```python
import torch

def _softmax(x, axis=-1):
    return torch.softmax(x, dim=axis)
def _gelu(x):
    return torch.nn.functional.gelu(x, approximate="tanh")
def _silu(x):
    return torch.nn.functional.silu(x)
def _sigmoid(x):
    return torch.sigmoid(x)
def _layernorm(x, axis=-1, eps=1e-5):
    m = x.mean(dim=axis, keepdim=True)
    v = x.var(dim=axis, keepdim=True, unbiased=False)
    return (x - m) / torch.sqrt(v + eps)
def _scatter_add(tgt, idx, src, axis=0):
    return tgt.clone().index_add_(axis, idx, src)
def _segment_sum(src, seg, n):
    out = torch.zeros((n,) + src.shape[1:], dtype=src.dtype)
    return out.index_add_(0, seg, src)
def _pad(x, pw):
    flat = tuple(v for pair in reversed(pw) for v in pair)
    return torch.nn.functional.pad(x, flat)

kernel_name = 'concat_heads__gqa_block_cached__gqa_block_cached__near_ridge'

def setup_inputs():

    _w = (lambda a: (lambda t: t.to(torch.bfloat16) if t.is_floating_point() else t)(torch.as_tensor(a)))
    def _A_setup():
        B, Sq, D = 8, 256, 640
        Hq, Hkv, Sk, Dh = 32, 32, 1024, 192
        assert Hq % Hkv == 0, (Hq, Hkv)
        return {
            "x": _w((lambda *s: torch.randn(*s, dtype=torch.bfloat16) * 0.8)(B, Sq, D)),
            "k_cache": _w((lambda *s: torch.randn(*s, dtype=torch.bfloat16) * 0.8)(B, Hkv, Sk, Dh)),
            "v_cache": _w((lambda *s: torch.randn(*s, dtype=torch.bfloat16) * 0.05)(B, Hkv, Sk, Dh)),
            "wq": _w((lambda *s: torch.randn(*s, dtype=torch.bfloat16) * 0.8)(D, Hq * Dh) * (D ** -0.5)),
            "wo": _w((lambda *s: torch.randn(*s, dtype=torch.bfloat16) * 0.05)(Hq * Dh, D) * ((Hq * Dh) ** -0.5)),
        }
    _A_in = _A_setup()

    def _B_setup():
        B, Sq, D = 8, 256, 640
        Hq, Hkv, Sk, Dh = 32, 32, 1024, 192
        assert Hq % Hkv == 0, (Hq, Hkv)
        return {
            "x": _w((lambda *s: torch.randn(*s, dtype=torch.bfloat16) * 0.8)(B, Sq, D)),
            "k_cache": _w((lambda *s: torch.randn(*s, dtype=torch.bfloat16) * 0.8)(B, Hkv, Sk, Dh)),
            "v_cache": _w((lambda *s: torch.randn(*s, dtype=torch.bfloat16) * 0.05)(B, Hkv, Sk, Dh)),
            "wq": _w((lambda *s: torch.randn(*s, dtype=torch.bfloat16) * 0.8)(D, Hq * Dh) * (D ** -0.5)),
            "wo": _w((lambda *s: torch.randn(*s, dtype=torch.bfloat16) * 0.05)(Hq * Dh, D) * ((Hq * Dh) ** -0.5)),
        }
    _B_in = _B_setup()

    return {
        "A_x": _A_in["x"],
        "A_k_cache": _A_in["k_cache"],
        "A_v_cache": _A_in["v_cache"],
        "A_wq": _A_in["wq"],
        "A_wo": _A_in["wo"],
        "B_x": _B_in["x"],
        "B_k_cache": _B_in["k_cache"],
        "B_v_cache": _B_in["v_cache"],
        "B_wq": _B_in["wq"],
        "B_wo": _B_in["wo"],
    }

def reference(A_x, A_k_cache, A_v_cache, A_wq, A_wo, B_x, B_k_cache, B_v_cache, B_wq, B_wo):
    def _A_ref(x, k_cache, v_cache, wq, wo):
        B, Sq, D = x.shape
        Hq, Dh = 32, 192
        Hkv = k_cache.shape[1]
        rep = Hq // Hkv
        q = (lambda x, *d: x.permute(*d))((x @ wq).reshape(B, Sq, Hq, Dh), 0, 2, 1, 3)
        k = (lambda x, n, axis: torch.repeat_interleave(x, n, dim=axis))(k_cache, rep, axis=1)
        v = (lambda x, n, axis: torch.repeat_interleave(x, n, dim=axis))(v_cache, rep, axis=1)
        scores = torch.einsum("bhqd,bhkd->bhqk", q, k) * (Dh ** -0.5)
        p = _softmax(scores, axis=-1)
        o = torch.einsum("bhqk,bhkd->bhqd", p, v)
        return (lambda x, *d: x.permute(*d))(o, 0, 2, 1, 3).reshape(B, Sq, Hq * Dh) @ wo

    def _B_ref(x, k_cache, v_cache, wq, wo):
        B, Sq, D = x.shape
        Hq, Dh = 32, 192
        Hkv = k_cache.shape[1]
        rep = Hq // Hkv
        q = (lambda x, *d: x.permute(*d))((x @ wq).reshape(B, Sq, Hq, Dh), 0, 2, 1, 3)
        k = (lambda x, n, axis: torch.repeat_interleave(x, n, dim=axis))(k_cache, rep, axis=1)
        v = (lambda x, n, axis: torch.repeat_interleave(x, n, dim=axis))(v_cache, rep, axis=1)
        scores = torch.einsum("bhqd,bhkd->bhqk", q, k) * (Dh ** -0.5)
        p = _softmax(scores, axis=-1)
        o = torch.einsum("bhqk,bhkd->bhqd", p, v)
        return (lambda x, *d: x.permute(*d))(o, 0, 2, 1, 3).reshape(B, Sq, Hq * Dh) @ wo

    a = _A_ref(A_x, A_k_cache, A_v_cache, A_wq, A_wo)
    b = _B_ref(B_x, B_k_cache, B_v_cache, B_wq, B_wo)
    return torch.concatenate([a, b], axis=-1)
```

```cpp
#include <hip/hip_runtime.h>
#include <hip/hip_bf16.h>
#include <stdint.h>

typedef __hip_bfloat16 bf16;

constexpr int LDS_BYTES = 163840;

__global__ __launch_bounds__(256, 1) void attn_kernel(const bf16* XA, const bf16* WqA, const bf16* KA, const bf16* VA, const bf16* WoA,
                                                      const bf16* XB, const bf16* WqB, const bf16* KB, const bf16* VB, const bf16* WoB,
                                                      void* ws, bf16* out) {
  __shared__ __attribute__((aligned(1024))) char lds[LDS_BYTES];
  uint64_t args = (uint64_t)__builtin_amdgcn_kernarg_segment_ptr();
  uint32_t tid = threadIdx.x, bid = blockIdx.x, lds0 = (uint32_t)(uintptr_t)&lds[0];
  asm volatile(
"s_bfe_u32 s8, s2, 0x10002\n"
"s_load_dwordx4 s[56:59], s[0:1], 80\n"
"s_mul_i32 s12, s8, 40\n"
"v_readfirstlane_b32 s4, v0\n"
"s_add_u32 s86, s0, s12\n"
"s_addc_u32 s87, s1, 0\n"
"s_load_dwordx2 s[52:53], s[86:87], 0\n"
"s_load_dwordx2 s[48:49], s[86:87], 8\n"
"s_load_dwordx4 s[40:43], s[86:87], 16\n"
"s_load_dwordx2 s[28:29], s[86:87], 32\n"
"s_movk_i32 s11, 8192\n"
"s_lshr_b32 s4, s4, 6\n"
"s_bfe_u32 s6, s2, 0x20003\n"
"s_mul_i32 s77, s4, 3072\n"
"v_lshlrev_b32 v198, 6, v0\n"
"s_mov_b32 m0, s77\n"
"s_getpc_b64 s[98:99]\n"
"v_lshl_or_b32 v198, s6, 14, v198\n"
"global_load_lds_dword v198, s[98:99]\n"
"s_and_b32 s5, s2, 3\n"
"s_lshr_b32 s7, s2, 5\n"
"v_and_b32 v192, 63, v0\n"
"v_bfe_u32 v193, v0, 4, 2\n"
"v_bfe_u32 v199, v0, 3, 3\n"
"s_lshl3_add_u32 s3, s5, s6\n"
"s_lshl2_add_u32 s12, s7, s4\n"
"v_mul_u32_u24 v199, 1280, v199\n"
"v_bitop3_b32 v200, v0, v193, 7 bitop3:0x6c\n"
"s_lshr_b32 s64, s5, 1\n"
"s_and_b32 s10, s5, 1\n"
"s_lshl1_add_u32 s12, s12, s64\n"
"s_mul_i32 s10, s10, 20480\n"
"s_mul_i32 s75, s12, 40960\n"
"s_lshl_b32 s36, s4, 13\n"
"v_lshl_or_b32 v228, v200, 4, v199\n"
"v_bfe_u32 v195, v0, 2, 2\n"
"s_cmp_lt_u32 s4, 2\n"
"s_cselect_b32 s37, 98304, s11\n"
"v_xor_b32 v229, 64, v228\n"
"v_and_b32 v196, 3, v0\n"
"s_xor_b32 s12, s10, 20480\n"
"s_add_u32 s10, s10, 2048\n"
"v_add_u32 v229, 9216, v229\n"
"s_add_u32 s37, s37, s36\n"
"v_add_u32 v230, s12, v228\n"
"v_add_u32 v231, s12, v229\n"
"v_add_u32 v228, s10, v228\n"
"v_add_u32 v229, s10, v229\n"
"s_mul_i32 s64, s64, 4294885376\n"
"s_add_u32 s64, s64, 40960\n"
"s_mov_b32 s54, 2623488\n"
"s_mov_b32 s55, 0x20000\n"
"v_lshrrev_b32 v199, 2, v0\n"
"v_lshlrev_b32 v196, 4, v196\n"
"v_and_b32 v200, 1, v193\n"
"v_mul_u32_u24 v199, 12288, v199\n"
"v_mul_u32_u24 v200, 49088, v200\n"
"s_movk_i32 s12, 2048\n"
"v_add3_u32 v199, v199, v196, s12\n"
"s_mul_i32 s74, s3, 384\n"
"s_lshl_b32 s79, s4, 11\n"
"v_sub_u32 v242, v199, v200\n"
"s_mov_b32 s50, 7866368\n"
"s_mov_b32 s51, 0x20000\n"
"s_add_u32 s79, s79, 114688\n"
"v_add_u32 v243, 48128, v242\n"
"s_add_u32 s68, s36, 73728\n"
"s_cmp_lt_u32 s4, 3\n"
"s_cselect_b32 s68, s68, 40960\n"
"s_mov_b32 s65, s75\n"
"s_add_u32 s69, s74, 1536\n"
"v_lshlrev_b32 v251, 4, v192\n"
"v_and_b32 v194, 15, v192\n"
"s_add_u32 s78, s77, 49152\n"
"s_lshl3_add_u32 s9, s8, s7\n"
"v_lshrrev_b32 v199, 1, v194\n"
"v_add_u32 v253, 4096, v251\n"
"v_xor_b32 v199, v199, v193\n"
"v_mov_b32 v252, v251\n"
"v_lshlrev_b32 v199, 4, v199\n"
"s_movk_i32 s12, 384\n"
"v_lshl_add_u32 v200, v194, 7, v199\n"
"v_bitop3_b32 v199, v194, v193, 6 bitop3:0x6c\n"
"v_lshlrev_b32 v199, 4, v199\n"
"v_mad_u32_u24 v245, v194, s12, v199\n"
"v_add_u32 v232, s37, v200\n"
"v_add_u32 v217, s68, v200\n"
"v_xor_b32 v246, 64, v245\n"
"v_xor_b32 v233, 64, v232\n"
"v_xor_b32 v219, 64, v217\n"
"v_lshrrev_b32 v197, 1, v193\n"
"v_lshl_add_u32 v199, v193, 8, v251\n"
"v_lshl_or_b32 v196, v193, 6, v196\n"
"v_lshl_add_u32 v199, v197, 10, v199\n"
"v_lshlrev_b32 v200, 6, v193\n"
"v_xad_u32 v244, v251, v200, s78\n"
"v_and_b32 v200, 3, v0\n"
"v_lshrrev_b32 v221, 2, v251\n"
"v_mul_u32_u24 v200, 0x144, v200\n"
"v_xad_u32 v221, v221, v200, s78\n"
"v_add_u32 v250, 114688, v199\n"
"v_lshrrev_b32 v201, 2, v192\n"
"v_mul_u32_u24 v199, 1366, v192\n"
"v_mul_u32_u24 v247, s12, v201\n"
"v_add_u32 v200, 87424, v199\n"
"v_lshlrev_b32 v248, 4, v201\n"
"v_add_u32 v201, 174848, v199\n"
"s_movk_i32 s12, 96\n"
"v_lshrrev_b32 v199, 11, v199\n"
"v_lshrrev_b32 v200, 11, v200\n"
"v_lshrrev_b32 v201, 11, v201\n"
"s_mov_b32 m0, s37\n"
"s_waitcnt lgkmcnt(0)\n"
"s_add_u32 s52, s52, 4294965248\n"
"s_addc_u32 s53, s53, -1\n"
"buffer_load_dwordx4 v228, s[52:55], s75 offen offset:0 lds\n"
"v_bitop3_b32 v236, v199, v251, s12 bitop3:0x6c\n"
"v_bitop3_b32 v237, v200, v251, s12 bitop3:0x6c\n"
"v_bitop3_b32 v238, v201, v251, s12 bitop3:0x6c\n"
"v_bitop3_b32 v247, v248, v247, s12 bitop3:0x6c\n"
"v_and_b32 v199, 3, v192\n"
"v_and_b32 v200, 16, v192\n"
"v_lshlrev_b32 v199, 3, v199\n"
"v_add_u32 v247, 49152, v247\n"
"v_add_u32 v241, 30720, v238\n"
"v_add_u32 v247, v247, v199\n"
"buffer_load_dwordx4 v229, s[52:55], s75 offen offset:1024 lds\n"
"v_lshl_or_b32 v199, v200, 1, v199\n"
"v_xor_b32 v240, s12, v247\n"
"v_lshl_or_b32 v199, v195, 6, v199\n"
"s_movk_i32 s12, 1280\n"
"v_xor_b32 v239, 64, v247\n"
"v_mad_u32_u24 v235, v193, s12, v199\n"
"v_xor_b32 v248, 32, v247\n"
"v_xor_b32 v249, 32, v235\n"
"v_mad_u32_u24 v199, v195, s12, v196\n"
"s_mul_i32 s12, s4, 20480\n"
"buffer_load_dwordx4 v230, s[52:55], s75 offen offset:2048 lds\n"
"s_add_u32 s12, s12, 3904\n"
"v_add_u32 v224, s12, v199\n"
"v_xor_b32 v199, 32, v224\n"
"v_cmp_lt_u32 vcc, 15, v192\n"
"v_add_u32 v199, 4800, v199\n"
"v_add_u32 v200, 9600, v224\n"
"v_cndmask_b32 v225, v224, v199, vcc\n"
"v_cmp_lt_u32 vcc, 31, v192\n"
"v_add_u32 v225, 4294966528, v225\n"
"v_cndmask_b32 v226, v199, v200, vcc\n"
"buffer_load_dwordx4 v231, s[52:55], s75 offen offset:3072 lds\n"
"v_cmp_lt_u32 vcc, 47, v192\n"
"v_add_u32 v226, 4294965760, v226\n"
"v_cndmask_b32 v227, v224, v199, vcc\n"
"v_add_u32 v234, 10624, v199\n"
"v_add_u32 v227, 7296, v227\n"
"s_mov_b32 s12, 858993459\n"
"s_cmp_eq_u32 s5, 1\n"
"s_cselect_b32 s12, 1111696420, s12\n"
"s_cmp_eq_u32 s5, 2\n"
"s_cselect_b32 s12, 357765461, s12\n"
"s_cmp_eq_u32 s5, 3\n"
"s_cselect_b32 s12, 610468422, s12\n"
"v_lshlrev_b32 v199, 2, v193\n"
"s_add_u32 m0, s37, 4096\n"
"s_add_u32 s97, s75, s64\n"
"buffer_load_dwordx4 v228, s[52:55], s97 offen offset:0 lds\n"
"v_lshrrev_b32 v199, v199, s12\n"
"v_bfe_u32 v200, v199, 0, 4\n"
"v_lshl_add_u32 v224, v200, 6, v224\n"
"v_bfe_u32 v200, v199, 16, 4\n"
"v_lshl_add_u32 v225, v200, 6, v225\n"
"v_bfe_u32 v200, v199, 12, 4\n"
"v_lshl_add_u32 v226, v200, 6, v226\n"
"v_bfe_u32 v200, v199, 8, 4\n"
"v_lshl_add_u32 v227, v200, 6, v227\n"
"v_bfe_u32 v200, v199, 4, 4\n"
"buffer_load_dwordx4 v229, s[52:55], s97 offen offset:1024 lds\n"
"v_lshl_add_u32 v234, v200, 6, v234\n"
"s_lshl_b32 s12, s7, 5\n"
"s_add_u32 s12, s12, s3\n"
"s_lshl2_add_u32 s12, s12, s4\n"
"s_mul_i32 s60, s12, 98304\n"
"s_lshr_b32 s12, 9, s4\n"
"s_and_b32 s12, s12, 1\n"
"s_mul_i32 s12, s12, 384\n"
"s_add_u32 s16, s12, 103296\n"
"s_cmp_eq_u32 s4, 2\n"
"s_cselect_b32 s12, 384, 0\n"
"buffer_load_dwordx4 v230, s[52:55], s97 offen offset:2048 lds\n"
"s_add_u32 s17, s12, 103296\n"
"s_cmp_eq_u32 s4, 0\n"
"s_cselect_b32 s12, 0, 384\n"
"s_mul_i32 s62, s4, 4294901632\n"
"s_add_u32 s62, s62, s12\n"
"s_add_u32 s62, s62, 1920\n"
"s_add_u32 s12, s16, s17\n"
"s_add_u32 s12, s12, s62\n"
"s_add_u32 s84, s12, 86016\n"
"s_mov_b32 s85, 0\n"
"s_add_u32 s61, s60, 1572864\n"
"buffer_load_dwordx4 v231, s[52:55], s97 offen offset:3072 lds\n"
"s_mul_i32 s10, s9, 32768\n"
"s_mul_i32 s12, s4, 8192\n"
"s_add_u32 s10, s10, s12\n"
"s_mul_i32 s12, s3, 1572864\n"
"s_add_u32 s66, s12, s10\n"
"s_add_u32 s67, s66, 6291456\n"
"s_mul_i32 s12, s7, 11\n"
"s_lshr_b32 s12, s12, 5\n"
"s_mul_i32 s12, s12, 786048\n"
"s_lshl_b32 s36, s7, 7\n"
"s_add_u32 s75, s75, 128\n"
"s_add_u32 s48, s48, 4294965248\n"
"s_addc_u32 s49, s49, -1\n"
"s_mov_b32 m0, s79\n"
"s_nop 0\n"
"buffer_load_dwordx4 v242, s[48:51], s74 offen offset:0 lds\n"
"s_add_u32 s12, s12, s36\n"
"s_add_u32 s33, s12, s69\n"
"s_mul_i32 s36, s7, 786432\n"
"s_sub_u32 s36, s36, s12\n"
"s_add_u32 s34, s36, 1572864\n"
"s_mul_i32 s36, s5, 1966080\n"
"s_mul_i32 s12, s6, 320\n"
"s_add_u32 s12, s12, s36\n"
"v_writelane_b32 v255, s12, 0\n"
"s_lshr_b32 s12, s2, 3\n"
"buffer_load_dwordx4 v243, s[48:51], s74 offen offset:1024 lds\n"
"s_lshl2_add_u32 s12, s12, s4\n"
"s_mul_i32 s12, s12, 2560\n"
"s_add_u32 s12, s12, s36\n"
"s_add_u32 s12, s12, 4096\n"
"v_writelane_b32 v255, s12, 30\n"
"s_mul_i32 s12, s5, 12582912\n"
"s_add_u32 s12, s12, s10\n"
"v_writelane_b32 v255, s12, 2\n"
"s_lshl2_add_u32 s10, s9, s6\n"
"s_lshl2_add_u32 s10, s10, s4\n"
"s_add_u32 s74, s74, 128\n"
"s_add_u32 m0, s79, 8192\n"
"s_nop 0\n"
"buffer_load_dwordx4 v242, s[48:51], s74 offen offset:0 lds\n"
"s_mul_i32 s36, s10, 20480\n"
"s_mul_i32 s12, s5, 5242880\n"
"s_add_u32 s12, s12, s36\n"
"v_writelane_b32 v255, s12, 4\n"
"s_mul_i32 s12, s5, 5120\n"
"s_add_u32 s12, s12, s36\n"
"v_writelane_b32 v255, s12, 5\n"
"s_lshl_b32 s10, s10, 7\n"
"s_add_u32 s10, s10, 36864\n"
"s_lshl4_add_u32 s12, s5, s10\n"
"buffer_load_dwordx4 v243, s[48:51], s74 offen offset:1024 lds\n"
"v_writelane_b32 v255, s12, 24\n"
"s_xor_b32 s12, s5, 3\n"
"s_lshl4_add_u32 s12, s12, s10\n"
"s_add_u32 s12, s12, 4\n"
"v_writelane_b32 v255, s12, 12\n"
"s_xor_b32 s12, s5, 2\n"
"s_lshl4_add_u32 s12, s12, s10\n"
"s_add_u32 s12, s12, 8\n"
"v_writelane_b32 v255, s12, 13\n"
"s_xor_b32 s12, s5, 1\n"
"s_add_u32 s74, s74, 128\n"
"s_mov_b32 m0, s68\n"
"s_nop 0\n"
"buffer_load_dwordx4 v228, s[52:55], s75 offen offset:0 lds\n"
"s_lshl4_add_u32 s12, s12, s10\n"
"s_add_u32 s12, s12, 12\n"
"v_writelane_b32 v255, s12, 15\n"
"s_lshl2_add_u32 s12, s7, s4\n"
"s_lshl2_add_u32 s12, s12, s5\n"
"s_mul_i32 s12, s12, 40960\n"
"s_lshl2_add_u32 s36, s8, s6\n"
"s_mul_i32 s36, s36, 320\n"
"s_add_u32 s12, s12, s36\n"
"v_writelane_b32 v255, s12, 6\n"
"s_lshl_b32 s12, s9, 7\n"
"v_writelane_b32 v255, s12, 7\n"
"buffer_load_dwordx4 v229, s[52:55], s75 offen offset:1024 lds\n"
"v_mov_b32 v216, s12\n"
"s_lshl2_add_u32 s12, s9, s5\n"
"s_lshl2_add_u32 s12, s12, s4\n"
"s_lshl_b32 s12, s12, 7\n"
"s_add_u32 s12, s12, 4096\n"
"v_writelane_b32 v255, s12, 10\n"
"s_lshl2_add_u32 s36, s6, s12\n"
"v_writelane_b32 v255, s36, 8\n"
"s_add_u32 s12, s12, 16\n"
"v_writelane_b32 v255, s12, 11\n"
"s_add_u32 s36, s36, 16\n"
"v_writelane_b32 v255, s36, 9\n"
"buffer_load_dwordx4 v230, s[52:55], s75 offen offset:2048 lds\n"
"s_lshl2_add_u32 s12, s2, s4\n"
"s_lshl2_add_u32 s12, s12, 69632\n"
"v_writelane_b32 v255, s12, 14\n"
"s_mul_i32 s12, s4, 5120\n"
"v_writelane_b32 v255, s12, 26\n"
"v_writelane_b32 v255, s5, 27\n"
"s_mul_i32 s12, s6, 245760\n"
"s_add_u32 s12, s12, 819200\n"
"v_writelane_b32 v255, s12, 1\n"
"v_writelane_b32 v255, s6, 3\n"
"s_mov_b32 s72, s60\n"
"s_mov_b32 s73, s60\n"
"buffer_load_dwordx4 v231, s[52:55], s75 offen offset:3072 lds\n"
"s_mov_b32 s88, 0x3dd53b95\n"
"s_mov_b32 s89, s88\n"
"s_mov_b32 s81, 0\n"
"s_mov_b32 s76, 0xf0000000\n"
"s_mov_b32 s26, 2\n"
"s_mov_b32 s18, s76\n"
"s_mov_b32 s24, s76\n"
"s_mov_b32 s19, s77\n"
"s_mov_b32 s25, s78\n"
"s_mov_b32 s80, 4\n"
"s_mov_b32 s70, 16777216\n"
"s_mov_b32 s91, s76\n"
"s_add_u32 m0, s68, 4096\n"
"s_add_u32 s97, s75, s64\n"
"buffer_load_dwordx4 v228, s[52:55], s97 offen offset:0 lds\n"
"s_mov_b32 s27, s76\n"
"s_mov_b64 s[14:15], s[56:57]\n"
"v_writelane_b32 v255, s58, 20\n"
"v_writelane_b32 v255, s59, 21\n"
"s_add_u32 s58, s56, 50462720\n"
"s_addc_u32 s59, s57, 0\n"
"v_writelane_b32 v255, s58, 16\n"
"v_writelane_b32 v255, s59, 17\n"
"s_add_u32 s56, s56, 131072\n"
"s_addc_u32 s57, s57, 0\n"
"s_mov_b32 s58, 50331648\n"
"s_mov_b32 s59, s55\n"
"s_mov_b64 s[44:45], s[42:43]\n"
"s_mov_b32 s43, s55\n"
"buffer_load_dwordx4 v229, s[52:55], s97 offen offset:1024 lds\n"
"s_mov_b32 s47, s55\n"
"s_add_u32 s28, s28, 4294963200\n"
"s_addc_u32 s29, s29, -1\n"
"s_mov_b32 s30, 7868416\n"
"s_mov_b32 s31, s55\n"
"s_mov_b64 s[20:21], s[28:29]\n"
"s_mov_b64 s[22:23], s[30:31]\n"
"s_add_u32 s42, s60, s84\n"
"s_mov_b32 s46, s42\n"
"s_mul_i32 s12, s2, 1\n"
"s_and_b32 s12, s12, 255\n"
"s_mul_i32 s36, s12, 205\n"
"s_lshr_b32 s36, s36, 10\n"
"buffer_load_dwordx4 v230, s[52:55], s97 offen offset:2048 lds\n"
"s_mul_i32 s36, s36, 5\n"
"s_sub_u32 s10, s12, s36\n"
"s_add_u32 s12, s16, s17\n"
"s_sub_u32 s9, 4294884352, s12\n"
"s_mul_i32 s12, s10, 4294961152\n"
"s_add_u32 s62, s62, s12\n"
"s_add_u32 s62, s62, 24576\n"
"s_sub_u32 s10, s10, 1\n"
"s_cselect_b32 s0, s16, 3072\n"
"s_cselect_b32 s10, 4, s10\n"
"s_cselect_b32 s16, s17, s16\n"
"s_cselect_b32 s17, s9, s17\n"
"s_cselect_b32 s9, s0, s9\n"
"s_sub_u32 s10, s10, 1\n"
"s_cselect_b32 s1, s16, 3072\n"
"s_cselect_b32 s10, 4, s10\n"
"s_cselect_b32 s16, s17, s16\n"
"s_cselect_b32 s17, s9, s17\n"
"s_cselect_b32 s9, s1, s9\n"
"v_mov_b64 v[190:191], 0\n"
"v_mov_b64 v[188:189], 0\n"
"v_mov_b64 v[186:187], 0\n"
"buffer_load_dwordx4 v231, s[52:55], s97 offen offset:3072 lds\n"
"v_mov_b64 v[184:185], 0\n"
"v_mov_b64 v[182:183], 0\n"
"v_mov_b64 v[180:181], 0\n"
"v_mov_b64 v[178:179], 0\n"
"v_mov_b64 v[176:177], 0\n"
"v_mov_b64 v[174:175], 0\n"
"v_mov_b64 v[172:173], 0\n"
"v_mov_b64 v[170:171], 0\n"
"v_mov_b64 v[168:169], 0\n"
"v_mov_b64 v[166:167], 0\n"
"v_mov_b64 v[164:165], 0\n"
"v_mov_b64 v[162:163], 0\n"
"s_add_u32 s75, s75, 128\n"
"s_add_u32 m0, s79, 16384\n"
"s_nop 0\n"
"buffer_load_dwordx4 v242, s[48:51], s74 offen offset:0 lds\n"
"v_mov_b64 v[160:161], 0\n"
"v_mov_b64 v[158:159], 0\n"
"v_mov_b64 v[156:157], 0\n"
"v_mov_b64 v[154:155], 0\n"
"v_mov_b64 v[152:153], 0\n"
"v_mov_b64 v[150:151], 0\n"
"v_mov_b64 v[148:149], 0\n"
"v_mov_b64 v[146:147], 0\n"
"v_mov_b64 v[144:145], 0\n"
"v_mov_b64 v[142:143], 0\n"
"v_mov_b64 v[140:141], 0\n"
"v_mov_b64 v[138:139], 0\n"
"buffer_load_dwordx4 v243, s[48:51], s74 offen offset:1024 lds\n"
"v_mov_b64 v[136:137], 0\n"
"v_mov_b64 v[134:135], 0\n"
"v_mov_b64 v[132:133], 0\n"
"v_mov_b64 v[130:131], 0\n"
"v_mov_b64 v[128:129], 0\n"
"v_mov_b64 v[126:127], 0\n"
"v_mov_b64 v[124:125], 0\n"
"v_mov_b64 v[122:123], 0\n"
"v_mov_b64 v[120:121], 0\n"
"v_mov_b64 v[118:119], 0\n"
"v_mov_b64 v[116:117], 0\n"
"v_mov_b64 v[114:115], 0\n"
"s_add_u32 s74, s74, 786176\n"
"s_add_u32 m0, s79, 24576\n"
"s_nop 0\n"
"buffer_load_dwordx4 v242, s[48:51], s74 offen offset:0 lds\n"
"v_mov_b64 v[112:113], 0\n"
"v_mov_b64 v[110:111], 0\n"
"v_mov_b64 v[108:109], 0\n"
"v_mov_b64 v[106:107], 0\n"
"v_mov_b64 v[104:105], 0\n"
"v_mov_b64 v[102:103], 0\n"
"v_mov_b64 v[100:101], 0\n"
"v_mov_b64 v[98:99], 0\n"
"v_mov_b64 v[96:97], 0\n"
"v_mov_b64 v[94:95], 0\n"
"v_mov_b64 v[92:93], 0\n"
"v_mov_b64 v[90:91], 0\n"
"buffer_load_dwordx4 v243, s[48:51], s74 offen offset:1024 lds\n"
"v_mov_b64 v[88:89], 0\n"
"v_mov_b64 v[86:87], 0\n"
"v_mov_b64 v[84:85], 0\n"
"v_mov_b64 v[82:83], 0\n"
"v_mov_b64 v[80:81], 0\n"
"v_mov_b64 v[78:79], 0\n"
"v_mov_b64 v[76:77], 0\n"
"v_mov_b64 v[74:75], 0\n"
"v_mov_b64 v[72:73], 0\n"
"v_mov_b64 v[70:71], 0\n"
"v_mov_b64 v[68:69], 0\n"
"v_mov_b64 v[66:67], 0\n"
"s_add_u32 s74, s74, 128\n"
"s_add_u32 m0, s79, 32768\n"
"s_nop 0\n"
"buffer_load_dwordx4 v242, s[48:51], s74 offen offset:0 lds\n"
"v_mov_b64 v[64:65], 0\n"
"v_mov_b64 v[62:63], 0\n"
"v_mov_b64 v[60:61], 0\n"
"v_mov_b64 v[58:59], 0\n"
"v_mov_b64 v[56:57], 0\n"
"v_mov_b64 v[54:55], 0\n"
"v_mov_b64 v[52:53], 0\n"
"v_mov_b64 v[50:51], 0\n"
"v_mov_b64 v[48:49], 0\n"
"v_mov_b64 v[46:47], 0\n"
"v_mov_b64 v[44:45], 0\n"
"v_mov_b64 v[42:43], 0\n"
"buffer_load_dwordx4 v243, s[48:51], s74 offen offset:1024 lds\n"
"v_mov_b64 v[40:41], 0\n"
"v_mov_b64 v[38:39], 0\n"
"v_mov_b64 v[36:37], 0\n"
"v_mov_b64 v[34:35], 0\n"
"v_mov_b64 v[32:33], 0\n"
"v_mov_b64 v[30:31], 0\n"
"v_mov_b64 v[28:29], 0\n"
"v_mov_b64 v[26:27], 0\n"
"v_mov_b64 v[24:25], 0\n"
"v_mov_b64 v[22:23], 0\n"
"v_mov_b64 v[20:21], 0\n"
"v_mov_b64 v[18:19], 0\n"
"s_add_u32 s74, s74, 128\n"
"v_mov_b64 v[16:17], 0\n"
"v_mov_b64 v[14:15], 0\n"
"v_mov_b64 v[12:13], 0\n"
"v_mov_b64 v[10:11], 0\n"
"v_mov_b64 v[8:9], 0\n"
"v_mov_b64 v[6:7], 0\n"
"v_mov_b64 v[4:5], 0\n"
"v_mov_b64 v[2:3], 0\n"
"v_mov_b64 v[0:1], 0\n"
"global_load_dword v222, v216, s[14:15] sc1\n"
"buffer_load_dword v223, v252, s[20:23], s91 offen\n"
"buffer_load_dword v223, v252, s[20:23], s91 offen\n"
"buffer_load_dword v223, v252, s[20:23], s91 offen\n"
"s_waitcnt vmcnt(18)\n"
"s_barrier\n"
"s_memtime s[94:95]\n"
"ds_read_b128 a[192:195], v232 offset:0\n"
"ds_read_b64_tr_b16 v[192:193], v250 offset:0\n"
"ds_read_b64_tr_b16 v[194:195], v250 offset:1024\n"
"ds_read_b128 a[208:211], v233 offset:0\n"
"ds_read_b64_tr_b16 v[196:197], v250 offset:4096\n"
"ds_read_b64_tr_b16 v[198:199], v250 offset:5120\n"
"ds_read_b128 a[212:215], v233 offset:2048\n"
"ds_read_b128 a[196:199], v232 offset:2048\n"
"ds_read_b128 a[200:203], v232 offset:4096\n"
"ds_read_b128 a[216:219], v233 offset:4096\n"
"ds_read_b128 a[220:223], v233 offset:6144\n"
"ds_read_b128 a[204:207], v232 offset:6144\n"
"ds_read_b64_tr_b16 v[200:201], v250 offset:8\n"
"ds_read_b64_tr_b16 v[202:203], v250 offset:1032\n"
".Lat_q:\n"
"s_waitcnt lgkmcnt(4)\n"
"v_mfma_f32_16x16x32_bf16 v[0:3], v[192:195], a[192:195], v[0:3]\n"
"s_waitcnt lgkmcnt(2)\n"
"ds_read_b64_tr_b16 v[204:205], v250 offset:4104\n"
"v_mfma_f32_16x16x32_bf16 v[0:3], v[196:199], a[208:211], v[0:3]\n"
"v_cmp_le_i32 s[98:99], s85, v237\n"
"v_mfma_f32_16x16x32_bf16 v[48:51], v[196:199], a[212:215], v[48:51]\n"
"s_mov_b32 m0, s37\n"
"ds_read_b64_tr_b16 v[206:207], v250 offset:5128\n"
"v_mfma_f32_16x16x32_bf16 v[48:51], v[192:195], a[196:199], v[48:51]\n"
"buffer_load_dwordx4 v228, s[52:55], s75 offen offset:0 lds\n"
"v_mfma_f32_16x16x32_bf16 v[96:99], v[192:195], a[200:203], v[96:99]\n"
"ds_read_b64_tr_b16 a[4:5], v250 offset:256\n"
"v_add_u32 v210, 30720, v237\n"
"v_mfma_f32_16x16x32_bf16 v[96:99], v[196:199], a[216:219], v[96:99]\n"
"v_mfma_f32_16x16x32_bf16 v[144:147], v[196:199], a[220:223], v[144:147]\n"
"ds_read_b64_tr_b16 a[6:7], v250 offset:1280\n"
"v_cndmask_b32 v210, v237, v210, s[98:99]\n"
"v_mfma_f32_16x16x32_bf16 v[144:147], v[192:195], a[204:207], v[144:147]\n"
"s_waitcnt lgkmcnt(4)\n"
"buffer_load_dwordx4 v229, s[52:55], s75 offen offset:1024 lds\n"
"v_mfma_f32_16x16x32_bf16 v[148:151], v[200:203], a[204:207], v[148:151]\n"
"s_waitcnt lgkmcnt(2)\n"
"ds_read_b64_tr_b16 a[52:53], v250 offset:4352\n"
"v_mfma_f32_16x16x32_bf16 v[148:151], v[204:207], a[220:223], v[148:151]\n"
"v_mfma_f32_16x16x32_bf16 v[100:103], v[204:207], a[216:219], v[100:103]\n"
"ds_read_b64_tr_b16 a[54:55], v250 offset:5376\n"
"v_mfma_f32_16x16x32_bf16 v[100:103], v[200:203], a[200:203], v[100:103]\n"
"buffer_load_dwordx4 v230, s[52:55], s75 offen offset:2048 lds\n"
"v_mfma_f32_16x16x32_bf16 v[52:55], v[200:203], a[196:199], v[52:55]\n"
"ds_read_b64_tr_b16 v[192:193], v250 offset:264\n"
"v_mfma_f32_16x16x32_bf16 v[52:55], v[204:207], a[212:215], v[52:55]\n"
"v_mfma_f32_16x16x32_bf16 v[4:7], v[204:207], a[208:211], v[4:7]\n"
"ds_read_b64_tr_b16 v[194:195], v250 offset:1288\n"
"v_mfma_f32_16x16x32_bf16 v[4:7], v[200:203], a[192:195], v[4:7]\n"
"s_waitcnt lgkmcnt(4)\n"
"buffer_load_dwordx4 v231, s[52:55], s75 offen offset:3072 lds\n"
"v_mfma_f32_16x16x32_bf16 v[8:11], a[4:7], a[192:195], v[8:11]\n"
"s_waitcnt lgkmcnt(2)\n"
"ds_read_b64_tr_b16 v[196:197], v250 offset:4360\n"
"v_mfma_f32_16x16x32_bf16 v[8:11], a[52:55], a[208:211], v[8:11]\n"
"s_add_u32 m0, s37, 4096\n"
"v_mfma_f32_16x16x32_bf16 v[56:59], a[52:55], a[212:215], v[56:59]\n"
"s_add_u32 s97, s75, s64\n"
"ds_read_b64_tr_b16 v[198:199], v250 offset:5384\n"
"v_mfma_f32_16x16x32_bf16 v[56:59], a[4:7], a[196:199], v[56:59]\n"
"buffer_load_dwordx4 v228, s[52:55], s97 offen offset:0 lds\n"
"v_mfma_f32_16x16x32_bf16 v[104:107], a[4:7], a[200:203], v[104:107]\n"
"ds_read_b64_tr_b16 v[200:201], v250 offset:8192\n"
"v_mfma_f32_16x16x32_bf16 v[104:107], a[52:55], a[216:219], v[104:107]\n"
"v_mfma_f32_16x16x32_bf16 v[152:155], a[52:55], a[220:223], v[152:155]\n"
"ds_read_b64_tr_b16 v[202:203], v250 offset:9216\n"
"v_mfma_f32_16x16x32_bf16 v[152:155], a[4:7], a[204:207], v[152:155]\n"
"s_waitcnt lgkmcnt(4)\n"
"buffer_load_dwordx4 v229, s[52:55], s97 offen offset:1024 lds\n"
"v_mfma_f32_16x16x32_bf16 v[156:159], v[192:195], a[204:207], v[156:159]\n"
"s_waitcnt lgkmcnt(2)\n"
"ds_read_b64_tr_b16 v[204:205], v250 offset:12288\n"
"v_mfma_f32_16x16x32_bf16 v[156:159], v[196:199], a[220:223], v[156:159]\n"
"v_mfma_f32_16x16x32_bf16 v[108:111], v[196:199], a[216:219], v[108:111]\n"
"ds_read_b64_tr_b16 v[206:207], v250 offset:13312\n"
"v_mfma_f32_16x16x32_bf16 v[108:111], v[192:195], a[200:203], v[108:111]\n"
"buffer_load_dwordx4 v230, s[52:55], s97 offen offset:2048 lds\n"
"v_mfma_f32_16x16x32_bf16 v[60:63], v[192:195], a[196:199], v[60:63]\n"
"ds_read_b64_tr_b16 a[4:5], v250 offset:8200\n"
"v_mfma_f32_16x16x32_bf16 v[60:63], v[196:199], a[212:215], v[60:63]\n"
"v_mfma_f32_16x16x32_bf16 v[12:15], v[196:199], a[208:211], v[12:15]\n"
"ds_read_b64_tr_b16 a[6:7], v250 offset:9224\n"
"v_mfma_f32_16x16x32_bf16 v[12:15], v[192:195], a[192:195], v[12:15]\n"
"s_waitcnt lgkmcnt(4)\n"
"buffer_load_dwordx4 v231, s[52:55], s97 offen offset:3072 lds\n"
"v_mfma_f32_16x16x32_bf16 v[16:19], v[200:203], a[192:195], v[16:19]\n"
"s_waitcnt lgkmcnt(2)\n"
"s_add_u32 s75, s75, 128\n"
"ds_read_b64_tr_b16 a[52:53], v250 offset:12296\n"
"v_mfma_f32_16x16x32_bf16 v[16:19], v[204:207], a[208:211], v[16:19]\n"
"s_waitcnt vmcnt(16)\n"
"s_barrier\n"
"v_mfma_f32_16x16x32_bf16 v[64:67], v[204:207], a[212:215], v[64:67]\n"
"s_add_u32 m0, s79, 40960\n"
"ds_read_b64_tr_b16 a[54:55], v250 offset:13320\n"
"v_mfma_f32_16x16x32_bf16 v[64:67], v[200:203], a[196:199], v[64:67]\n"
"buffer_load_dwordx4 v242, s[48:51], s74 offen offset:0 lds\n"
"v_mfma_f32_16x16x32_bf16 v[112:115], v[200:203], a[200:203], v[112:115]\n"
"ds_read_b64_tr_b16 v[192:193], v250 offset:8448\n"
"v_mfma_f32_16x16x32_bf16 v[112:115], v[204:207], a[216:219], v[112:115]\n"
"buffer_load_dwordx4 v243, s[48:51], s74 offen offset:1024 lds\n"
"v_mfma_f32_16x16x32_bf16 v[160:163], v[204:207], a[220:223], v[160:163]\n"
"ds_read_b64_tr_b16 v[194:195], v250 offset:9472\n"
"v_mfma_f32_16x16x32_bf16 v[160:163], v[200:203], a[204:207], v[160:163]\n"
"s_waitcnt lgkmcnt(4)\n"
"v_mfma_f32_16x16x32_bf16 v[164:167], a[4:7], a[204:207], v[164:167]\n"
"s_waitcnt lgkmcnt(2)\n"
"s_add_u32 s74, s74, 786176\n"
"ds_read_b64_tr_b16 v[196:197], v250 offset:12544\n"
"v_mfma_f32_16x16x32_bf16 v[164:167], a[52:55], a[220:223], v[164:167]\n"
"buffer_load_dword v223, v252, s[20:23], s91 offen\n"
"v_mfma_f32_16x16x32_bf16 v[116:119], a[52:55], a[216:219], v[116:119]\n"
"s_add_u32 s91, s91, s70\n"
"ds_read_b64_tr_b16 v[198:199], v250 offset:13568\n"
"v_mfma_f32_16x16x32_bf16 v[116:119], a[4:7], a[200:203], v[116:119]\n"
"v_mfma_f32_16x16x32_bf16 v[68:71], a[4:7], a[196:199], v[68:71]\n"
"ds_read_b64_tr_b16 v[200:201], v250 offset:8456\n"
"v_mfma_f32_16x16x32_bf16 v[68:71], a[52:55], a[212:215], v[68:71]\n"
"v_mfma_f32_16x16x32_bf16 v[20:23], a[52:55], a[208:211], v[20:23]\n"
"ds_read_b64_tr_b16 v[202:203], v250 offset:9480\n"
"v_mfma_f32_16x16x32_bf16 v[20:23], a[4:7], a[192:195], v[20:23]\n"
"s_waitcnt lgkmcnt(4)\n"
"v_mfma_f32_16x16x32_bf16 v[24:27], v[192:195], a[192:195], v[24:27]\n"
"s_waitcnt lgkmcnt(2)\n"
"ds_read_b64_tr_b16 v[204:205], v250 offset:12552\n"
"v_mfma_f32_16x16x32_bf16 v[24:27], v[196:199], a[208:211], v[24:27]\n"
"ds_read_b128 a[224:227], v217 offset:0\n"
"v_mfma_f32_16x16x32_bf16 v[72:75], v[196:199], a[212:215], v[72:75]\n"
"s_mov_b32 m0, s19\n"
"ds_read_b64_tr_b16 v[206:207], v250 offset:13576\n"
"v_mfma_f32_16x16x32_bf16 v[72:75], v[192:195], a[196:199], v[72:75]\n"
"buffer_load_dwordx4 v236, s[40:43], s18 offen offset:0 nt lds\n"
"v_mfma_f32_16x16x32_bf16 v[120:123], v[192:195], a[200:203], v[120:123]\n"
"ds_read_b64_tr_b16 a[4:5], v250 offset:16384\n"
"v_mfma_f32_16x16x32_bf16 v[120:123], v[196:199], a[216:219], v[120:123]\n"
"ds_read_b128 a[240:243], v219 offset:0\n"
"v_mfma_f32_16x16x32_bf16 v[168:171], v[196:199], a[220:223], v[168:171]\n"
"ds_read_b64_tr_b16 a[6:7], v250 offset:17408\n"
"v_mfma_f32_16x16x32_bf16 v[168:171], v[192:195], a[204:207], v[168:171]\n"
"s_waitcnt lgkmcnt(6)\n"
"v_mfma_f32_16x16x32_bf16 v[172:175], v[200:203], a[204:207], v[172:175]\n"
"s_waitcnt lgkmcnt(3)\n"
"ds_read_b64_tr_b16 a[52:53], v250 offset:20480\n"
"v_mfma_f32_16x16x32_bf16 v[172:175], v[204:207], a[220:223], v[172:175]\n"
"ds_read_b128 a[244:247], v219 offset:2048\n"
"v_mfma_f32_16x16x32_bf16 v[124:127], v[204:207], a[216:219], v[124:127]\n"
"ds_read_b64_tr_b16 a[54:55], v250 offset:21504\n"
"v_mfma_f32_16x16x32_bf16 v[124:127], v[200:203], a[200:203], v[124:127]\n"
"buffer_load_dwordx4 v210, s[40:43], s18 offen offset:1024 nt lds\n"
"v_mfma_f32_16x16x32_bf16 v[76:79], v[200:203], a[196:199], v[76:79]\n"
"ds_read_b64_tr_b16 v[192:193], v250 offset:16392\n"
"v_mfma_f32_16x16x32_bf16 v[76:79], v[204:207], a[212:215], v[76:79]\n"
"ds_read_b128 a[228:231], v217 offset:2048\n"
"v_mfma_f32_16x16x32_bf16 v[28:31], v[204:207], a[208:211], v[28:31]\n"
"ds_read_b64_tr_b16 v[194:195], v250 offset:17416\n"
"v_mfma_f32_16x16x32_bf16 v[28:31], v[200:203], a[192:195], v[28:31]\n"
"s_waitcnt lgkmcnt(6)\n"
"v_mfma_f32_16x16x32_bf16 v[32:35], a[4:7], a[192:195], v[32:35]\n"
"s_waitcnt lgkmcnt(3)\n"
"ds_read_b64_tr_b16 v[196:197], v250 offset:20488\n"
"v_mfma_f32_16x16x32_bf16 v[32:35], a[52:55], a[208:211], v[32:35]\n"
"ds_read_b128 a[232:235], v217 offset:4096\n"
"v_mfma_f32_16x16x32_bf16 v[80:83], a[52:55], a[212:215], v[80:83]\n"
"ds_read_b64_tr_b16 v[198:199], v250 offset:21512\n"
"v_mfma_f32_16x16x32_bf16 v[80:83], a[4:7], a[196:199], v[80:83]\n"
"buffer_load_dwordx4 v241, s[40:43], s18 offen offset:2048 nt lds\n"
"v_mfma_f32_16x16x32_bf16 v[128:131], a[4:7], a[200:203], v[128:131]\n"
"ds_read_b64_tr_b16 v[200:201], v250 offset:16640\n"
"v_mfma_f32_16x16x32_bf16 v[128:131], a[52:55], a[216:219], v[128:131]\n"
"ds_read_b128 a[248:251], v219 offset:4096\n"
"v_mfma_f32_16x16x32_bf16 v[176:179], a[52:55], a[220:223], v[176:179]\n"
"ds_read_b64_tr_b16 v[202:203], v250 offset:17664\n"
"v_mfma_f32_16x16x32_bf16 v[176:179], a[4:7], a[204:207], v[176:179]\n"
"s_waitcnt lgkmcnt(6)\n"
"v_mfma_f32_16x16x32_bf16 v[180:183], v[192:195], a[204:207], v[180:183]\n"
"s_waitcnt lgkmcnt(3)\n"
"ds_read_b64_tr_b16 v[204:205], v250 offset:20736\n"
"v_mfma_f32_16x16x32_bf16 v[180:183], v[196:199], a[220:223], v[180:183]\n"
"ds_read_b128 a[252:255], v219 offset:6144\n"
"v_mfma_f32_16x16x32_bf16 v[132:135], v[196:199], a[216:219], v[132:135]\n"
"ds_read_b64_tr_b16 v[206:207], v250 offset:21760\n"
"v_mfma_f32_16x16x32_bf16 v[132:135], v[192:195], a[200:203], v[132:135]\n"
"v_mfma_f32_16x16x32_bf16 v[84:87], v[192:195], a[196:199], v[84:87]\n"
"ds_read_b64_tr_b16 a[4:5], v250 offset:16648\n"
"v_mfma_f32_16x16x32_bf16 v[84:87], v[196:199], a[212:215], v[84:87]\n"
"ds_read_b128 a[236:239], v217 offset:6144\n"
"v_mfma_f32_16x16x32_bf16 v[36:39], v[196:199], a[208:211], v[36:39]\n"
"ds_read_b64_tr_b16 a[6:7], v250 offset:17672\n"
"v_mfma_f32_16x16x32_bf16 v[36:39], v[192:195], a[192:195], v[36:39]\n"
"s_waitcnt lgkmcnt(6)\n"
"v_mfma_f32_16x16x32_bf16 v[40:43], v[200:203], a[192:195], v[40:43]\n"
"s_waitcnt lgkmcnt(3)\n"
"ds_read_b64_tr_b16 a[52:53], v250 offset:20744\n"
"v_mfma_f32_16x16x32_bf16 v[40:43], v[204:207], a[208:211], v[40:43]\n"
"s_waitcnt vmcnt(14)\n"
"s_barrier\n"
"v_mfma_f32_16x16x32_bf16 v[88:91], v[204:207], a[212:215], v[88:91]\n"
"s_mov_b32 m0, s79\n"
"ds_read_b64_tr_b16 a[54:55], v250 offset:21768\n"
"v_mfma_f32_16x16x32_bf16 v[88:91], v[200:203], a[196:199], v[88:91]\n"
"buffer_load_dwordx4 v242, s[48:51], s74 offen offset:0 lds\n"
"v_mfma_f32_16x16x32_bf16 v[136:139], v[200:203], a[200:203], v[136:139]\n"
"ds_read_b64_tr_b16 v[192:193], v250 offset:24576\n"
"v_mfma_f32_16x16x32_bf16 v[136:139], v[204:207], a[216:219], v[136:139]\n"
"buffer_load_dwordx4 v243, s[48:51], s74 offen offset:1024 lds\n"
"v_mfma_f32_16x16x32_bf16 v[184:187], v[204:207], a[220:223], v[184:187]\n"
"ds_read_b64_tr_b16 v[194:195], v250 offset:25600\n"
"v_mfma_f32_16x16x32_bf16 v[184:187], v[200:203], a[204:207], v[184:187]\n"
"s_waitcnt lgkmcnt(4)\n"
"s_add_u32 s74, s74, 128\n"
"v_mfma_f32_16x16x32_bf16 v[188:191], a[4:7], a[204:207], v[188:191]\n"
"s_waitcnt lgkmcnt(2)\n"
"s_add_u32 m0, s79, 8192\n"
"ds_read_b64_tr_b16 v[196:197], v250 offset:28672\n"
"v_mfma_f32_16x16x32_bf16 v[188:191], a[52:55], a[220:223], v[188:191]\n"
"buffer_load_dwordx4 v242, s[48:51], s74 offen offset:0 lds\n"
"v_mfma_f32_16x16x32_bf16 v[140:143], a[52:55], a[216:219], v[140:143]\n"
"ds_read_b64_tr_b16 v[198:199], v250 offset:29696\n"
"v_mfma_f32_16x16x32_bf16 v[140:143], a[4:7], a[200:203], v[140:143]\n"
"buffer_load_dwordx4 v243, s[48:51], s74 offen offset:1024 lds\n"
"v_mfma_f32_16x16x32_bf16 v[92:95], a[4:7], a[196:199], v[92:95]\n"
"s_add_u32 s74, s74, 128\n"
"ds_read_b64_tr_b16 v[200:201], v250 offset:24584\n"
"v_mfma_f32_16x16x32_bf16 v[92:95], a[52:55], a[212:215], v[92:95]\n"
"v_mfma_f32_16x16x32_bf16 v[44:47], a[52:55], a[208:211], v[44:47]\n"
"ds_read_b64_tr_b16 v[202:203], v250 offset:25608\n"
"v_mfma_f32_16x16x32_bf16 v[44:47], a[4:7], a[192:195], v[44:47]\n"
"s_waitcnt lgkmcnt(4)\n"
"v_mfma_f32_16x16x32_bf16 v[0:3], v[192:195], a[224:227], v[0:3]\n"
"s_waitcnt lgkmcnt(2)\n"
"ds_read_b64_tr_b16 v[204:205], v250 offset:28680\n"
"v_mfma_f32_16x16x32_bf16 v[0:3], v[196:199], a[240:243], v[0:3]\n"
"v_mfma_f32_16x16x32_bf16 v[48:51], v[196:199], a[244:247], v[48:51]\n"
"s_mov_b32 m0, s68\n"
"ds_read_b64_tr_b16 v[206:207], v250 offset:29704\n"
"v_mfma_f32_16x16x32_bf16 v[48:51], v[192:195], a[228:231], v[48:51]\n"
"buffer_load_dwordx4 v228, s[52:55], s75 offen offset:0 lds\n"
"v_mfma_f32_16x16x32_bf16 v[96:99], v[192:195], a[232:235], v[96:99]\n"
"ds_read_b64_tr_b16 a[4:5], v250 offset:24832\n"
"v_mfma_f32_16x16x32_bf16 v[96:99], v[196:199], a[248:251], v[96:99]\n"
"v_mfma_f32_16x16x32_bf16 v[144:147], v[196:199], a[252:255], v[144:147]\n"
"ds_read_b64_tr_b16 a[6:7], v250 offset:25856\n"
"v_mfma_f32_16x16x32_bf16 v[144:147], v[192:195], a[236:239], v[144:147]\n"
"s_waitcnt lgkmcnt(4)\n"
"buffer_load_dwordx4 v229, s[52:55], s75 offen offset:1024 lds\n"
"v_mfma_f32_16x16x32_bf16 v[148:151], v[200:203], a[236:239], v[148:151]\n"
"s_waitcnt lgkmcnt(2)\n"
"ds_read_b64_tr_b16 a[52:53], v250 offset:28928\n"
"v_mfma_f32_16x16x32_bf16 v[148:151], v[204:207], a[252:255], v[148:151]\n"
"v_mfma_f32_16x16x32_bf16 v[100:103], v[204:207], a[248:251], v[100:103]\n"
"ds_read_b64_tr_b16 a[54:55], v250 offset:29952\n"
"v_mfma_f32_16x16x32_bf16 v[100:103], v[200:203], a[232:235], v[100:103]\n"
"buffer_load_dwordx4 v230, s[52:55], s75 offen offset:2048 lds\n"
"v_mfma_f32_16x16x32_bf16 v[52:55], v[200:203], a[228:231], v[52:55]\n"
"ds_read_b64_tr_b16 v[192:193], v250 offset:24840\n"
"v_mfma_f32_16x16x32_bf16 v[52:55], v[204:207], a[244:247], v[52:55]\n"
"v_mfma_f32_16x16x32_bf16 v[4:7], v[204:207], a[240:243], v[4:7]\n"
"ds_read_b64_tr_b16 v[194:195], v250 offset:25864\n"
"v_mfma_f32_16x16x32_bf16 v[4:7], v[200:203], a[224:227], v[4:7]\n"
"s_waitcnt lgkmcnt(4)\n"
"buffer_load_dwordx4 v231, s[52:55], s75 offen offset:3072 lds\n"
"v_mfma_f32_16x16x32_bf16 v[8:11], a[4:7], a[224:227], v[8:11]\n"
"s_waitcnt lgkmcnt(2)\n"
"ds_read_b64_tr_b16 v[196:197], v250 offset:28936\n"
"v_mfma_f32_16x16x32_bf16 v[8:11], a[52:55], a[240:243], v[8:11]\n"
"s_add_u32 m0, s68, 4096\n"
"v_mfma_f32_16x16x32_bf16 v[56:59], a[52:55], a[244:247], v[56:59]\n"
"s_add_u32 s97, s75, s64\n"
"ds_read_b64_tr_b16 v[198:199], v250 offset:29960\n"
"v_mfma_f32_16x16x32_bf16 v[56:59], a[4:7], a[228:231], v[56:59]\n"
"buffer_load_dwordx4 v228, s[52:55], s97 offen offset:0 lds\n"
"v_mfma_f32_16x16x32_bf16 v[104:107], a[4:7], a[232:235], v[104:107]\n"
"ds_read_b64_tr_b16 v[200:201], v250 offset:32768\n"
"v_mfma_f32_16x16x32_bf16 v[104:107], a[52:55], a[248:251], v[104:107]\n"
"v_mfma_f32_16x16x32_bf16 v[152:155], a[52:55], a[252:255], v[152:155]\n"
"ds_read_b64_tr_b16 v[202:203], v250 offset:33792\n"
"v_mfma_f32_16x16x32_bf16 v[152:155], a[4:7], a[236:239], v[152:155]\n"
"s_waitcnt lgkmcnt(4)\n"
"buffer_load_dwordx4 v229, s[52:55], s97 offen offset:1024 lds\n"
"v_mfma_f32_16x16x32_bf16 v[156:159], v[192:195], a[236:239], v[156:159]\n"
"s_waitcnt lgkmcnt(2)\n"
"ds_read_b64_tr_b16 v[204:205], v250 offset:36864\n"
"v_mfma_f32_16x16x32_bf16 v[156:159], v[196:199], a[252:255], v[156:159]\n"
"v_mfma_f32_16x16x32_bf16 v[108:111], v[196:199], a[248:251], v[108:111]\n"
"ds_read_b64_tr_b16 v[206:207], v250 offset:37888\n"
"v_mfma_f32_16x16x32_bf16 v[108:111], v[192:195], a[232:235], v[108:111]\n"
"buffer_load_dwordx4 v230, s[52:55], s97 offen offset:2048 lds\n"
"v_mfma_f32_16x16x32_bf16 v[60:63], v[192:195], a[228:231], v[60:63]\n"
"ds_read_b64_tr_b16 a[4:5], v250 offset:32776\n"
"v_mfma_f32_16x16x32_bf16 v[60:63], v[196:199], a[244:247], v[60:63]\n"
"v_mfma_f32_16x16x32_bf16 v[12:15], v[196:199], a[240:243], v[12:15]\n"
"ds_read_b64_tr_b16 a[6:7], v250 offset:33800\n"
"v_mfma_f32_16x16x32_bf16 v[12:15], v[192:195], a[224:227], v[12:15]\n"
"s_waitcnt lgkmcnt(4)\n"
"buffer_load_dwordx4 v231, s[52:55], s97 offen offset:3072 lds\n"
"v_mfma_f32_16x16x32_bf16 v[16:19], v[200:203], a[224:227], v[16:19]\n"
"s_waitcnt lgkmcnt(2)\n"
"s_add_u32 s75, s75, 128\n"
"ds_read_b64_tr_b16 a[52:53], v250 offset:36872\n"
"v_mfma_f32_16x16x32_bf16 v[16:19], v[204:207], a[240:243], v[16:19]\n"
"s_waitcnt vmcnt(16)\n"
"s_barrier\n"
"v_mfma_f32_16x16x32_bf16 v[64:67], v[204:207], a[244:247], v[64:67]\n"
"s_add_u32 m0, s79, 16384\n"
"ds_read_b64_tr_b16 a[54:55], v250 offset:37896\n"
"v_mfma_f32_16x16x32_bf16 v[64:67], v[200:203], a[228:231], v[64:67]\n"
"buffer_load_dwordx4 v242, s[48:51], s74 offen offset:0 lds\n"
"v_mfma_f32_16x16x32_bf16 v[112:115], v[200:203], a[232:235], v[112:115]\n"
"ds_read_b64_tr_b16 v[192:193], v250 offset:33024\n"
"v_mfma_f32_16x16x32_bf16 v[112:115], v[204:207], a[248:251], v[112:115]\n"
"buffer_load_dwordx4 v243, s[48:51], s74 offen offset:1024 lds\n"
"v_mfma_f32_16x16x32_bf16 v[160:163], v[204:207], a[252:255], v[160:163]\n"
"ds_read_b64_tr_b16 v[194:195], v250 offset:34048\n"
"v_mfma_f32_16x16x32_bf16 v[160:163], v[200:203], a[236:239], v[160:163]\n"
"s_waitcnt lgkmcnt(4)\n"
"v_mfma_f32_16x16x32_bf16 v[164:167], a[4:7], a[236:239], v[164:167]\n"
"s_waitcnt lgkmcnt(2)\n"
"s_add_u32 s74, s74, 786176\n"
"ds_read_b64_tr_b16 v[196:197], v250 offset:37120\n"
"v_mfma_f32_16x16x32_bf16 v[164:167], a[52:55], a[252:255], v[164:167]\n"
"buffer_load_dword v223, v252, s[20:23], s91 offen\n"
"v_mfma_f32_16x16x32_bf16 v[116:119], a[52:55], a[248:251], v[116:119]\n"
"s_add_u32 s91, s91, s70\n"
"ds_read_b64_tr_b16 v[198:199], v250 offset:38144\n"
"v_mfma_f32_16x16x32_bf16 v[116:119], a[4:7], a[232:235], v[116:119]\n"
"v_mfma_f32_16x16x32_bf16 v[68:71], a[4:7], a[228:231], v[68:71]\n"
"ds_read_b64_tr_b16 v[200:201], v250 offset:33032\n"
"v_mfma_f32_16x16x32_bf16 v[68:71], a[52:55], a[244:247], v[68:71]\n"
"v_mfma_f32_16x16x32_bf16 v[20:23], a[52:55], a[240:243], v[20:23]\n"
"ds_read_b64_tr_b16 v[202:203], v250 offset:34056\n"
"v_mfma_f32_16x16x32_bf16 v[20:23], a[4:7], a[224:227], v[20:23]\n"
"s_waitcnt lgkmcnt(4)\n"
"v_mfma_f32_16x16x32_bf16 v[24:27], v[192:195], a[224:227], v[24:27]\n"
"s_waitcnt lgkmcnt(2)\n"
"ds_read_b64_tr_b16 v[204:205], v250 offset:37128\n"
"v_mfma_f32_16x16x32_bf16 v[24:27], v[196:199], a[240:243], v[24:27]\n"
"ds_read_b128 a[192:195], v232 offset:0\n"
"v_mfma_f32_16x16x32_bf16 v[72:75], v[196:199], a[244:247], v[72:75]\n"
"s_mov_b32 m0, s25\n"
"ds_read_b64_tr_b16 v[206:207], v250 offset:38152\n"
"v_mfma_f32_16x16x32_bf16 v[72:75], v[192:195], a[228:231], v[72:75]\n"
"buffer_load_dwordx4 v236, s[44:47], s24 offen offset:0 nt lds\n"
"v_mfma_f32_16x16x32_bf16 v[120:123], v[192:195], a[232:235], v[120:123]\n"
"ds_read_b64_tr_b16 a[4:5], v250 offset:40960\n"
"v_mfma_f32_16x16x32_bf16 v[120:123], v[196:199], a[248:251], v[120:123]\n"
"ds_read_b128 a[208:211], v233 offset:0\n"
"v_mfma_f32_16x16x32_bf16 v[168:171], v[196:199], a[252:255], v[168:171]\n"
"ds_read_b64_tr_b16 a[6:7], v250 offset:41984\n"
"v_mfma_f32_16x16x32_bf16 v[168:171], v[192:195], a[236:239], v[168:171]\n"
"s_waitcnt lgkmcnt(6)\n"
"v_mfma_f32_16x16x32_bf16 v[172:175], v[200:203], a[236:239], v[172:175]\n"
"s_waitcnt lgkmcnt(3)\n"
"ds_read_b64_tr_b16 a[52:53], v250 offset:45056\n"
"v_mfma_f32_16x16x32_bf16 v[172:175], v[204:207], a[252:255], v[172:175]\n"
"ds_read_b128 a[212:215], v233 offset:2048\n"
"v_mfma_f32_16x16x32_bf16 v[124:127], v[204:207], a[248:251], v[124:127]\n"
"ds_read_b64_tr_b16 a[54:55], v250 offset:46080\n"
"v_mfma_f32_16x16x32_bf16 v[124:127], v[200:203], a[232:235], v[124:127]\n"
"buffer_load_dwordx4 v210, s[44:47], s24 offen offset:1024 nt lds\n"
"v_mfma_f32_16x16x32_bf16 v[76:79], v[200:203], a[228:231], v[76:79]\n"
"ds_read_b64_tr_b16 v[192:193], v250 offset:40968\n"
"v_mfma_f32_16x16x32_bf16 v[76:79], v[204:207], a[244:247], v[76:79]\n"
"ds_read_b128 a[196:199], v232 offset:2048\n"
"v_mfma_f32_16x16x32_bf16 v[28:31], v[204:207], a[240:243], v[28:31]\n"
"ds_read_b64_tr_b16 v[194:195], v250 offset:41992\n"
"v_mfma_f32_16x16x32_bf16 v[28:31], v[200:203], a[224:227], v[28:31]\n"
"s_waitcnt lgkmcnt(6)\n"
"v_mfma_f32_16x16x32_bf16 v[32:35], a[4:7], a[224:227], v[32:35]\n"
"s_waitcnt lgkmcnt(3)\n"
"ds_read_b64_tr_b16 v[196:197], v250 offset:45064\n"
"v_mfma_f32_16x16x32_bf16 v[32:35], a[52:55], a[240:243], v[32:35]\n"
"ds_read_b128 a[200:203], v232 offset:4096\n"
"v_mfma_f32_16x16x32_bf16 v[80:83], a[52:55], a[244:247], v[80:83]\n"
"ds_read_b64_tr_b16 v[198:199], v250 offset:46088\n"
"v_mfma_f32_16x16x32_bf16 v[80:83], a[4:7], a[228:231], v[80:83]\n"
"buffer_load_dwordx4 v241, s[44:47], s24 offen offset:2048 nt lds\n"
"v_mfma_f32_16x16x32_bf16 v[128:131], a[4:7], a[232:235], v[128:131]\n"
"s_sub_u32 s26, s26, 1\n"
"s_mul_i32 s97, s26, 4294955008\n"
"ds_read_b64_tr_b16 v[200:201], v250 offset:41216\n"
"v_mfma_f32_16x16x32_bf16 v[128:131], a[52:55], a[248:251], v[128:131]\n"
"s_add_u32 s12, s77, s97\n"
"s_add_u32 s36, s78, s97\n"
"ds_read_b128 a[216:219], v233 offset:4096\n"
"v_mfma_f32_16x16x32_bf16 v[176:179], a[52:55], a[252:255], v[176:179]\n"
"s_cmp_lt_u32 s97, 24576\n"
"s_cselect_b32 s18, s72, 0xf0000000\n"
"ds_read_b64_tr_b16 v[202:203], v250 offset:42240\n"
"v_mfma_f32_16x16x32_bf16 v[176:179], a[4:7], a[236:239], v[176:179]\n"
"s_waitcnt lgkmcnt(6)\n"
"s_cselect_b32 s24, s73, 0xf0000000\n"
"s_cselect_b32 s19, s12, s77\n"
"v_mfma_f32_16x16x32_bf16 v[180:183], v[192:195], a[236:239], v[180:183]\n"
"s_waitcnt lgkmcnt(3)\n"
"s_cselect_b32 s25, s36, s78\n"
"ds_read_b64_tr_b16 v[204:205], v250 offset:45312\n"
"v_mfma_f32_16x16x32_bf16 v[180:183], v[196:199], a[252:255], v[180:183]\n"
"s_cselect_b32 s97, 33792, 0\n"
"s_add_u32 s72, s72, s97\n"
"ds_read_b128 a[220:223], v233 offset:6144\n"
"v_mfma_f32_16x16x32_bf16 v[132:135], v[196:199], a[248:251], v[132:135]\n"
"s_add_u32 s73, s73, s97\n"
"s_mul_i32 s85, s26, 768\n"
"ds_read_b64_tr_b16 v[206:207], v250 offset:46336\n"
"v_mfma_f32_16x16x32_bf16 v[132:135], v[192:195], a[232:235], v[132:135]\n"
"s_add_u32 s85, s85, 896\n"
"s_sub_u32 s80, s80, 1\n"
"v_mfma_f32_16x16x32_bf16 v[84:87], v[192:195], a[228:231], v[84:87]\n"
"ds_read_b64_tr_b16 a[4:5], v250 offset:41224\n"
"v_mfma_f32_16x16x32_bf16 v[84:87], v[196:199], a[244:247], v[84:87]\n"
"ds_read_b128 a[204:207], v232 offset:6144\n"
"v_mfma_f32_16x16x32_bf16 v[36:39], v[196:199], a[240:243], v[36:39]\n"
"ds_read_b64_tr_b16 a[6:7], v250 offset:42248\n"
"v_mfma_f32_16x16x32_bf16 v[36:39], v[192:195], a[224:227], v[36:39]\n"
"s_waitcnt lgkmcnt(6)\n"
"v_mfma_f32_16x16x32_bf16 v[40:43], v[200:203], a[224:227], v[40:43]\n"
"s_waitcnt lgkmcnt(3)\n"
"ds_read_b64_tr_b16 a[52:53], v250 offset:45320\n"
"v_mfma_f32_16x16x32_bf16 v[40:43], v[204:207], a[240:243], v[40:43]\n"
"s_waitcnt vmcnt(14)\n"
"s_barrier\n"
"v_mfma_f32_16x16x32_bf16 v[88:91], v[204:207], a[244:247], v[88:91]\n"
"s_add_u32 m0, s79, 24576\n"
"ds_read_b64_tr_b16 a[54:55], v250 offset:46344\n"
"v_mfma_f32_16x16x32_bf16 v[88:91], v[200:203], a[228:231], v[88:91]\n"
"buffer_load_dwordx4 v242, s[48:51], s74 offen offset:0 lds\n"
"v_mfma_f32_16x16x32_bf16 v[136:139], v[200:203], a[232:235], v[136:139]\n"
"s_cmp_eq_u32 s80, 3\n"
"s_cselect_b32 s91, s27, s91\n"
"ds_read_b64_tr_b16 v[192:193], v250 offset:0\n"
"v_mfma_f32_16x16x32_bf16 v[136:139], v[204:207], a[248:251], v[136:139]\n"
"buffer_load_dwordx4 v243, s[48:51], s74 offen offset:1024 lds\n"
"v_mfma_f32_16x16x32_bf16 v[184:187], v[204:207], a[252:255], v[184:187]\n"
"s_cmp_eq_u32 s80, 9\n"
"s_cselect_b32 s91, 0xf0000000, s91\n"
"ds_read_b64_tr_b16 v[194:195], v250 offset:1024\n"
"v_mfma_f32_16x16x32_bf16 v[184:187], v[200:203], a[236:239], v[184:187]\n"
"s_waitcnt lgkmcnt(4)\n"
"s_add_u32 s74, s74, 128\n"
"v_mfma_f32_16x16x32_bf16 v[188:191], a[4:7], a[236:239], v[188:191]\n"
"s_waitcnt lgkmcnt(2)\n"
"s_add_u32 m0, s79, 32768\n"
"ds_read_b64_tr_b16 v[196:197], v250 offset:4096\n"
"v_mfma_f32_16x16x32_bf16 v[188:191], a[52:55], a[252:255], v[188:191]\n"
"buffer_load_dwordx4 v242, s[48:51], s74 offen offset:0 lds\n"
"v_mfma_f32_16x16x32_bf16 v[140:143], a[52:55], a[248:251], v[140:143]\n"
"ds_read_b64_tr_b16 v[198:199], v250 offset:5120\n"
"v_mfma_f32_16x16x32_bf16 v[140:143], a[4:7], a[232:235], v[140:143]\n"
"buffer_load_dwordx4 v243, s[48:51], s74 offen offset:1024 lds\n"
"v_mfma_f32_16x16x32_bf16 v[92:95], a[4:7], a[228:231], v[92:95]\n"
"s_add_u32 s74, s74, 128\n"
"ds_read_b64_tr_b16 v[200:201], v250 offset:8\n"
"v_mfma_f32_16x16x32_bf16 v[92:95], a[52:55], a[244:247], v[92:95]\n"
"v_mfma_f32_16x16x32_bf16 v[44:47], a[52:55], a[240:243], v[44:47]\n"
"s_cmp_lg_u32 s80, 0\n"
"ds_read_b64_tr_b16 v[202:203], v250 offset:1032\n"
"v_mfma_f32_16x16x32_bf16 v[44:47], a[4:7], a[224:227], v[44:47]\n"
"s_cbranch_scc1 .Lat_q\n"
"s_cbranch_vccz .Lat_t1\n"
"s_waitcnt lgkmcnt(4)\n"
"v_mfma_f32_16x16x32_bf16 v[0:3], v[192:195], a[192:195], v[0:3]\n"
"s_waitcnt lgkmcnt(2)\n"
"ds_read_b64_tr_b16 v[204:205], v250 offset:4104\n"
"v_mfma_f32_16x16x32_bf16 v[0:3], v[196:199], a[208:211], v[0:3]\n"
"s_add_u32 s73, s60, s62\n"
"s_mov_b32 s72, s73\n"
"v_mfma_f32_16x16x32_bf16 v[48:51], v[196:199], a[212:215], v[48:51]\n"
"ds_read_b64_tr_b16 v[206:207], v250 offset:5128\n"
"v_mfma_f32_16x16x32_bf16 v[48:51], v[192:195], a[196:199], v[48:51]\n"
"v_mfma_f32_16x16x32_bf16 v[96:99], v[192:195], a[200:203], v[96:99]\n"
"ds_read_b64_tr_b16 a[4:5], v250 offset:256\n"
"v_mfma_f32_16x16x32_bf16 v[96:99], v[196:199], a[216:219], v[96:99]\n"
"v_mfma_f32_16x16x32_bf16 v[144:147], v[196:199], a[220:223], v[144:147]\n"
"ds_read_b64_tr_b16 a[6:7], v250 offset:1280\n"
"v_mfma_f32_16x16x32_bf16 v[144:147], v[192:195], a[204:207], v[144:147]\n"
"s_waitcnt lgkmcnt(4)\n"
"v_mfma_f32_16x16x32_bf16 v[148:151], v[200:203], a[204:207], v[148:151]\n"
"s_waitcnt lgkmcnt(2)\n"
"ds_read_b64_tr_b16 a[52:53], v250 offset:4352\n"
"v_mfma_f32_16x16x32_bf16 v[148:151], v[204:207], a[220:223], v[148:151]\n"
"v_mfma_f32_16x16x32_bf16 v[100:103], v[204:207], a[216:219], v[100:103]\n"
"ds_read_b64_tr_b16 a[54:55], v250 offset:5376\n"
"v_mfma_f32_16x16x32_bf16 v[100:103], v[200:203], a[200:203], v[100:103]\n"
"v_mfma_f32_16x16x32_bf16 v[52:55], v[200:203], a[196:199], v[52:55]\n"
"ds_read_b64_tr_b16 v[192:193], v250 offset:264\n"
"v_mfma_f32_16x16x32_bf16 v[52:55], v[204:207], a[212:215], v[52:55]\n"
"v_mfma_f32_16x16x32_bf16 v[4:7], v[204:207], a[208:211], v[4:7]\n"
"ds_read_b64_tr_b16 v[194:195], v250 offset:1288\n"
"v_mfma_f32_16x16x32_bf16 v[4:7], v[200:203], a[192:195], v[4:7]\n"
"s_waitcnt lgkmcnt(4)\n"
"v_mfma_f32_16x16x32_bf16 v[8:11], a[4:7], a[192:195], v[8:11]\n"
"s_waitcnt lgkmcnt(2)\n"
"ds_read_b64_tr_b16 v[196:197], v250 offset:4360\n"
"v_mfma_f32_16x16x32_bf16 v[8:11], a[52:55], a[208:211], v[8:11]\n"
"v_mfma_f32_16x16x32_bf16 v[56:59], a[52:55], a[212:215], v[56:59]\n"
"ds_read_b64_tr_b16 v[198:199], v250 offset:5384\n"
"v_mfma_f32_16x16x32_bf16 v[56:59], a[4:7], a[196:199], v[56:59]\n"
"v_mfma_f32_16x16x32_bf16 v[104:107], a[4:7], a[200:203], v[104:107]\n"
"ds_read_b64_tr_b16 v[200:201], v250 offset:8192\n"
"v_mfma_f32_16x16x32_bf16 v[104:107], a[52:55], a[216:219], v[104:107]\n"
"v_mfma_f32_16x16x32_bf16 v[152:155], a[52:55], a[220:223], v[152:155]\n"
"ds_read_b64_tr_b16 v[202:203], v250 offset:9216\n"
"v_mfma_f32_16x16x32_bf16 v[152:155], a[4:7], a[204:207], v[152:155]\n"
"s_waitcnt lgkmcnt(4)\n"
"v_mfma_f32_16x16x32_bf16 v[156:159], v[192:195], a[204:207], v[156:159]\n"
"s_waitcnt lgkmcnt(2)\n"
"ds_read_b64_tr_b16 v[204:205], v250 offset:12288\n"
"v_mfma_f32_16x16x32_bf16 v[156:159], v[196:199], a[220:223], v[156:159]\n"
"v_mfma_f32_16x16x32_bf16 v[108:111], v[196:199], a[216:219], v[108:111]\n"
"ds_read_b64_tr_b16 v[206:207], v250 offset:13312\n"
"v_mfma_f32_16x16x32_bf16 v[108:111], v[192:195], a[200:203], v[108:111]\n"
"v_mfma_f32_16x16x32_bf16 v[60:63], v[192:195], a[196:199], v[60:63]\n"
"ds_read_b64_tr_b16 a[4:5], v250 offset:8200\n"
"v_mfma_f32_16x16x32_bf16 v[60:63], v[196:199], a[212:215], v[60:63]\n"
"v_mfma_f32_16x16x32_bf16 v[12:15], v[196:199], a[208:211], v[12:15]\n"
"ds_read_b64_tr_b16 a[6:7], v250 offset:9224\n"
"v_mfma_f32_16x16x32_bf16 v[12:15], v[192:195], a[192:195], v[12:15]\n"
"s_waitcnt lgkmcnt(4)\n"
"v_mfma_f32_16x16x32_bf16 v[16:19], v[200:203], a[192:195], v[16:19]\n"
"s_waitcnt lgkmcnt(2)\n"
"ds_read_b64_tr_b16 a[52:53], v250 offset:12296\n"
"v_mfma_f32_16x16x32_bf16 v[16:19], v[204:207], a[208:211], v[16:19]\n"
"s_waitcnt vmcnt(8)\n"
"s_barrier\n"
"v_mfma_f32_16x16x32_bf16 v[64:67], v[204:207], a[212:215], v[64:67]\n"
"s_add_u32 m0, s79, 40960\n"
"ds_read_b64_tr_b16 a[54:55], v250 offset:13320\n"
"v_mfma_f32_16x16x32_bf16 v[64:67], v[200:203], a[196:199], v[64:67]\n"
"buffer_load_dwordx4 v242, s[48:51], s74 offen offset:0 lds\n"
"v_mfma_f32_16x16x32_bf16 v[112:115], v[200:203], a[200:203], v[112:115]\n"
"ds_read_b64_tr_b16 v[192:193], v250 offset:8448\n"
"v_mfma_f32_16x16x32_bf16 v[112:115], v[204:207], a[216:219], v[112:115]\n"
"buffer_load_dwordx4 v243, s[48:51], s74 offen offset:1024 lds\n"
"v_mfma_f32_16x16x32_bf16 v[160:163], v[204:207], a[220:223], v[160:163]\n"
"s_add_u32 s74, s74, 786176\n"
"ds_read_b64_tr_b16 v[194:195], v250 offset:9472\n"
"v_mfma_f32_16x16x32_bf16 v[160:163], v[200:203], a[204:207], v[160:163]\n"
"s_waitcnt lgkmcnt(4)\n"
"v_mfma_f32_16x16x32_bf16 v[164:167], a[4:7], a[204:207], v[164:167]\n"
"s_waitcnt lgkmcnt(2)\n"
"ds_read_b64_tr_b16 v[196:197], v250 offset:12544\n"
"v_mfma_f32_16x16x32_bf16 v[164:167], a[52:55], a[220:223], v[164:167]\n"
"v_mfma_f32_16x16x32_bf16 v[116:119], a[52:55], a[216:219], v[116:119]\n"
"ds_read_b64_tr_b16 v[198:199], v250 offset:13568\n"
"v_mfma_f32_16x16x32_bf16 v[116:119], a[4:7], a[200:203], v[116:119]\n"
"v_mfma_f32_16x16x32_bf16 v[68:71], a[4:7], a[196:199], v[68:71]\n"
"ds_read_b64_tr_b16 v[200:201], v250 offset:8456\n"
"v_mfma_f32_16x16x32_bf16 v[68:71], a[52:55], a[212:215], v[68:71]\n"
"v_mfma_f32_16x16x32_bf16 v[20:23], a[52:55], a[208:211], v[20:23]\n"
"ds_read_b64_tr_b16 v[202:203], v250 offset:9480\n"
"v_mfma_f32_16x16x32_bf16 v[20:23], a[4:7], a[192:195], v[20:23]\n"
"s_waitcnt lgkmcnt(4)\n"
"v_mfma_f32_16x16x32_bf16 v[24:27], v[192:195], a[192:195], v[24:27]\n"
"s_waitcnt lgkmcnt(2)\n"
"ds_read_b64_tr_b16 v[204:205], v250 offset:12552\n"
"v_mfma_f32_16x16x32_bf16 v[24:27], v[196:199], a[208:211], v[24:27]\n"
"ds_read_b128 a[224:227], v217 offset:0\n"
"v_mfma_f32_16x16x32_bf16 v[72:75], v[196:199], a[212:215], v[72:75]\n"
"ds_read_b64_tr_b16 v[206:207], v250 offset:13576\n"
"v_mfma_f32_16x16x32_bf16 v[72:75], v[192:195], a[196:199], v[72:75]\n"
"v_mfma_f32_16x16x32_bf16 v[120:123], v[192:195], a[200:203], v[120:123]\n"
"s_add_u32 m0, s77, 24576\n"
"ds_read_b64_tr_b16 a[4:5], v250 offset:16384\n"
"v_mfma_f32_16x16x32_bf16 v[120:123], v[196:199], a[216:219], v[120:123]\n"
"buffer_load_dwordx4 v236, s[40:43], s72 offen offset:0 nt lds\n"
"v_mfma_f32_16x16x32_bf16 v[168:171], v[196:199], a[220:223], v[168:171]\n"
"ds_read_b64_tr_b16 a[6:7], v250 offset:17408\n"
"v_mfma_f32_16x16x32_bf16 v[168:171], v[192:195], a[204:207], v[168:171]\n"
"s_waitcnt lgkmcnt(5)\n"
"ds_read_b128 a[240:243], v219 offset:0\n"
"v_mfma_f32_16x16x32_bf16 v[172:175], v[200:203], a[204:207], v[172:175]\n"
"s_waitcnt lgkmcnt(3)\n"
"ds_read_b64_tr_b16 a[52:53], v250 offset:20480\n"
"v_mfma_f32_16x16x32_bf16 v[172:175], v[204:207], a[220:223], v[172:175]\n"
"v_mfma_f32_16x16x32_bf16 v[124:127], v[204:207], a[216:219], v[124:127]\n"
"ds_read_b64_tr_b16 a[54:55], v250 offset:21504\n"
"v_mfma_f32_16x16x32_bf16 v[124:127], v[200:203], a[200:203], v[124:127]\n"
"ds_read_b128 a[244:247], v219 offset:2048\n"
"v_mfma_f32_16x16x32_bf16 v[76:79], v[200:203], a[196:199], v[76:79]\n"
"ds_read_b64_tr_b16 v[192:193], v250 offset:16392\n"
"v_mfma_f32_16x16x32_bf16 v[76:79], v[204:207], a[212:215], v[76:79]\n"
"buffer_load_dwordx4 v237, s[40:43], s72 offen offset:1024 nt lds\n"
"v_mfma_f32_16x16x32_bf16 v[28:31], v[204:207], a[208:211], v[28:31]\n"
"ds_read_b64_tr_b16 v[194:195], v250 offset:17416\n"
"v_mfma_f32_16x16x32_bf16 v[28:31], v[200:203], a[192:195], v[28:31]\n"
"s_waitcnt lgkmcnt(6)\n"
"ds_read_b128 a[228:231], v217 offset:2048\n"
"v_mfma_f32_16x16x32_bf16 v[32:35], a[4:7], a[192:195], v[32:35]\n"
"s_waitcnt lgkmcnt(4)\n"
"ds_read_b64_tr_b16 v[196:197], v250 offset:20488\n"
"v_mfma_f32_16x16x32_bf16 v[32:35], a[52:55], a[208:211], v[32:35]\n"
"v_mfma_f32_16x16x32_bf16 v[80:83], a[52:55], a[212:215], v[80:83]\n"
"ds_read_b64_tr_b16 v[198:199], v250 offset:21512\n"
"v_mfma_f32_16x16x32_bf16 v[80:83], a[4:7], a[196:199], v[80:83]\n"
"ds_read_b128 a[232:235], v217 offset:4096\n"
"v_mfma_f32_16x16x32_bf16 v[128:131], a[4:7], a[200:203], v[128:131]\n"
"ds_read_b64_tr_b16 v[200:201], v250 offset:16640\n"
"v_mfma_f32_16x16x32_bf16 v[128:131], a[52:55], a[216:219], v[128:131]\n"
"buffer_load_dwordx4 v238, s[40:43], s72 offen offset:2048 nt lds\n"
"v_mfma_f32_16x16x32_bf16 v[176:179], a[52:55], a[220:223], v[176:179]\n"
"s_add_u32 s72, s72, 3072\n"
"ds_read_b64_tr_b16 v[202:203], v250 offset:17664\n"
"v_mfma_f32_16x16x32_bf16 v[176:179], a[4:7], a[204:207], v[176:179]\n"
"s_waitcnt lgkmcnt(6)\n"
"ds_read_b128 a[248:251], v219 offset:4096\n"
"v_mfma_f32_16x16x32_bf16 v[180:183], v[192:195], a[204:207], v[180:183]\n"
"s_waitcnt lgkmcnt(4)\n"
"ds_read_b64_tr_b16 v[204:205], v250 offset:20736\n"
"v_mfma_f32_16x16x32_bf16 v[180:183], v[196:199], a[220:223], v[180:183]\n"
"v_mfma_f32_16x16x32_bf16 v[132:135], v[196:199], a[216:219], v[132:135]\n"
"ds_read_b64_tr_b16 v[206:207], v250 offset:21760\n"
"v_mfma_f32_16x16x32_bf16 v[132:135], v[192:195], a[200:203], v[132:135]\n"
"ds_read_b128 a[252:255], v219 offset:6144\n"
"v_mfma_f32_16x16x32_bf16 v[84:87], v[192:195], a[196:199], v[84:87]\n"
"ds_read_b64_tr_b16 a[4:5], v250 offset:16648\n"
"v_mfma_f32_16x16x32_bf16 v[84:87], v[196:199], a[212:215], v[84:87]\n"
"v_mfma_f32_16x16x32_bf16 v[36:39], v[196:199], a[208:211], v[36:39]\n"
"ds_read_b64_tr_b16 a[6:7], v250 offset:17672\n"
"v_mfma_f32_16x16x32_bf16 v[36:39], v[192:195], a[192:195], v[36:39]\n"
"s_waitcnt lgkmcnt(6)\n"
"ds_read_b128 a[236:239], v217 offset:6144\n"
"v_mfma_f32_16x16x32_bf16 v[40:43], v[200:203], a[192:195], v[40:43]\n"
"s_waitcnt lgkmcnt(4)\n"
"ds_read_b64_tr_b16 a[52:53], v250 offset:20744\n"
"v_mfma_f32_16x16x32_bf16 v[40:43], v[204:207], a[208:211], v[40:43]\n"
"s_waitcnt vmcnt(5)\n"
"s_barrier\n"
"v_mfma_f32_16x16x32_bf16 v[88:91], v[204:207], a[212:215], v[88:91]\n"
"ds_read_b64_tr_b16 a[54:55], v250 offset:21768\n"
"v_readfirstlane_b32 s13, v222\n"
"v_mfma_f32_16x16x32_bf16 v[88:91], v[200:203], a[196:199], v[88:91]\n"
"v_mfma_f32_16x16x32_bf16 v[136:139], v[200:203], a[200:203], v[136:139]\n"
"s_add_u32 s13, s13, 1\n"
"ds_read_b64_tr_b16 v[192:193], v250 offset:24576\n"
"v_mfma_f32_16x16x32_bf16 v[136:139], v[204:207], a[216:219], v[136:139]\n"
"s_mov_b32 s80, 8\n"
"v_mfma_f32_16x16x32_bf16 v[184:187], v[204:207], a[220:223], v[184:187]\n"
"s_mov_b32 s90, 0x47800000\n"
"ds_read_b64_tr_b16 v[194:195], v250 offset:25600\n"
"v_mfma_f32_16x16x32_bf16 v[184:187], v[200:203], a[204:207], v[184:187]\n"
"s_waitcnt lgkmcnt(5)\n"
"v_mfma_f32_16x16x32_bf16 v[188:191], a[4:7], a[204:207], v[188:191]\n"
"s_waitcnt lgkmcnt(2)\n"
"ds_read_b64_tr_b16 v[196:197], v250 offset:28672\n"
"v_mfma_f32_16x16x32_bf16 v[188:191], a[52:55], a[220:223], v[188:191]\n"
"s_mov_b32 s82, 0xc1e00000\n"
"v_mfma_f32_16x16x32_bf16 v[140:143], a[52:55], a[216:219], v[140:143]\n"
"s_mov_b32 s83, 0xc1e00000\n"
"ds_read_b64_tr_b16 v[198:199], v250 offset:29696\n"
"v_mfma_f32_16x16x32_bf16 v[140:143], a[4:7], a[200:203], v[140:143]\n"
"v_mfma_f32_16x16x32_bf16 v[92:95], a[4:7], a[196:199], v[92:95]\n"
"ds_read_b64_tr_b16 v[200:201], v250 offset:24584\n"
"v_mfma_f32_16x16x32_bf16 v[92:95], a[52:55], a[212:215], v[92:95]\n"
"v_mfma_f32_16x16x32_bf16 v[44:47], a[52:55], a[208:211], v[44:47]\n"
"ds_read_b64_tr_b16 v[202:203], v250 offset:25608\n"
"v_mfma_f32_16x16x32_bf16 v[44:47], a[4:7], a[192:195], v[44:47]\n"
"s_waitcnt lgkmcnt(4)\n"
"v_mfma_f32_16x16x32_bf16 v[0:3], v[192:195], a[224:227], v[0:3]\n"
"s_waitcnt lgkmcnt(2)\n"
"ds_read_b64_tr_b16 v[204:205], v250 offset:28680\n"
"v_mfma_f32_16x16x32_bf16 v[0:3], v[196:199], a[240:243], v[0:3]\n"
"s_mov_b32 s26, s69\n"
"v_mfma_f32_16x16x32_bf16 v[48:51], v[196:199], a[244:247], v[48:51]\n"
"s_mov_b32 s32, s79\n"
"ds_read_b64_tr_b16 v[206:207], v250 offset:29704\n"
"v_mfma_f32_16x16x32_bf16 v[48:51], v[192:195], a[228:231], v[48:51]\n"
"s_add_u32 s24, s63, 147456\n"
"v_mfma_f32_16x16x32_bf16 v[96:99], v[192:195], a[232:235], v[96:99]\n"
"s_mov_b32 s70, 238\n"
"ds_read_b64_tr_b16 a[4:5], v250 offset:24832\n"
"v_mfma_f32_16x16x32_bf16 v[96:99], v[196:199], a[248:251], v[96:99]\n"
"s_mov_b64 s[20:21], s[48:49]\n"
"v_mfma_f32_16x16x32_bf16 v[144:147], v[196:199], a[252:255], v[144:147]\n"
"s_mov_b64 s[22:23], s[50:51]\n"
"ds_read_b64_tr_b16 a[6:7], v250 offset:25856\n"
"v_mfma_f32_16x16x32_bf16 v[144:147], v[192:195], a[236:239], v[144:147]\n"
"s_waitcnt lgkmcnt(4)\n"
"v_mfma_f32_16x16x32_bf16 v[148:151], v[200:203], a[236:239], v[148:151]\n"
"s_waitcnt lgkmcnt(2)\n"
"ds_read_b64_tr_b16 a[52:53], v250 offset:28928\n"
"v_mfma_f32_16x16x32_bf16 v[148:151], v[204:207], a[252:255], v[148:151]\n"
"s_mov_b32 s18, s33\n"
"v_mfma_f32_16x16x32_bf16 v[100:103], v[204:207], a[248:251], v[100:103]\n"
"s_mov_b32 s11, 6144\n"
"ds_read_b64_tr_b16 a[54:55], v250 offset:29952\n"
"v_mfma_f32_16x16x32_bf16 v[100:103], v[200:203], a[232:235], v[100:103]\n"
"s_mov_b32 s71, 786432\n"
"v_mfma_f32_16x16x32_bf16 v[52:55], v[200:203], a[228:231], v[52:55]\n"
"s_mov_b32 s35, 24576\n"
"ds_read_b64_tr_b16 v[192:193], v250 offset:24840\n"
"v_mfma_f32_16x16x32_bf16 v[52:55], v[204:207], a[244:247], v[52:55]\n"
"s_add_u32 s33, s24, 4096\n"
"v_mfma_f32_16x16x32_bf16 v[4:7], v[204:207], a[240:243], v[4:7]\n"
"s_mov_b32 s25, 0\n"
"ds_read_b64_tr_b16 v[194:195], v250 offset:25864\n"
"v_mfma_f32_16x16x32_bf16 v[4:7], v[200:203], a[224:227], v[4:7]\n"
"s_waitcnt lgkmcnt(4)\n"
"v_mfma_f32_16x16x32_bf16 v[8:11], a[4:7], a[224:227], v[8:11]\n"
"s_waitcnt lgkmcnt(2)\n"
"ds_read_b64_tr_b16 v[196:197], v250 offset:28936\n"
"v_mfma_f32_16x16x32_bf16 v[8:11], a[52:55], a[240:243], v[8:11]\n"
"s_mov_b32 s19, 0\n"
"v_mfma_f32_16x16x32_bf16 v[56:59], a[52:55], a[244:247], v[56:59]\n"
"ds_read_b64_tr_b16 v[198:199], v250 offset:29960\n"
"v_mfma_f32_16x16x32_bf16 v[56:59], a[4:7], a[228:231], v[56:59]\n"
"v_mfma_f32_16x16x32_bf16 v[104:107], a[4:7], a[232:235], v[104:107]\n"
"ds_read_b64_tr_b16 v[200:201], v250 offset:32768\n"
"v_mfma_f32_16x16x32_bf16 v[104:107], a[52:55], a[248:251], v[104:107]\n"
"v_mfma_f32_16x16x32_bf16 v[152:155], a[52:55], a[252:255], v[152:155]\n"
"ds_read_b64_tr_b16 v[202:203], v250 offset:33792\n"
"v_mfma_f32_16x16x32_bf16 v[152:155], a[4:7], a[236:239], v[152:155]\n"
"s_waitcnt lgkmcnt(4)\n"
"v_mfma_f32_16x16x32_bf16 v[156:159], v[192:195], a[236:239], v[156:159]\n"
"s_waitcnt lgkmcnt(2)\n"
"ds_read_b64_tr_b16 v[204:205], v250 offset:36864\n"
"v_mfma_f32_16x16x32_bf16 v[156:159], v[196:199], a[252:255], v[156:159]\n"
"v_mfma_f32_16x16x32_bf16 v[108:111], v[196:199], a[248:251], v[108:111]\n"
"ds_read_b64_tr_b16 v[206:207], v250 offset:37888\n"
"v_mfma_f32_16x16x32_bf16 v[108:111], v[192:195], a[232:235], v[108:111]\n"
"v_mfma_f32_16x16x32_bf16 v[60:63], v[192:195], a[228:231], v[60:63]\n"
"ds_read_b64_tr_b16 a[4:5], v250 offset:32776\n"
"v_mfma_f32_16x16x32_bf16 v[60:63], v[196:199], a[244:247], v[60:63]\n"
"v_mfma_f32_16x16x32_bf16 v[12:15], v[196:199], a[240:243], v[12:15]\n"
"ds_read_b64_tr_b16 a[6:7], v250 offset:33800\n"
"v_mfma_f32_16x16x32_bf16 v[12:15], v[192:195], a[224:227], v[12:15]\n"
"s_waitcnt lgkmcnt(4)\n"
"v_mfma_f32_16x16x32_bf16 v[16:19], v[200:203], a[224:227], v[16:19]\n"
"s_waitcnt lgkmcnt(2)\n"
"ds_read_b64_tr_b16 a[52:53], v250 offset:36872\n"
"v_mfma_f32_16x16x32_bf16 v[16:19], v[204:207], a[240:243], v[16:19]\n"
"s_waitcnt vmcnt(3)\n"
"s_barrier\n"
"v_mfma_f32_16x16x32_bf16 v[64:67], v[204:207], a[244:247], v[64:67]\n"
"ds_read_b64_tr_b16 a[54:55], v250 offset:37896\n"
"v_mfma_f32_16x16x32_bf16 v[64:67], v[200:203], a[228:231], v[64:67]\n"
"v_mfma_f32_16x16x32_bf16 v[112:115], v[200:203], a[232:235], v[112:115]\n"
"s_add_u32 m0, s77, 36864\n"
"ds_read_b64_tr_b16 v[192:193], v250 offset:33024\n"
"v_mfma_f32_16x16x32_bf16 v[112:115], v[204:207], a[248:251], v[112:115]\n"
"buffer_load_dwordx4 v236, s[40:43], s72 offen offset:0 nt lds\n"
"v_mfma_f32_16x16x32_bf16 v[160:163], v[204:207], a[252:255], v[160:163]\n"
"ds_read_b64_tr_b16 v[194:195], v250 offset:34048\n"
"v_mfma_f32_16x16x32_bf16 v[160:163], v[200:203], a[236:239], v[160:163]\n"
"s_waitcnt lgkmcnt(4)\n"
"v_mfma_f32_16x16x32_bf16 v[164:167], a[4:7], a[236:239], v[164:167]\n"
"s_waitcnt lgkmcnt(2)\n"
"ds_read_b64_tr_b16 v[196:197], v250 offset:37120\n"
"v_mfma_f32_16x16x32_bf16 v[164:167], a[52:55], a[252:255], v[164:167]\n"
"buffer_load_dwordx4 v237, s[40:43], s72 offen offset:1024 nt lds\n"
"v_mfma_f32_16x16x32_bf16 v[116:119], a[52:55], a[248:251], v[116:119]\n"
"ds_read_b64_tr_b16 v[198:199], v250 offset:38144\n"
"v_mfma_f32_16x16x32_bf16 v[116:119], a[4:7], a[232:235], v[116:119]\n"
"v_mfma_f32_16x16x32_bf16 v[68:71], a[4:7], a[228:231], v[68:71]\n"
"ds_read_b64_tr_b16 v[200:201], v250 offset:33032\n"
"v_mfma_f32_16x16x32_bf16 v[68:71], a[52:55], a[244:247], v[68:71]\n"
"buffer_load_dwordx4 v238, s[40:43], s72 offen offset:2048 nt lds\n"
"v_mfma_f32_16x16x32_bf16 v[20:23], a[52:55], a[240:243], v[20:23]\n"
"s_add_u32 s72, s72, s0\n"
"ds_read_b64_tr_b16 v[202:203], v250 offset:34056\n"
"v_mfma_f32_16x16x32_bf16 v[20:23], a[4:7], a[224:227], v[20:23]\n"
"s_waitcnt lgkmcnt(4)\n"
"v_mfma_f32_16x16x32_bf16 v[24:27], v[192:195], a[224:227], v[24:27]\n"
"s_waitcnt lgkmcnt(2)\n"
"ds_read_b64_tr_b16 v[204:205], v250 offset:37128\n"
"v_mfma_f32_16x16x32_bf16 v[24:27], v[196:199], a[240:243], v[24:27]\n"
"v_mfma_f32_16x16x32_bf16 v[72:75], v[196:199], a[244:247], v[72:75]\n"
"s_add_u32 m0, s78, 24576\n"
"ds_read_b64_tr_b16 v[206:207], v250 offset:38152\n"
"v_mfma_f32_16x16x32_bf16 v[72:75], v[192:195], a[228:231], v[72:75]\n"
"buffer_load_dwordx4 v236, s[44:47], s73 offen offset:0 nt lds\n"
"v_mfma_f32_16x16x32_bf16 v[120:123], v[192:195], a[232:235], v[120:123]\n"
"ds_read_b64_tr_b16 a[4:5], v250 offset:40960\n"
"v_mfma_f32_16x16x32_bf16 v[120:123], v[196:199], a[248:251], v[120:123]\n"
"v_mfma_f32_16x16x32_bf16 v[168:171], v[196:199], a[252:255], v[168:171]\n"
"ds_read_b64_tr_b16 a[6:7], v250 offset:41984\n"
"v_mfma_f32_16x16x32_bf16 v[168:171], v[192:195], a[236:239], v[168:171]\n"
"s_waitcnt lgkmcnt(4)\n"
"v_mfma_f32_16x16x32_bf16 v[172:175], v[200:203], a[236:239], v[172:175]\n"
"s_waitcnt lgkmcnt(2)\n"
"ds_read_b64_tr_b16 a[52:53], v250 offset:45056\n"
"v_mfma_f32_16x16x32_bf16 v[172:175], v[204:207], a[252:255], v[172:175]\n"
"buffer_load_dwordx4 v237, s[44:47], s73 offen offset:1024 nt lds\n"
"v_mfma_f32_16x16x32_bf16 v[124:127], v[204:207], a[248:251], v[124:127]\n"
"ds_read_b64_tr_b16 a[54:55], v250 offset:46080\n"
"v_mfma_f32_16x16x32_bf16 v[124:127], v[200:203], a[232:235], v[124:127]\n"
"v_mfma_f32_16x16x32_bf16 v[76:79], v[200:203], a[228:231], v[76:79]\n"
"ds_read_b64_tr_b16 v[192:193], v250 offset:40968\n"
"v_mfma_f32_16x16x32_bf16 v[76:79], v[204:207], a[244:247], v[76:79]\n"
"v_mfma_f32_16x16x32_bf16 v[28:31], v[204:207], a[240:243], v[28:31]\n"
"ds_read_b64_tr_b16 v[194:195], v250 offset:41992\n"
"v_mfma_f32_16x16x32_bf16 v[28:31], v[200:203], a[224:227], v[28:31]\n"
"s_waitcnt lgkmcnt(4)\n"
"buffer_load_dwordx4 v238, s[44:47], s73 offen offset:2048 nt lds\n"
"v_mfma_f32_16x16x32_bf16 v[32:35], a[4:7], a[224:227], v[32:35]\n"
"s_waitcnt lgkmcnt(2)\n"
"ds_read_b64_tr_b16 v[196:197], v250 offset:45064\n"
"v_mfma_f32_16x16x32_bf16 v[32:35], a[52:55], a[240:243], v[32:35]\n"
"s_add_u32 s73, s73, 3072\n"
"v_mfma_f32_16x16x32_bf16 v[80:83], a[52:55], a[244:247], v[80:83]\n"
"ds_read_b64_tr_b16 v[198:199], v250 offset:46088\n"
"v_readlane_b32 s12, v255, 14\n"
"v_mfma_f32_16x16x32_bf16 v[80:83], a[4:7], a[228:231], v[80:83]\n"
"v_mfma_f32_16x16x32_bf16 v[128:131], a[4:7], a[232:235], v[128:131]\n"
"ds_read_b64_tr_b16 v[200:201], v250 offset:41216\n"
"v_mov_b32 v252, s12\n"
"v_mfma_f32_16x16x32_bf16 v[128:131], a[52:55], a[248:251], v[128:131]\n"
"v_mfma_f32_16x16x32_bf16 v[176:179], a[52:55], a[252:255], v[176:179]\n"
"ds_read_b64_tr_b16 v[202:203], v250 offset:42240\n"
"v_mfma_f32_16x16x32_bf16 v[176:179], a[4:7], a[236:239], v[176:179]\n"
"s_waitcnt lgkmcnt(4)\n"
"v_mfma_f32_16x16x32_bf16 v[180:183], v[192:195], a[236:239], v[180:183]\n"
"s_waitcnt lgkmcnt(2)\n"
"ds_read_b64_tr_b16 v[204:205], v250 offset:45312\n"
"v_mfma_f32_16x16x32_bf16 v[180:183], v[196:199], a[252:255], v[180:183]\n"
"v_mfma_f32_16x16x32_bf16 v[132:135], v[196:199], a[248:251], v[132:135]\n"
"ds_read_b64_tr_b16 v[206:207], v250 offset:46336\n"
"v_mfma_f32_16x16x32_bf16 v[132:135], v[192:195], a[232:235], v[132:135]\n"
"v_mfma_f32_16x16x32_bf16 v[84:87], v[192:195], a[228:231], v[84:87]\n"
"ds_read_b64_tr_b16 a[4:5], v250 offset:41224\n"
"v_mfma_f32_16x16x32_bf16 v[84:87], v[196:199], a[244:247], v[84:87]\n"
"v_mfma_f32_16x16x32_bf16 v[36:39], v[196:199], a[240:243], v[36:39]\n"
"ds_read_b64_tr_b16 a[6:7], v250 offset:42248\n"
"v_mfma_f32_16x16x32_bf16 v[36:39], v[192:195], a[224:227], v[36:39]\n"
"s_waitcnt lgkmcnt(4)\n"
"v_mfma_f32_16x16x32_bf16 v[40:43], v[200:203], a[224:227], v[40:43]\n"
"s_waitcnt lgkmcnt(2)\n"
"ds_read_b64_tr_b16 a[52:53], v250 offset:45320\n"
"v_mfma_f32_16x16x32_bf16 v[40:43], v[204:207], a[240:243], v[40:43]\n"
"s_barrier\n"
"v_mfma_f32_16x16x32_bf16 v[88:91], v[204:207], a[244:247], v[88:91]\n"
"ds_read_b64_tr_b16 a[54:55], v250 offset:46344\n"
"v_mfma_f32_16x16x32_bf16 v[88:91], v[200:203], a[228:231], v[88:91]\n"
"v_mfma_f32_16x16x32_bf16 v[136:139], v[200:203], a[232:235], v[136:139]\n"
"v_mfma_f32_16x16x32_bf16 v[136:139], v[204:207], a[248:251], v[136:139]\n"
"v_mfma_f32_16x16x32_bf16 v[184:187], v[204:207], a[252:255], v[184:187]\n"
"v_mfma_f32_16x16x32_bf16 v[184:187], v[200:203], a[236:239], v[184:187]\n"
"s_waitcnt lgkmcnt(2)\n"
"v_mfma_f32_16x16x32_bf16 v[188:191], a[4:7], a[236:239], v[188:191]\n"
"s_waitcnt lgkmcnt(0)\n"
"v_mfma_f32_16x16x32_bf16 v[188:191], a[52:55], a[252:255], v[188:191]\n"
"v_mfma_f32_16x16x32_bf16 v[140:143], a[52:55], a[248:251], v[140:143]\n"
"v_mfma_f32_16x16x32_bf16 v[140:143], a[4:7], a[232:235], v[140:143]\n"
"v_mfma_f32_16x16x32_bf16 v[92:95], a[4:7], a[228:231], v[92:95]\n"
"v_mfma_f32_16x16x32_bf16 v[92:95], a[52:55], a[244:247], v[92:95]\n"
"v_mfma_f32_16x16x32_bf16 v[44:47], a[52:55], a[240:243], v[44:47]\n"
"v_mfma_f32_16x16x32_bf16 v[44:47], a[4:7], a[224:227], v[44:47]\n"
"s_branch .Lat_t2\n"
".Lat_t1:\n"
"s_waitcnt lgkmcnt(4)\n"
"v_mfma_f32_16x16x32_bf16 v[0:3], v[192:195], a[192:195], v[0:3]\n"
"s_waitcnt lgkmcnt(2)\n"
"ds_read_b64_tr_b16 v[204:205], v250 offset:4104\n"
"v_accvgpr_read_b32 v212, a116\n"
"v_mfma_f32_16x16x32_bf16 v[0:3], v[196:199], a[208:211], v[0:3]\n"
"s_add_u32 s73, s61, s62\n"
"s_mov_b32 s72, s73\n"
"v_accvgpr_read_b32 v213, a117\n"
"v_accvgpr_read_b32 v214, a118\n"
"v_mfma_f32_16x16x32_bf16 v[48:51], v[196:199], a[212:215], v[48:51]\n"
"ds_read_b64_tr_b16 v[206:207], v250 offset:5128\n"
"v_accvgpr_read_b32 v215, a119\n"
"v_mfma_f32_16x16x32_bf16 v[48:51], v[192:195], a[196:199], v[48:51]\n"
"v_mul_f32_dpp v212, v220, v212 row_newbcast:0 row_mask:0xf bank_mask:0xf\n"
"v_mul_f32_dpp v213, v220, v213 row_newbcast:4 row_mask:0xf bank_mask:0xf\n"
"v_mul_f32_dpp v214, v220, v214 row_newbcast:8 row_mask:0xf bank_mask:0xf\n"
"v_mfma_f32_16x16x32_bf16 v[96:99], v[192:195], a[200:203], v[96:99]\n"
"ds_read_b64_tr_b16 a[4:5], v250 offset:256\n"
"v_mul_f32_dpp v215, v220, v215 row_newbcast:12 row_mask:0xf bank_mask:0xf\n"
"v_mfma_f32_16x16x32_bf16 v[96:99], v[196:199], a[216:219], v[96:99]\n"
"v_cvt_pk_bf16_f32 v212, v212, v213\n"
"v_cvt_pk_bf16_f32 v213, v214, v215\n"
"v_accvgpr_read_b32 v208, a112\n"
"v_mfma_f32_16x16x32_bf16 v[144:147], v[196:199], a[220:223], v[144:147]\n"
"ds_read_b64_tr_b16 a[6:7], v250 offset:1280\n"
"ds_write_b64 v244, v[212:213] offset:51208\n"
"v_accvgpr_read_b32 v209, a113\n"
"v_mfma_f32_16x16x32_bf16 v[144:147], v[192:195], a[204:207], v[144:147]\n"
"s_waitcnt lgkmcnt(5)\n"
"v_accvgpr_read_b32 v210, a114\n"
"v_accvgpr_read_b32 v211, a115\n"
"v_mfma_f32_16x16x32_bf16 v[148:151], v[200:203], a[204:207], v[148:151]\n"
"s_waitcnt lgkmcnt(3)\n"
"ds_read_b64_tr_b16 a[52:53], v250 offset:4352\n"
"v_mul_f32_dpp v208, v220, v208 row_newbcast:0 row_mask:0xf bank_mask:0xf\n"
"v_mul_f32_dpp v209, v220, v209 row_newbcast:4 row_mask:0xf bank_mask:0xf\n"
"v_mfma_f32_16x16x32_bf16 v[148:151], v[204:207], a[220:223], v[148:151]\n"
"v_mul_f32_dpp v210, v220, v210 row_newbcast:8 row_mask:0xf bank_mask:0xf\n"
"v_mul_f32_dpp v211, v220, v211 row_newbcast:12 row_mask:0xf bank_mask:0xf\n"
"v_mfma_f32_16x16x32_bf16 v[100:103], v[204:207], a[216:219], v[100:103]\n"
"ds_read_b64_tr_b16 a[54:55], v250 offset:5376\n"
"v_cvt_pk_bf16_f32 v211, v210, v211\n"
"v_cvt_pk_bf16_f32 v210, v208, v209\n"
"v_mfma_f32_16x16x32_bf16 v[100:103], v[200:203], a[200:203], v[100:103]\n"
"ds_write_b64 v244, v[210:211] offset:51200\n"
"v_accvgpr_read_b32 v212, a164\n"
"v_mfma_f32_16x16x32_bf16 v[52:55], v[200:203], a[196:199], v[52:55]\n"
"ds_read_b64_tr_b16 v[192:193], v250 offset:264\n"
"v_accvgpr_read_b32 v213, a165\n"
"v_mfma_f32_16x16x32_bf16 v[52:55], v[204:207], a[212:215], v[52:55]\n"
"v_accvgpr_read_b32 v214, a166\n"
"v_accvgpr_read_b32 v215, a167\n"
"v_mul_f32_dpp v212, v222, v212 row_newbcast:0 row_mask:0xf bank_mask:0xf\n"
"v_mfma_f32_16x16x32_bf16 v[4:7], v[204:207], a[208:211], v[4:7]\n"
"ds_read_b64_tr_b16 v[194:195], v250 offset:1288\n"
"ds_read_b64_tr_b16 a[112:113], v221 offset:51200\n"
"v_mfma_f32_16x16x32_bf16 v[4:7], v[200:203], a[192:195], v[4:7]\n"
"s_waitcnt lgkmcnt(7)\n"
"ds_read_b64_tr_b16 a[114:115], v221 offset:51208\n"
"v_mul_f32_dpp v213, v222, v213 row_newbcast:4 row_mask:0xf bank_mask:0xf\n"
"v_mul_f32_dpp v214, v222, v214 row_newbcast:8 row_mask:0xf bank_mask:0xf\n"
"v_mfma_f32_16x16x32_bf16 v[8:11], a[4:7], a[192:195], v[8:11]\n"
"s_waitcnt lgkmcnt(5)\n"
"ds_read_b64_tr_b16 v[196:197], v250 offset:4360\n"
"v_mul_f32_dpp v215, v222, v215 row_newbcast:12 row_mask:0xf bank_mask:0xf\n"
"v_cvt_pk_bf16_f32 v212, v212, v213\n"
"v_mfma_f32_16x16x32_bf16 v[8:11], a[52:55], a[208:211], v[8:11]\n"
"v_cvt_pk_bf16_f32 v213, v214, v215\n"
"v_accvgpr_read_b32 v208, a160\n"
"v_mfma_f32_16x16x32_bf16 v[56:59], a[52:55], a[212:215], v[56:59]\n"
"ds_read_b64_tr_b16 v[198:199], v250 offset:5384\n"
"ds_write_b64 v244, v[212:213] offset:51208\n"
"v_accvgpr_read_b32 v209, a161\n"
"v_mfma_f32_16x16x32_bf16 v[56:59], a[4:7], a[196:199], v[56:59]\n"
"v_accvgpr_read_b32 v210, a162\n"
"v_accvgpr_read_b32 v211, a163\n"
"v_mfma_f32_16x16x32_bf16 v[104:107], a[4:7], a[200:203], v[104:107]\n"
"ds_read_b64_tr_b16 v[200:201], v250 offset:8192\n"
"v_mul_f32_dpp v208, v222, v208 row_newbcast:0 row_mask:0xf bank_mask:0xf\n"
"v_mul_f32_dpp v209, v222, v209 row_newbcast:4 row_mask:0xf bank_mask:0xf\n"
"v_mfma_f32_16x16x32_bf16 v[104:107], a[52:55], a[216:219], v[104:107]\n"
"s_waitcnt lgkmcnt(4)\n"
"buffer_store_dwordx4 a[112:115], v253, s[56:59], s76 offen offset:0 sc1\n"
"v_mul_f32_dpp v210, v222, v210 row_newbcast:8 row_mask:0xf bank_mask:0xf\n"
"v_mul_f32_dpp v211, v222, v211 row_newbcast:12 row_mask:0xf bank_mask:0xf\n"
"v_cvt_pk_bf16_f32 v211, v210, v211\n"
"v_cvt_pk_bf16_f32 v210, v208, v209\n"
"v_mfma_f32_16x16x32_bf16 v[152:155], a[52:55], a[220:223], v[152:155]\n"
"ds_read_b64_tr_b16 v[202:203], v250 offset:9216\n"
"ds_write_b64 v244, v[210:211] offset:51200\n"
"v_mfma_f32_16x16x32_bf16 v[152:155], a[4:7], a[204:207], v[152:155]\n"
"v_accvgpr_read_b32 v212, a28\n"
"v_accvgpr_read_b32 v213, a29\n"
"v_accvgpr_read_b32 v214, a30\n"
"v_mfma_f32_16x16x32_bf16 v[156:159], v[192:195], a[204:207], v[156:159]\n"
"s_waitcnt lgkmcnt(4)\n"
"ds_read_b64_tr_b16 v[204:205], v250 offset:12288\n"
"v_accvgpr_read_b32 v215, a31\n"
"v_mfma_f32_16x16x32_bf16 v[156:159], v[196:199], a[220:223], v[156:159]\n"
"ds_read_b64_tr_b16 a[160:161], v221 offset:51200\n"
"ds_read_b64_tr_b16 a[162:163], v221 offset:51208\n"
"v_mul_f32_dpp v212, v216, v212 row_newbcast:0 row_mask:0xf bank_mask:0xf\n"
"v_mfma_f32_16x16x32_bf16 v[108:111], v[196:199], a[216:219], v[108:111]\n"
"ds_read_b64_tr_b16 v[206:207], v250 offset:13312\n"
"v_mul_f32_dpp v213, v216, v213 row_newbcast:4 row_mask:0xf bank_mask:0xf\n"
"v_mul_f32_dpp v214, v216, v214 row_newbcast:8 row_mask:0xf bank_mask:0xf\n"
"v_mfma_f32_16x16x32_bf16 v[108:111], v[192:195], a[200:203], v[108:111]\n"
"v_mul_f32_dpp v215, v216, v215 row_newbcast:12 row_mask:0xf bank_mask:0xf\n"
"v_cvt_pk_bf16_f32 v212, v212, v213\n"
"v_mfma_f32_16x16x32_bf16 v[60:63], v[192:195], a[196:199], v[60:63]\n"
"ds_read_b64_tr_b16 a[4:5], v250 offset:8200\n"
"v_cvt_pk_bf16_f32 v213, v214, v215\n"
"v_accvgpr_read_b32 v208, a24\n"
"v_mfma_f32_16x16x32_bf16 v[60:63], v[196:199], a[212:215], v[60:63]\n"
"ds_write_b64 v244, v[212:213] offset:51208\n"
"v_mfma_f32_16x16x32_bf16 v[12:15], v[196:199], a[208:211], v[12:15]\n"
"ds_read_b64_tr_b16 a[6:7], v250 offset:9224\n"
"v_accvgpr_read_b32 v209, a25\n"
"v_accvgpr_read_b32 v210, a26\n"
"v_mfma_f32_16x16x32_bf16 v[12:15], v[192:195], a[192:195], v[12:15]\n"
"s_waitcnt lgkmcnt(8)\n"
"v_accvgpr_read_b32 v211, a27\n"
"v_mul_f32_dpp v208, v216, v208 row_newbcast:0 row_mask:0xf bank_mask:0xf\n"
"v_mul_f32_dpp v209, v216, v209 row_newbcast:4 row_mask:0xf bank_mask:0xf\n"
"v_mfma_f32_16x16x32_bf16 v[16:19], v[200:203], a[192:195], v[16:19]\n"
"s_waitcnt lgkmcnt(3)\n"
"buffer_store_dwordx4 a[160:163], v253, s[56:59], s76 offen offset:2048 sc1\n"
"ds_read_b64_tr_b16 a[52:53], v250 offset:12296\n"
"v_mul_f32_dpp v210, v216, v210 row_newbcast:8 row_mask:0xf bank_mask:0xf\n"
"v_mul_f32_dpp v211, v216, v211 row_newbcast:12 row_mask:0xf bank_mask:0xf\n"
"v_mfma_f32_16x16x32_bf16 v[16:19], v[204:207], a[208:211], v[16:19]\n"
"s_waitcnt vmcnt(10)\n"
"s_barrier\n"
"v_cvt_pk_bf16_f32 v211, v210, v211\n"
"v_mfma_f32_16x16x32_bf16 v[64:67], v[204:207], a[212:215], v[64:67]\n"
"s_add_u32 m0, s79, 40960\n"
"ds_read_b64_tr_b16 a[54:55], v250 offset:13320\n"
"v_cvt_pk_bf16_f32 v210, v208, v209\n"
"v_mfma_f32_16x16x32_bf16 v[64:67], v[200:203], a[196:199], v[64:67]\n"
"buffer_load_dwordx4 v242, s[48:51], s74 offen offset:0 lds\n"
"v_accvgpr_read_b32 v212, a76\n"
"v_mfma_f32_16x16x32_bf16 v[112:115], v[200:203], a[200:203], v[112:115]\n"
"ds_read_b64_tr_b16 v[192:193], v250 offset:8448\n"
"ds_write_b64 v244, v[210:211] offset:51200\n"
"v_accvgpr_read_b32 v213, a77\n"
"v_mfma_f32_16x16x32_bf16 v[112:115], v[204:207], a[216:219], v[112:115]\n"
"buffer_load_dwordx4 v243, s[48:51], s74 offen offset:1024 lds\n"
"v_mfma_f32_16x16x32_bf16 v[160:163], v[204:207], a[220:223], v[160:163]\n"
"s_add_u32 s74, s74, 786176\n"
"ds_read_b64_tr_b16 v[194:195], v250 offset:9472\n"
"v_accvgpr_read_b32 v214, a78\n"
"v_accvgpr_read_b32 v215, a79\n"
"v_mfma_f32_16x16x32_bf16 v[160:163], v[200:203], a[204:207], v[160:163]\n"
"s_waitcnt lgkmcnt(5)\n"
"ds_read_b64_tr_b16 a[24:25], v221 offset:51200\n"
"ds_read_b64_tr_b16 a[26:27], v221 offset:51208\n"
"v_mfma_f32_16x16x32_bf16 v[164:167], a[4:7], a[204:207], v[164:167]\n"
"s_waitcnt lgkmcnt(5)\n"
"ds_read_b64_tr_b16 v[196:197], v250 offset:12544\n"
"v_mul_f32_dpp v212, v218, v212 row_newbcast:0 row_mask:0xf bank_mask:0xf\n"
"v_mul_f32_dpp v213, v218, v213 row_newbcast:4 row_mask:0xf bank_mask:0xf\n"
"v_mfma_f32_16x16x32_bf16 v[164:167], a[52:55], a[220:223], v[164:167]\n"
"v_mul_f32_dpp v214, v218, v214 row_newbcast:8 row_mask:0xf bank_mask:0xf\n"
"v_mul_f32_dpp v215, v218, v215 row_newbcast:12 row_mask:0xf bank_mask:0xf\n"
"v_cvt_pk_bf16_f32 v212, v212, v213\n"
"v_mfma_f32_16x16x32_bf16 v[116:119], a[52:55], a[216:219], v[116:119]\n"
"ds_read_b64_tr_b16 v[198:199], v250 offset:13568\n"
"v_cvt_pk_bf16_f32 v213, v214, v215\n"
"v_mfma_f32_16x16x32_bf16 v[116:119], a[4:7], a[200:203], v[116:119]\n"
"ds_write_b64 v244, v[212:213] offset:51208\n"
"v_accvgpr_read_b32 v208, a72\n"
"v_mfma_f32_16x16x32_bf16 v[68:71], a[4:7], a[196:199], v[68:71]\n"
"ds_read_b64_tr_b16 v[200:201], v250 offset:8456\n"
"v_accvgpr_read_b32 v209, a73\n"
"v_mfma_f32_16x16x32_bf16 v[68:71], a[52:55], a[212:215], v[68:71]\n"
"s_waitcnt lgkmcnt(4)\n"
"buffer_store_dwordx4 a[24:27], v251, s[56:59], s76 offen offset:1024 sc1\n"
"v_accvgpr_read_b32 v210, a74\n"
"v_accvgpr_read_b32 v211, a75\n"
"v_mul_f32_dpp v208, v218, v208 row_newbcast:0 row_mask:0xf bank_mask:0xf\n"
"v_mul_f32_dpp v209, v218, v209 row_newbcast:4 row_mask:0xf bank_mask:0xf\n"
"v_mfma_f32_16x16x32_bf16 v[20:23], a[52:55], a[208:211], v[20:23]\n"
"ds_read_b64_tr_b16 v[202:203], v250 offset:9480\n"
"v_mul_f32_dpp v210, v218, v210 row_newbcast:8 row_mask:0xf bank_mask:0xf\n"
"v_mul_f32_dpp v211, v218, v211 row_newbcast:12 row_mask:0xf bank_mask:0xf\n"
"v_mfma_f32_16x16x32_bf16 v[20:23], a[4:7], a[192:195], v[20:23]\n"
"v_cvt_pk_bf16_f32 v211, v210, v211\n"
"v_mfma_f32_16x16x32_bf16 v[24:27], v[192:195], a[192:195], v[24:27]\n"
"s_waitcnt lgkmcnt(3)\n"
"ds_read_b64_tr_b16 v[204:205], v250 offset:12552\n"
"v_cvt_pk_bf16_f32 v210, v208, v209\n"
"v_mfma_f32_16x16x32_bf16 v[24:27], v[196:199], a[208:211], v[24:27]\n"
"ds_read_b128 a[224:227], v217 offset:0\n"
"ds_write_b64 v244, v[210:211] offset:51200\n"
"v_accvgpr_read_b32 v212, a124\n"
"v_mfma_f32_16x16x32_bf16 v[72:75], v[196:199], a[212:215], v[72:75]\n"
"ds_read_b64_tr_b16 v[206:207], v250 offset:13576\n"
"v_accvgpr_read_b32 v213, a125\n"
"v_mfma_f32_16x16x32_bf16 v[72:75], v[192:195], a[196:199], v[72:75]\n"
"v_accvgpr_read_b32 v214, a126\n"
"v_accvgpr_read_b32 v215, a127\n"
"v_mul_f32_dpp v212, v220, v212 row_newbcast:0 row_mask:0xf bank_mask:0xf\n"
"v_mfma_f32_16x16x32_bf16 v[120:123], v[192:195], a[200:203], v[120:123]\n"
"s_add_u32 m0, s77, 24576\n"
"ds_read_b64_tr_b16 a[4:5], v250 offset:16384\n"
"ds_read_b64_tr_b16 a[72:73], v221 offset:51200\n"
"v_mfma_f32_16x16x32_bf16 v[120:123], v[196:199], a[216:219], v[120:123]\n"
"buffer_load_dwordx4 v236, s[40:43], s72 offen offset:0 nt lds\n"
"ds_read_b64_tr_b16 a[74:75], v221 offset:51208\n"
"v_mfma_f32_16x16x32_bf16 v[168:171], v[196:199], a[220:223], v[168:171]\n"
"ds_read_b64_tr_b16 a[6:7], v250 offset:17408\n"
"v_mul_f32_dpp v213, v220, v213 row_newbcast:4 row_mask:0xf bank_mask:0xf\n"
"v_mul_f32_dpp v214, v220, v214 row_newbcast:8 row_mask:0xf bank_mask:0xf\n"
"v_mfma_f32_16x16x32_bf16 v[168:171], v[192:195], a[204:207], v[168:171]\n"
"s_waitcnt lgkmcnt(8)\n"
"ds_read_b128 a[240:243], v219 offset:0\n"
"v_mul_f32_dpp v215, v220, v215 row_newbcast:12 row_mask:0xf bank_mask:0xf\n"
"v_mfma_f32_16x16x32_bf16 v[172:175], v[200:203], a[204:207], v[172:175]\n"
"s_waitcnt lgkmcnt(5)\n"
"ds_read_b64_tr_b16 a[52:53], v250 offset:20480\n"
"v_cvt_pk_bf16_f32 v212, v212, v213\n"
"v_cvt_pk_bf16_f32 v213, v214, v215\n"
"v_mfma_f32_16x16x32_bf16 v[172:175], v[204:207], a[220:223], v[172:175]\n"
"ds_write_b64 v244, v[212:213] offset:51208\n"
"v_mfma_f32_16x16x32_bf16 v[124:127], v[204:207], a[216:219], v[124:127]\n"
"ds_read_b64_tr_b16 a[54:55], v250 offset:21504\n"
"v_accvgpr_read_b32 v208, a120\n"
"v_accvgpr_read_b32 v209, a121\n"
"v_mfma_f32_16x16x32_bf16 v[124:127], v[200:203], a[200:203], v[124:127]\n"
"s_waitcnt lgkmcnt(5)\n"
"buffer_store_dwordx4 a[72:75], v251, s[56:59], s76 offen offset:3072 sc1\n"
"ds_read_b128 a[244:247], v219 offset:2048\n"
"v_accvgpr_read_b32 v210, a122\n"
"v_accvgpr_read_b32 v211, a123\n"
"v_mul_f32_dpp v208, v220, v208 row_newbcast:0 row_mask:0xf bank_mask:0xf\n"
"v_mfma_f32_16x16x32_bf16 v[76:79], v[200:203], a[196:199], v[76:79]\n"
"ds_read_b64_tr_b16 v[192:193], v250 offset:16392\n"
"v_mul_f32_dpp v209, v220, v209 row_newbcast:4 row_mask:0xf bank_mask:0xf\n"
"v_mfma_f32_16x16x32_bf16 v[76:79], v[204:207], a[212:215], v[76:79]\n"
"buffer_load_dwordx4 v237, s[40:43], s72 offen offset:1024 nt lds\n"
"v_mul_f32_dpp v210, v220, v210 row_newbcast:8 row_mask:0xf bank_mask:0xf\n"
"v_mfma_f32_16x16x32_bf16 v[28:31], v[204:207], a[208:211], v[28:31]\n"
"ds_read_b64_tr_b16 v[194:195], v250 offset:17416\n"
"v_mul_f32_dpp v211, v220, v211 row_newbcast:12 row_mask:0xf bank_mask:0xf\n"
"v_mfma_f32_16x16x32_bf16 v[28:31], v[200:203], a[192:195], v[28:31]\n"
"s_waitcnt lgkmcnt(7)\n"
"ds_read_b128 a[228:231], v217 offset:2048\n"
"v_cvt_pk_bf16_f32 v211, v210, v211\n"
"v_cvt_pk_bf16_f32 v210, v208, v209\n"
"v_mfma_f32_16x16x32_bf16 v[32:35], a[4:7], a[192:195], v[32:35]\n"
"s_waitcnt lgkmcnt(4)\n"
"ds_read_b64_tr_b16 v[196:197], v250 offset:20488\n"
"ds_write_b64 v244, v[210:211] offset:51200\n"
"v_accvgpr_read_b32 v212, a172\n"
"v_mfma_f32_16x16x32_bf16 v[32:35], a[52:55], a[208:211], v[32:35]\n"
"v_accvgpr_read_b32 v213, a173\n"
"v_accvgpr_read_b32 v214, a174\n"
"v_mfma_f32_16x16x32_bf16 v[80:83], a[52:55], a[212:215], v[80:83]\n"
"ds_read_b64_tr_b16 v[198:199], v250 offset:21512\n"
"v_accvgpr_read_b32 v215, a175\n"
"v_mul_f32_dpp v212, v222, v212 row_newbcast:0 row_mask:0xf bank_mask:0xf\n"
"v_mfma_f32_16x16x32_bf16 v[80:83], a[4:7], a[196:199], v[80:83]\n"
"ds_read_b128 a[232:235], v217 offset:4096\n"
"ds_read_b64_tr_b16 a[120:121], v221 offset:51200\n"
"v_mfma_f32_16x16x32_bf16 v[128:131], a[4:7], a[200:203], v[128:131]\n"
"ds_read_b64_tr_b16 v[200:201], v250 offset:16640\n"
"ds_read_b64_tr_b16 a[122:123], v221 offset:51208\n"
"v_mul_f32_dpp v213, v222, v213 row_newbcast:4 row_mask:0xf bank_mask:0xf\n"
"v_mfma_f32_16x16x32_bf16 v[128:131], a[52:55], a[216:219], v[128:131]\n"
"buffer_load_dwordx4 v238, s[40:43], s72 offen offset:2048 nt lds\n"
"v_mul_f32_dpp v214, v222, v214 row_newbcast:8 row_mask:0xf bank_mask:0xf\n"
"v_mfma_f32_16x16x32_bf16 v[176:179], a[52:55], a[220:223], v[176:179]\n"
"s_add_u32 s72, s72, 3072\n"
"ds_read_b64_tr_b16 v[202:203], v250 offset:17664\n"
"v_mul_f32_dpp v215, v222, v215 row_newbcast:12 row_mask:0xf bank_mask:0xf\n"
"v_mfma_f32_16x16x32_bf16 v[176:179], a[4:7], a[204:207], v[176:179]\n"
"s_waitcnt lgkmcnt(9)\n"
"ds_read_b128 a[248:251], v219 offset:4096\n"
"v_cvt_pk_bf16_f32 v212, v212, v213\n"
"v_cvt_pk_bf16_f32 v213, v214, v215\n"
"v_mfma_f32_16x16x32_bf16 v[180:183], v[192:195], a[204:207], v[180:183]\n"
"s_waitcnt lgkmcnt(6)\n"
"ds_read_b64_tr_b16 v[204:205], v250 offset:20736\n"
"ds_write_b64 v244, v[212:213] offset:51208\n"
"v_mfma_f32_16x16x32_bf16 v[180:183], v[196:199], a[220:223], v[180:183]\n"
"v_accvgpr_read_b32 v208, a168\n"
"v_accvgpr_read_b32 v209, a169\n"
"v_accvgpr_read_b32 v210, a170\n"
"v_mfma_f32_16x16x32_bf16 v[132:135], v[196:199], a[216:219], v[132:135]\n"
"s_waitcnt lgkmcnt(4)\n"
"buffer_store_dwordx4 a[120:123], v253, s[56:59], s76 offen offset:1024 sc1\n"
"ds_read_b64_tr_b16 v[206:207], v250 offset:21760\n"
"v_accvgpr_read_b32 v211, a171\n"
"v_mul_f32_dpp v208, v222, v208 row_newbcast:0 row_mask:0xf bank_mask:0xf\n"
"v_mul_f32_dpp v209, v222, v209 row_newbcast:4 row_mask:0xf bank_mask:0xf\n"
"v_mfma_f32_16x16x32_bf16 v[132:135], v[192:195], a[200:203], v[132:135]\n"
"ds_read_b128 a[252:255], v219 offset:6144\n"
"v_mul_f32_dpp v210, v222, v210 row_newbcast:8 row_mask:0xf bank_mask:0xf\n"
"v_mfma_f32_16x16x32_bf16 v[84:87], v[192:195], a[196:199], v[84:87]\n"
"ds_read_b64_tr_b16 a[4:5], v250 offset:16648\n"
"v_mul_f32_dpp v211, v222, v211 row_newbcast:12 row_mask:0xf bank_mask:0xf\n"
"v_cvt_pk_bf16_f32 v211, v210, v211\n"
"v_mfma_f32_16x16x32_bf16 v[84:87], v[196:199], a[212:215], v[84:87]\n"
"v_cvt_pk_bf16_f32 v210, v208, v209\n"
"v_accvgpr_read_b32 v212, a36\n"
"v_mfma_f32_16x16x32_bf16 v[36:39], v[196:199], a[208:211], v[36:39]\n"
"ds_read_b64_tr_b16 a[6:7], v250 offset:17672\n"
"ds_write_b64 v244, v[210:211] offset:51200\n"
"v_accvgpr_read_b32 v213, a37\n"
"v_mfma_f32_16x16x32_bf16 v[36:39], v[192:195], a[192:195], v[36:39]\n"
"s_waitcnt lgkmcnt(8)\n"
"ds_read_b128 a[236:239], v217 offset:6144\n"
"v_accvgpr_read_b32 v214, a38\n"
"v_accvgpr_read_b32 v215, a39\n"
"v_mfma_f32_16x16x32_bf16 v[40:43], v[200:203], a[192:195], v[40:43]\n"
"s_waitcnt lgkmcnt(5)\n"
"ds_read_b64_tr_b16 a[52:53], v250 offset:20744\n"
"ds_read_b64_tr_b16 a[168:169], v221 offset:51200\n"
"v_mfma_f32_16x16x32_bf16 v[40:43], v[204:207], a[208:211], v[40:43]\n"
"s_waitcnt vmcnt(10)\n"
"s_barrier\n"
"ds_read_b64_tr_b16 a[170:171], v221 offset:51208\n"
"v_mfma_f32_16x16x32_bf16 v[88:91], v[204:207], a[212:215], v[88:91]\n"
"s_mov_b32 s80, 8\n"
"ds_read_b64_tr_b16 a[54:55], v250 offset:21768\n"
"v_mul_f32_dpp v212, v216, v212 row_newbcast:0 row_mask:0xf bank_mask:0xf\n"
"v_mfma_f32_16x16x32_bf16 v[88:91], v[200:203], a[196:199], v[88:91]\n"
"v_mul_f32_dpp v213, v216, v213 row_newbcast:4 row_mask:0xf bank_mask:0xf\n"
"v_mul_f32_dpp v214, v216, v214 row_newbcast:8 row_mask:0xf bank_mask:0xf\n"
"v_mul_f32_dpp v215, v216, v215 row_newbcast:12 row_mask:0xf bank_mask:0xf\n"
"v_mfma_f32_16x16x32_bf16 v[136:139], v[200:203], a[200:203], v[136:139]\n"
"ds_read_b64_tr_b16 v[192:193], v250 offset:24576\n"
"v_cvt_pk_bf16_f32 v212, v212, v213\n"
"v_cvt_pk_bf16_f32 v213, v214, v215\n"
"v_mfma_f32_16x16x32_bf16 v[136:139], v[204:207], a[216:219], v[136:139]\n"
"ds_write_b64 v244, v[212:213] offset:51208\n"
"v_mfma_f32_16x16x32_bf16 v[184:187], v[204:207], a[220:223], v[184:187]\n"
"ds_read_b64_tr_b16 v[194:195], v250 offset:25600\n"
"v_accvgpr_read_b32 v208, a32\n"
"v_accvgpr_read_b32 v209, a33\n"
"v_mfma_f32_16x16x32_bf16 v[184:187], v[200:203], a[204:207], v[184:187]\n"
"s_waitcnt lgkmcnt(9)\n"
"v_accvgpr_read_b32 v210, a34\n"
"v_accvgpr_read_b32 v211, a35\n"
"v_mfma_f32_16x16x32_bf16 v[188:191], a[4:7], a[204:207], v[188:191]\n"
"s_waitcnt lgkmcnt(3)\n"
"buffer_store_dwordx4 a[168:171], v253, s[56:59], s76 offen offset:3072 sc1\n"
"ds_read_b64_tr_b16 v[196:197], v250 offset:28672\n"
"s_add_u32 s76, s76, 524288\n"
"v_mul_f32_dpp v208, v216, v208 row_newbcast:0 row_mask:0xf bank_mask:0xf\n"
"v_mul_f32_dpp v209, v216, v209 row_newbcast:4 row_mask:0xf bank_mask:0xf\n"
"v_mfma_f32_16x16x32_bf16 v[188:191], a[52:55], a[220:223], v[188:191]\n"
"v_mul_f32_dpp v210, v216, v210 row_newbcast:8 row_mask:0xf bank_mask:0xf\n"
"v_mul_f32_dpp v211, v216, v211 row_newbcast:12 row_mask:0xf bank_mask:0xf\n"
"v_cvt_pk_bf16_f32 v211, v210, v211\n"
"v_mfma_f32_16x16x32_bf16 v[140:143], a[52:55], a[216:219], v[140:143]\n"
"ds_read_b64_tr_b16 v[198:199], v250 offset:29696\n"
"v_cvt_pk_bf16_f32 v210, v208, v209\n"
"v_mfma_f32_16x16x32_bf16 v[140:143], a[4:7], a[200:203], v[140:143]\n"
"ds_write_b64 v244, v[210:211] offset:51200\n"
"v_accvgpr_read_b32 v212, a84\n"
"v_mfma_f32_16x16x32_bf16 v[92:95], a[4:7], a[196:199], v[92:95]\n"
"ds_read_b64_tr_b16 v[200:201], v250 offset:24584\n"
"v_accvgpr_read_b32 v213, a85\n"
"v_mfma_f32_16x16x32_bf16 v[92:95], a[52:55], a[212:215], v[92:95]\n"
"v_accvgpr_read_b32 v214, a86\n"
"v_accvgpr_read_b32 v215, a87\n"
"v_mul_f32_dpp v212, v218, v212 row_newbcast:0 row_mask:0xf bank_mask:0xf\n"
"v_mfma_f32_16x16x32_bf16 v[44:47], a[52:55], a[208:211], v[44:47]\n"
"ds_read_b64_tr_b16 v[202:203], v250 offset:25608\n"
"ds_read_b64_tr_b16 a[32:33], v221 offset:51200\n"
"ds_read_b64_tr_b16 a[34:35], v221 offset:51208\n"
"v_mfma_f32_16x16x32_bf16 v[44:47], a[4:7], a[192:195], v[44:47]\n"
"s_waitcnt lgkmcnt(7)\n"
"v_mul_f32_dpp v213, v218, v213 row_newbcast:4 row_mask:0xf bank_mask:0xf\n"
"v_mfma_f32_16x16x32_bf16 v[0:3], v[192:195], a[224:227], v[0:3]\n"
"s_waitcnt lgkmcnt(5)\n"
"ds_read_b64_tr_b16 v[204:205], v250 offset:28680\n"
"v_readlane_b32 s26, v255, 0\n"
"v_mfma_f32_16x16x32_bf16 v[0:3], v[196:199], a[240:243], v[0:3]\n"
"v_mul_f32_dpp v214, v218, v214 row_newbcast:8 row_mask:0xf bank_mask:0xf\n"
"v_mul_f32_dpp v215, v218, v215 row_newbcast:12 row_mask:0xf bank_mask:0xf\n"
"v_cvt_pk_bf16_f32 v212, v212, v213\n"
"v_mfma_f32_16x16x32_bf16 v[48:51], v[196:199], a[244:247], v[48:51]\n"
"ds_read_b64_tr_b16 v[206:207], v250 offset:29704\n"
"v_readlane_b32 s32, v255, 26\n"
"v_mfma_f32_16x16x32_bf16 v[48:51], v[192:195], a[228:231], v[48:51]\n"
"v_cvt_pk_bf16_f32 v213, v214, v215\n"
"v_accvgpr_read_b32 v208, a80\n"
"v_accvgpr_read_b32 v209, a81\n"
"v_mfma_f32_16x16x32_bf16 v[96:99], v[192:195], a[232:235], v[96:99]\n"
"s_add_u32 s32, s32, 102400\n"
"ds_read_b64_tr_b16 a[4:5], v250 offset:24832\n"
"ds_write_b64 v244, v[212:213] offset:51208\n"
"v_mfma_f32_16x16x32_bf16 v[96:99], v[196:199], a[248:251], v[96:99]\n"
"s_waitcnt lgkmcnt(4)\n"
"buffer_store_dwordx4 a[32:35], v251, s[56:59], s76 offen offset:0 sc1\n"
"s_add_u32 s24, s63, 147456\n"
"v_accvgpr_read_b32 v210, a82\n"
"v_accvgpr_read_b32 v211, a83\n"
"v_mul_f32_dpp v208, v218, v208 row_newbcast:0 row_mask:0xf bank_mask:0xf\n"
"v_mfma_f32_16x16x32_bf16 v[144:147], v[196:199], a[252:255], v[144:147]\n"
"s_mov_b32 s70, 186\n"
"ds_read_b64_tr_b16 a[6:7], v250 offset:25856\n"
"v_mul_f32_dpp v209, v218, v209 row_newbcast:4 row_mask:0xf bank_mask:0xf\n"
"v_mul_f32_dpp v210, v218, v210 row_newbcast:8 row_mask:0xf bank_mask:0xf\n"
"v_mfma_f32_16x16x32_bf16 v[144:147], v[192:195], a[236:239], v[144:147]\n"
"v_mul_f32_dpp v211, v218, v211 row_newbcast:12 row_mask:0xf bank_mask:0xf\n"
"v_cvt_pk_bf16_f32 v211, v210, v211\n"
"v_mfma_f32_16x16x32_bf16 v[148:151], v[200:203], a[236:239], v[148:151]\n"
"s_waitcnt lgkmcnt(3)\n"
"ds_read_b64_tr_b16 a[52:53], v250 offset:28928\n"
"v_cvt_pk_bf16_f32 v210, v208, v209\n"
"v_accvgpr_read_b32 v212, a132\n"
"v_mfma_f32_16x16x32_bf16 v[148:151], v[204:207], a[252:255], v[148:151]\n"
"s_mov_b64 s[20:21], s[28:29]\n"
"ds_write_b64 v244, v[210:211] offset:51200\n"
"v_mfma_f32_16x16x32_bf16 v[100:103], v[204:207], a[248:251], v[100:103]\n"
"s_mov_b64 s[22:23], s[30:31]\n"
"ds_read_b64_tr_b16 a[54:55], v250 offset:29952\n"
"v_accvgpr_read_b32 v213, a133\n"
"v_accvgpr_read_b32 v214, a134\n"
"v_mfma_f32_16x16x32_bf16 v[100:103], v[200:203], a[232:235], v[100:103]\n"
"s_mov_b32 s18, 0xf0000000\n"
"v_accvgpr_read_b32 v215, a135\n"
"v_mul_f32_dpp v212, v220, v212 row_newbcast:0 row_mask:0xf bank_mask:0xf\n"
"v_mul_f32_dpp v213, v220, v213 row_newbcast:4 row_mask:0xf bank_mask:0xf\n"
"v_mfma_f32_16x16x32_bf16 v[52:55], v[200:203], a[228:231], v[52:55]\n"
"s_mov_b32 s11, 0\n"
"ds_read_b64_tr_b16 v[192:193], v250 offset:24840\n"
"ds_read_b64_tr_b16 a[80:81], v221 offset:51200\n"
"v_mfma_f32_16x16x32_bf16 v[52:55], v[204:207], a[244:247], v[52:55]\n"
"s_mov_b32 s71, 81920\n"
"ds_read_b64_tr_b16 a[82:83], v221 offset:51208\n"
"v_mul_f32_dpp v214, v220, v214 row_newbcast:8 row_mask:0xf bank_mask:0xf\n"
"v_mul_f32_dpp v215, v220, v215 row_newbcast:12 row_mask:0xf bank_mask:0xf\n"
"v_mfma_f32_16x16x32_bf16 v[4:7], v[204:207], a[240:243], v[4:7]\n"
"s_mov_b32 s35, 20480\n"
"ds_read_b64_tr_b16 v[194:195], v250 offset:25864\n"
"v_cvt_pk_bf16_f32 v212, v212, v213\n"
"v_mfma_f32_16x16x32_bf16 v[4:7], v[200:203], a[224:227], v[4:7]\n"
"s_waitcnt lgkmcnt(7)\n"
"v_cvt_pk_bf16_f32 v213, v214, v215\n"
"v_accvgpr_read_b32 v208, a128\n"
"v_accvgpr_read_b32 v209, a129\n"
"v_mfma_f32_16x16x32_bf16 v[8:11], a[4:7], a[224:227], v[8:11]\n"
"s_waitcnt lgkmcnt(4)\n"
"ds_read_b64_tr_b16 v[196:197], v250 offset:28936\n"
"ds_write_b64 v244, v[212:213] offset:51208\n"
"v_accvgpr_read_b32 v210, a130\n"
"v_mfma_f32_16x16x32_bf16 v[8:11], a[52:55], a[240:243], v[8:11]\n"
"s_add_u32 s33, s24, 4096\n"
"v_accvgpr_read_b32 v211, a131\n"
"v_mul_f32_dpp v208, v220, v208 row_newbcast:0 row_mask:0xf bank_mask:0xf\n"
"v_mfma_f32_16x16x32_bf16 v[56:59], a[52:55], a[244:247], v[56:59]\n"
"s_mov_b32 s25, 0\n"
"ds_read_b64_tr_b16 v[198:199], v250 offset:29960\n"
"v_mul_f32_dpp v209, v220, v209 row_newbcast:4 row_mask:0xf bank_mask:0xf\n"
"v_mul_f32_dpp v210, v220, v210 row_newbcast:8 row_mask:0xf bank_mask:0xf\n"
"v_mfma_f32_16x16x32_bf16 v[56:59], a[4:7], a[228:231], v[56:59]\n"
"s_waitcnt lgkmcnt(4)\n"
"buffer_store_dwordx4 a[80:83], v251, s[56:59], s76 offen offset:2048 sc1\n"
"s_mov_b32 s19, 0\n"
"v_mul_f32_dpp v211, v220, v211 row_newbcast:12 row_mask:0xf bank_mask:0xf\n"
"v_cvt_pk_bf16_f32 v211, v210, v211\n"
"v_mfma_f32_16x16x32_bf16 v[104:107], a[4:7], a[232:235], v[104:107]\n"
"ds_read_b64_tr_b16 v[200:201], v250 offset:32768\n"
"v_cvt_pk_bf16_f32 v210, v208, v209\n"
"v_accvgpr_read_b32 v212, a180\n"
"v_mfma_f32_16x16x32_bf16 v[104:107], a[52:55], a[248:251], v[104:107]\n"
"ds_write_b64 v244, v[210:211] offset:51200\n"
"v_accvgpr_read_b32 v213, a181\n"
"v_mfma_f32_16x16x32_bf16 v[152:155], a[52:55], a[252:255], v[152:155]\n"
"ds_read_b64_tr_b16 v[202:203], v250 offset:33792\n"
"v_accvgpr_read_b32 v214, a182\n"
"v_mfma_f32_16x16x32_bf16 v[152:155], a[4:7], a[236:239], v[152:155]\n"
"s_waitcnt lgkmcnt(6)\n"
"v_accvgpr_read_b32 v215, a183\n"
"v_mul_f32_dpp v212, v222, v212 row_newbcast:0 row_mask:0xf bank_mask:0xf\n"
"v_mul_f32_dpp v213, v222, v213 row_newbcast:4 row_mask:0xf bank_mask:0xf\n"
"v_mfma_f32_16x16x32_bf16 v[156:159], v[192:195], a[236:239], v[156:159]\n"
"s_waitcnt lgkmcnt(3)\n"
"ds_read_b64_tr_b16 v[204:205], v250 offset:36864\n"
"ds_read_b64_tr_b16 a[128:129], v221 offset:51200\n"
"v_mfma_f32_16x16x32_bf16 v[156:159], v[196:199], a[252:255], v[156:159]\n"
"ds_read_b64_tr_b16 a[130:131], v221 offset:51208\n"
"v_mul_f32_dpp v214, v222, v214 row_newbcast:8 row_mask:0xf bank_mask:0xf\n"
"v_mul_f32_dpp v215, v222, v215 row_newbcast:12 row_mask:0xf bank_mask:0xf\n"
"v_mfma_f32_16x16x32_bf16 v[108:111], v[196:199], a[248:251], v[108:111]\n"
"ds_read_b64_tr_b16 v[206:207], v250 offset:37888\n"
"v_cvt_pk_bf16_f32 v212, v212, v213\n"
"v_cvt_pk_bf16_f32 v213, v214, v215\n"
"v_mfma_f32_16x16x32_bf16 v[108:111], v[192:195], a[232:235], v[108:111]\n"
"ds_write_b64 v244, v[212:213] offset:51208\n"
"v_mfma_f32_16x16x32_bf16 v[60:63], v[192:195], a[228:231], v[60:63]\n"
"ds_read_b64_tr_b16 a[4:5], v250 offset:32776\n"
"v_accvgpr_read_b32 v208, a176\n"
"v_accvgpr_read_b32 v209, a177\n"
"v_mfma_f32_16x16x32_bf16 v[60:63], v[196:199], a[244:247], v[60:63]\n"
"v_accvgpr_read_b32 v210, a178\n"
"v_accvgpr_read_b32 v211, a179\n"
"v_mfma_f32_16x16x32_bf16 v[12:15], v[196:199], a[240:243], v[12:15]\n"
"ds_read_b64_tr_b16 a[6:7], v250 offset:33800\n"
"v_mul_f32_dpp v208, v222, v208 row_newbcast:0 row_mask:0xf bank_mask:0xf\n"
"v_mul_f32_dpp v209, v222, v209 row_newbcast:4 row_mask:0xf bank_mask:0xf\n"
"v_mfma_f32_16x16x32_bf16 v[12:15], v[192:195], a[224:227], v[12:15]\n"
"s_waitcnt lgkmcnt(7)\n"
"v_mul_f32_dpp v210, v222, v210 row_newbcast:8 row_mask:0xf bank_mask:0xf\n"
"v_mul_f32_dpp v211, v222, v211 row_newbcast:12 row_mask:0xf bank_mask:0xf\n"
"v_cvt_pk_bf16_f32 v211, v210, v211\n"
"v_mfma_f32_16x16x32_bf16 v[16:19], v[200:203], a[224:227], v[16:19]\n"
"s_waitcnt lgkmcnt(3)\n"
"buffer_store_dwordx4 a[128:131], v253, s[56:59], s76 offen offset:0 sc1\n"
"ds_read_b64_tr_b16 a[52:53], v250 offset:36872\n"
"v_cvt_pk_bf16_f32 v210, v208, v209\n"
"v_accvgpr_read_b32 v212, a44\n"
"v_mfma_f32_16x16x32_bf16 v[16:19], v[204:207], a[240:243], v[16:19]\n"
"s_waitcnt vmcnt(10)\n"
"s_barrier\n"
"v_accvgpr_read_b32 v213, a45\n"
"v_mfma_f32_16x16x32_bf16 v[64:67], v[204:207], a[244:247], v[64:67]\n"
"ds_read_b64_tr_b16 a[54:55], v250 offset:37896\n"
"ds_write_b64 v244, v[210:211] offset:51200\n"
"v_mfma_f32_16x16x32_bf16 v[64:67], v[200:203], a[228:231], v[64:67]\n"
"v_accvgpr_read_b32 v214, a46\n"
"v_accvgpr_read_b32 v215, a47\n"
"v_mul_f32_dpp v212, v216, v212 row_newbcast:0 row_mask:0xf bank_mask:0xf\n"
"v_mfma_f32_16x16x32_bf16 v[112:115], v[200:203], a[232:235], v[112:115]\n"
"s_add_u32 m0, s77, 36864\n"
"ds_read_b64_tr_b16 v[192:193], v250 offset:33024\n"
"ds_read_b64_tr_b16 a[176:177], v221 offset:51200\n"
"ds_read_b64_tr_b16 a[178:179], v221 offset:51208\n"
"v_mfma_f32_16x16x32_bf16 v[112:115], v[204:207], a[248:251], v[112:115]\n"
"buffer_load_dwordx4 v236, s[40:43], s72 offen offset:0 nt lds\n"
"v_mfma_f32_16x16x32_bf16 v[160:163], v[204:207], a[252:255], v[160:163]\n"
"ds_read_b64_tr_b16 v[194:195], v250 offset:34048\n"
"v_mul_f32_dpp v213, v216, v213 row_newbcast:4 row_mask:0xf bank_mask:0xf\n"
"v_mul_f32_dpp v214, v216, v214 row_newbcast:8 row_mask:0xf bank_mask:0xf\n"
"v_mfma_f32_16x16x32_bf16 v[160:163], v[200:203], a[236:239], v[160:163]\n"
"s_waitcnt lgkmcnt(7)\n"
"v_mul_f32_dpp v215, v216, v215 row_newbcast:12 row_mask:0xf bank_mask:0xf\n"
"v_cvt_pk_bf16_f32 v212, v212, v213\n"
"v_mfma_f32_16x16x32_bf16 v[164:167], a[4:7], a[236:239], v[164:167]\n"
"s_waitcnt lgkmcnt(5)\n"
"ds_read_b64_tr_b16 v[196:197], v250 offset:37120\n"
"v_cvt_pk_bf16_f32 v213, v214, v215\n"
"v_accvgpr_read_b32 v208, a40\n"
"v_mfma_f32_16x16x32_bf16 v[164:167], a[52:55], a[252:255], v[164:167]\n"
"buffer_load_dwordx4 v237, s[40:43], s72 offen offset:1024 nt lds\n"
"v_accvgpr_read_b32 v209, a41\n"
"v_mfma_f32_16x16x32_bf16 v[116:119], a[52:55], a[248:251], v[116:119]\n"
"ds_read_b64_tr_b16 v[198:199], v250 offset:38144\n"
"ds_write_b64 v244, v[212:213] offset:51208\n"
"v_mfma_f32_16x16x32_bf16 v[116:119], a[4:7], a[232:235], v[116:119]\n"
"s_waitcnt lgkmcnt(4)\n"
"buffer_store_dwordx4 a[176:179], v253, s[56:59], s76 offen offset:2048 sc1\n"
"v_accvgpr_read_b32 v210, a42\n"
"v_accvgpr_read_b32 v211, a43\n"
"v_mul_f32_dpp v208, v216, v208 row_newbcast:0 row_mask:0xf bank_mask:0xf\n"
"v_mul_f32_dpp v209, v216, v209 row_newbcast:4 row_mask:0xf bank_mask:0xf\n"
"v_mfma_f32_16x16x32_bf16 v[68:71], a[4:7], a[228:231], v[68:71]\n"
"ds_read_b64_tr_b16 v[200:201], v250 offset:33032\n"
"v_mul_f32_dpp v210, v216, v210 row_newbcast:8 row_mask:0xf bank_mask:0xf\n"
"v_mfma_f32_16x16x32_bf16 v[68:71], a[52:55], a[244:247], v[68:71]\n"
"buffer_load_dwordx4 v238, s[40:43], s72 offen offset:2048 nt lds\n"
"v_mul_f32_dpp v211, v216, v211 row_newbcast:12 row_mask:0xf bank_mask:0xf\n"
"v_mfma_f32_16x16x32_bf16 v[20:23], a[52:55], a[240:243], v[20:23]\n"
"s_add_u32 s72, s72, s0\n"
"ds_read_b64_tr_b16 v[202:203], v250 offset:34056\n"
"v_cvt_pk_bf16_f32 v211, v210, v211\n"
"v_cvt_pk_bf16_f32 v210, v208, v209\n"
"v_mfma_f32_16x16x32_bf16 v[20:23], a[4:7], a[224:227], v[20:23]\n"
"s_waitcnt lgkmcnt(5)\n"
"ds_write_b64 v244, v[210:211] offset:51200\n"
"v_mfma_f32_16x16x32_bf16 v[24:27], v[192:195], a[224:227], v[24:27]\n"
"s_waitcnt lgkmcnt(4)\n"
"ds_read_b64_tr_b16 v[204:205], v250 offset:37128\n"
"v_accvgpr_read_b32 v212, a92\n"
"v_mfma_f32_16x16x32_bf16 v[24:27], v[196:199], a[240:243], v[24:27]\n"
"v_accvgpr_read_b32 v213, a93\n"
"v_accvgpr_read_b32 v214, a94\n"
"v_accvgpr_read_b32 v215, a95\n"
"v_mfma_f32_16x16x32_bf16 v[72:75], v[196:199], a[244:247], v[72:75]\n"
"s_add_u32 m0, s78, 24576\n"
"ds_read_b64_tr_b16 v[206:207], v250 offset:38152\n"
"ds_read_b64_tr_b16 a[40:41], v221 offset:51200\n"
"v_mfma_f32_16x16x32_bf16 v[72:75], v[192:195], a[228:231], v[72:75]\n"
"buffer_load_dwordx4 v236, s[44:47], s73 offen offset:0 nt lds\n"
"ds_read_b64_tr_b16 a[42:43], v221 offset:51208\n"
"v_mfma_f32_16x16x32_bf16 v[120:123], v[192:195], a[232:235], v[120:123]\n"
"ds_read_b64_tr_b16 a[4:5], v250 offset:40960\n"
"v_mul_f32_dpp v212, v218, v212 row_newbcast:0 row_mask:0xf bank_mask:0xf\n"
"v_mfma_f32_16x16x32_bf16 v[120:123], v[196:199], a[248:251], v[120:123]\n"
"v_mul_f32_dpp v213, v218, v213 row_newbcast:4 row_mask:0xf bank_mask:0xf\n"
"v_mul_f32_dpp v214, v218, v214 row_newbcast:8 row_mask:0xf bank_mask:0xf\n"
"v_mul_f32_dpp v215, v218, v215 row_newbcast:12 row_mask:0xf bank_mask:0xf\n"
"v_mfma_f32_16x16x32_bf16 v[168:171], v[196:199], a[252:255], v[168:171]\n"
"ds_read_b64_tr_b16 a[6:7], v250 offset:41984\n"
"v_cvt_pk_bf16_f32 v212, v212, v213\n"
"v_cvt_pk_bf16_f32 v213, v214, v215\n"
"v_mfma_f32_16x16x32_bf16 v[168:171], v[192:195], a[236:239], v[168:171]\n"
"s_waitcnt lgkmcnt(7)\n"
"ds_write_b64 v244, v[212:213] offset:51208\n"
"v_mfma_f32_16x16x32_bf16 v[172:175], v[200:203], a[236:239], v[172:175]\n"
"s_waitcnt lgkmcnt(5)\n"
"ds_read_b64_tr_b16 a[52:53], v250 offset:45056\n"
"v_accvgpr_read_b32 v208, a88\n"
"v_accvgpr_read_b32 v209, a89\n"
"v_mfma_f32_16x16x32_bf16 v[172:175], v[204:207], a[252:255], v[172:175]\n"
"buffer_load_dwordx4 v237, s[44:47], s73 offen offset:1024 nt lds\n"
"v_mfma_f32_16x16x32_bf16 v[124:127], v[204:207], a[248:251], v[124:127]\n"
"s_waitcnt lgkmcnt(4)\n"
"buffer_store_dwordx4 a[40:43], v251, s[56:59], s76 offen offset:1024 sc1\n"
"ds_read_b64_tr_b16 a[54:55], v250 offset:46080\n"
"v_accvgpr_read_b32 v210, a90\n"
"v_accvgpr_read_b32 v211, a91\n"
"v_mul_f32_dpp v208, v218, v208 row_newbcast:0 row_mask:0xf bank_mask:0xf\n"
"v_mfma_f32_16x16x32_bf16 v[124:127], v[200:203], a[232:235], v[124:127]\n"
"v_mul_f32_dpp v209, v218, v209 row_newbcast:4 row_mask:0xf bank_mask:0xf\n"
"v_mul_f32_dpp v210, v218, v210 row_newbcast:8 row_mask:0xf bank_mask:0xf\n"
"v_mul_f32_dpp v211, v218, v211 row_newbcast:12 row_mask:0xf bank_mask:0xf\n"
"v_mfma_f32_16x16x32_bf16 v[76:79], v[200:203], a[228:231], v[76:79]\n"
"ds_read_b64_tr_b16 v[192:193], v250 offset:40968\n"
"v_cvt_pk_bf16_f32 v211, v210, v211\n"
"v_mfma_f32_16x16x32_bf16 v[76:79], v[204:207], a[244:247], v[76:79]\n"
"v_cvt_pk_bf16_f32 v210, v208, v209\n"
"v_accvgpr_read_b32 v212, a140\n"
"v_accvgpr_read_b32 v213, a141\n"
"v_mfma_f32_16x16x32_bf16 v[28:31], v[204:207], a[240:243], v[28:31]\n"
"ds_read_b64_tr_b16 v[194:195], v250 offset:41992\n"
"ds_write_b64 v244, v[210:211] offset:51200\n"
"v_mfma_f32_16x16x32_bf16 v[28:31], v[200:203], a[224:227], v[28:31]\n"
"s_waitcnt lgkmcnt(6)\n"
"buffer_load_dwordx4 v238, s[44:47], s73 offen offset:2048 nt lds\n"
"v_accvgpr_read_b32 v214, a142\n"
"v_mfma_f32_16x16x32_bf16 v[32:35], a[4:7], a[224:227], v[32:35]\n"
"s_waitcnt lgkmcnt(3)\n"
"ds_read_b64_tr_b16 v[196:197], v250 offset:45064\n"
"v_accvgpr_read_b32 v215, a143\n"
"v_mul_f32_dpp v212, v220, v212 row_newbcast:0 row_mask:0xf bank_mask:0xf\n"
"v_mfma_f32_16x16x32_bf16 v[32:35], a[52:55], a[240:243], v[32:35]\n"
"s_add_u32 s73, s73, 3072\n"
"ds_read_b64_tr_b16 a[88:89], v221 offset:51200\n"
"ds_read_b64_tr_b16 a[90:91], v221 offset:51208\n"
"v_mfma_f32_16x16x32_bf16 v[80:83], a[52:55], a[244:247], v[80:83]\n"
"ds_read_b64_tr_b16 v[198:199], v250 offset:46088\n"
"v_readlane_b32 s12, v255, 8\n"
"v_mul_f32_dpp v213, v220, v213 row_newbcast:4 row_mask:0xf bank_mask:0xf\n"
"v_mfma_f32_16x16x32_bf16 v[80:83], a[4:7], a[228:231], v[80:83]\n"
"v_mul_f32_dpp v214, v220, v214 row_newbcast:8 row_mask:0xf bank_mask:0xf\n"
"v_mul_f32_dpp v215, v220, v215 row_newbcast:12 row_mask:0xf bank_mask:0xf\n"
"v_mfma_f32_16x16x32_bf16 v[128:131], a[4:7], a[232:235], v[128:131]\n"
"ds_read_b64_tr_b16 v[200:201], v250 offset:41216\n"
"v_mov_b32 v252, s12\n"
"v_cvt_pk_bf16_f32 v212, v212, v213\n"
"v_mfma_f32_16x16x32_bf16 v[128:131], a[52:55], a[248:251], v[128:131]\n"
"v_cvt_pk_bf16_f32 v213, v214, v215\n"
"v_accvgpr_read_b32 v208, a136\n"
"v_accvgpr_read_b32 v209, a137\n"
"v_mfma_f32_16x16x32_bf16 v[176:179], a[52:55], a[252:255], v[176:179]\n"
"ds_read_b64_tr_b16 v[202:203], v250 offset:42240\n"
"ds_write_b64 v244, v[212:213] offset:51208\n"
"v_mfma_f32_16x16x32_bf16 v[176:179], a[4:7], a[236:239], v[176:179]\n"
"s_waitcnt lgkmcnt(8)\n"
"v_accvgpr_read_b32 v210, a138\n"
"v_accvgpr_read_b32 v211, a139\n"
"v_mul_f32_dpp v208, v220, v208 row_newbcast:0 row_mask:0xf bank_mask:0xf\n"
"v_mfma_f32_16x16x32_bf16 v[180:183], v[192:195], a[236:239], v[180:183]\n"
"s_waitcnt lgkmcnt(3)\n"
"buffer_store_dwordx4 a[88:91], v251, s[56:59], s76 offen offset:3072 sc1\n"
"ds_read_b64_tr_b16 v[204:205], v250 offset:45312\n"
"v_mul_f32_dpp v209, v220, v209 row_newbcast:4 row_mask:0xf bank_mask:0xf\n"
"v_mul_f32_dpp v210, v220, v210 row_newbcast:8 row_mask:0xf bank_mask:0xf\n"
"v_mfma_f32_16x16x32_bf16 v[180:183], v[196:199], a[252:255], v[180:183]\n"
"v_mul_f32_dpp v211, v220, v211 row_newbcast:12 row_mask:0xf bank_mask:0xf\n"
"v_cvt_pk_bf16_f32 v211, v210, v211\n"
"v_cvt_pk_bf16_f32 v210, v208, v209\n"
"v_mfma_f32_16x16x32_bf16 v[132:135], v[196:199], a[248:251], v[132:135]\n"
"ds_read_b64_tr_b16 v[206:207], v250 offset:46336\n"
"ds_write_b64 v244, v[210:211] offset:51200\n"
"v_accvgpr_read_b32 v212, a188\n"
"v_mfma_f32_16x16x32_bf16 v[132:135], v[192:195], a[232:235], v[132:135]\n"
"v_accvgpr_read_b32 v213, a189\n"
"v_accvgpr_read_b32 v214, a190\n"
"v_mfma_f32_16x16x32_bf16 v[84:87], v[192:195], a[228:231], v[84:87]\n"
"ds_read_b64_tr_b16 a[4:5], v250 offset:41224\n"
"v_accvgpr_read_b32 v215, a191\n"
"v_mul_f32_dpp v212, v222, v212 row_newbcast:0 row_mask:0xf bank_mask:0xf\n"
"v_mfma_f32_16x16x32_bf16 v[84:87], v[196:199], a[244:247], v[84:87]\n"
"ds_read_b64_tr_b16 a[136:137], v221 offset:51200\n"
"ds_read_b64_tr_b16 a[138:139], v221 offset:51208\n"
"v_mfma_f32_16x16x32_bf16 v[36:39], v[196:199], a[240:243], v[36:39]\n"
"ds_read_b64_tr_b16 a[6:7], v250 offset:42248\n"
"v_mul_f32_dpp v213, v222, v213 row_newbcast:4 row_mask:0xf bank_mask:0xf\n"
"v_mul_f32_dpp v214, v222, v214 row_newbcast:8 row_mask:0xf bank_mask:0xf\n"
"v_mfma_f32_16x16x32_bf16 v[36:39], v[192:195], a[224:227], v[36:39]\n"
"s_waitcnt lgkmcnt(8)\n"
"v_mul_f32_dpp v215, v222, v215 row_newbcast:12 row_mask:0xf bank_mask:0xf\n"
"v_cvt_pk_bf16_f32 v212, v212, v213\n"
"v_cvt_pk_bf16_f32 v213, v214, v215\n"
"v_mfma_f32_16x16x32_bf16 v[40:43], v[200:203], a[224:227], v[40:43]\n"
"s_waitcnt lgkmcnt(5)\n"
"ds_read_b64_tr_b16 a[52:53], v250 offset:45320\n"
"ds_write_b64 v244, v[212:213] offset:51208\n"
"v_mfma_f32_16x16x32_bf16 v[40:43], v[204:207], a[240:243], v[40:43]\n"
"s_barrier\n"
"v_accvgpr_read_b32 v208, a184\n"
"v_mfma_f32_16x16x32_bf16 v[88:91], v[204:207], a[244:247], v[88:91]\n"
"ds_read_b64_tr_b16 a[54:55], v250 offset:46344\n"
"v_accvgpr_read_b32 v209, a185\n"
"v_mfma_f32_16x16x32_bf16 v[88:91], v[200:203], a[228:231], v[88:91]\n"
"s_waitcnt lgkmcnt(4)\n"
"buffer_store_dwordx4 a[136:139], v253, s[56:59], s76 offen offset:1024 sc1\n"
"v_accvgpr_read_b32 v210, a186\n"
"v_accvgpr_read_b32 v211, a187\n"
"v_mul_f32_dpp v208, v222, v208 row_newbcast:0 row_mask:0xf bank_mask:0xf\n"
"v_mul_f32_dpp v209, v222, v209 row_newbcast:4 row_mask:0xf bank_mask:0xf\n"
"v_mfma_f32_16x16x32_bf16 v[136:139], v[200:203], a[232:235], v[136:139]\n"
"v_mul_f32_dpp v210, v222, v210 row_newbcast:8 row_mask:0xf bank_mask:0xf\n"
"v_mul_f32_dpp v211, v222, v211 row_newbcast:12 row_mask:0xf bank_mask:0xf\n"
"v_cvt_pk_bf16_f32 v211, v210, v211\n"
"v_mfma_f32_16x16x32_bf16 v[136:139], v[204:207], a[248:251], v[136:139]\n"
"v_cvt_pk_bf16_f32 v210, v208, v209\n"
"v_mfma_f32_16x16x32_bf16 v[184:187], v[204:207], a[252:255], v[184:187]\n"
"ds_write_b64 v244, v[210:211] offset:51200\n"
"v_mfma_f32_16x16x32_bf16 v[184:187], v[200:203], a[236:239], v[184:187]\n"
"s_waitcnt lgkmcnt(4)\n"
"v_mfma_f32_16x16x32_bf16 v[188:191], a[4:7], a[236:239], v[188:191]\n"
"s_waitcnt lgkmcnt(1)\n"
"ds_read_b64_tr_b16 a[184:185], v221 offset:51200\n"
"ds_read_b64_tr_b16 a[186:187], v221 offset:51208\n"
"v_mfma_f32_16x16x32_bf16 v[188:191], a[52:55], a[252:255], v[188:191]\n"
"v_mfma_f32_16x16x32_bf16 v[140:143], a[52:55], a[248:251], v[140:143]\n"
"v_mfma_f32_16x16x32_bf16 v[140:143], a[4:7], a[232:235], v[140:143]\n"
"v_mfma_f32_16x16x32_bf16 v[92:95], a[4:7], a[228:231], v[92:95]\n"
"v_mfma_f32_16x16x32_bf16 v[92:95], a[52:55], a[244:247], v[92:95]\n"
"v_mfma_f32_16x16x32_bf16 v[44:47], a[52:55], a[240:243], v[44:47]\n"
"s_waitcnt lgkmcnt(0)\n"
"buffer_store_dwordx4 a[184:187], v253, s[56:59], s76 offen offset:3072 sc1\n"
"v_mfma_f32_16x16x32_bf16 v[44:47], a[4:7], a[224:227], v[44:47]\n"
".Lat_t2:\n"
"s_memtime s[92:93]\n"
".p2align 3\n"
".Lat_cv:\n"
"v_pk_mul_f32 v[0:1], v[0:1], s[88:89]\n"
"v_pk_mul_f32 v[2:3], v[2:3], s[88:89]\n"
"v_pk_mul_f32 v[4:5], v[4:5], s[88:89]\n"
"v_pk_mul_f32 v[6:7], v[6:7], s[88:89]\n"
"v_pk_mul_f32 v[48:49], v[48:49], s[88:89]\n"
"v_pk_mul_f32 v[50:51], v[50:51], s[88:89]\n"
"v_pk_mul_f32 v[52:53], v[52:53], s[88:89]\n"
"v_pk_mul_f32 v[54:55], v[54:55], s[88:89]\n"
"v_pk_mul_f32 v[96:97], v[96:97], s[88:89]\n"
"v_pk_mul_f32 v[98:99], v[98:99], s[88:89]\n"
"v_pk_mul_f32 v[100:101], v[100:101], s[88:89]\n"
"v_pk_mul_f32 v[102:103], v[102:103], s[88:89]\n"
"v_pk_mul_f32 v[144:145], v[144:145], s[88:89]\n"
"v_pk_mul_f32 v[146:147], v[146:147], s[88:89]\n"
"v_pk_mul_f32 v[148:149], v[148:149], s[88:89]\n"
"v_pk_mul_f32 v[150:151], v[150:151], s[88:89]\n"
"v_pk_mul_f32 v[8:9], v[8:9], s[88:89]\n"
"v_pk_mul_f32 v[10:11], v[10:11], s[88:89]\n"
"v_pk_mul_f32 v[12:13], v[12:13], s[88:89]\n"
"v_pk_mul_f32 v[14:15], v[14:15], s[88:89]\n"
"v_pk_mul_f32 v[56:57], v[56:57], s[88:89]\n"
"v_pk_mul_f32 v[58:59], v[58:59], s[88:89]\n"
"v_pk_mul_f32 v[60:61], v[60:61], s[88:89]\n"
"v_pk_mul_f32 v[62:63], v[62:63], s[88:89]\n"
"v_pk_mul_f32 v[104:105], v[104:105], s[88:89]\n"
"v_pk_mul_f32 v[106:107], v[106:107], s[88:89]\n"
"v_pk_mul_f32 v[108:109], v[108:109], s[88:89]\n"
"v_pk_mul_f32 v[110:111], v[110:111], s[88:89]\n"
"v_pk_mul_f32 v[152:153], v[152:153], s[88:89]\n"
"v_pk_mul_f32 v[154:155], v[154:155], s[88:89]\n"
"v_pk_mul_f32 v[156:157], v[156:157], s[88:89]\n"
"v_pk_mul_f32 v[158:159], v[158:159], s[88:89]\n"
"v_pk_mul_f32 v[16:17], v[16:17], s[88:89]\n"
"v_pk_mul_f32 v[18:19], v[18:19], s[88:89]\n"
"v_pk_mul_f32 v[20:21], v[20:21], s[88:89]\n"
"v_pk_mul_f32 v[22:23], v[22:23], s[88:89]\n"
"v_pk_mul_f32 v[64:65], v[64:65], s[88:89]\n"
"v_pk_mul_f32 v[66:67], v[66:67], s[88:89]\n"
"v_pk_mul_f32 v[68:69], v[68:69], s[88:89]\n"
"v_pk_mul_f32 v[70:71], v[70:71], s[88:89]\n"
"v_pk_mul_f32 v[112:113], v[112:113], s[88:89]\n"
"v_pk_mul_f32 v[114:115], v[114:115], s[88:89]\n"
"v_pk_mul_f32 v[116:117], v[116:117], s[88:89]\n"
"v_pk_mul_f32 v[118:119], v[118:119], s[88:89]\n"
"v_pk_mul_f32 v[160:161], v[160:161], s[88:89]\n"
"v_pk_mul_f32 v[162:163], v[162:163], s[88:89]\n"
"v_pk_mul_f32 v[164:165], v[164:165], s[88:89]\n"
"v_pk_mul_f32 v[166:167], v[166:167], s[88:89]\n"
"v_pk_mul_f32 v[24:25], v[24:25], s[88:89]\n"
"v_pk_mul_f32 v[26:27], v[26:27], s[88:89]\n"
"v_pk_mul_f32 v[28:29], v[28:29], s[88:89]\n"
"v_pk_mul_f32 v[30:31], v[30:31], s[88:89]\n"
"v_pk_mul_f32 v[72:73], v[72:73], s[88:89]\n"
"v_pk_mul_f32 v[74:75], v[74:75], s[88:89]\n"
"v_pk_mul_f32 v[76:77], v[76:77], s[88:89]\n"
"v_pk_mul_f32 v[78:79], v[78:79], s[88:89]\n"
"v_pk_mul_f32 v[120:121], v[120:121], s[88:89]\n"
"v_pk_mul_f32 v[122:123], v[122:123], s[88:89]\n"
"v_pk_mul_f32 v[124:125], v[124:125], s[88:89]\n"
"v_pk_mul_f32 v[126:127], v[126:127], s[88:89]\n"
"v_pk_mul_f32 v[168:169], v[168:169], s[88:89]\n"
"v_pk_mul_f32 v[170:171], v[170:171], s[88:89]\n"
"v_pk_mul_f32 v[172:173], v[172:173], s[88:89]\n"
"v_pk_mul_f32 v[174:175], v[174:175], s[88:89]\n"
"v_pk_mul_f32 v[32:33], v[32:33], s[88:89]\n"
"v_pk_mul_f32 v[34:35], v[34:35], s[88:89]\n"
"v_pk_mul_f32 v[36:37], v[36:37], s[88:89]\n"
"v_pk_mul_f32 v[38:39], v[38:39], s[88:89]\n"
"v_pk_mul_f32 v[80:81], v[80:81], s[88:89]\n"
"v_pk_mul_f32 v[82:83], v[82:83], s[88:89]\n"
"v_pk_mul_f32 v[84:85], v[84:85], s[88:89]\n"
"v_pk_mul_f32 v[86:87], v[86:87], s[88:89]\n"
"v_pk_mul_f32 v[128:129], v[128:129], s[88:89]\n"
"v_pk_mul_f32 v[130:131], v[130:131], s[88:89]\n"
"v_pk_mul_f32 v[132:133], v[132:133], s[88:89]\n"
"v_pk_mul_f32 v[134:135], v[134:135], s[88:89]\n"
"v_pk_mul_f32 v[176:177], v[176:177], s[88:89]\n"
"v_pk_mul_f32 v[178:179], v[178:179], s[88:89]\n"
"v_pk_mul_f32 v[180:181], v[180:181], s[88:89]\n"
"v_pk_mul_f32 v[182:183], v[182:183], s[88:89]\n"
"v_pk_mul_f32 v[40:41], v[40:41], s[88:89]\n"
"v_pk_mul_f32 v[42:43], v[42:43], s[88:89]\n"
"v_pk_mul_f32 v[44:45], v[44:45], s[88:89]\n"
"v_pk_mul_f32 v[46:47], v[46:47], s[88:89]\n"
"v_pk_mul_f32 v[88:89], v[88:89], s[88:89]\n"
"v_pk_mul_f32 v[90:91], v[90:91], s[88:89]\n"
"v_pk_mul_f32 v[92:93], v[92:93], s[88:89]\n"
"v_pk_mul_f32 v[94:95], v[94:95], s[88:89]\n"
"v_pk_mul_f32 v[136:137], v[136:137], s[88:89]\n"
"v_pk_mul_f32 v[138:139], v[138:139], s[88:89]\n"
"v_pk_mul_f32 v[140:141], v[140:141], s[88:89]\n"
"v_pk_mul_f32 v[142:143], v[142:143], s[88:89]\n"
"v_pk_mul_f32 v[184:185], v[184:185], s[88:89]\n"
"v_pk_mul_f32 v[186:187], v[186:187], s[88:89]\n"
"v_pk_mul_f32 v[188:189], v[188:189], s[88:89]\n"
"v_pk_mul_f32 v[190:191], v[190:191], s[88:89]\n"
"v_cvt_pk_bf16_f32 v152, v152, v153\n"
"v_cvt_pk_bf16_f32 v154, v154, v155\n"
"v_cvt_pk_bf16_f32 v156, v156, v157\n"
"v_cvt_pk_bf16_f32 v158, v158, v159\n"
"v_accvgpr_write_b32 a244, v152\n"
"v_accvgpr_write_b32 a245, v154\n"
"v_accvgpr_write_b32 a246, v156\n"
"v_accvgpr_write_b32 a247, v158\n"
"v_cvt_pk_bf16_f32 v152, v176, v177\n"
"v_cvt_pk_bf16_f32 v153, v178, v179\n"
"v_cvt_pk_bf16_f32 v154, v180, v181\n"
"v_cvt_pk_bf16_f32 v155, v182, v183\n"
"v_cvt_pk_bf16_f32 v156, v184, v185\n"
"v_cvt_pk_bf16_f32 v157, v186, v187\n"
"v_cvt_pk_bf16_f32 v158, v188, v189\n"
"v_cvt_pk_bf16_f32 v159, v190, v191\n"
"v_cvt_pk_bf16_f32 v160, v160, v161\n"
"v_cvt_pk_bf16_f32 v162, v162, v163\n"
"v_cvt_pk_bf16_f32 v164, v164, v165\n"
"v_cvt_pk_bf16_f32 v166, v166, v167\n"
"v_accvgpr_write_b32 a248, v160\n"
"v_accvgpr_write_b32 a249, v162\n"
"v_accvgpr_write_b32 a250, v164\n"
"v_accvgpr_write_b32 a251, v166\n"
"v_cvt_pk_bf16_f32 v168, v168, v169\n"
"v_cvt_pk_bf16_f32 v170, v170, v171\n"
"v_cvt_pk_bf16_f32 v172, v172, v173\n"
"v_cvt_pk_bf16_f32 v174, v174, v175\n"
"v_accvgpr_write_b32 a252, v168\n"
"v_accvgpr_write_b32 a253, v170\n"
"v_accvgpr_write_b32 a254, v172\n"
"v_accvgpr_write_b32 a255, v174\n"
"s_waitcnt lgkmcnt(0)\n"
"ds_read_b128 v[160:163], v245 offset:0\n"
"ds_read_b128 v[164:167], v245 offset:6144\n"
"ds_read_b128 v[168:171], v246 offset:0\n"
"ds_read_b128 v[172:175], v246 offset:6144\n"
"ds_read_b128 v[176:179], v245 offset:128\n"
"v_cvt_pk_bf16_f32 v144, v144, v145\n"
"v_cvt_pk_bf16_f32 v146, v146, v147\n"
"v_cvt_pk_bf16_f32 v148, v148, v149\n"
"v_cvt_pk_bf16_f32 v150, v150, v151\n"
"v_accvgpr_write_b32 a240, v144\n"
"v_accvgpr_write_b32 a241, v146\n"
"v_accvgpr_write_b32 a242, v148\n"
"v_accvgpr_write_b32 a243, v150\n"
"v_cvt_pk_bf16_f32 v0, v0, v1\n"
"v_cvt_pk_bf16_f32 v2, v2, v3\n"
"v_cvt_pk_bf16_f32 v4, v4, v5\n"
"v_cvt_pk_bf16_f32 v6, v6, v7\n"
"v_accvgpr_write_b32 a192, v0\n"
"v_accvgpr_write_b32 a193, v2\n"
"v_accvgpr_write_b32 a194, v4\n"
"v_accvgpr_write_b32 a195, v6\n"
"v_cvt_pk_bf16_f32 v8, v8, v9\n"
"v_cvt_pk_bf16_f32 v10, v10, v11\n"
"v_cvt_pk_bf16_f32 v12, v12, v13\n"
"v_cvt_pk_bf16_f32 v14, v14, v15\n"
"v_accvgpr_write_b32 a196, v8\n"
"v_accvgpr_write_b32 a197, v10\n"
"v_accvgpr_write_b32 a198, v12\n"
"v_accvgpr_write_b32 a199, v14\n"
"v_cvt_pk_bf16_f32 v16, v16, v17\n"
"v_cvt_pk_bf16_f32 v18, v18, v19\n"
"v_cvt_pk_bf16_f32 v20, v20, v21\n"
"v_cvt_pk_bf16_f32 v22, v22, v23\n"
"v_accvgpr_write_b32 a200, v16\n"
"v_accvgpr_write_b32 a201, v18\n"
"v_accvgpr_write_b32 a202, v20\n"
"v_accvgpr_write_b32 a203, v22\n"
"v_cvt_pk_bf16_f32 v24, v24, v25\n"
"v_cvt_pk_bf16_f32 v26, v26, v27\n"
"v_cvt_pk_bf16_f32 v28, v28, v29\n"
"v_cvt_pk_bf16_f32 v30, v30, v31\n"
"v_accvgpr_write_b32 a204, v24\n"
"v_accvgpr_write_b32 a205, v26\n"
"v_accvgpr_write_b32 a206, v28\n"
"v_accvgpr_write_b32 a207, v30\n"
"v_cvt_pk_bf16_f32 v48, v48, v49\n"
"v_cvt_pk_bf16_f32 v50, v50, v51\n"
"v_cvt_pk_bf16_f32 v52, v52, v53\n"
"v_cvt_pk_bf16_f32 v54, v54, v55\n"
"v_accvgpr_write_b32 a208, v48\n"
"v_accvgpr_write_b32 a209, v50\n"
"v_accvgpr_write_b32 a210, v52\n"
"v_accvgpr_write_b32 a211, v54\n"
"v_cvt_pk_bf16_f32 v96, v96, v97\n"
"v_cvt_pk_bf16_f32 v98, v98, v99\n"
"v_cvt_pk_bf16_f32 v100, v100, v101\n"
"v_cvt_pk_bf16_f32 v102, v102, v103\n"
"v_accvgpr_write_b32 a224, v96\n"
"v_accvgpr_write_b32 a225, v98\n"
"v_accvgpr_write_b32 a226, v100\n"
"v_accvgpr_write_b32 a227, v102\n"
"v_mov_b64 v[208:209], s[82:83]\n"
"v_mov_b64 v[210:211], s[82:83]\n"
"v_cvt_pk_bf16_f32 v56, v56, v57\n"
"v_cvt_pk_bf16_f32 v58, v58, v59\n"
"v_cvt_pk_bf16_f32 v60, v60, v61\n"
"v_cvt_pk_bf16_f32 v62, v62, v63\n"
"v_accvgpr_write_b32 a212, v56\n"
"v_accvgpr_write_b32 a213, v58\n"
"v_accvgpr_write_b32 a214, v60\n"
"v_accvgpr_write_b32 a215, v62\n"
"v_cvt_pk_bf16_f32 v104, v104, v105\n"
"v_cvt_pk_bf16_f32 v106, v106, v107\n"
"v_cvt_pk_bf16_f32 v108, v108, v109\n"
"v_cvt_pk_bf16_f32 v110, v110, v111\n"
"v_accvgpr_write_b32 a228, v104\n"
"v_accvgpr_write_b32 a229, v106\n"
"v_accvgpr_write_b32 a230, v108\n"
"v_accvgpr_write_b32 a231, v110\n"
"v_cvt_pk_bf16_f32 v64, v64, v65\n"
"v_cvt_pk_bf16_f32 v66, v66, v67\n"
".Lat_p0:\n"
"s_waitcnt lgkmcnt(4)\n"
"v_mfma_f32_16x16x32_bf16 v[0:3], v[160:163], a[192:195], v[208:211]\n"
"s_sub_u32 s92, s92, s94\n"
"ds_read_b128 v[180:183], v245 offset:6272\n"
"v_cvt_pk_bf16_f32 v68, v68, v69\n"
"v_mfma_f32_16x16x32_bf16 v[8:11], v[160:163], a[208:211], v[208:211]\n"
"s_cmp_gt_u32 s92, 17900\n"
"v_cvt_pk_bf16_f32 v70, v70, v71\n"
"v_accvgpr_write_b32 a216, v64\n"
"v_mfma_f32_16x16x32_bf16 v[16:19], v[160:163], a[224:227], v[208:211]\n"
"s_cselect_b32 s92, 1, 0\n"
"v_accvgpr_write_b32 a217, v66\n"
"v_accvgpr_write_b32 a218, v68\n"
"v_mfma_f32_16x16x32_bf16 v[24:27], v[160:163], a[240:243], v[208:211]\n"
"s_waitcnt lgkmcnt(4)\n"
"v_accvgpr_write_b32 a219, v70\n"
"v_cvt_pk_bf16_f32 v112, v112, v113\n"
"v_mfma_f32_16x16x32_bf16 v[28:31], v[164:167], a[240:243], v[208:211]\n"
"s_cselect_b32 s18, s18, 0xf0000000\n"
"ds_read_b128 v[184:187], v246 offset:128\n"
"v_cvt_pk_bf16_f32 v114, v114, v115\n"
"v_mfma_f32_16x16x32_bf16 v[20:23], v[164:167], a[224:227], v[208:211]\n"
"s_cmp_eq_u32 s81, 0\n"
"v_cvt_pk_bf16_f32 v116, v116, v117\n"
"v_cvt_pk_bf16_f32 v118, v118, v119\n"
"v_mfma_f32_16x16x32_bf16 v[12:15], v[164:167], a[208:211], v[208:211]\n"
"s_cselect_b32 s95, s92, s95\n"
"v_accvgpr_write_b32 a232, v112\n"
"v_accvgpr_write_b32 a233, v114\n"
"v_mfma_f32_16x16x32_bf16 v[4:7], v[164:167], a[192:195], v[208:211]\n"
"s_waitcnt lgkmcnt(4)\n"
"v_accvgpr_write_b32 a234, v116\n"
"v_accvgpr_write_b32 a235, v118\n"
"v_mfma_f32_16x16x32_bf16 v[0:3], v[168:171], a[196:199], v[0:3]\n"
"s_cselect_b32 s76, s66, s67\n"
"ds_read_b128 v[188:191], v246 offset:6272\n"
"v_cvt_pk_bf16_f32 v72, v72, v73\n"
"v_mfma_f32_16x16x32_bf16 v[8:11], v[168:171], a[212:215], v[8:11]\n"
"s_cselect_b64 vcc, -1, 0\n"
"v_cvt_pk_bf16_f32 v74, v74, v75\n"
"v_cvt_pk_bf16_f32 v76, v76, v77\n"
"v_mfma_f32_16x16x32_bf16 v[16:19], v[168:171], a[228:231], v[16:19]\n"
"v_cvt_pk_bf16_f32 v78, v78, v79\n"
"v_accvgpr_write_b32 a220, v72\n"
"v_mfma_f32_16x16x32_bf16 v[24:27], v[168:171], a[244:247], v[24:27]\n"
"s_waitcnt lgkmcnt(4)\n"
"v_accvgpr_write_b32 a221, v74\n"
"v_accvgpr_write_b32 a222, v76\n"
"v_mfma_f32_16x16x32_bf16 v[28:31], v[172:175], a[244:247], v[28:31]\n"
"ds_read_b128 v[160:163], v245 offset:256\n"
"v_accvgpr_write_b32 a223, v78\n"
"v_mfma_f32_16x16x32_bf16 v[20:23], v[172:175], a[228:231], v[20:23]\n"
"v_cvt_pk_bf16_f32 v120, v120, v121\n"
"v_cvt_pk_bf16_f32 v122, v122, v123\n"
"v_mfma_f32_16x16x32_bf16 v[12:15], v[172:175], a[212:215], v[12:15]\n"
"s_add_u32 m0, s63, 114688\n"
"v_cvt_pk_bf16_f32 v124, v124, v125\n"
"v_cvt_pk_bf16_f32 v126, v126, v127\n"
"v_mfma_f32_16x16x32_bf16 v[4:7], v[172:175], a[196:199], v[4:7]\n"
"s_waitcnt lgkmcnt(4)\n"
"buffer_load_dwordx4 v242, s[48:51], s18 offen offset:0 lds\n"
"v_mfma_f32_16x16x32_bf16 v[0:3], v[176:179], a[200:203], v[0:3]\n"
"ds_read_b128 v[164:167], v245 offset:6400\n"
"v_accvgpr_write_b32 a236, v120\n"
"v_mfma_f32_16x16x32_bf16 v[8:11], v[176:179], a[216:219], v[8:11]\n"
"v_accvgpr_write_b32 a237, v122\n"
"v_accvgpr_write_b32 a238, v124\n"
"v_mfma_f32_16x16x32_bf16 v[16:19], v[176:179], a[232:235], v[16:19]\n"
"v_accvgpr_write_b32 a239, v126\n"
"v_cvt_pk_bf16_f32 v144, v128, v129\n"
"v_mfma_f32_16x16x32_bf16 v[24:27], v[176:179], a[248:251], v[24:27]\n"
"s_waitcnt lgkmcnt(4)\n"
"buffer_load_dwordx4 v243, s[48:51], s18 offen offset:1024 lds\n"
"v_mfma_f32_16x16x32_bf16 v[28:31], v[180:183], a[248:251], v[28:31]\n"
"ds_read_b128 v[168:171], v246 offset:256\n"
"v_cvt_pk_bf16_f32 v145, v130, v131\n"
"v_mfma_f32_16x16x32_bf16 v[20:23], v[180:183], a[232:235], v[20:23]\n"
"v_cvt_pk_bf16_f32 v146, v132, v133\n"
"v_cvt_pk_bf16_f32 v147, v134, v135\n"
"v_mfma_f32_16x16x32_bf16 v[12:15], v[180:183], a[216:219], v[12:15]\n"
"s_add_u32 s18, s18, s34\n"
"v_cvt_pk_bf16_f32 v148, v136, v137\n"
"v_cvt_pk_bf16_f32 v149, v138, v139\n"
"v_mfma_f32_16x16x32_bf16 v[4:7], v[180:183], a[200:203], v[4:7]\n"
"s_waitcnt lgkmcnt(4)\n"
"buffer_load_dwordx4 v242, s[48:51], s18 offen offset:0 lds\n"
"v_mfma_f32_16x16x32_bf16 v[0:3], v[184:187], a[204:207], v[0:3]\n"
"ds_read_b128 v[172:175], v246 offset:6400\n"
"v_cvt_pk_bf16_f32 v150, v140, v141\n"
"v_mfma_f32_16x16x32_bf16 v[8:11], v[184:187], a[220:223], v[8:11]\n"
"v_cvt_pk_bf16_f32 v151, v142, v143\n"
"v_cvt_pk_bf16_f32 v128, v32, v33\n"
"v_mfma_f32_16x16x32_bf16 v[16:19], v[184:187], a[236:239], v[16:19]\n"
"v_cvt_pk_bf16_f32 v129, v34, v35\n"
"v_cvt_pk_bf16_f32 v130, v36, v37\n"
"v_mfma_f32_16x16x32_bf16 v[24:27], v[184:187], a[252:255], v[24:27]\n"
"s_waitcnt lgkmcnt(4)\n"
"buffer_load_dwordx4 v243, s[48:51], s18 offen offset:1024 lds\n"
"v_mfma_f32_16x16x32_bf16 v[28:31], v[188:191], a[252:255], v[28:31]\n"
"v_cvt_pk_bf16_f32 v131, v38, v39\n"
"v_cvt_pk_bf16_f32 v136, v80, v81\n"
"v_mfma_f32_16x16x32_bf16 v[20:23], v[188:191], a[236:239], v[20:23]\n"
"v_cvt_pk_bf16_f32 v137, v82, v83\n"
"v_cvt_pk_bf16_f32 v138, v84, v85\n"
"v_cvt_pk_bf16_f32 v139, v86, v87\n"
"v_mfma_f32_16x16x32_bf16 v[12:15], v[188:191], a[220:223], v[12:15]\n"
"s_add_u32 s18, s18, 128\n"
"v_cvt_pk_bf16_f32 v132, v40, v41\n"
"v_cvt_pk_bf16_f32 v133, v42, v43\n"
"v_mfma_f32_16x16x32_bf16 v[4:7], v[188:191], a[204:207], v[4:7]\n"
"s_waitcnt lgkmcnt(3)\n"
"buffer_load_dwordx4 v242, s[48:51], s18 offen offset:0 lds\n"
"v_mfma_f32_16x16x32_bf16 v[0:3], v[160:163], v[128:131], v[0:3]\n"
"v_cvt_pk_bf16_f32 v134, v44, v45\n"
"v_cvt_pk_bf16_f32 v135, v46, v47\n"
"v_mfma_f32_16x16x32_bf16 v[8:11], v[160:163], v[136:139], v[8:11]\n"
"s_waitcnt lgkmcnt(0)\n"
"s_barrier\n"
"v_mfma_f32_16x16x32_bf16 v[16:19], v[160:163], v[144:147], v[16:19]\n"
"v_cvt_pk_bf16_f32 v140, v88, v89\n"
"v_cvt_pk_bf16_f32 v141, v90, v91\n"
"v_mfma_f32_16x16x32_bf16 v[24:27], v[160:163], v[152:155], v[24:27]\n"
"buffer_load_dwordx4 v243, s[48:51], s18 offen offset:1024 lds\n"
"v_mfma_f32_16x16x32_bf16 v[28:31], v[164:167], v[152:155], v[28:31]\n"
"s_mul_i32 s70, s70, s95\n"
"ds_read_b128 v[176:179], v245 offset:12288\n"
"v_cvt_pk_bf16_f32 v142, v92, v93\n"
"v_mfma_f32_16x16x32_bf16 v[20:23], v[164:167], v[144:147], v[20:23]\n"
"s_mul_i32 s93, s95, 184\n"
"v_cvt_pk_bf16_f32 v143, v94, v95\n"
"v_mov_b32 v212, 0xf0000000\n"
"v_mfma_f32_16x16x32_bf16 v[12:15], v[164:167], v[136:139], v[12:15]\n"
"s_add_u32 s18, s18, 128\n"
"v_add_u32 v216, 4294965376, v242\n"
"v_add_u32 v218, 4294965376, v243\n"
"v_mfma_f32_16x16x32_bf16 v[4:7], v[164:167], v[128:131], v[4:7]\n"
"buffer_load_dwordx4 v242, s[48:51], s18 offen offset:0 lds\n"
"v_mfma_f32_16x16x32_bf16 v[0:3], v[168:171], v[132:135], v[0:3]\n"
"ds_read_b128 v[180:183], v246 offset:12288\n"
"v_mov_b64 v[204:205], 0\n"
"v_mfma_f32_16x16x32_bf16 v[8:11], v[168:171], v[140:143], v[8:11]\n"
"v_mov_b64 v[206:207], 0\n"
"v_mov_b32 v223, s13\n"
"v_mfma_f32_16x16x32_bf16 v[16:19], v[168:171], v[148:151], v[16:19]\n"
"v_cndmask_b32 v214, v224, v242, vcc\n"
"v_cndmask_b32 v215, v225, v243, vcc\n"
"v_mfma_f32_16x16x32_bf16 v[24:27], v[168:171], v[156:159], v[24:27]\n"
"buffer_load_dwordx4 v243, s[48:51], s18 offen offset:1024 lds\n"
"v_mfma_f32_16x16x32_bf16 v[28:31], v[172:175], v[156:159], v[28:31]\n"
"s_mov_b32 s18, 0xf0000000\n"
"ds_read_b128 v[184:187], v245 offset:12416\n"
"v_cndmask_b32 v216, v226, v216, vcc\n"
"v_mfma_f32_16x16x32_bf16 v[20:23], v[172:175], v[148:151], v[20:23]\n"
"v_cndmask_b32 v218, v227, v218, vcc\n"
"v_cndmask_b32 v220, v234, v212, vcc\n"
"v_mfma_f32_16x16x32_bf16 v[12:15], v[172:175], v[140:143], v[12:15]\n"
"v_mfma_f32_16x16x32_bf16 v[4:7], v[172:175], v[132:135], v[4:7]\n"
"s_waitcnt lgkmcnt(2)\n"
"v_mfma_f32_16x16x32_bf16 v[32:35], v[176:179], a[192:195], v[208:211]\n"
"s_waitcnt lgkmcnt(1)\n"
"ds_read_b128 v[188:191], v246 offset:12416\n"
"v_mfma_f32_16x16x32_bf16 v[32:35], v[180:183], a[196:199], v[32:35]\n"
"s_mov_b32 s12, 0x29800000\n"
"v_exp_f32 v192, v0\n"
"v_mfma_f32_16x16x32_bf16 v[40:43], v[180:183], a[212:215], v[208:211]\n"
"v_exp_f32 v193, v1\n"
"v_mfma_f32_16x16x32_bf16 v[40:43], v[176:179], a[208:211], v[40:43]\n"
"v_add_f32 v200, v204, v192\n"
"v_add_f32 v200, v200, v193\n"
"v_mfma_f32_16x16x32_bf16 v[48:51], v[176:179], a[224:227], v[208:211]\n"
"ds_read_b128 v[160:163], v245 offset:12544\n"
"v_exp_f32 v194, v2\n"
"v_mfma_f32_16x16x32_bf16 v[48:51], v[180:183], a[228:231], v[48:51]\n"
"v_cvt_pk_bf16_f32 v96, v192, v193\n"
"v_exp_f32 v195, v3\n"
"v_mfma_f32_16x16x32_bf16 v[56:59], v[180:183], a[244:247], v[208:211]\n"
"v_add_f32 v200, v200, v194\n"
"v_exp_f32 v196, v4\n"
"v_add_f32 v200, v200, v195\n"
"v_mfma_f32_16x16x32_bf16 v[56:59], v[176:179], a[240:243], v[56:59]\n"
"s_waitcnt lgkmcnt(2)\n"
"v_exp_f32 v197, v5\n"
"v_mfma_f32_16x16x32_bf16 v[56:59], v[184:187], a[248:251], v[56:59]\n"
"s_waitcnt lgkmcnt(1)\n"
"ds_read_b128 v[164:167], v246 offset:12544\n"
"v_cvt_pk_bf16_f32 v97, v194, v195\n"
"v_mfma_f32_16x16x32_bf16 v[56:59], v[188:191], a[252:255], v[56:59]\n"
"v_add_f32 v200, v200, v196\n"
"v_add_f32 v200, v200, v197\n"
"v_mfma_f32_16x16x32_bf16 v[48:51], v[188:191], a[236:239], v[48:51]\n"
"v_cvt_pk_bf16_f32 v98, v196, v197\n"
"v_exp_f32 v198, v6\n"
"v_mfma_f32_16x16x32_bf16 v[48:51], v[184:187], a[232:235], v[48:51]\n"
"v_exp_f32 v199, v7\n"
"v_mfma_f32_16x16x32_bf16 v[40:43], v[184:187], a[216:219], v[40:43]\n"
"ds_read_b128 v[168:171], v245 offset:18432\n"
"v_add_f32 v200, v200, v198\n"
"v_mfma_f32_16x16x32_bf16 v[40:43], v[188:191], a[220:223], v[40:43]\n"
"v_add_f32 v200, v200, v199\n"
"v_cvt_pk_bf16_f32 v99, v198, v199\n"
"v_exp_f32 v192, v8\n"
"v_mfma_f32_16x16x32_bf16 v[32:35], v[188:191], a[204:207], v[32:35]\n"
"v_exp_f32 v193, v9\n"
"v_mfma_f32_16x16x32_bf16 v[32:35], v[184:187], a[200:203], v[32:35]\n"
"s_waitcnt lgkmcnt(2)\n"
"v_add_f32 v201, v205, v192\n"
"v_add_f32 v201, v201, v193\n"
"v_mfma_f32_16x16x32_bf16 v[32:35], v[160:163], v[128:131], v[32:35]\n"
"s_waitcnt lgkmcnt(1)\n"
"ds_read_b128 v[172:175], v246 offset:18432\n"
"v_cvt_pk_bf16_f32 v100, v192, v193\n"
"v_mfma_f32_16x16x32_bf16 v[32:35], v[164:167], v[132:135], v[32:35]\n"
"v_exp_f32 v194, v10\n"
"v_mfma_f32_16x16x32_bf16 v[40:43], v[164:167], v[140:143], v[40:43]\n"
"v_exp_f32 v195, v11\n"
"v_mfma_f32_16x16x32_bf16 v[40:43], v[160:163], v[136:139], v[40:43]\n"
"v_add_f32 v201, v201, v194\n"
"v_add_f32 v201, v201, v195\n"
"v_mfma_f32_16x16x32_bf16 v[48:51], v[160:163], v[144:147], v[48:51]\n"
"ds_read_b128 v[176:179], v245 offset:18560\n"
"v_cvt_pk_bf16_f32 v101, v194, v195\n"
"v_exp_f32 v196, v12\n"
"v_mfma_f32_16x16x32_bf16 v[48:51], v[164:167], v[148:151], v[48:51]\n"
"v_exp_f32 v197, v13\n"
"v_mfma_f32_16x16x32_bf16 v[56:59], v[164:167], v[156:159], v[56:59]\n"
"v_add_f32 v201, v201, v196\n"
"v_add_f32 v201, v201, v197\n"
"v_mfma_f32_16x16x32_bf16 v[56:59], v[160:163], v[152:155], v[56:59]\n"
"s_waitcnt lgkmcnt(2)\n"
"v_cvt_pk_bf16_f32 v102, v196, v197\n"
"v_exp_f32 v198, v14\n"
"v_mfma_f32_16x16x32_bf16 v[60:63], v[168:171], a[240:243], v[208:211]\n"
"s_waitcnt lgkmcnt(1)\n"
"ds_read_b128 v[180:183], v246 offset:18560\n"
"v_exp_f32 v199, v15\n"
"v_mfma_f32_16x16x32_bf16 v[60:63], v[172:175], a[244:247], v[60:63]\n"
"v_add_f32 v201, v201, v198\n"
"v_add_f32 v201, v201, v199\n"
"v_mfma_f32_16x16x32_bf16 v[52:55], v[172:175], a[228:231], v[208:211]\n"
"v_cvt_pk_bf16_f32 v103, v198, v199\n"
"v_exp_f32 v192, v16\n"
"v_mfma_f32_16x16x32_bf16 v[52:55], v[168:171], a[224:227], v[52:55]\n"
"v_exp_f32 v193, v17\n"
"v_add_f32 v202, v206, v192\n"
"v_mfma_f32_16x16x32_bf16 v[44:47], v[168:171], a[208:211], v[208:211]\n"
"ds_read_b128 v[184:187], v245 offset:18688\n"
"v_exp_f32 v194, v18\n"
"v_mfma_f32_16x16x32_bf16 v[44:47], v[172:175], a[212:215], v[44:47]\n"
"v_add_f32 v202, v202, v193\n"
"v_cvt_pk_bf16_f32 v104, v192, v193\n"
"v_mfma_f32_16x16x32_bf16 v[36:39], v[172:175], a[196:199], v[208:211]\n"
"v_add_f32 v202, v202, v194\n"
"v_exp_f32 v195, v19\n"
"v_mfma_f32_16x16x32_bf16 v[36:39], v[168:171], a[192:195], v[36:39]\n"
"s_waitcnt lgkmcnt(2)\n"
"v_exp_f32 v196, v20\n"
"v_mfma_f32_16x16x32_bf16 v[36:39], v[176:179], a[200:203], v[36:39]\n"
"s_waitcnt lgkmcnt(1)\n"
"ds_read_b128 v[188:191], v246 offset:18688\n"
"ds_read_b64_tr_b16 v[160:161], v247 offset:0\n"
"v_mfma_f32_16x16x32_bf16 v[36:39], v[180:183], a[204:207], v[36:39]\n"
"v_add_f32 v202, v202, v195\n"
"v_cvt_pk_bf16_f32 v105, v194, v195\n"
"v_mfma_f32_16x16x32_bf16 v[44:47], v[180:183], a[220:223], v[44:47]\n"
"ds_read_b64_tr_b16 v[162:163], v247 offset:6144\n"
"v_add_f32 v202, v202, v196\n"
"v_exp_f32 v197, v21\n"
"v_mfma_f32_16x16x32_bf16 v[44:47], v[176:179], a[216:219], v[44:47]\n"
"v_exp_f32 v198, v22\n"
"v_mfma_f32_16x16x32_bf16 v[52:55], v[176:179], a[232:235], v[52:55]\n"
"ds_read_b64_tr_b16 v[164:165], v248 offset:0\n"
"v_add_f32 v202, v202, v197\n"
"v_mfma_f32_16x16x32_bf16 v[52:55], v[180:183], a[236:239], v[52:55]\n"
"v_cvt_pk_bf16_f32 v106, v196, v197\n"
"v_add_f32 v202, v202, v198\n"
"v_mfma_f32_16x16x32_bf16 v[60:63], v[180:183], a[252:255], v[60:63]\n"
"ds_read_b64_tr_b16 v[166:167], v248 offset:6144\n"
"v_exp_f32 v199, v23\n"
"v_mfma_f32_16x16x32_bf16 v[60:63], v[176:179], a[248:251], v[60:63]\n"
"s_waitcnt lgkmcnt(5)\n"
"v_exp_f32 v192, v24\n"
"v_mfma_f32_16x16x32_bf16 v[60:63], v[184:187], v[152:155], v[60:63]\n"
"s_waitcnt lgkmcnt(4)\n"
"ds_read_b64_tr_b16 v[168:169], v239 offset:0\n"
"v_add_f32 v202, v202, v199\n"
"v_mfma_f32_16x16x32_bf16 v[60:63], v[188:191], v[156:159], v[60:63]\n"
"v_cvt_pk_bf16_f32 v107, v198, v199\n"
"v_add_f32 v203, v207, v192\n"
"v_exp_f32 v193, v25\n"
"v_mfma_f32_16x16x32_bf16 v[52:55], v[188:191], v[148:151], v[52:55]\n"
"ds_read_b64_tr_b16 v[170:171], v239 offset:6144\n"
"v_exp_f32 v194, v26\n"
"v_mfma_f32_16x16x32_bf16 v[52:55], v[184:187], v[144:147], v[52:55]\n"
"v_add_f32 v203, v203, v193\n"
"v_cvt_pk_bf16_f32 v108, v192, v193\n"
"v_mfma_f32_16x16x32_bf16 v[44:47], v[184:187], v[136:139], v[44:47]\n"
"ds_read_b64_tr_b16 v[172:173], v240 offset:0\n"
"v_add_f32 v203, v203, v194\n"
"v_mfma_f32_16x16x32_bf16 v[44:47], v[188:191], v[140:143], v[44:47]\n"
"v_exp_f32 v195, v27\n"
"v_mfma_f32_16x16x32_bf16 v[36:39], v[188:191], v[132:135], v[36:39]\n"
"ds_read_b64_tr_b16 v[174:175], v240 offset:6144\n"
"v_exp_f32 v196, v28\n"
"v_mfma_f32_16x16x32_bf16 v[36:39], v[184:187], v[128:131], v[36:39]\n"
"v_add_f32 v203, v203, v195\n"
"v_cvt_pk_bf16_f32 v109, v194, v195\n"
"v_add_f32 v203, v203, v196\n"
"v_exp_f32 v197, v29\n"
"v_exp_f32 v198, v30\n"
"v_add_f32 v203, v203, v197\n"
"v_cvt_pk_bf16_f32 v110, v196, v197\n"
"v_add_f32 v203, v203, v198\n"
"v_exp_f32 v199, v31\n"
"s_nop 0\n"
"v_add_f32 v203, v203, v199\n"
"v_cvt_pk_bf16_f32 v111, v198, v199\n"
"v_add_f32 v212, v200, v201\n"
"v_add_f32 v213, v202, v203\n"
"v_add_f32 v212, v212, v213\n"
"v_min3_f32 v213, v200, v201, v202\n"
"v_cmp_nge_f32 vcc, s90, v212\n"
"v_min_f32 v213, v213, v203\n"
"v_cmp_nle_f32 s[98:99], s12, v213\n"
"v_mov_b64 v[64:65], v[208:209]\n"
"v_mov_b64 v[66:67], v[208:209]\n"
"v_mov_b64 v[68:69], v[208:209]\n"
"v_mov_b64 v[70:71], v[208:209]\n"
"v_mov_b64 v[72:73], v[208:209]\n"
"v_mov_b64 v[74:75], v[208:209]\n"
"v_mov_b64 v[76:77], v[208:209]\n"
"v_mov_b64 v[78:79], v[208:209]\n"
"s_or_b64 vcc, vcc, s[98:99]\n"
"s_cbranch_vccnz .Lat_tr0\n"
".Lat_bk0:\n"
"s_waitcnt lgkmcnt(6)\n"
"v_mfma_f32_16x16x32_bf16 a[0:3], v[96:99], v[160:163], 0\n"
"s_waitcnt lgkmcnt(4)\n"
"ds_read_b64_tr_b16 v[176:177], v247 offset:128\n"
"v_mfma_f32_16x16x32_bf16 a[4:7], v[96:99], v[164:167], 0\n"
"v_mfma_f32_16x16x32_bf16 a[52:55], v[100:103], v[164:167], 0\n"
"ds_read_b64_tr_b16 v[178:179], v247 offset:6272\n"
"v_exp_f32 v192, v32\n"
"v_mfma_f32_16x16x32_bf16 a[48:51], v[100:103], v[160:163], 0\n"
"s_memrealtime s[98:99]\n"
"v_add_f32 v204, v200, v192\n"
"v_exp_f32 v193, v33\n"
"v_mfma_f32_16x16x32_bf16 a[96:99], v[104:107], v[160:163], 0\n"
"ds_read_b64_tr_b16 v[180:181], v248 offset:128\n"
"v_add_f32 v204, v204, v193\n"
"v_mfma_f32_16x16x32_bf16 a[100:103], v[104:107], v[164:167], 0\n"
"v_exp_f32 v194, v34\n"
"v_mfma_f32_16x16x32_bf16 a[148:151], v[108:111], v[164:167], 0\n"
"s_mov_b32 m0, s77\n"
"ds_read_b64_tr_b16 v[182:183], v248 offset:6272\n"
"v_cvt_pk_bf16_f32 v112, v192, v193\n"
"v_exp_f32 v195, v35\n"
"v_mfma_f32_16x16x32_bf16 a[144:147], v[108:111], v[160:163], 0\n"
"s_waitcnt lgkmcnt(6)\n"
"buffer_load_dwordx4 v236, s[40:43], s72 offen offset:0 nt lds\n"
"v_mfma_f32_16x16x32_bf16 a[152:155], v[108:111], v[168:171], 0\n"
"s_waitcnt lgkmcnt(4)\n"
"ds_read_b64_tr_b16 v[184:185], v239 offset:128\n"
"v_add_f32 v204, v204, v194\n"
"v_mfma_f32_16x16x32_bf16 a[156:159], v[108:111], v[172:175], 0\n"
"v_exp_f32 v196, v36\n"
"v_mfma_f32_16x16x32_bf16 a[108:111], v[104:107], v[172:175], 0\n"
"ds_read_b64_tr_b16 v[186:187], v239 offset:6272\n"
"v_add_f32 v204, v204, v195\n"
"v_mfma_f32_16x16x32_bf16 a[104:107], v[104:107], v[168:171], 0\n"
"v_exp_f32 v197, v37\n"
"v_mfma_f32_16x16x32_bf16 a[56:59], v[100:103], v[168:171], 0\n"
"ds_read_b64_tr_b16 v[188:189], v240 offset:128\n"
"v_cvt_pk_bf16_f32 v113, v194, v195\n"
"v_mfma_f32_16x16x32_bf16 a[60:63], v[100:103], v[172:175], 0\n"
"v_add_f32 v204, v204, v196\n"
"v_add_f32 v204, v204, v197\n"
"v_cvt_pk_bf16_f32 v114, v196, v197\n"
"v_mfma_f32_16x16x32_bf16 a[12:15], v[96:99], v[172:175], 0\n"
"ds_read_b64_tr_b16 v[190:191], v240 offset:6272\n"
"v_exp_f32 v198, v38\n"
"v_mfma_f32_16x16x32_bf16 a[8:11], v[96:99], v[168:171], 0\n"
"s_waitcnt lgkmcnt(6)\n"
"buffer_load_dwordx4 v237, s[40:43], s72 offen offset:1024 nt lds\n"
"v_mfma_f32_16x16x32_bf16 a[16:19], v[96:99], v[176:179], 0\n"
"s_waitcnt lgkmcnt(4)\n"
"ds_read_b64_tr_b16 v[160:161], v247 offset:256\n"
"v_add_f32 v204, v204, v198\n"
"v_mfma_f32_16x16x32_bf16 a[20:23], v[96:99], v[180:183], 0\n"
"v_exp_f32 v199, v39\n"
"v_mfma_f32_16x16x32_bf16 a[68:71], v[100:103], v[180:183], 0\n"
"ds_read_b64_tr_b16 v[162:163], v247 offset:6400\n"
"v_add_f32 v204, v204, v199\n"
"v_mfma_f32_16x16x32_bf16 a[64:67], v[100:103], v[176:179], 0\n"
"v_cvt_pk_bf16_f32 v115, v198, v199\n"
"v_exp_f32 v192, v40\n"
"v_mfma_f32_16x16x32_bf16 a[112:115], v[104:107], v[176:179], 0\n"
"ds_read_b64_tr_b16 v[164:165], v248 offset:256\n"
"v_add_f32 v205, v201, v192\n"
"v_exp_f32 v193, v41\n"
"v_mfma_f32_16x16x32_bf16 a[116:119], v[104:107], v[180:183], 0\n"
"v_add_f32 v205, v205, v193\n"
"v_cvt_pk_bf16_f32 v116, v192, v193\n"
"v_mfma_f32_16x16x32_bf16 a[164:167], v[108:111], v[180:183], 0\n"
"ds_read_b64_tr_b16 v[166:167], v248 offset:6400\n"
"v_exp_f32 v194, v42\n"
"v_mfma_f32_16x16x32_bf16 a[160:163], v[108:111], v[176:179], 0\n"
"s_waitcnt lgkmcnt(6)\n"
"buffer_load_dwordx4 v238, s[40:43], s72 offen offset:2048 nt lds\n"
"v_mfma_f32_16x16x32_bf16 a[168:171], v[108:111], v[184:187], 0\n"
"s_waitcnt lgkmcnt(4)\n"
"ds_read_b64_tr_b16 v[168:169], v239 offset:256\n"
"v_add_f32 v205, v205, v194\n"
"v_mfma_f32_16x16x32_bf16 a[172:175], v[108:111], v[188:191], 0\n"
"s_add_u32 s72, s72, 3072\n"
"v_exp_f32 v195, v43\n"
"v_mfma_f32_16x16x32_bf16 a[124:127], v[104:107], v[188:191], 0\n"
"ds_read_b64_tr_b16 v[170:171], v239 offset:6400\n"
"v_add_f32 v205, v205, v195\n"
"v_mfma_f32_16x16x32_bf16 a[120:123], v[104:107], v[184:187], 0\n"
"v_cvt_pk_bf16_f32 v117, v194, v195\n"
"v_exp_f32 v196, v44\n"
"v_exp_f32 v197, v45\n"
"v_mfma_f32_16x16x32_bf16 a[72:75], v[100:103], v[184:187], 0\n"
"ds_read_b64_tr_b16 v[172:173], v240 offset:256\n"
"v_add_f32 v205, v205, v196\n"
"v_mfma_f32_16x16x32_bf16 a[76:79], v[100:103], v[188:191], 0\n"
"v_add_f32 v205, v205, v197\n"
"v_cvt_pk_bf16_f32 v118, v196, v197\n"
"v_mfma_f32_16x16x32_bf16 a[28:31], v[96:99], v[188:191], 0\n"
"ds_read_b64_tr_b16 v[174:175], v240 offset:6400\n"
"v_exp_f32 v198, v46\n"
"v_mfma_f32_16x16x32_bf16 a[24:27], v[96:99], v[184:187], 0\n"
"s_waitcnt lgkmcnt(6)\n"
"v_exp_f32 v199, v47\n"
"v_mfma_f32_16x16x32_bf16 a[32:35], v[96:99], v[160:163], 0\n"
"s_waitcnt lgkmcnt(4)\n"
"v_add_f32 v205, v205, v198\n"
"v_add_f32 v205, v205, v199\n"
"v_mfma_f32_16x16x32_bf16 a[36:39], v[96:99], v[164:167], 0\n"
"v_cvt_pk_bf16_f32 v119, v198, v199\n"
"v_exp_f32 v192, v48\n"
"v_mfma_f32_16x16x32_bf16 a[84:87], v[100:103], v[164:167], 0\n"
"v_exp_f32 v193, v49\n"
"v_add_f32 v206, v202, v192\n"
"v_mfma_f32_16x16x32_bf16 a[80:83], v[100:103], v[160:163], 0\n"
"s_waitcnt vmcnt(11) lgkmcnt(0)\n"
"s_barrier\n"
"v_mfma_f32_16x16x32_bf16 a[128:131], v[104:107], v[160:163], 0\n"
"s_add_u32 s91, s98, s70\n"
"ds_read_b128 v[176:179], v245 offset:24576\n"
"v_exp_f32 v194, v50\n"
"v_mfma_f32_16x16x32_bf16 a[132:135], v[104:107], v[164:167], 0\n"
"s_sub_u32 s91, s91, 45\n"
"v_add_f32 v206, v206, v193\n"
"v_cvt_pk_bf16_f32 v120, v192, v193\n"
"v_mfma_f32_16x16x32_bf16 a[180:183], v[108:111], v[164:167], 0\n"
"v_add_f32 v206, v206, v194\n"
"v_exp_f32 v195, v51\n"
"v_mfma_f32_16x16x32_bf16 a[176:179], v[108:111], v[160:163], 0\n"
"v_exp_f32 v196, v52\n"
"v_mfma_f32_16x16x32_bf16 a[184:187], v[108:111], v[168:171], 0\n"
"ds_read_b128 v[180:183], v246 offset:24576\n"
"v_add_f32 v206, v206, v195\n"
"v_mfma_f32_16x16x32_bf16 a[188:191], v[108:111], v[172:175], 0\n"
"v_cvt_pk_bf16_f32 v121, v194, v195\n"
"v_add_f32 v206, v206, v196\n"
"v_exp_f32 v197, v53\n"
"v_mfma_f32_16x16x32_bf16 a[140:143], v[104:107], v[172:175], 0\n"
"v_exp_f32 v198, v54\n"
"v_mfma_f32_16x16x32_bf16 a[136:139], v[104:107], v[168:171], 0\n"
"v_add_f32 v206, v206, v197\n"
"v_cvt_pk_bf16_f32 v122, v196, v197\n"
"v_mfma_f32_16x16x32_bf16 a[88:91], v[100:103], v[168:171], 0\n"
"ds_read_b128 v[184:187], v245 offset:24704\n"
"v_add_f32 v206, v206, v198\n"
"v_mfma_f32_16x16x32_bf16 a[92:95], v[100:103], v[172:175], 0\n"
"v_exp_f32 v199, v55\n"
"v_mfma_f32_16x16x32_bf16 a[44:47], v[96:99], v[172:175], 0\n"
"v_add_f32 v206, v206, v199\n"
"v_exp_f32 v192, v56\n"
"v_mfma_f32_16x16x32_bf16 a[40:43], v[96:99], v[168:171], 0\n"
"s_waitcnt lgkmcnt(2)\n"
"v_cvt_pk_bf16_f32 v123, v198, v199\n"
"v_mfma_f32_16x16x32_bf16 v[0:3], v[176:179], a[192:195], v[64:67]\n"
"s_waitcnt lgkmcnt(1)\n"
"ds_read_b128 v[188:191], v246 offset:24704\n"
"v_add_f32 v207, v203, v192\n"
"v_mfma_f32_16x16x32_bf16 v[0:3], v[180:183], a[196:199], v[0:3]\n"
"v_exp_f32 v193, v57\n"
"v_mfma_f32_16x16x32_bf16 v[8:11], v[180:183], a[212:215], v[68:71]\n"
"s_add_u32 m0, s77, 12288\n"
"v_exp_f32 v194, v58\n"
"v_mfma_f32_16x16x32_bf16 v[8:11], v[176:179], a[208:211], v[8:11]\n"
"buffer_load_dwordx4 v236, s[40:43], s72 offen offset:0 nt lds\n"
"v_mfma_f32_16x16x32_bf16 v[16:19], v[176:179], a[224:227], v[72:75]\n"
"ds_read_b128 v[160:163], v245 offset:24832\n"
"v_add_f32 v207, v207, v193\n"
"v_cvt_pk_bf16_f32 v124, v192, v193\n"
"v_mfma_f32_16x16x32_bf16 v[16:19], v[180:183], a[228:231], v[16:19]\n"
"v_add_f32 v207, v207, v194\n"
"v_mfma_f32_16x16x32_bf16 v[24:27], v[180:183], a[244:247], v[76:79]\n"
"ds_read_b128 v[164:167], v246 offset:24832\n"
"v_mfma_f32_16x16x32_bf16 v[24:27], v[176:179], a[240:243], v[24:27]\n"
"s_waitcnt lgkmcnt(3)\n"
"v_exp_f32 v195, v59\n"
"v_mfma_f32_16x16x32_bf16 v[24:27], v[184:187], a[248:251], v[24:27]\n"
"s_waitcnt lgkmcnt(2)\n"
"v_exp_f32 v196, v60\n"
"v_mfma_f32_16x16x32_bf16 v[24:27], v[188:191], a[252:255], v[24:27]\n"
"v_add_f32 v207, v207, v195\n"
"v_cvt_pk_bf16_f32 v125, v194, v195\n"
"v_add_f32 v207, v207, v196\n"
"v_mfma_f32_16x16x32_bf16 v[16:19], v[188:191], a[236:239], v[16:19]\n"
"ds_read_b128 v[168:171], v245 offset:30720\n"
"v_mfma_f32_16x16x32_bf16 v[16:19], v[184:187], a[232:235], v[16:19]\n"
"buffer_load_dwordx4 v237, s[40:43], s72 offen offset:1024 nt lds\n"
"v_mfma_f32_16x16x32_bf16 v[8:11], v[184:187], a[216:219], v[8:11]\n"
"ds_read_b128 v[172:175], v246 offset:30720\n"
"v_mfma_f32_16x16x32_bf16 v[8:11], v[188:191], a[220:223], v[8:11]\n"
"v_exp_f32 v197, v61\n"
"v_mfma_f32_16x16x32_bf16 v[0:3], v[188:191], a[204:207], v[0:3]\n"
"ds_read_b128 v[176:179], v245 offset:30848\n"
"v_add_f32 v207, v207, v197\n"
"v_mfma_f32_16x16x32_bf16 v[0:3], v[184:187], a[200:203], v[0:3]\n"
"s_waitcnt lgkmcnt(4)\n"
"v_exp_f32 v198, v62\n"
"v_cvt_pk_bf16_f32 v126, v196, v197\n"
"v_mfma_f32_16x16x32_bf16 v[0:3], v[160:163], v[128:131], v[0:3]\n"
"s_waitcnt lgkmcnt(3)\n"
"ds_read_b128 v[180:183], v246 offset:30848\n"
"v_add_f32 v207, v207, v198\n"
"v_mfma_f32_16x16x32_bf16 v[0:3], v[164:167], v[132:135], v[0:3]\n"
"v_exp_f32 v199, v63\n"
"v_mfma_f32_16x16x32_bf16 v[8:11], v[164:167], v[140:143], v[8:11]\n"
"ds_read_b128 v[184:187], v245 offset:30976\n"
"v_add_f32 v207, v207, v199\n"
"v_mfma_f32_16x16x32_bf16 v[8:11], v[160:163], v[136:139], v[8:11]\n"
"buffer_load_dwordx4 v238, s[40:43], s72 offen offset:2048 nt lds\n"
"v_cvt_pk_bf16_f32 v127, v198, v199\n"
"v_mfma_f32_16x16x32_bf16 v[16:19], v[160:163], v[144:147], v[16:19]\n"
"ds_read_b128 v[188:191], v246 offset:30976\n"
"v_add_f32 v212, v204, v205\n"
"v_mfma_f32_16x16x32_bf16 v[16:19], v[164:167], v[148:151], v[16:19]\n"
"v_add_f32 v213, v206, v207\n"
"v_add_f32 v212, v212, v213\n"
"v_mfma_f32_16x16x32_bf16 v[24:27], v[164:167], v[156:159], v[24:27]\n"
"v_cmp_nge_f32 vcc, s90, v212\n"
"v_mfma_f32_16x16x32_bf16 v[24:27], v[160:163], v[152:155], v[24:27]\n"
"s_cbranch_vccnz .Lat_tr1\n"
".Lat_bk1:\n"
"s_waitcnt lgkmcnt(5)\n"
"v_mfma_f32_16x16x32_bf16 v[4:7], v[168:171], a[192:195], v[64:67]\n"
"s_waitcnt lgkmcnt(4)\n"
"v_exp_f32 v192, v0\n"
"v_mfma_f32_16x16x32_bf16 v[4:7], v[172:175], a[196:199], v[4:7]\n"
"s_add_u32 s72, s72, s1\n"
"v_exp_f32 v193, v1\n"
"v_add_f32 v200, v204, v192\n"
"v_mfma_f32_16x16x32_bf16 v[12:15], v[172:175], a[212:215], v[68:71]\n"
"s_add_u32 m0, s78, 36864\n"
"v_add_f32 v200, v200, v193\n"
"v_mfma_f32_16x16x32_bf16 v[12:15], v[168:171], a[208:211], v[12:15]\n"
"s_waitcnt lgkmcnt(3)\n"
"buffer_load_dwordx4 v236, s[44:47], s73 offen offset:0 nt lds\n"
"v_mfma_f32_16x16x32_bf16 v[12:15], v[176:179], a[216:219], v[12:15]\n"
"s_waitcnt lgkmcnt(2)\n"
"v_exp_f32 v194, v2\n"
"v_mfma_f32_16x16x32_bf16 v[12:15], v[180:183], a[220:223], v[12:15]\n"
"v_cvt_pk_bf16_f32 v32, v192, v193\n"
"v_exp_f32 v195, v3\n"
"v_mfma_f32_16x16x32_bf16 v[4:7], v[180:183], a[204:207], v[4:7]\n"
"v_add_f32 v200, v200, v194\n"
"v_add_f32 v200, v200, v195\n"
"v_mfma_f32_16x16x32_bf16 v[4:7], v[176:179], a[200:203], v[4:7]\n"
"s_waitcnt lgkmcnt(1)\n"
"v_cvt_pk_bf16_f32 v33, v194, v195\n"
"v_mfma_f32_16x16x32_bf16 v[4:7], v[184:187], v[128:131], v[4:7]\n"
"s_waitcnt lgkmcnt(0)\n"
"v_exp_f32 v192, v8\n"
"v_mfma_f32_16x16x32_bf16 v[4:7], v[188:191], v[132:135], v[4:7]\n"
"v_exp_f32 v193, v9\n"
"v_mfma_f32_16x16x32_bf16 v[12:15], v[188:191], v[140:143], v[12:15]\n"
"v_add_f32 v201, v205, v192\n"
"v_add_f32 v201, v201, v193\n"
"v_mfma_f32_16x16x32_bf16 v[12:15], v[184:187], v[136:139], v[12:15]\n"
"v_exp_f32 v194, v10\n"
"v_mfma_f32_16x16x32_bf16 v[20:23], v[168:171], a[224:227], v[72:75]\n"
"ds_read_b128 v[160:163], v245 offset:36864\n"
"v_exp_f32 v195, v11\n"
"v_mfma_f32_16x16x32_bf16 v[20:23], v[172:175], a[228:231], v[20:23]\n"
"v_cvt_pk_bf16_f32 v40, v192, v193\n"
"v_add_f32 v201, v201, v194\n"
"v_mfma_f32_16x16x32_bf16 v[28:31], v[172:175], a[244:247], v[76:79]\n"
"v_add_f32 v201, v201, v195\n"
"v_cvt_pk_bf16_f32 v41, v194, v195\n"
"v_mfma_f32_16x16x32_bf16 v[28:31], v[168:171], a[240:243], v[28:31]\n"
"v_exp_f32 v192, v16\n"
"v_mfma_f32_16x16x32_bf16 v[28:31], v[176:179], a[248:251], v[28:31]\n"
"ds_read_b128 v[164:167], v246 offset:36864\n"
"v_exp_f32 v196, v4\n"
"v_mfma_f32_16x16x32_bf16 v[28:31], v[180:183], a[252:255], v[28:31]\n"
"v_exp_f32 v197, v5\n"
"v_mfma_f32_16x16x32_bf16 v[20:23], v[180:183], a[236:239], v[20:23]\n"
"v_exp_f32 v198, v6\n"
"v_mfma_f32_16x16x32_bf16 v[20:23], v[176:179], a[232:235], v[20:23]\n"
"v_add_f32 v200, v200, v196\n"
"v_add_f32 v200, v200, v197\n"
"v_mfma_f32_16x16x32_bf16 v[20:23], v[184:187], v[144:147], v[20:23]\n"
"ds_read_b128 v[168:171], v245 offset:36992\n"
"v_cvt_pk_bf16_f32 v34, v196, v197\n"
"v_mfma_f32_16x16x32_bf16 v[20:23], v[188:191], v[148:151], v[20:23]\n"
"v_add_f32 v200, v200, v198\n"
"v_exp_f32 v199, v7\n"
"v_mfma_f32_16x16x32_bf16 v[28:31], v[188:191], v[156:159], v[28:31]\n"
"v_exp_f32 v196, v12\n"
"v_mfma_f32_16x16x32_bf16 v[28:31], v[184:187], v[152:155], v[28:31]\n"
"v_add_f32 v200, v200, v199\n"
"v_cvt_pk_bf16_f32 v35, v198, v199\n"
"v_exp_f32 v197, v13\n"
"v_exp_f32 v198, v14\n"
"v_exp_f32 v199, v15\n"
"v_add_f32 v201, v201, v196\n"
"v_add_f32 v201, v201, v197\n"
"v_cvt_pk_bf16_f32 v42, v196, v197\n"
"s_branch .Lat_j3\n"
".p2align 5\ns_nop 0\ns_nop 0\ns_nop 0\ns_nop 0\n"
".Lat_j0:\n"
"s_waitcnt lgkmcnt(2)\n"
"v_mfma_f32_16x16x32_bf16 v[0:3], v[160:163], a[192:195], v[64:67]\n"
"s_waitcnt lgkmcnt(1)\n"
"ds_read_b128 v[172:175], v246 offset:128\n"
"v_add_f32 v206, v206, v194\n"
"v_mfma_f32_16x16x32_bf16 v[0:3], v[164:167], a[196:199], v[0:3]\n"
"v_exp_f32 v195, v107\n"
"v_mfma_f32_16x16x32_bf16 v[8:11], v[164:167], a[212:215], v[68:71]\n"
"s_add_u32 m0, s77, 24576\n"
"v_exp_f32 v196, v52\n"
"v_mfma_f32_16x16x32_bf16 v[8:11], v[160:163], a[208:211], v[8:11]\n"
"buffer_load_dwordx4 v236, s[40:43], s72 offen offset:0 nt lds\n"
"v_mfma_f32_16x16x32_bf16 v[16:19], v[160:163], a[224:227], v[72:75]\n"
"ds_read_b128 v[176:179], v245 offset:256\n"
"v_add_f32 v206, v206, v195\n"
"v_mfma_f32_16x16x32_bf16 v[16:19], v[164:167], a[228:231], v[16:19]\n"
"v_cvt_pk_bf16_f32 v89, v194, v195\n"
"v_add_f32 v206, v206, v196\n"
"v_mfma_f32_16x16x32_bf16 v[24:27], v[164:167], a[244:247], v[76:79]\n"
"v_exp_f32 v197, v53\n"
"v_mfma_f32_16x16x32_bf16 v[24:27], v[160:163], a[240:243], v[24:27]\n"
"s_waitcnt lgkmcnt(2)\n"
"v_exp_f32 v198, v54\n"
"v_mfma_f32_16x16x32_bf16 v[24:27], v[168:171], a[248:251], v[24:27]\n"
"s_waitcnt lgkmcnt(1)\n"
"ds_read_b128 v[180:183], v246 offset:256\n"
"v_add_f32 v206, v206, v197\n"
"v_mfma_f32_16x16x32_bf16 v[24:27], v[172:175], a[252:255], v[24:27]\n"
"buffer_load_dwordx4 v237, s[40:43], s72 offen offset:1024 nt lds\n"
"v_mfma_f32_16x16x32_bf16 v[16:19], v[172:175], a[236:239], v[16:19]\n"
"v_cvt_pk_bf16_f32 v90, v196, v197\n"
"v_add_f32 v206, v206, v198\n"
"v_mfma_f32_16x16x32_bf16 v[16:19], v[168:171], a[232:235], v[16:19]\n"
"v_exp_f32 v199, v55\n"
"v_mfma_f32_16x16x32_bf16 v[8:11], v[168:171], a[216:219], v[8:11]\n"
"ds_read_b128 v[184:187], v245 offset:6144\n"
"v_add_f32 v206, v206, v199\n"
"v_mfma_f32_16x16x32_bf16 v[8:11], v[172:175], a[220:223], v[8:11]\n"
"v_exp_f32 v192, v108\n"
"v_mfma_f32_16x16x32_bf16 v[0:3], v[172:175], a[204:207], v[0:3]\n"
"v_cvt_pk_bf16_f32 v91, v198, v199\n"
"v_add_f32 v207, v203, v192\n"
"v_mfma_f32_16x16x32_bf16 v[0:3], v[168:171], a[200:203], v[0:3]\n"
"s_waitcnt lgkmcnt(2)\n"
"buffer_load_dwordx4 v238, s[40:43], s72 offen offset:2048 nt lds\n"
"v_mfma_f32_16x16x32_bf16 v[0:3], v[176:179], v[128:131], v[0:3]\n"
"s_waitcnt lgkmcnt(1)\n"
"ds_read_b128 v[188:191], v246 offset:6144\n"
"v_mfma_f32_16x16x32_bf16 v[0:3], v[180:183], v[132:135], v[0:3]\n"
"s_add_u32 s72, s72, 3072\n"
"v_exp_f32 v193, v109\n"
"v_mfma_f32_16x16x32_bf16 v[8:11], v[180:183], v[140:143], v[8:11]\n"
"v_exp_f32 v194, v110\n"
"v_mfma_f32_16x16x32_bf16 v[8:11], v[176:179], v[136:139], v[8:11]\n"
"v_add_f32 v207, v207, v193\n"
"v_cvt_pk_bf16_f32 v92, v192, v193\n"
"v_mfma_f32_16x16x32_bf16 v[16:19], v[176:179], v[144:147], v[16:19]\n"
"s_add_u32 m0, s77, 36864\n"
"ds_read_b128 v[160:163], v245 offset:6272\n"
"v_add_f32 v207, v207, v194\n"
"v_mfma_f32_16x16x32_bf16 v[16:19], v[180:183], v[148:151], v[16:19]\n"
"buffer_load_dwordx4 v236, s[40:43], s72 offen offset:0 nt lds\n"
"v_mfma_f32_16x16x32_bf16 v[24:27], v[180:183], v[156:159], v[24:27]\n"
"v_exp_f32 v195, v111\n"
"v_mfma_f32_16x16x32_bf16 v[24:27], v[176:179], v[152:155], v[24:27]\n"
"s_waitcnt lgkmcnt(2)\n"
"v_exp_f32 v196, v60\n"
"v_mfma_f32_16x16x32_bf16 v[28:31], v[184:187], a[240:243], v[76:79]\n"
"s_waitcnt lgkmcnt(1)\n"
"ds_read_b128 v[164:167], v246 offset:6272\n"
"v_add_f32 v207, v207, v195\n"
"v_mfma_f32_16x16x32_bf16 v[28:31], v[188:191], a[244:247], v[28:31]\n"
"v_cvt_pk_bf16_f32 v93, v194, v195\n"
"v_add_f32 v207, v207, v196\n"
"v_mfma_f32_16x16x32_bf16 v[20:23], v[188:191], a[228:231], v[72:75]\n"
"v_exp_f32 v197, v61\n"
"v_mfma_f32_16x16x32_bf16 v[20:23], v[184:187], a[224:227], v[20:23]\n"
"buffer_load_dwordx4 v237, s[40:43], s72 offen offset:1024 nt lds\n"
"v_mfma_f32_16x16x32_bf16 v[12:15], v[184:187], a[208:211], v[68:71]\n"
"ds_read_b128 v[168:171], v245 offset:6400\n"
"v_add_f32 v207, v207, v197\n"
"v_mfma_f32_16x16x32_bf16 v[12:15], v[188:191], a[212:215], v[12:15]\n"
"v_exp_f32 v198, v62\n"
"v_mfma_f32_16x16x32_bf16 v[4:7], v[188:191], a[196:199], v[64:67]\n"
"v_cvt_pk_bf16_f32 v94, v196, v197\n"
"v_add_f32 v207, v207, v198\n"
"v_mfma_f32_16x16x32_bf16 v[4:7], v[184:187], a[192:195], v[4:7]\n"
"s_waitcnt lgkmcnt(2)\n"
"v_exp_f32 v199, v63\n"
"v_mfma_f32_16x16x32_bf16 v[4:7], v[160:163], a[200:203], v[4:7]\n"
"s_waitcnt lgkmcnt(1)\n"
"ds_read_b128 v[172:175], v246 offset:6400\n"
"ds_read_b64_tr_b16 v[176:177], v239 offset:12416\n"
"v_mfma_f32_16x16x32_bf16 v[4:7], v[164:167], a[204:207], v[4:7]\n"
"buffer_load_dwordx4 v238, s[40:43], s72 offen offset:2048 nt lds\n"
"v_mfma_f32_16x16x32_bf16 v[12:15], v[164:167], a[220:223], v[12:15]\n"
"s_add_u32 s72, s72, s0\n"
"ds_read_b64_tr_b16 v[178:179], v239 offset:18560\n"
"v_add_f32_e64 v207, v207, v199\n"
"v_mfma_f32_16x16x32_bf16 v[12:15], v[160:163], a[216:219], v[12:15]\n"
"v_cvt_pk_bf16_f32 v95, v198, v199\n"
"v_add_f32 v212, v204, v205\n"
"v_mfma_f32_16x16x32_bf16 v[20:23], v[160:163], a[232:235], v[20:23]\n"
"ds_read_b64_tr_b16 v[180:181], v239 offset:24704\n"
"v_add_f32 v213, v206, v207\n"
"v_mfma_f32_16x16x32_bf16 v[20:23], v[164:167], a[236:239], v[20:23]\n"
"v_add_f32 v212, v212, v213\n"
"v_cmp_nge_f32 vcc, s90, v212\n"
"v_mfma_f32_16x16x32_bf16 v[28:31], v[164:167], a[252:255], v[28:31]\n"
"ds_read_b64_tr_b16 v[182:183], v239 offset:30848\n"
"v_mfma_f32_16x16x32_bf16 v[28:31], v[160:163], a[248:251], v[28:31]\n"
"s_waitcnt lgkmcnt(5)\n"
"v_mfma_f32_16x16x32_bf16 v[28:31], v[168:171], v[152:155], v[28:31]\n"
"s_waitcnt lgkmcnt(4)\n"
"ds_read_b64_tr_b16 v[184:185], v240 offset:12416\n"
"v_mfma_f32_16x16x32_bf16 v[28:31], v[172:175], v[156:159], v[28:31]\n"
"v_mfma_f32_16x16x32_bf16 v[20:23], v[172:175], v[148:151], v[20:23]\n"
"s_mov_b32 m0, s24\n"
"ds_read_b64_tr_b16 v[186:187], v240 offset:18560\n"
"v_mfma_f32_16x16x32_bf16 v[20:23], v[168:171], v[144:147], v[20:23]\n"
"buffer_load_dwordx4 v214, s[20:23], s18 offen offset:0 lds\n"
"v_mfma_f32_16x16x32_bf16 v[12:15], v[168:171], v[136:139], v[12:15]\n"
"ds_read_b64_tr_b16 v[188:189], v240 offset:24704\n"
"v_mfma_f32_16x16x32_bf16 v[12:15], v[172:175], v[140:143], v[12:15]\n"
"buffer_load_dwordx4 v215, s[20:23], s18 offen offset:1024 lds\n"
"v_mfma_f32_16x16x32_bf16 v[4:7], v[172:175], v[132:135], v[4:7]\n"
"ds_read_b64_tr_b16 v[190:191], v240 offset:30848\n"
"v_mfma_f32_16x16x32_bf16 v[4:7], v[168:171], v[128:131], v[4:7]\n"
"s_cbranch_vccnz .Lat_tr2\n"
".Lat_bk2:\n"
"s_waitcnt lgkmcnt(6)\n"
"v_mfma_f32_16x16x32_bf16 a[24:27], v[112:115], v[176:179], a[24:27]\n"
"s_waitcnt lgkmcnt(4)\n"
"ds_read_b64_tr_b16 v[160:161], v247 offset:12544\n"
"v_mfma_f32_16x16x32_bf16 a[24:27], v[32:35], v[180:183], a[24:27]\n"
"v_mfma_f32_16x16x32_bf16 a[72:75], v[40:43], v[180:183], a[72:75]\n"
"ds_read_b64_tr_b16 v[162:163], v247 offset:18688\n"
"v_mfma_f32_16x16x32_bf16 a[72:75], v[116:119], v[176:179], a[72:75]\n"
"v_mfma_f32_16x16x32_bf16 a[120:123], v[120:123], v[176:179], a[120:123]\n"
"ds_read_b64_tr_b16 v[164:165], v247 offset:24832\n"
"v_mfma_f32_16x16x32_bf16 a[120:123], v[48:51], v[180:183], a[120:123]\n"
"v_mfma_f32_16x16x32_bf16 a[168:171], v[56:59], v[180:183], a[168:171]\n"
"ds_read_b64_tr_b16 v[166:167], v247 offset:30976\n"
"v_mfma_f32_16x16x32_bf16 a[168:171], v[124:127], v[176:179], a[168:171]\n"
"s_waitcnt lgkmcnt(6)\n"
"v_mfma_f32_16x16x32_bf16 a[172:175], v[124:127], v[184:187], a[172:175]\n"
"s_waitcnt lgkmcnt(4)\n"
"ds_read_b64_tr_b16 v[168:169], v248 offset:12544\n"
"v_mfma_f32_16x16x32_bf16 a[172:175], v[56:59], v[188:191], a[172:175]\n"
"v_mfma_f32_16x16x32_bf16 a[124:127], v[48:51], v[188:191], a[124:127]\n"
"ds_read_b64_tr_b16 v[170:171], v248 offset:18688\n"
"v_mfma_f32_16x16x32_bf16 a[124:127], v[120:123], v[184:187], a[124:127]\n"
"v_exp_f32 v192, v0\n"
"v_mfma_f32_16x16x32_bf16 a[76:79], v[116:119], v[184:187], a[76:79]\n"
"ds_read_b64_tr_b16 v[172:173], v248 offset:24832\n"
"v_add_f32 v200, v204, v192\n"
"v_mfma_f32_16x16x32_bf16 a[76:79], v[40:43], v[188:191], a[76:79]\n"
"v_exp_f32 v193, v1\n"
"v_mfma_f32_16x16x32_bf16 a[28:31], v[32:35], v[188:191], a[28:31]\n"
"ds_read_b64_tr_b16 v[174:175], v248 offset:30976\n"
"v_add_f32 v200, v200, v193\n"
"v_mfma_f32_16x16x32_bf16 a[28:31], v[112:115], v[184:187], a[28:31]\n"
"s_waitcnt lgkmcnt(6)\n"
"v_exp_f32 v194, v2\n"
"v_mfma_f32_16x16x32_bf16 a[32:35], v[112:115], v[160:163], a[32:35]\n"
"s_waitcnt lgkmcnt(4)\n"
"ds_read_b64_tr_b16 v[176:177], v239 offset:12544\n"
"v_cvt_pk_bf16_f32 v96, v192, v193\n"
"v_mfma_f32_16x16x32_bf16 a[32:35], v[32:35], v[164:167], a[32:35]\n"
"v_exp_f32 v195, v3\n"
"v_mfma_f32_16x16x32_bf16 a[80:83], v[40:43], v[164:167], a[80:83]\n"
"ds_read_b64_tr_b16 v[178:179], v239 offset:18688\n"
"v_add_f32 v200, v200, v194\n"
"v_mfma_f32_16x16x32_bf16 a[80:83], v[116:119], v[160:163], a[80:83]\n"
"v_exp_f32 v196, v4\n"
"v_mfma_f32_16x16x32_bf16 a[128:131], v[120:123], v[160:163], a[128:131]\n"
"ds_read_b64_tr_b16 v[180:181], v239 offset:24832\n"
"v_add_f32 v200, v200, v195\n"
"v_mfma_f32_16x16x32_bf16 a[128:131], v[48:51], v[164:167], a[128:131]\n"
"v_exp_f32 v197, v5\n"
"v_mfma_f32_16x16x32_bf16 a[176:179], v[56:59], v[164:167], a[176:179]\n"
"ds_read_b64_tr_b16 v[182:183], v239 offset:30976\n"
"v_cvt_pk_bf16_f32 v97, v194, v195\n"
"v_mfma_f32_16x16x32_bf16 a[176:179], v[124:127], v[160:163], a[176:179]\n"
"s_waitcnt lgkmcnt(6)\n"
"v_exp_f32 v198, v6\n"
"v_mfma_f32_16x16x32_bf16 a[180:183], v[124:127], v[168:171], a[180:183]\n"
"s_waitcnt lgkmcnt(4)\n"
"ds_read_b64_tr_b16 v[184:185], v240 offset:12544\n"
"v_add_f32 v200, v200, v196\n"
"v_mfma_f32_16x16x32_bf16 a[180:183], v[56:59], v[172:175], a[180:183]\n"
"v_add_f32 v200, v200, v197\n"
"v_cvt_pk_bf16_f32 v98, v196, v197\n"
"v_mfma_f32_16x16x32_bf16 a[132:135], v[48:51], v[172:175], a[132:135]\n"
"ds_read_b64_tr_b16 v[186:187], v240 offset:18688\n"
"v_add_f32 v200, v200, v198\n"
"v_mfma_f32_16x16x32_bf16 a[132:135], v[120:123], v[168:171], a[132:135]\n"
"v_exp_f32 v199, v7\n"
"v_mfma_f32_16x16x32_bf16 a[84:87], v[116:119], v[168:171], a[84:87]\n"
"ds_read_b64_tr_b16 v[188:189], v240 offset:24832\n"
"v_add_f32 v200, v200, v199\n"
"v_mfma_f32_16x16x32_bf16 a[84:87], v[40:43], v[172:175], a[84:87]\n"
"v_exp_f32 v192, v8\n"
"v_mfma_f32_16x16x32_bf16 a[36:39], v[32:35], v[172:175], a[36:39]\n"
"ds_read_b64_tr_b16 v[190:191], v240 offset:30976\n"
"v_cvt_pk_bf16_f32 v99, v198, v199\n"
"v_mfma_f32_16x16x32_bf16 a[36:39], v[112:115], v[168:171], a[36:39]\n"
"s_waitcnt lgkmcnt(6)\n"
"v_exp_f32 v193, v9\n"
"v_mfma_f32_16x16x32_bf16 a[40:43], v[112:115], v[176:179], a[40:43]\n"
"s_waitcnt lgkmcnt(4)\n"
"v_add_f32 v201, v205, v192\n"
"v_add_f32 v201, v201, v193\n"
"v_mfma_f32_16x16x32_bf16 a[40:43], v[32:35], v[180:183], a[40:43]\n"
"v_exp_f32 v194, v10\n"
"v_mfma_f32_16x16x32_bf16 a[88:91], v[40:43], v[180:183], a[88:91]\n"
"v_exp_f32 v195, v11\n"
"v_mfma_f32_16x16x32_bf16 a[88:91], v[116:119], v[176:179], a[88:91]\n"
"s_waitcnt vmcnt(10) lgkmcnt(0)\n"
"s_barrier\n"
"v_mfma_f32_16x16x32_bf16 a[136:139], v[120:123], v[176:179], a[136:139]\n"
"ds_read_b128 v[160:163], v245 offset:12288\n"
"v_cvt_pk_bf16_f32 v100, v192, v193\n"
"v_mfma_f32_16x16x32_bf16 a[136:139], v[48:51], v[180:183], a[136:139]\n"
"v_exp_f32 v196, v12\n"
"v_mfma_f32_16x16x32_bf16 a[184:187], v[56:59], v[180:183], a[184:187]\n"
"v_add_f32 v201, v201, v194\n"
"v_add_f32 v201, v201, v195\n"
"v_mfma_f32_16x16x32_bf16 a[184:187], v[124:127], v[176:179], a[184:187]\n"
"v_exp_f32 v197, v13\n"
"v_mfma_f32_16x16x32_bf16 a[188:191], v[124:127], v[184:187], a[188:191]\n"
"s_add_u32 m0, s78, 12288\n"
"ds_read_b128 v[164:167], v246 offset:12288\n"
"v_cvt_pk_bf16_f32 v101, v194, v195\n"
"v_mfma_f32_16x16x32_bf16 a[188:191], v[56:59], v[188:191], a[188:191]\n"
"buffer_load_dwordx4 v236, s[44:47], s73 offen offset:0 nt lds\n"
"v_mfma_f32_16x16x32_bf16 a[140:143], v[48:51], v[188:191], a[140:143]\n"
"v_exp_f32 v198, v14\n"
"v_mfma_f32_16x16x32_bf16 a[140:143], v[120:123], v[184:187], a[140:143]\n"
"v_exp_f32 v199, v15\n"
"v_mfma_f32_16x16x32_bf16 a[92:95], v[116:119], v[184:187], a[92:95]\n"
"ds_read_b128 v[168:171], v245 offset:12416\n"
"v_add_f32 v201, v201, v196\n"
"v_mfma_f32_16x16x32_bf16 a[92:95], v[40:43], v[188:191], a[92:95]\n"
"v_add_f32 v201, v201, v197\n"
"v_cvt_pk_bf16_f32 v102, v196, v197\n"
"v_mfma_f32_16x16x32_bf16 a[44:47], v[32:35], v[188:191], a[44:47]\n"
"v_exp_f32 v192, v16\n"
"v_mfma_f32_16x16x32_bf16 a[44:47], v[112:115], v[184:187], a[44:47]\n"
"s_waitcnt lgkmcnt(2)\n"
"v_mfma_f32_16x16x32_bf16 v[32:35], v[160:163], a[192:195], v[64:67]\n"
"s_waitcnt lgkmcnt(1)\n"
"ds_read_b128 v[172:175], v246 offset:12416\n"
"v_add_f32 v201, v201, v198\n"
"v_mfma_f32_16x16x32_bf16 v[32:35], v[164:167], a[196:199], v[32:35]\n"
"v_exp_f32 v193, v17\n"
"v_mfma_f32_16x16x32_bf16 v[40:43], v[164:167], a[212:215], v[68:71]\n"
"v_exp_f32 v194, v18\n"
"v_mfma_f32_16x16x32_bf16 v[40:43], v[160:163], a[208:211], v[40:43]\n"
"buffer_load_dwordx4 v237, s[44:47], s73 offen offset:1024 nt lds\n"
"v_mfma_f32_16x16x32_bf16 v[48:51], v[160:163], a[224:227], v[72:75]\n"
"ds_read_b128 v[176:179], v245 offset:12544\n"
"v_add_f32 v201, v201, v199\n"
"v_mfma_f32_16x16x32_bf16 v[48:51], v[164:167], a[228:231], v[48:51]\n"
"v_cvt_pk_bf16_f32 v103, v198, v199\n"
"v_add_f32 v202, v206, v192\n"
"v_mfma_f32_16x16x32_bf16 v[56:59], v[164:167], a[244:247], v[76:79]\n"
"v_exp_f32 v195, v19\n"
"v_mfma_f32_16x16x32_bf16 v[56:59], v[160:163], a[240:243], v[56:59]\n"
"s_waitcnt lgkmcnt(2)\n"
"v_exp_f32 v196, v20\n"
"v_mfma_f32_16x16x32_bf16 v[56:59], v[168:171], a[248:251], v[56:59]\n"
"s_waitcnt lgkmcnt(1)\n"
"ds_read_b128 v[180:183], v246 offset:12544\n"
"v_add_f32 v202, v202, v193\n"
"v_mfma_f32_16x16x32_bf16 v[56:59], v[172:175], a[252:255], v[56:59]\n"
"buffer_load_dwordx4 v238, s[44:47], s73 offen offset:2048 nt lds\n"
"v_mfma_f32_16x16x32_bf16 v[48:51], v[172:175], a[236:239], v[48:51]\n"
"s_add_u32 s73, s73, s1\n"
"v_exp_f32 v197, v21\n"
"v_mfma_f32_16x16x32_bf16 v[48:51], v[168:171], a[232:235], v[48:51]\n"
"v_cvt_pk_bf16_f32 v104, v192, v193\n"
"v_add_f32_e64 v202, v202, v194\n"
"v_mfma_f32_16x16x32_bf16 v[40:43], v[168:171], a[216:219], v[40:43]\n"
"ds_read_b128 v[184:187], v245 offset:18432\n"
"v_add_f32 v202, v202, v195\n"
"v_mfma_f32_16x16x32_bf16 v[40:43], v[172:175], a[220:223], v[40:43]\n"
"v_exp_f32 v198, v22\n"
"v_mfma_f32_16x16x32_bf16 v[32:35], v[172:175], a[204:207], v[32:35]\n"
"s_add_u32 m0, s78, 24576\n"
"v_exp_f32 v199, v23\n"
"v_mfma_f32_16x16x32_bf16 v[32:35], v[168:171], a[200:203], v[32:35]\n"
"s_waitcnt lgkmcnt(2)\n"
"buffer_load_dwordx4 v236, s[44:47], s73 offen offset:0 nt lds\n"
"v_mfma_f32_16x16x32_bf16 v[32:35], v[176:179], v[128:131], v[32:35]\n"
"s_waitcnt lgkmcnt(1)\n"
"ds_read_b128 v[188:191], v246 offset:18432\n"
"v_cvt_pk_bf16_f32 v105, v194, v195\n"
"v_mfma_f32_16x16x32_bf16 v[32:35], v[180:183], v[132:135], v[32:35]\n"
"v_exp_f32 v192, v24\n"
"v_mfma_f32_16x16x32_bf16 v[40:43], v[180:183], v[140:143], v[40:43]\n"
"v_add_f32 v202, v202, v196\n"
"v_add_f32 v202, v202, v197\n"
"v_mfma_f32_16x16x32_bf16 v[40:43], v[176:179], v[136:139], v[40:43]\n"
"v_exp_f32 v193, v25\n"
"v_mfma_f32_16x16x32_bf16 v[48:51], v[176:179], v[144:147], v[48:51]\n"
"ds_read_b128 v[160:163], v245 offset:18560\n"
"v_cvt_pk_bf16_f32 v106, v196, v197\n"
"v_mfma_f32_16x16x32_bf16 v[48:51], v[180:183], v[148:151], v[48:51]\n"
"buffer_load_dwordx4 v237, s[44:47], s73 offen offset:1024 nt lds\n"
"v_mfma_f32_16x16x32_bf16 v[56:59], v[180:183], v[156:159], v[56:59]\n"
"v_exp_f32 v194, v26\n"
"v_mfma_f32_16x16x32_bf16 v[56:59], v[176:179], v[152:155], v[56:59]\n"
"s_waitcnt lgkmcnt(2)\n"
"v_exp_f32 v195, v27\n"
"v_mfma_f32_16x16x32_bf16 v[60:63], v[184:187], a[240:243], v[76:79]\n"
"s_waitcnt lgkmcnt(1)\n"
"ds_read_b128 v[164:167], v246 offset:18560\n"
"v_add_f32 v202, v202, v198\n"
"v_mfma_f32_16x16x32_bf16 v[60:63], v[188:191], a[244:247], v[60:63]\n"
"v_add_f32 v202, v202, v199\n"
"v_cvt_pk_bf16_f32 v107, v198, v199\n"
"v_mfma_f32_16x16x32_bf16 v[52:55], v[188:191], a[228:231], v[72:75]\n"
"v_exp_f32 v196, v28\n"
"v_mfma_f32_16x16x32_bf16 v[52:55], v[184:187], a[224:227], v[52:55]\n"
"buffer_load_dwordx4 v238, s[44:47], s73 offen offset:2048 nt lds\n"
"v_mfma_f32_16x16x32_bf16 v[44:47], v[184:187], a[208:211], v[68:71]\n"
"s_add_u32 s73, s73, 3072\n"
"ds_read_b128 v[168:171], v245 offset:18688\n"
"v_add_f32 v203, v207, v192\n"
"v_mfma_f32_16x16x32_bf16 v[44:47], v[188:191], a[212:215], v[44:47]\n"
"s_sub_u32 s10, s10, 1\n"
"v_exp_f32 v197, v29\n"
"v_mfma_f32_16x16x32_bf16 v[36:39], v[188:191], a[196:199], v[64:67]\n"
"s_cselect_b32 s1, s16, 3072\n"
"v_exp_f32 v198, v30\n"
"v_mfma_f32_16x16x32_bf16 v[36:39], v[184:187], a[192:195], v[36:39]\n"
"s_waitcnt lgkmcnt(2)\n"
"v_add_f32 v203, v203, v193\n"
"v_cvt_pk_bf16_f32 v108, v192, v193\n"
"v_mfma_f32_16x16x32_bf16 v[36:39], v[160:163], a[200:203], v[36:39]\n"
"s_waitcnt lgkmcnt(1)\n"
"ds_read_b128 v[172:175], v246 offset:18688\n"
"ds_read_b64_tr_b16 v[176:177], v247 offset:36864\n"
"v_mfma_f32_16x16x32_bf16 v[36:39], v[164:167], a[204:207], v[36:39]\n"
"s_cselect_b32 s10, 4, s10\n"
"v_exp_f32 v199, v31\n"
"v_mfma_f32_16x16x32_bf16 v[44:47], v[164:167], a[220:223], v[44:47]\n"
"s_cselect_b32 s16, s17, s16\n"
"ds_read_b64_tr_b16 v[178:179], v247 offset:43008\n"
"v_add_f32 v203, v203, v194\n"
"v_mfma_f32_16x16x32_bf16 v[44:47], v[160:163], a[216:219], v[44:47]\n"
"s_cselect_b32 s17, s9, s17\n"
"v_add_f32 v203, v203, v195\n"
"v_cvt_pk_bf16_f32 v109, v194, v195\n"
"v_mfma_f32_16x16x32_bf16 v[52:55], v[160:163], a[232:235], v[52:55]\n"
"s_cselect_b32 s9, s1, s9\n"
"ds_read_b64_tr_b16 v[180:181], v247 offset:0\n"
"v_add_f32_e64 v203, v203, v196\n"
"v_mfma_f32_16x16x32_bf16 v[52:55], v[164:167], a[236:239], v[52:55]\n"
"v_add_f32 v203, v203, v197\n"
"v_cvt_pk_bf16_f32 v110, v196, v197\n"
"v_mfma_f32_16x16x32_bf16 v[60:63], v[164:167], a[252:255], v[60:63]\n"
"ds_read_b64_tr_b16 v[182:183], v247 offset:6144\n"
"v_add_f32 v203, v203, v198\n"
"v_mfma_f32_16x16x32_bf16 v[60:63], v[160:163], a[248:251], v[60:63]\n"
"s_waitcnt lgkmcnt(5)\n"
"v_add_f32 v203, v203, v199\n"
"v_cvt_pk_bf16_f32 v111, v198, v199\n"
"v_mfma_f32_16x16x32_bf16 v[60:63], v[168:171], v[152:155], v[60:63]\n"
"s_waitcnt lgkmcnt(4)\n"
"ds_read_b64_tr_b16 v[184:185], v248 offset:36864\n"
"v_add_f32 v212, v200, v201\n"
"v_mfma_f32_16x16x32_bf16 v[60:63], v[172:175], v[156:159], v[60:63]\n"
"v_add_f32 v213, v202, v203\n"
"v_mfma_f32_16x16x32_bf16 v[52:55], v[172:175], v[148:151], v[52:55]\n"
"s_add_u32 m0, s24, s11\n"
"ds_read_b64_tr_b16 v[186:187], v248 offset:43008\n"
"v_add_f32 v212, v212, v213\n"
"v_mfma_f32_16x16x32_bf16 v[52:55], v[168:171], v[144:147], v[52:55]\n"
"buffer_load_dwordx4 v216, s[20:23], s18 offen offset:2048 lds\n"
"v_mfma_f32_16x16x32_bf16 v[44:47], v[168:171], v[136:139], v[44:47]\n"
"ds_read_b64_tr_b16 v[188:189], v248 offset:0\n"
"v_cmp_nge_f32 vcc, s90, v212\n"
"v_mfma_f32_16x16x32_bf16 v[44:47], v[172:175], v[140:143], v[44:47]\n"
"v_mfma_f32_16x16x32_bf16 v[36:39], v[172:175], v[132:135], v[36:39]\n"
"ds_read_b64_tr_b16 v[190:191], v248 offset:6144\n"
"v_mfma_f32_16x16x32_bf16 v[36:39], v[168:171], v[128:131], v[36:39]\n"
"s_cbranch_vccnz .Lat_tr3\n"
".Lat_bk3:\n"
"s_waitcnt lgkmcnt(6)\n"
"v_mfma_f32_16x16x32_bf16 a[0:3], v[80:83], v[176:179], a[0:3]\n"
"s_waitcnt lgkmcnt(4)\n"
"ds_read_b64_tr_b16 v[160:161], v239 offset:36864\n"
"v_mfma_f32_16x16x32_bf16 a[0:3], v[96:99], v[180:183], a[0:3]\n"
"s_memrealtime s[98:99]\n"
"v_mfma_f32_16x16x32_bf16 a[48:51], v[100:103], v[180:183], a[48:51]\n"
"ds_read_b64_tr_b16 v[162:163], v239 offset:43008\n"
"v_mfma_f32_16x16x32_bf16 a[48:51], v[84:87], v[176:179], a[48:51]\n"
"s_sub_u32 s70, s70, 8\n"
"v_exp_f32 v192, v32\n"
"v_mfma_f32_16x16x32_bf16 a[96:99], v[88:91], v[176:179], a[96:99]\n"
"ds_read_b64_tr_b16 v[164:165], v239 offset:0\n"
"v_add_f32 v204, v200, v192\n"
"v_mfma_f32_16x16x32_bf16 a[96:99], v[104:107], v[180:183], a[96:99]\n"
"s_max_i32 s70, s70, s93\n"
"v_exp_f32 v193, v33\n"
"v_mfma_f32_16x16x32_bf16 a[144:147], v[108:111], v[180:183], a[144:147]\n"
"ds_read_b64_tr_b16 v[166:167], v239 offset:6144\n"
"v_add_f32 v204, v204, v193\n"
"v_mfma_f32_16x16x32_bf16 a[144:147], v[92:95], v[176:179], a[144:147]\n"
"s_waitcnt lgkmcnt(6)\n"
"v_exp_f32 v194, v34\n"
"v_mfma_f32_16x16x32_bf16 a[148:151], v[92:95], v[184:187], a[148:151]\n"
"s_waitcnt lgkmcnt(4)\n"
"ds_read_b64_tr_b16 v[168:169], v240 offset:36864\n"
"v_cvt_pk_bf16_f32 v112, v192, v193\n"
"v_mfma_f32_16x16x32_bf16 a[148:151], v[108:111], v[188:191], a[148:151]\n"
"v_exp_f32 v195, v35\n"
"v_mfma_f32_16x16x32_bf16 a[100:103], v[104:107], v[188:191], a[100:103]\n"
"ds_read_b64_tr_b16 v[170:171], v240 offset:43008\n"
"v_add_f32 v204, v204, v194\n"
"v_mfma_f32_16x16x32_bf16 a[100:103], v[88:91], v[184:187], a[100:103]\n"
"v_exp_f32 v196, v36\n"
"v_mfma_f32_16x16x32_bf16 a[52:55], v[84:87], v[184:187], a[52:55]\n"
"ds_read_b64_tr_b16 v[172:173], v240 offset:0\n"
"v_add_f32 v204, v204, v195\n"
"v_mfma_f32_16x16x32_bf16 a[52:55], v[100:103], v[188:191], a[52:55]\n"
"v_exp_f32 v197, v37\n"
"v_mfma_f32_16x16x32_bf16 a[4:7], v[96:99], v[188:191], a[4:7]\n"
"ds_read_b64_tr_b16 v[174:175], v240 offset:6144\n"
"v_cvt_pk_bf16_f32 v113, v194, v195\n"
"v_mfma_f32_16x16x32_bf16 a[4:7], v[80:83], v[184:187], a[4:7]\n"
"s_waitcnt lgkmcnt(6)\n"
"v_add_f32 v204, v204, v196\n"
"v_add_f32 v204, v204, v197\n"
"v_mfma_f32_16x16x32_bf16 a[8:11], v[80:83], v[160:163], a[8:11]\n"
"s_waitcnt lgkmcnt(4)\n"
"ds_read_b64_tr_b16 v[176:177], v247 offset:36992\n"
"v_cvt_pk_bf16_f32 v114, v196, v197\n"
"v_mfma_f32_16x16x32_bf16 a[8:11], v[96:99], v[164:167], a[8:11]\n"
"v_exp_f32 v198, v38\n"
"v_mfma_f32_16x16x32_bf16 a[56:59], v[100:103], v[164:167], a[56:59]\n"
"ds_read_b64_tr_b16 v[178:179], v247 offset:43136\n"
"v_add_f32 v204, v204, v198\n"
"v_mfma_f32_16x16x32_bf16 a[56:59], v[84:87], v[160:163], a[56:59]\n"
"v_exp_f32 v199, v39\n"
"v_mfma_f32_16x16x32_bf16 a[104:107], v[88:91], v[160:163], a[104:107]\n"
"ds_read_b64_tr_b16 v[180:181], v247 offset:128\n"
"v_add_f32 v204, v204, v199\n"
"v_mfma_f32_16x16x32_bf16 a[104:107], v[104:107], v[164:167], a[104:107]\n"
"v_cvt_pk_bf16_f32 v115, v198, v199\n"
"v_mfma_f32_16x16x32_bf16 a[152:155], v[108:111], v[164:167], a[152:155]\n"
"ds_read_b64_tr_b16 v[182:183], v247 offset:6272\n"
"v_mfma_f32_16x16x32_bf16 a[152:155], v[92:95], v[160:163], a[152:155]\n"
"s_waitcnt lgkmcnt(6)\n"
"v_exp_f32 v192, v40\n"
"v_mfma_f32_16x16x32_bf16 a[156:159], v[92:95], v[168:171], a[156:159]\n"
"s_waitcnt lgkmcnt(4)\n"
"ds_read_b64_tr_b16 v[184:185], v248 offset:36992\n"
"v_add_f32 v205, v201, v192\n"
"v_mfma_f32_16x16x32_bf16 a[156:159], v[108:111], v[172:175], a[156:159]\n"
"v_exp_f32 v193, v41\n"
"v_mfma_f32_16x16x32_bf16 a[108:111], v[104:107], v[172:175], a[108:111]\n"
"ds_read_b64_tr_b16 v[186:187], v248 offset:43136\n"
"v_add_f32 v205, v205, v193\n"
"v_mfma_f32_16x16x32_bf16 a[108:111], v[88:91], v[168:171], a[108:111]\n"
"v_cvt_pk_bf16_f32 v116, v192, v193\n"
"v_mfma_f32_16x16x32_bf16 a[60:63], v[84:87], v[168:171], a[60:63]\n"
"ds_read_b64_tr_b16 v[188:189], v248 offset:128\n"
"v_mfma_f32_16x16x32_bf16 a[60:63], v[100:103], v[172:175], a[60:63]\n"
"v_exp_f32 v194, v42\n"
"v_mfma_f32_16x16x32_bf16 a[12:15], v[96:99], v[172:175], a[12:15]\n"
"ds_read_b64_tr_b16 v[190:191], v248 offset:6272\n"
"v_add_f32 v205, v205, v194\n"
"v_mfma_f32_16x16x32_bf16 a[12:15], v[80:83], v[168:171], a[12:15]\n"
"s_waitcnt lgkmcnt(6)\n"
"v_exp_f32 v195, v43\n"
"v_mfma_f32_16x16x32_bf16 a[16:19], v[80:83], v[176:179], a[16:19]\n"
"s_waitcnt lgkmcnt(4)\n"
"v_add_f32 v205, v205, v195\n"
"v_cvt_pk_bf16_f32 v117, v194, v195\n"
"v_mfma_f32_16x16x32_bf16 a[16:19], v[96:99], v[180:183], a[16:19]\n"
"v_exp_f32 v196, v44\n"
"v_mfma_f32_16x16x32_bf16 a[64:67], v[100:103], v[180:183], a[64:67]\n"
"v_exp_f32 v197, v45\n"
"v_mfma_f32_16x16x32_bf16 a[64:67], v[84:87], v[176:179], a[64:67]\n"
"s_waitcnt vmcnt(12) lgkmcnt(0)\n"
"s_barrier\n"
"v_mfma_f32_16x16x32_bf16 a[112:115], v[88:91], v[176:179], a[112:115]\n"
"s_sub_u32 s97, s91, s98\n"
"ds_read_b128 v[160:163], v245 offset:24576\n"
"v_add_f32 v205, v205, v196\n"
"v_mfma_f32_16x16x32_bf16 a[112:115], v[104:107], v[180:183], a[112:115]\n"
"s_max_i32 s91, s97, -20\n"
"v_add_f32 v205, v205, v197\n"
"v_cvt_pk_bf16_f32 v118, v196, v197\n"
"v_mfma_f32_16x16x32_bf16 a[160:163], v[108:111], v[180:183], a[160:163]\n"
"v_exp_f32 v198, v46\n"
"v_mfma_f32_16x16x32_bf16 a[160:163], v[92:95], v[176:179], a[160:163]\n"
"v_exp_f32 v199, v47\n"
"v_mfma_f32_16x16x32_bf16 a[164:167], v[92:95], v[184:187], a[164:167]\n"
"ds_read_b128 v[164:167], v246 offset:24576\n"
"v_add_f32 v205, v205, v198\n"
"v_mfma_f32_16x16x32_bf16 a[164:167], v[108:111], v[188:191], a[164:167]\n"
"s_cmp_gt_i32 s97, 0\n"
"v_add_f32 v205, v205, v199\n"
"v_cvt_pk_bf16_f32 v119, v198, v199\n"
"v_mfma_f32_16x16x32_bf16 a[116:119], v[104:107], v[188:191], a[116:119]\n"
"s_cbranch_scc1 .Lat_pace5\n"
".Lat_pbk5:\n"
"v_exp_f32 v192, v48\n"
"v_mfma_f32_16x16x32_bf16 a[116:119], v[88:91], v[184:187], a[116:119]\n"
"s_add_u32 s91, s91, s98\n"
"v_exp_f32 v193, v49\n"
"v_mfma_f32_16x16x32_bf16 a[68:71], v[84:87], v[184:187], a[68:71]\n"
"s_add_u32 s91, s91, s70\n"
"ds_read_b128 v[168:171], v245 offset:24704\n"
"v_add_f32 v206, v202, v192\n"
"v_mfma_f32_16x16x32_bf16 a[68:71], v[100:103], v[188:191], a[68:71]\n"
"v_exp_f32 v194, v50\n"
"v_mfma_f32_16x16x32_bf16 a[20:23], v[96:99], v[188:191], a[20:23]\n"
"v_add_f32 v206, v206, v193\n"
"v_cvt_pk_bf16_f32 v120, v192, v193\n"
"v_mfma_f32_16x16x32_bf16 a[20:23], v[80:83], v[184:187], a[20:23]\n"
"s_waitcnt lgkmcnt(2)\n"
"v_mfma_f32_16x16x32_bf16 v[0:3], v[160:163], a[192:195], v[64:67]\n"
"s_waitcnt lgkmcnt(1)\n"
"ds_read_b128 v[172:175], v246 offset:24704\n"
"v_add_f32 v206, v206, v194\n"
"v_mfma_f32_16x16x32_bf16 v[0:3], v[164:167], a[196:199], v[0:3]\n"
"v_exp_f32 v195, v51\n"
"v_mfma_f32_16x16x32_bf16 v[8:11], v[164:167], a[212:215], v[68:71]\n"
"s_mov_b32 m0, s77\n"
"v_exp_f32 v196, v52\n"
"v_mfma_f32_16x16x32_bf16 v[8:11], v[160:163], a[208:211], v[8:11]\n"
"buffer_load_dwordx4 v236, s[40:43], s72 offen offset:0 nt lds\n"
"v_mfma_f32_16x16x32_bf16 v[16:19], v[160:163], a[224:227], v[72:75]\n"
"ds_read_b128 v[176:179], v245 offset:24832\n"
"v_add_f32 v206, v206, v195\n"
"v_mfma_f32_16x16x32_bf16 v[16:19], v[164:167], a[228:231], v[16:19]\n"
"v_cvt_pk_bf16_f32 v121, v194, v195\n"
"v_add_f32 v206, v206, v196\n"
"v_mfma_f32_16x16x32_bf16 v[24:27], v[164:167], a[244:247], v[76:79]\n"
"v_exp_f32 v197, v53\n"
"v_mfma_f32_16x16x32_bf16 v[24:27], v[160:163], a[240:243], v[24:27]\n"
"s_waitcnt lgkmcnt(2)\n"
"v_exp_f32 v198, v54\n"
"v_mfma_f32_16x16x32_bf16 v[24:27], v[168:171], a[248:251], v[24:27]\n"
"s_waitcnt lgkmcnt(1)\n"
"ds_read_b128 v[180:183], v246 offset:24832\n"
"v_add_f32 v206, v206, v197\n"
"v_mfma_f32_16x16x32_bf16 v[24:27], v[172:175], a[252:255], v[24:27]\n"
"buffer_load_dwordx4 v237, s[40:43], s72 offen offset:1024 nt lds\n"
"v_mfma_f32_16x16x32_bf16 v[16:19], v[172:175], a[236:239], v[16:19]\n"
"v_cvt_pk_bf16_f32 v122, v196, v197\n"
"v_add_f32 v206, v206, v198\n"
"v_mfma_f32_16x16x32_bf16 v[16:19], v[168:171], a[232:235], v[16:19]\n"
"v_exp_f32 v199, v55\n"
"v_mfma_f32_16x16x32_bf16 v[8:11], v[168:171], a[216:219], v[8:11]\n"
"ds_read_b128 v[184:187], v245 offset:30720\n"
"v_add_f32 v206, v206, v199\n"
"v_mfma_f32_16x16x32_bf16 v[8:11], v[172:175], a[220:223], v[8:11]\n"
"v_exp_f32 v192, v56\n"
"v_mfma_f32_16x16x32_bf16 v[0:3], v[172:175], a[204:207], v[0:3]\n"
"v_cvt_pk_bf16_f32 v123, v198, v199\n"
"v_add_f32 v207, v203, v192\n"
"v_mfma_f32_16x16x32_bf16 v[0:3], v[168:171], a[200:203], v[0:3]\n"
"s_waitcnt lgkmcnt(2)\n"
"buffer_load_dwordx4 v238, s[40:43], s72 offen offset:2048 nt lds\n"
"v_mfma_f32_16x16x32_bf16 v[0:3], v[176:179], v[128:131], v[0:3]\n"
"s_waitcnt lgkmcnt(1)\n"
"ds_read_b128 v[188:191], v246 offset:30720\n"
"v_mfma_f32_16x16x32_bf16 v[0:3], v[180:183], v[132:135], v[0:3]\n"
"s_add_u32 s72, s72, 3072\n"
"v_exp_f32 v193, v57\n"
"v_mfma_f32_16x16x32_bf16 v[8:11], v[180:183], v[140:143], v[8:11]\n"
"v_exp_f32 v194, v58\n"
"v_mfma_f32_16x16x32_bf16 v[8:11], v[176:179], v[136:139], v[8:11]\n"
"v_add_f32 v207, v207, v193\n"
"v_cvt_pk_bf16_f32 v124, v192, v193\n"
"v_mfma_f32_16x16x32_bf16 v[16:19], v[176:179], v[144:147], v[16:19]\n"
"s_add_u32 m0, s77, 12288\n"
"ds_read_b128 v[160:163], v245 offset:30848\n"
"v_add_f32 v207, v207, v194\n"
"v_mfma_f32_16x16x32_bf16 v[16:19], v[180:183], v[148:151], v[16:19]\n"
"buffer_load_dwordx4 v236, s[40:43], s72 offen offset:0 nt lds\n"
"v_mfma_f32_16x16x32_bf16 v[24:27], v[180:183], v[156:159], v[24:27]\n"
"v_exp_f32 v195, v59\n"
"v_mfma_f32_16x16x32_bf16 v[24:27], v[176:179], v[152:155], v[24:27]\n"
"s_waitcnt lgkmcnt(2)\n"
"v_exp_f32 v196, v60\n"
"v_mfma_f32_16x16x32_bf16 v[28:31], v[184:187], a[240:243], v[76:79]\n"
"s_waitcnt lgkmcnt(1)\n"
"ds_read_b128 v[164:167], v246 offset:30848\n"
"v_add_f32 v207, v207, v195\n"
"v_mfma_f32_16x16x32_bf16 v[28:31], v[188:191], a[244:247], v[28:31]\n"
"v_cvt_pk_bf16_f32 v125, v194, v195\n"
"v_add_f32 v207, v207, v196\n"
"v_mfma_f32_16x16x32_bf16 v[20:23], v[188:191], a[228:231], v[72:75]\n"
"v_exp_f32 v197, v61\n"
"v_mfma_f32_16x16x32_bf16 v[20:23], v[184:187], a[224:227], v[20:23]\n"
"buffer_load_dwordx4 v237, s[40:43], s72 offen offset:1024 nt lds\n"
"v_mfma_f32_16x16x32_bf16 v[12:15], v[184:187], a[208:211], v[68:71]\n"
"ds_read_b128 v[168:171], v245 offset:30976\n"
"v_add_f32 v207, v207, v197\n"
"v_mfma_f32_16x16x32_bf16 v[12:15], v[188:191], a[212:215], v[12:15]\n"
"v_exp_f32 v198, v62\n"
"v_mfma_f32_16x16x32_bf16 v[4:7], v[188:191], a[196:199], v[64:67]\n"
"v_cvt_pk_bf16_f32 v126, v196, v197\n"
"v_add_f32 v207, v207, v198\n"
"v_mfma_f32_16x16x32_bf16 v[4:7], v[184:187], a[192:195], v[4:7]\n"
"s_waitcnt lgkmcnt(2)\n"
"v_exp_f32 v199, v63\n"
"v_mfma_f32_16x16x32_bf16 v[4:7], v[160:163], a[200:203], v[4:7]\n"
"s_waitcnt lgkmcnt(1)\n"
"ds_read_b128 v[172:175], v246 offset:30976\n"
"ds_read_b64_tr_b16 v[176:177], v239 offset:36992\n"
"v_mfma_f32_16x16x32_bf16 v[4:7], v[164:167], a[204:207], v[4:7]\n"
"buffer_load_dwordx4 v238, s[40:43], s72 offen offset:2048 nt lds\n"
"v_mfma_f32_16x16x32_bf16 v[12:15], v[164:167], a[220:223], v[12:15]\n"
"s_add_u32 s72, s72, s1\n"
"ds_read_b64_tr_b16 v[178:179], v239 offset:43136\n"
"v_add_f32_e64 v207, v207, v199\n"
"v_mfma_f32_16x16x32_bf16 v[12:15], v[160:163], a[216:219], v[12:15]\n"
"v_cvt_pk_bf16_f32 v127, v198, v199\n"
"v_add_f32 v212, v204, v205\n"
"v_mfma_f32_16x16x32_bf16 v[20:23], v[160:163], a[232:235], v[20:23]\n"
"ds_read_b64_tr_b16 v[180:181], v239 offset:128\n"
"v_add_f32 v213, v206, v207\n"
"v_mfma_f32_16x16x32_bf16 v[20:23], v[164:167], a[236:239], v[20:23]\n"
"v_add_f32 v212, v212, v213\n"
"v_cmp_nge_f32 vcc, s90, v212\n"
"v_mfma_f32_16x16x32_bf16 v[28:31], v[164:167], a[252:255], v[28:31]\n"
"ds_read_b64_tr_b16 v[182:183], v239 offset:6272\n"
"v_mfma_f32_16x16x32_bf16 v[28:31], v[160:163], a[248:251], v[28:31]\n"
"s_waitcnt lgkmcnt(5)\n"
"v_mfma_f32_16x16x32_bf16 v[28:31], v[168:171], v[152:155], v[28:31]\n"
"s_waitcnt lgkmcnt(4)\n"
"ds_read_b64_tr_b16 v[184:185], v240 offset:36992\n"
"v_mfma_f32_16x16x32_bf16 v[28:31], v[172:175], v[156:159], v[28:31]\n"
"v_mfma_f32_16x16x32_bf16 v[20:23], v[172:175], v[148:151], v[20:23]\n"
"s_add_u32 m0, s24, s11\n"
"ds_read_b64_tr_b16 v[186:187], v240 offset:43136\n"
"v_mfma_f32_16x16x32_bf16 v[20:23], v[168:171], v[144:147], v[20:23]\n"
"buffer_load_dwordx4 v218, s[20:23], s18 offen offset:3072 lds\n"
"v_mfma_f32_16x16x32_bf16 v[12:15], v[168:171], v[136:139], v[12:15]\n"
"ds_read_b64_tr_b16 v[188:189], v240 offset:128\n"
"v_mfma_f32_16x16x32_bf16 v[12:15], v[172:175], v[140:143], v[12:15]\n"
"v_mfma_f32_16x16x32_bf16 v[4:7], v[172:175], v[132:135], v[4:7]\n"
"ds_read_b64_tr_b16 v[190:191], v240 offset:6272\n"
"v_mfma_f32_16x16x32_bf16 v[4:7], v[168:171], v[128:131], v[4:7]\n"
"s_cbranch_vccnz .Lat_tr4\n"
".Lat_bk4:\n"
"s_waitcnt lgkmcnt(6)\n"
"v_mfma_f32_16x16x32_bf16 a[24:27], v[80:83], v[176:179], a[24:27]\n"
"s_waitcnt lgkmcnt(4)\n"
"ds_read_b64_tr_b16 v[160:161], v247 offset:37120\n"
"v_mfma_f32_16x16x32_bf16 a[24:27], v[96:99], v[180:183], a[24:27]\n"
"v_mfma_f32_16x16x32_bf16 a[72:75], v[100:103], v[180:183], a[72:75]\n"
"ds_read_b64_tr_b16 v[162:163], v247 offset:43264\n"
"v_mfma_f32_16x16x32_bf16 a[72:75], v[84:87], v[176:179], a[72:75]\n"
"v_mfma_f32_16x16x32_bf16 a[120:123], v[88:91], v[176:179], a[120:123]\n"
"ds_read_b64_tr_b16 v[164:165], v247 offset:256\n"
"v_mfma_f32_16x16x32_bf16 a[120:123], v[104:107], v[180:183], a[120:123]\n"
"v_mfma_f32_16x16x32_bf16 a[168:171], v[108:111], v[180:183], a[168:171]\n"
"ds_read_b64_tr_b16 v[166:167], v247 offset:6400\n"
"v_mfma_f32_16x16x32_bf16 a[168:171], v[92:95], v[176:179], a[168:171]\n"
"s_waitcnt lgkmcnt(6)\n"
"v_mfma_f32_16x16x32_bf16 a[172:175], v[92:95], v[184:187], a[172:175]\n"
"s_waitcnt lgkmcnt(4)\n"
"ds_read_b64_tr_b16 v[168:169], v248 offset:37120\n"
"v_mfma_f32_16x16x32_bf16 a[172:175], v[108:111], v[188:191], a[172:175]\n"
"v_mfma_f32_16x16x32_bf16 a[124:127], v[104:107], v[188:191], a[124:127]\n"
"ds_read_b64_tr_b16 v[170:171], v248 offset:43264\n"
"v_mfma_f32_16x16x32_bf16 a[124:127], v[88:91], v[184:187], a[124:127]\n"
"v_exp_f32 v192, v0\n"
"v_mfma_f32_16x16x32_bf16 a[76:79], v[84:87], v[184:187], a[76:79]\n"
"ds_read_b64_tr_b16 v[172:173], v248 offset:256\n"
"v_add_f32 v200, v204, v192\n"
"v_mfma_f32_16x16x32_bf16 a[76:79], v[100:103], v[188:191], a[76:79]\n"
"v_exp_f32 v193, v1\n"
"v_mfma_f32_16x16x32_bf16 a[28:31], v[96:99], v[188:191], a[28:31]\n"
"ds_read_b64_tr_b16 v[174:175], v248 offset:6400\n"
"v_add_f32 v200, v200, v193\n"
"v_mfma_f32_16x16x32_bf16 a[28:31], v[80:83], v[184:187], a[28:31]\n"
"s_waitcnt lgkmcnt(6)\n"
"v_exp_f32 v194, v2\n"
"v_mfma_f32_16x16x32_bf16 a[32:35], v[80:83], v[160:163], a[32:35]\n"
"s_waitcnt lgkmcnt(4)\n"
"ds_read_b64_tr_b16 v[176:177], v239 offset:37120\n"
"v_cvt_pk_bf16_f32 v32, v192, v193\n"
"v_mfma_f32_16x16x32_bf16 a[32:35], v[96:99], v[164:167], a[32:35]\n"
"v_exp_f32 v195, v3\n"
"v_mfma_f32_16x16x32_bf16 a[80:83], v[100:103], v[164:167], a[80:83]\n"
"ds_read_b64_tr_b16 v[178:179], v239 offset:43264\n"
"v_add_f32 v200, v200, v194\n"
"v_mfma_f32_16x16x32_bf16 a[80:83], v[84:87], v[160:163], a[80:83]\n"
"v_exp_f32 v196, v4\n"
"v_mfma_f32_16x16x32_bf16 a[128:131], v[88:91], v[160:163], a[128:131]\n"
"ds_read_b64_tr_b16 v[180:181], v239 offset:256\n"
"v_add_f32 v200, v200, v195\n"
"v_mfma_f32_16x16x32_bf16 a[128:131], v[104:107], v[164:167], a[128:131]\n"
"v_exp_f32 v197, v5\n"
"v_mfma_f32_16x16x32_bf16 a[176:179], v[108:111], v[164:167], a[176:179]\n"
"ds_read_b64_tr_b16 v[182:183], v239 offset:6400\n"
"v_cvt_pk_bf16_f32 v33, v194, v195\n"
"v_mfma_f32_16x16x32_bf16 a[176:179], v[92:95], v[160:163], a[176:179]\n"
"s_waitcnt lgkmcnt(6)\n"
"v_exp_f32 v198, v6\n"
"v_mfma_f32_16x16x32_bf16 a[180:183], v[92:95], v[168:171], a[180:183]\n"
"s_waitcnt lgkmcnt(4)\n"
"ds_read_b64_tr_b16 v[184:185], v240 offset:37120\n"
"v_add_f32 v200, v200, v196\n"
"v_mfma_f32_16x16x32_bf16 a[180:183], v[108:111], v[172:175], a[180:183]\n"
"v_add_f32 v200, v200, v197\n"
"v_cvt_pk_bf16_f32 v34, v196, v197\n"
"v_mfma_f32_16x16x32_bf16 a[132:135], v[104:107], v[172:175], a[132:135]\n"
"ds_read_b64_tr_b16 v[186:187], v240 offset:43264\n"
"v_add_f32 v200, v200, v198\n"
"v_mfma_f32_16x16x32_bf16 a[132:135], v[88:91], v[168:171], a[132:135]\n"
"v_exp_f32 v199, v7\n"
"v_mfma_f32_16x16x32_bf16 a[84:87], v[84:87], v[168:171], a[84:87]\n"
"ds_read_b64_tr_b16 v[188:189], v240 offset:256\n"
"v_add_f32 v200, v200, v199\n"
"v_mfma_f32_16x16x32_bf16 a[84:87], v[100:103], v[172:175], a[84:87]\n"
"v_exp_f32 v192, v8\n"
"v_mfma_f32_16x16x32_bf16 a[36:39], v[96:99], v[172:175], a[36:39]\n"
"ds_read_b64_tr_b16 v[190:191], v240 offset:6400\n"
"v_cvt_pk_bf16_f32 v35, v198, v199\n"
"v_mfma_f32_16x16x32_bf16 a[36:39], v[80:83], v[168:171], a[36:39]\n"
"s_waitcnt lgkmcnt(6)\n"
"v_exp_f32 v193, v9\n"
"v_mfma_f32_16x16x32_bf16 a[40:43], v[80:83], v[176:179], a[40:43]\n"
"s_waitcnt lgkmcnt(4)\n"
"v_add_f32 v201, v205, v192\n"
"v_add_f32 v201, v201, v193\n"
"v_mfma_f32_16x16x32_bf16 a[40:43], v[96:99], v[180:183], a[40:43]\n"
"v_exp_f32 v194, v10\n"
"v_mfma_f32_16x16x32_bf16 a[88:91], v[100:103], v[180:183], a[88:91]\n"
"v_exp_f32 v195, v11\n"
"v_mfma_f32_16x16x32_bf16 a[88:91], v[84:87], v[176:179], a[88:91]\n"
"s_waitcnt vmcnt(8) lgkmcnt(0)\n"
"s_barrier\n"
"v_mfma_f32_16x16x32_bf16 a[136:139], v[88:91], v[176:179], a[136:139]\n"
"ds_read_b128 v[160:163], v245 offset:36864\n"
"v_cvt_pk_bf16_f32 v40, v192, v193\n"
"v_mfma_f32_16x16x32_bf16 a[136:139], v[104:107], v[180:183], a[136:139]\n"
"v_exp_f32 v196, v12\n"
"v_mfma_f32_16x16x32_bf16 a[184:187], v[108:111], v[180:183], a[184:187]\n"
"v_add_f32 v201, v201, v194\n"
"v_add_f32 v201, v201, v195\n"
"v_mfma_f32_16x16x32_bf16 a[184:187], v[92:95], v[176:179], a[184:187]\n"
"v_exp_f32 v197, v13\n"
"v_mfma_f32_16x16x32_bf16 a[188:191], v[92:95], v[184:187], a[188:191]\n"
"s_add_u32 m0, s78, 36864\n"
"ds_read_b128 v[164:167], v246 offset:36864\n"
"v_cvt_pk_bf16_f32 v41, v194, v195\n"
"v_mfma_f32_16x16x32_bf16 a[188:191], v[108:111], v[188:191], a[188:191]\n"
"buffer_load_dwordx4 v236, s[44:47], s73 offen offset:0 nt lds\n"
"v_mfma_f32_16x16x32_bf16 a[140:143], v[104:107], v[188:191], a[140:143]\n"
"v_exp_f32 v198, v14\n"
"v_mfma_f32_16x16x32_bf16 a[140:143], v[88:91], v[184:187], a[140:143]\n"
"v_exp_f32 v199, v15\n"
"v_mfma_f32_16x16x32_bf16 a[92:95], v[84:87], v[184:187], a[92:95]\n"
"ds_read_b128 v[168:171], v245 offset:36992\n"
"v_add_f32 v201, v201, v196\n"
"v_mfma_f32_16x16x32_bf16 a[92:95], v[100:103], v[188:191], a[92:95]\n"
"v_add_f32 v201, v201, v197\n"
"v_cvt_pk_bf16_f32 v42, v196, v197\n"
"v_mfma_f32_16x16x32_bf16 a[44:47], v[96:99], v[188:191], a[44:47]\n"
"v_exp_f32 v192, v16\n"
"v_mfma_f32_16x16x32_bf16 a[44:47], v[80:83], v[184:187], a[44:47]\n"
".Lat_j3:\n"
"s_waitcnt lgkmcnt(2)\n"
"v_mfma_f32_16x16x32_bf16 v[96:99], v[160:163], a[192:195], v[64:67]\n"
"s_waitcnt lgkmcnt(1)\n"
"ds_read_b128 v[172:175], v246 offset:36992\n"
"v_add_f32 v201, v201, v198\n"
"v_mfma_f32_16x16x32_bf16 v[96:99], v[164:167], a[196:199], v[96:99]\n"
"v_exp_f32 v193, v17\n"
"v_mfma_f32_16x16x32_bf16 v[100:103], v[164:167], a[212:215], v[68:71]\n"
"v_exp_f32 v194, v18\n"
"v_mfma_f32_16x16x32_bf16 v[100:103], v[160:163], a[208:211], v[100:103]\n"
"buffer_load_dwordx4 v237, s[44:47], s73 offen offset:1024 nt lds\n"
"v_mfma_f32_16x16x32_bf16 v[104:107], v[160:163], a[224:227], v[72:75]\n"
"ds_read_b128 v[176:179], v245 offset:37120\n"
"v_add_f32 v201, v201, v199\n"
"v_mfma_f32_16x16x32_bf16 v[104:107], v[164:167], a[228:231], v[104:107]\n"
"v_cvt_pk_bf16_f32 v43, v198, v199\n"
"v_add_f32 v202, v206, v192\n"
"v_mfma_f32_16x16x32_bf16 v[108:111], v[164:167], a[244:247], v[76:79]\n"
"v_exp_f32 v195, v19\n"
"v_mfma_f32_16x16x32_bf16 v[108:111], v[160:163], a[240:243], v[108:111]\n"
"s_waitcnt lgkmcnt(2)\n"
"v_exp_f32 v196, v20\n"
"v_mfma_f32_16x16x32_bf16 v[108:111], v[168:171], a[248:251], v[108:111]\n"
"s_waitcnt lgkmcnt(1)\n"
"ds_read_b128 v[180:183], v246 offset:37120\n"
"v_add_f32 v202, v202, v193\n"
"v_mfma_f32_16x16x32_bf16 v[108:111], v[172:175], a[252:255], v[108:111]\n"
"buffer_load_dwordx4 v238, s[44:47], s73 offen offset:2048 nt lds\n"
"v_mfma_f32_16x16x32_bf16 v[104:107], v[172:175], a[236:239], v[104:107]\n"
"s_add_u32 s73, s73, s0\n"
"v_exp_f32 v197, v21\n"
"v_mfma_f32_16x16x32_bf16 v[104:107], v[168:171], a[232:235], v[104:107]\n"
"v_cvt_pk_bf16_f32 v48, v192, v193\n"
"v_add_f32_e64 v202, v202, v194\n"
"v_mfma_f32_16x16x32_bf16 v[100:103], v[168:171], a[216:219], v[100:103]\n"
"ds_read_b128 v[184:187], v245 offset:43008\n"
"v_add_f32 v202, v202, v195\n"
"v_mfma_f32_16x16x32_bf16 v[100:103], v[172:175], a[220:223], v[100:103]\n"
"v_exp_f32 v198, v22\n"
"v_mfma_f32_16x16x32_bf16 v[96:99], v[172:175], a[204:207], v[96:99]\n"
"s_mov_b32 m0, s78\n"
"v_exp_f32 v199, v23\n"
"v_mfma_f32_16x16x32_bf16 v[96:99], v[168:171], a[200:203], v[96:99]\n"
"s_waitcnt lgkmcnt(2)\n"
"buffer_load_dwordx4 v236, s[44:47], s73 offen offset:0 nt lds\n"
"v_mfma_f32_16x16x32_bf16 v[96:99], v[176:179], v[128:131], v[96:99]\n"
"s_waitcnt lgkmcnt(1)\n"
"ds_read_b128 v[188:191], v246 offset:43008\n"
"v_cvt_pk_bf16_f32 v49, v194, v195\n"
"v_mfma_f32_16x16x32_bf16 v[96:99], v[180:183], v[132:135], v[96:99]\n"
"v_exp_f32 v192, v24\n"
"v_mfma_f32_16x16x32_bf16 v[100:103], v[180:183], v[140:143], v[100:103]\n"
"v_add_f32 v202, v202, v196\n"
"v_add_f32 v202, v202, v197\n"
"v_mfma_f32_16x16x32_bf16 v[100:103], v[176:179], v[136:139], v[100:103]\n"
"v_exp_f32 v193, v25\n"
"v_mfma_f32_16x16x32_bf16 v[104:107], v[176:179], v[144:147], v[104:107]\n"
"ds_read_b128 v[160:163], v245 offset:43136\n"
"v_cvt_pk_bf16_f32 v50, v196, v197\n"
"v_mfma_f32_16x16x32_bf16 v[104:107], v[180:183], v[148:151], v[104:107]\n"
"buffer_load_dwordx4 v237, s[44:47], s73 offen offset:1024 nt lds\n"
"v_mfma_f32_16x16x32_bf16 v[108:111], v[180:183], v[156:159], v[108:111]\n"
"v_exp_f32 v194, v26\n"
"v_mfma_f32_16x16x32_bf16 v[108:111], v[176:179], v[152:155], v[108:111]\n"
"s_waitcnt lgkmcnt(2)\n"
"v_exp_f32 v195, v27\n"
"v_mfma_f32_16x16x32_bf16 v[60:63], v[184:187], a[240:243], v[76:79]\n"
"s_waitcnt lgkmcnt(1)\n"
"ds_read_b128 v[164:167], v246 offset:43136\n"
"v_add_f32 v202, v202, v198\n"
"v_mfma_f32_16x16x32_bf16 v[60:63], v[188:191], a[244:247], v[60:63]\n"
"v_add_f32 v202, v202, v199\n"
"v_cvt_pk_bf16_f32 v51, v198, v199\n"
"v_mfma_f32_16x16x32_bf16 v[52:55], v[188:191], a[228:231], v[72:75]\n"
"v_exp_f32 v196, v28\n"
"v_mfma_f32_16x16x32_bf16 v[52:55], v[184:187], a[224:227], v[52:55]\n"
"buffer_load_dwordx4 v238, s[44:47], s73 offen offset:2048 nt lds\n"
"v_mfma_f32_16x16x32_bf16 v[44:47], v[184:187], a[208:211], v[68:71]\n"
"s_add_u32 s73, s73, 3072\n"
"ds_read_b128 v[168:171], v245 offset:43264\n"
"v_add_f32 v203, v207, v192\n"
"v_mfma_f32_16x16x32_bf16 v[44:47], v[188:191], a[212:215], v[44:47]\n"
"s_sub_u32 s10, s10, 1\n"
"v_exp_f32 v197, v29\n"
"v_mfma_f32_16x16x32_bf16 v[36:39], v[188:191], a[196:199], v[64:67]\n"
"s_cselect_b32 s0, s16, 3072\n"
"v_exp_f32 v198, v30\n"
"v_mfma_f32_16x16x32_bf16 v[36:39], v[184:187], a[192:195], v[36:39]\n"
"s_waitcnt lgkmcnt(2)\n"
"v_add_f32 v203, v203, v193\n"
"v_cvt_pk_bf16_f32 v56, v192, v193\n"
"v_mfma_f32_16x16x32_bf16 v[36:39], v[160:163], a[200:203], v[36:39]\n"
"s_waitcnt lgkmcnt(1)\n"
"ds_read_b128 v[172:175], v246 offset:43264\n"
"ds_read_b64_tr_b16 v[176:177], v247 offset:12288\n"
"v_mfma_f32_16x16x32_bf16 v[36:39], v[164:167], a[204:207], v[36:39]\n"
"s_cselect_b32 s10, 4, s10\n"
"v_exp_f32 v199, v31\n"
"v_mfma_f32_16x16x32_bf16 v[44:47], v[164:167], a[220:223], v[44:47]\n"
"s_cselect_b32 s16, s17, s16\n"
"ds_read_b64_tr_b16 v[178:179], v247 offset:18432\n"
"v_add_f32 v203, v203, v194\n"
"v_mfma_f32_16x16x32_bf16 v[44:47], v[160:163], a[216:219], v[44:47]\n"
"s_cselect_b32 s17, s9, s17\n"
"v_add_f32 v203, v203, v195\n"
"v_cvt_pk_bf16_f32 v57, v194, v195\n"
"v_mfma_f32_16x16x32_bf16 v[52:55], v[160:163], a[232:235], v[52:55]\n"
"s_cselect_b32 s9, s0, s9\n"
"ds_read_b64_tr_b16 v[180:181], v247 offset:24576\n"
"v_add_f32_e64 v203, v203, v196\n"
"v_mfma_f32_16x16x32_bf16 v[52:55], v[164:167], a[236:239], v[52:55]\n"
"v_add_f32 v203, v203, v197\n"
"v_cvt_pk_bf16_f32 v58, v196, v197\n"
"v_mfma_f32_16x16x32_bf16 v[60:63], v[164:167], a[252:255], v[60:63]\n"
"ds_read_b64_tr_b16 v[182:183], v247 offset:30720\n"
"v_add_f32 v203, v203, v198\n"
"v_mfma_f32_16x16x32_bf16 v[60:63], v[160:163], a[248:251], v[60:63]\n"
"s_waitcnt lgkmcnt(5)\n"
"v_add_f32 v203, v203, v199\n"
"v_cvt_pk_bf16_f32 v59, v198, v199\n"
"v_mfma_f32_16x16x32_bf16 v[60:63], v[168:171], v[152:155], v[60:63]\n"
"s_waitcnt lgkmcnt(4)\n"
"ds_read_b64_tr_b16 v[184:185], v248 offset:12288\n"
"v_add_f32 v212, v200, v201\n"
"v_mfma_f32_16x16x32_bf16 v[60:63], v[172:175], v[156:159], v[60:63]\n"
"global_store_dword v252, v223, s[14:15] sc1\n"
"v_add_f32 v213, v202, v203\n"
"v_mfma_f32_16x16x32_bf16 v[52:55], v[172:175], v[148:151], v[52:55]\n"
"s_add_u32 m0, s33, 4096\n"
"ds_read_b64_tr_b16 v[186:187], v248 offset:18432\n"
"v_add_f32 v212, v212, v213\n"
"v_mfma_f32_16x16x32_bf16 v[52:55], v[168:171], v[144:147], v[52:55]\n"
"buffer_load_dwordx4 v220, s[20:23], s18 offen offset:0 lds\n"
"v_mfma_f32_16x16x32_bf16 v[44:47], v[168:171], v[136:139], v[44:47]\n"
"ds_read_b64_tr_b16 v[188:189], v248 offset:24576\n"
"v_cmp_nge_f32 vcc, s90, v212\n"
"v_mfma_f32_16x16x32_bf16 v[44:47], v[172:175], v[140:143], v[44:47]\n"
"v_mfma_f32_16x16x32_bf16 v[36:39], v[172:175], v[132:135], v[36:39]\n"
"ds_read_b64_tr_b16 v[190:191], v248 offset:30720\n"
"v_mfma_f32_16x16x32_bf16 v[36:39], v[168:171], v[128:131], v[36:39]\n"
"s_cbranch_vccnz .Lat_tr5\n"
".Lat_bk5:\n"
"s_waitcnt lgkmcnt(6)\n"
"v_mfma_f32_16x16x32_bf16 a[0:3], v[112:115], v[176:179], a[0:3]\n"
"s_waitcnt lgkmcnt(4)\n"
"ds_read_b64_tr_b16 v[160:161], v239 offset:12288\n"
"v_mfma_f32_16x16x32_bf16 a[0:3], v[32:35], v[180:183], a[0:3]\n"
"s_memrealtime s[98:99]\n"
"v_mfma_f32_16x16x32_bf16 a[48:51], v[40:43], v[180:183], a[48:51]\n"
"ds_read_b64_tr_b16 v[162:163], v239 offset:18432\n"
"v_mfma_f32_16x16x32_bf16 a[48:51], v[116:119], v[176:179], a[48:51]\n"
"s_sub_u32 s70, s70, 8\n"
"v_exp_f32 v192, v96\n"
"v_mfma_f32_16x16x32_bf16 a[96:99], v[120:123], v[176:179], a[96:99]\n"
"ds_read_b64_tr_b16 v[164:165], v239 offset:24576\n"
"v_add_f32 v204, v200, v192\n"
"v_mfma_f32_16x16x32_bf16 a[96:99], v[48:51], v[180:183], a[96:99]\n"
"s_max_i32 s70, s70, s93\n"
"v_exp_f32 v193, v97\n"
"v_mfma_f32_16x16x32_bf16 a[144:147], v[56:59], v[180:183], a[144:147]\n"
"ds_read_b64_tr_b16 v[166:167], v239 offset:30720\n"
"v_add_f32 v204, v204, v193\n"
"v_mfma_f32_16x16x32_bf16 a[144:147], v[124:127], v[176:179], a[144:147]\n"
"s_waitcnt lgkmcnt(6)\n"
"v_exp_f32 v194, v98\n"
"v_mfma_f32_16x16x32_bf16 a[148:151], v[124:127], v[184:187], a[148:151]\n"
"s_waitcnt lgkmcnt(4)\n"
"ds_read_b64_tr_b16 v[168:169], v240 offset:12288\n"
"v_cvt_pk_bf16_f32 v80, v192, v193\n"
"v_mfma_f32_16x16x32_bf16 a[148:151], v[56:59], v[188:191], a[148:151]\n"
"s_sub_u32 s80, s80, 1\n"
"v_exp_f32 v195, v99\n"
"v_mfma_f32_16x16x32_bf16 a[100:103], v[48:51], v[188:191], a[100:103]\n"
"ds_read_b64_tr_b16 v[170:171], v240 offset:18432\n"
"v_add_f32 v204, v204, v194\n"
"v_mfma_f32_16x16x32_bf16 a[100:103], v[120:123], v[184:187], a[100:103]\n"
"s_add_u32 s18, s18, s19\n"
"v_exp_f32 v196, v36\n"
"v_mfma_f32_16x16x32_bf16 a[52:55], v[116:119], v[184:187], a[52:55]\n"
"ds_read_b64_tr_b16 v[172:173], v240 offset:24576\n"
"v_add_f32 v204, v204, v195\n"
"v_mfma_f32_16x16x32_bf16 a[52:55], v[40:43], v[188:191], a[52:55]\n"
"s_add_u32 s24, s24, s25\n"
"v_exp_f32 v197, v37\n"
"v_mfma_f32_16x16x32_bf16 a[4:7], v[32:35], v[188:191], a[4:7]\n"
"ds_read_b64_tr_b16 v[174:175], v240 offset:30720\n"
"v_cvt_pk_bf16_f32 v81, v194, v195\n"
"v_mfma_f32_16x16x32_bf16 a[4:7], v[112:115], v[184:187], a[4:7]\n"
"s_waitcnt lgkmcnt(6)\n"
"v_add_f32 v204, v204, v196\n"
"v_add_f32 v204, v204, v197\n"
"v_mfma_f32_16x16x32_bf16 a[8:11], v[112:115], v[160:163], a[8:11]\n"
"s_waitcnt lgkmcnt(4)\n"
"ds_read_b64_tr_b16 v[176:177], v247 offset:12416\n"
"v_cvt_pk_bf16_f32 v82, v196, v197\n"
"v_mfma_f32_16x16x32_bf16 a[8:11], v[32:35], v[164:167], a[8:11]\n"
"s_cmp_eq_u32 s80, 3\n"
"v_exp_f32 v198, v38\n"
"v_mfma_f32_16x16x32_bf16 a[56:59], v[40:43], v[164:167], a[56:59]\n"
"s_cselect_b32 s18, s26, s18\n"
"ds_read_b64_tr_b16 v[178:179], v247 offset:18560\n"
"v_add_f32 v204, v204, v198\n"
"v_mfma_f32_16x16x32_bf16 a[56:59], v[116:119], v[160:163], a[56:59]\n"
"s_cselect_b32 s19, s71, s19\n"
"v_exp_f32 v199, v39\n"
"v_mfma_f32_16x16x32_bf16 a[104:107], v[120:123], v[160:163], a[104:107]\n"
"s_cselect_b32 s24, s32, s24\n"
"ds_read_b64_tr_b16 v[180:181], v247 offset:24704\n"
"v_add_f32 v204, v204, v199\n"
"v_mfma_f32_16x16x32_bf16 a[104:107], v[48:51], v[164:167], a[104:107]\n"
"s_cselect_b32 s25, s35, s25\n"
"v_cvt_pk_bf16_f32 v83, v198, v199\n"
"v_mfma_f32_16x16x32_bf16 a[152:155], v[56:59], v[164:167], a[152:155]\n"
"ds_read_b64_tr_b16 v[182:183], v247 offset:30848\n"
"v_mfma_f32_16x16x32_bf16 a[152:155], v[124:127], v[160:163], a[152:155]\n"
"s_waitcnt lgkmcnt(6)\n"
"v_exp_f32 v192, v100\n"
"v_mfma_f32_16x16x32_bf16 a[156:159], v[124:127], v[168:171], a[156:159]\n"
"s_waitcnt lgkmcnt(4)\n"
"ds_read_b64_tr_b16 v[184:185], v248 offset:12416\n"
"v_add_f32 v205, v201, v192\n"
"v_mfma_f32_16x16x32_bf16 a[156:159], v[56:59], v[172:175], a[156:159]\n"
"v_exp_f32 v193, v101\n"
"v_mfma_f32_16x16x32_bf16 a[108:111], v[48:51], v[172:175], a[108:111]\n"
"ds_read_b64_tr_b16 v[186:187], v248 offset:18560\n"
"v_add_f32 v205, v205, v193\n"
"v_mfma_f32_16x16x32_bf16 a[108:111], v[120:123], v[168:171], a[108:111]\n"
"s_cmp_lg_u32 s81, 0\n"
"v_cvt_pk_bf16_f32 v84, v192, v193\n"
"v_mfma_f32_16x16x32_bf16 a[60:63], v[116:119], v[168:171], a[60:63]\n"
"ds_read_b64_tr_b16 v[188:189], v248 offset:24704\n"
"v_mfma_f32_16x16x32_bf16 a[60:63], v[40:43], v[172:175], a[60:63]\n"
"s_cselect_b32 s33, s24, s33\n"
"v_exp_f32 v194, v102\n"
"v_mfma_f32_16x16x32_bf16 a[12:15], v[32:35], v[172:175], a[12:15]\n"
"ds_read_b64_tr_b16 v[190:191], v248 offset:30848\n"
"v_add_f32 v205, v205, v194\n"
"v_mfma_f32_16x16x32_bf16 a[12:15], v[112:115], v[168:171], a[12:15]\n"
"s_waitcnt lgkmcnt(6)\n"
"v_exp_f32 v195, v103\n"
"v_mfma_f32_16x16x32_bf16 a[16:19], v[112:115], v[176:179], a[16:19]\n"
"s_waitcnt lgkmcnt(4)\n"
"v_add_f32 v205, v205, v195\n"
"v_cvt_pk_bf16_f32 v85, v194, v195\n"
"v_mfma_f32_16x16x32_bf16 a[16:19], v[32:35], v[180:183], a[16:19]\n"
"v_exp_f32 v196, v44\n"
"v_mfma_f32_16x16x32_bf16 a[64:67], v[40:43], v[180:183], a[64:67]\n"
"v_exp_f32 v197, v45\n"
"v_mfma_f32_16x16x32_bf16 a[64:67], v[116:119], v[176:179], a[64:67]\n"
"s_waitcnt vmcnt(11) lgkmcnt(0)\n"
"s_barrier\n"
"v_mfma_f32_16x16x32_bf16 a[112:115], v[120:123], v[176:179], a[112:115]\n"
"s_sub_u32 s97, s91, s98\n"
"ds_read_b128 v[160:163], v245 offset:0\n"
"v_add_f32 v205, v205, v196\n"
"v_mfma_f32_16x16x32_bf16 a[112:115], v[48:51], v[180:183], a[112:115]\n"
"s_max_i32 s91, s97, -20\n"
"v_add_f32 v205, v205, v197\n"
"v_cvt_pk_bf16_f32 v86, v196, v197\n"
"v_mfma_f32_16x16x32_bf16 a[160:163], v[56:59], v[180:183], a[160:163]\n"
"v_exp_f32 v198, v46\n"
"v_mfma_f32_16x16x32_bf16 a[160:163], v[124:127], v[176:179], a[160:163]\n"
"v_exp_f32 v199, v47\n"
"v_mfma_f32_16x16x32_bf16 a[164:167], v[124:127], v[184:187], a[164:167]\n"
"ds_read_b128 v[164:167], v246 offset:0\n"
"v_add_f32 v205, v205, v198\n"
"v_mfma_f32_16x16x32_bf16 a[164:167], v[56:59], v[188:191], a[164:167]\n"
"s_cmp_gt_i32 s97, 0\n"
"v_add_f32 v205, v205, v199\n"
"v_cvt_pk_bf16_f32 v87, v198, v199\n"
"v_mfma_f32_16x16x32_bf16 a[116:119], v[48:51], v[188:191], a[116:119]\n"
"s_cbranch_scc1 .Lat_pace7\n"
".Lat_pbk7:\n"
"v_exp_f32 v192, v104\n"
"v_mfma_f32_16x16x32_bf16 a[116:119], v[120:123], v[184:187], a[116:119]\n"
"s_add_u32 s91, s91, s98\n"
"v_exp_f32 v193, v105\n"
"v_mfma_f32_16x16x32_bf16 a[68:71], v[116:119], v[184:187], a[68:71]\n"
"s_add_u32 s91, s91, s70\n"
"ds_read_b128 v[168:171], v245 offset:128\n"
"v_add_f32 v206, v202, v192\n"
"v_mfma_f32_16x16x32_bf16 a[68:71], v[40:43], v[188:191], a[68:71]\n"
"v_exp_f32 v194, v106\n"
"v_mfma_f32_16x16x32_bf16 a[20:23], v[32:35], v[188:191], a[20:23]\n"
"s_cmp_gt_u32 s80, 1\n"
"v_add_f32 v206, v206, v193\n"
"v_cvt_pk_bf16_f32 v88, v192, v193\n"
"v_mfma_f32_16x16x32_bf16 a[20:23], v[112:115], v[184:187], a[20:23]\n"
"s_cbranch_scc1 .Lat_j0\n"
"s_cmp_eq_u32 s80, 0\n"
"s_cbranch_scc1 .Lat_lend\n"
"v_readlane_b32 s36, v255, 1\n"
"s_cmp_eq_u32 s81, 0\n"
"s_cselect_b32 s36, 4293394688, s36\n"
"s_cselect_b32 s12, 0xf0000000, 0\n"
"s_cselect_b32 s11, 22528, 0\n"
"v_or_b32 v216, s12, v216\n"
"v_or_b32 v218, s12, v218\n"
"s_cselect_b32 s12, 32768, 0\n"
"s_sub_u32 s24, s24, s12\n"
"s_add_u32 s18, s18, s36\n"
"v_readlane_b32 s12, v255, 10\n"
"s_mov_b32 s0, 0x40000000\n"
"s_nop 1\n"
"v_mov_b32 v208, s12\n"
"global_load_dwordx4 v[208:211], v208, s[14:15] sc1\n"
"s_branch .Lat_j0\n"
".Lat_lend:\n"
"ds_read_b64_tr_b16 v[160:161], v239 offset:12416\n"
"ds_read_b64_tr_b16 v[162:163], v239 offset:18560\n"
"ds_read_b64_tr_b16 v[164:165], v239 offset:24704\n"
"ds_read_b64_tr_b16 v[166:167], v239 offset:30848\n"
"ds_read_b64_tr_b16 v[168:169], v240 offset:12416\n"
"ds_read_b64_tr_b16 v[170:171], v240 offset:18560\n"
"ds_read_b64_tr_b16 v[172:173], v240 offset:24704\n"
"ds_read_b64_tr_b16 v[174:175], v240 offset:30848\n"
"s_add_u32 s81, s81, 1\n"
"s_cmp_lt_u32 s81, 2\n"
"s_cbranch_scc0 .Lat_drainB\n"
"s_mov_b32 s75, s65\n"
"v_add_f32 v206, v206, v194\n"
"v_exp_f32 v195, v107\n"
"v_exp_f32 v196, v52\n"
"v_add_f32 v206, v206, v195\n"
"v_cvt_pk_bf16_f32 v89, v194, v195\n"
"v_add_f32 v206, v206, v196\n"
"v_exp_f32 v197, v53\n"
"v_exp_f32 v198, v54\n"
"s_waitcnt lgkmcnt(6)\n"
"v_mfma_f32_16x16x32_bf16 a[24:27], v[112:115], v[160:163], a[24:27]\n"
"s_waitcnt lgkmcnt(4)\n"
"s_mov_b32 m0, s37\n"
"ds_read_b64_tr_b16 v[176:177], v247 offset:12544\n"
"v_mfma_f32_16x16x32_bf16 a[24:27], v[32:35], v[164:167], a[24:27]\n"
"buffer_load_dwordx4 v228, s[52:55], s75 offen offset:0 lds\n"
"v_mfma_f32_16x16x32_bf16 a[72:75], v[40:43], v[164:167], a[72:75]\n"
"ds_read_b64_tr_b16 v[178:179], v247 offset:18688\n"
"v_add_f32 v206, v206, v197\n"
"v_mfma_f32_16x16x32_bf16 a[72:75], v[116:119], v[160:163], a[72:75]\n"
"v_cvt_pk_bf16_f32 v90, v196, v197\n"
"v_add_f32 v206, v206, v198\n"
"v_mfma_f32_16x16x32_bf16 a[120:123], v[120:123], v[160:163], a[120:123]\n"
"ds_read_b64_tr_b16 v[180:181], v247 offset:24832\n"
"v_mfma_f32_16x16x32_bf16 a[120:123], v[48:51], v[164:167], a[120:123]\n"
"buffer_load_dwordx4 v229, s[52:55], s75 offen offset:1024 lds\n"
"v_mfma_f32_16x16x32_bf16 a[168:171], v[56:59], v[164:167], a[168:171]\n"
"ds_read_b64_tr_b16 v[182:183], v247 offset:30976\n"
"v_mfma_f32_16x16x32_bf16 a[168:171], v[124:127], v[160:163], a[168:171]\n"
"s_waitcnt lgkmcnt(6)\n"
"v_exp_f32 v199, v55\n"
"v_mfma_f32_16x16x32_bf16 a[172:175], v[124:127], v[168:171], a[172:175]\n"
"s_waitcnt lgkmcnt(4)\n"
"ds_read_b64_tr_b16 v[184:185], v248 offset:12544\n"
"v_add_f32 v206, v206, v199\n"
"v_mfma_f32_16x16x32_bf16 a[172:175], v[56:59], v[172:175], a[172:175]\n"
"buffer_load_dwordx4 v230, s[52:55], s75 offen offset:2048 lds\n"
"v_mfma_f32_16x16x32_bf16 a[124:127], v[48:51], v[172:175], a[124:127]\n"
"ds_read_b64_tr_b16 v[186:187], v248 offset:18688\n"
"v_exp_f32 v192, v108\n"
"v_mfma_f32_16x16x32_bf16 a[124:127], v[120:123], v[168:171], a[124:127]\n"
"v_cvt_pk_bf16_f32 v91, v198, v199\n"
"v_add_f32 v207, v203, v192\n"
"v_mfma_f32_16x16x32_bf16 a[76:79], v[116:119], v[168:171], a[76:79]\n"
"ds_read_b64_tr_b16 v[188:189], v248 offset:24832\n"
"v_mfma_f32_16x16x32_bf16 a[76:79], v[40:43], v[172:175], a[76:79]\n"
"buffer_load_dwordx4 v231, s[52:55], s75 offen offset:3072 lds\n"
"v_mfma_f32_16x16x32_bf16 a[28:31], v[32:35], v[172:175], a[28:31]\n"
"ds_read_b64_tr_b16 v[190:191], v248 offset:30976\n"
"v_mfma_f32_16x16x32_bf16 a[28:31], v[112:115], v[168:171], a[28:31]\n"
"s_waitcnt lgkmcnt(6)\n"
"s_add_u32 m0, s37, 4096\n"
"v_mfma_f32_16x16x32_bf16 a[32:35], v[112:115], v[176:179], a[32:35]\n"
"s_waitcnt lgkmcnt(4)\n"
"s_add_u32 s97, s75, s64\n"
"ds_read_b64_tr_b16 v[160:161], v239 offset:12544\n"
"v_mfma_f32_16x16x32_bf16 a[32:35], v[32:35], v[180:183], a[32:35]\n"
"buffer_load_dwordx4 v228, s[52:55], s97 offen offset:0 lds\n"
"v_mfma_f32_16x16x32_bf16 a[80:83], v[40:43], v[180:183], a[80:83]\n"
"ds_read_b64_tr_b16 v[162:163], v239 offset:18688\n"
"v_exp_f32 v193, v109\n"
"v_mfma_f32_16x16x32_bf16 a[80:83], v[116:119], v[176:179], a[80:83]\n"
"v_exp_f32 v194, v110\n"
"v_mfma_f32_16x16x32_bf16 a[128:131], v[120:123], v[176:179], a[128:131]\n"
"ds_read_b64_tr_b16 v[164:165], v239 offset:24832\n"
"v_add_f32 v207, v207, v193\n"
"v_cvt_pk_bf16_f32 v92, v192, v193\n"
"v_mfma_f32_16x16x32_bf16 a[128:131], v[48:51], v[180:183], a[128:131]\n"
"buffer_load_dwordx4 v229, s[52:55], s97 offen offset:1024 lds\n"
"v_mfma_f32_16x16x32_bf16 a[176:179], v[56:59], v[180:183], a[176:179]\n"
"ds_read_b64_tr_b16 v[166:167], v239 offset:30976\n"
"v_add_f32 v207, v207, v194\n"
"v_mfma_f32_16x16x32_bf16 a[176:179], v[124:127], v[176:179], a[176:179]\n"
"s_waitcnt lgkmcnt(6)\n"
"v_exp_f32 v195, v111\n"
"v_mfma_f32_16x16x32_bf16 a[180:183], v[124:127], v[184:187], a[180:183]\n"
"s_waitcnt lgkmcnt(4)\n"
"ds_read_b64_tr_b16 v[168:169], v240 offset:12544\n"
"v_mfma_f32_16x16x32_bf16 a[180:183], v[56:59], v[188:191], a[180:183]\n"
"buffer_load_dwordx4 v230, s[52:55], s97 offen offset:2048 lds\n"
"v_mfma_f32_16x16x32_bf16 a[132:135], v[48:51], v[188:191], a[132:135]\n"
"ds_read_b64_tr_b16 v[170:171], v240 offset:18688\n"
"v_mfma_f32_16x16x32_bf16 a[132:135], v[120:123], v[184:187], a[132:135]\n"
"v_exp_f32 v196, v60\n"
"v_mfma_f32_16x16x32_bf16 a[84:87], v[116:119], v[184:187], a[84:87]\n"
"ds_read_b64_tr_b16 v[172:173], v240 offset:24832\n"
"v_add_f32 v207, v207, v195\n"
"v_mfma_f32_16x16x32_bf16 a[84:87], v[40:43], v[188:191], a[84:87]\n"
"buffer_load_dwordx4 v231, s[52:55], s97 offen offset:3072 lds\n"
"v_mfma_f32_16x16x32_bf16 a[36:39], v[32:35], v[188:191], a[36:39]\n"
"ds_read_b64_tr_b16 v[174:175], v240 offset:30976\n"
"v_cvt_pk_bf16_f32 v93, v194, v195\n"
"v_add_f32 v207, v207, v196\n"
"v_mfma_f32_16x16x32_bf16 a[36:39], v[112:115], v[184:187], a[36:39]\n"
"s_waitcnt lgkmcnt(6)\n"
"v_exp_f32 v197, v61\n"
"v_mfma_f32_16x16x32_bf16 a[40:43], v[112:115], v[160:163], a[40:43]\n"
"s_waitcnt lgkmcnt(4)\n"
"v_add_f32 v207, v207, v197\n"
"v_mfma_f32_16x16x32_bf16 a[40:43], v[32:35], v[164:167], a[40:43]\n"
"v_exp_f32 v198, v62\n"
"v_mfma_f32_16x16x32_bf16 a[88:91], v[40:43], v[164:167], a[88:91]\n"
"v_cvt_pk_bf16_f32 v94, v196, v197\n"
"v_add_f32 v207, v207, v198\n"
"v_mfma_f32_16x16x32_bf16 a[88:91], v[116:119], v[160:163], a[88:91]\n"
"s_waitcnt vmcnt(13) lgkmcnt(0)\n"
"s_barrier\n"
"v_mfma_f32_16x16x32_bf16 a[136:139], v[120:123], v[160:163], a[136:139]\n"
"ds_read_b64_tr_b16 v[176:177], v247 offset:36864\n"
"v_mfma_f32_16x16x32_bf16 a[136:139], v[48:51], v[164:167], a[136:139]\n"
"ds_read_b64_tr_b16 v[178:179], v247 offset:43008\n"
"v_mfma_f32_16x16x32_bf16 a[184:187], v[56:59], v[164:167], a[184:187]\n"
"ds_read_b64_tr_b16 v[180:181], v248 offset:36864\n"
"v_mfma_f32_16x16x32_bf16 a[184:187], v[124:127], v[160:163], a[184:187]\n"
"ds_read_b64_tr_b16 v[182:183], v248 offset:43008\n"
"v_mfma_f32_16x16x32_bf16 a[188:191], v[124:127], v[168:171], a[188:191]\n"
"ds_read_b64_tr_b16 v[184:185], v239 offset:36864\n"
"v_exp_f32 v199, v63\n"
"v_mfma_f32_16x16x32_bf16 a[188:191], v[56:59], v[172:175], a[188:191]\n"
"v_add_f32 v207, v207, v199\n"
"v_cvt_pk_bf16_f32 v95, v198, v199\n"
"v_mfma_f32_16x16x32_bf16 a[140:143], v[48:51], v[172:175], a[140:143]\n"
"ds_read_b64_tr_b16 v[186:187], v239 offset:43008\n"
"v_add_f32 v212, v204, v205\n"
"v_mfma_f32_16x16x32_bf16 a[140:143], v[120:123], v[168:171], a[140:143]\n"
"v_add_f32 v213, v206, v207\n"
"v_add_f32 v212, v212, v213\n"
"v_mfma_f32_16x16x32_bf16 a[92:95], v[116:119], v[168:171], a[92:95]\n"
"ds_read_b64_tr_b16 v[188:189], v240 offset:36864\n"
"v_cmp_nge_f32 vcc, s90, v212\n"
"v_mfma_f32_16x16x32_bf16 a[92:95], v[40:43], v[172:175], a[92:95]\n"
"ds_read_b64_tr_b16 v[190:191], v240 offset:43008\n"
"v_mfma_f32_16x16x32_bf16 a[44:47], v[32:35], v[172:175], a[44:47]\n"
"v_mfma_f32_16x16x32_bf16 a[44:47], v[112:115], v[168:171], a[44:47]\n"
"s_nop 0\n"
"s_cbranch_vccnz .Lat_tr6\n"
".Lat_bk6:\n"
"s_waitcnt lgkmcnt(6)\n"
"v_mfma_f32_16x16x32_bf16 a[0:3], v[80:83], v[176:179], a[0:3]\n"
"s_waitcnt lgkmcnt(4)\n"
"s_add_u32 s75, s75, 128\n"
"ds_read_b64_tr_b16 v[160:161], v247 offset:36992\n"
"v_mfma_f32_16x16x32_bf16 a[4:7], v[80:83], v[180:183], a[4:7]\n"
"s_add_u32 s74, s69, 786688\n"
"v_permlane16_swap_b32 v204, v205\n"
"v_permlane16_swap_b32 v206, v207\n"
"v_mfma_f32_16x16x32_bf16 a[52:55], v[84:87], v[180:183], a[52:55]\n"
"s_mov_b32 s8, s0\n"
"ds_read_b64_tr_b16 v[162:163], v247 offset:43136\n"
"v_add_f32 v204, v204, v205\n"
"v_mfma_f32_16x16x32_bf16 a[48:51], v[84:87], v[176:179], a[48:51]\n"
"s_mov_b32 s0, s1\n"
"v_add_f32 v206, v206, v207\n"
"v_lshrrev_b32 v205, 4, v251\n"
"v_mfma_f32_16x16x32_bf16 a[96:99], v[88:91], v[176:179], a[96:99]\n"
"s_mov_b32 s1, s8\n"
"ds_read_b64_tr_b16 v[164:165], v248 offset:36992\n"
"v_permlane32_swap_b32 v204, v206\n"
"v_mfma_f32_16x16x32_bf16 a[100:103], v[88:91], v[180:183], a[100:103]\n"
"s_add_u32 s42, s61, s84\n"
"v_add_f32 v204, v204, v206\n"
"v_mfma_f32_16x16x32_bf16 a[148:151], v[92:95], v[180:183], a[148:151]\n"
"s_mov_b32 s46, s42\n"
"ds_read_b64_tr_b16 v[166:167], v248 offset:43136\n"
"v_mfma_f32_16x16x32_bf16 a[144:147], v[92:95], v[176:179], a[144:147]\n"
"s_waitcnt lgkmcnt(6)\n"
"v_rcp_f32 v204, v204\n"
"v_mfma_f32_16x16x32_bf16 a[152:155], v[92:95], v[184:187], a[152:155]\n"
"s_waitcnt lgkmcnt(4)\n"
"ds_read_b64_tr_b16 v[168:169], v239 offset:36992\n"
"ds_bpermute_b32 v216, v205, v204 offset:0\n"
"v_mfma_f32_16x16x32_bf16 a[156:159], v[92:95], v[188:191], a[156:159]\n"
"s_mov_b32 s72, s61\n"
"ds_bpermute_b32 v218, v205, v204 offset:64\n"
"v_mfma_f32_16x16x32_bf16 a[108:111], v[88:91], v[188:191], a[108:111]\n"
"s_mov_b32 s73, s61\n"
"ds_read_b64_tr_b16 v[170:171], v239 offset:43136\n"
"ds_bpermute_b32 v220, v205, v204 offset:128\n"
"v_mfma_f32_16x16x32_bf16 a[104:107], v[88:91], v[184:187], a[104:107]\n"
"s_mov_b32 s26, 1\n"
"ds_bpermute_b32 v222, v205, v204 offset:192\n"
"v_mfma_f32_16x16x32_bf16 a[56:59], v[84:87], v[184:187], a[56:59]\n"
"s_mov_b32 s24, 0xf0000000\n"
"ds_read_b64_tr_b16 v[172:173], v240 offset:36992\n"
"v_mfma_f32_16x16x32_bf16 a[60:63], v[84:87], v[188:191], a[60:63]\n"
"s_mov_b32 s25, s78\n"
"v_accvgpr_read_b32 v212, a4\n"
"v_accvgpr_read_b32 v213, a5\n"
"v_mfma_f32_16x16x32_bf16 a[12:15], v[80:83], v[188:191], a[12:15]\n"
"s_mov_b32 s18, 0xf0000000\n"
"ds_read_b64_tr_b16 v[174:175], v240 offset:43136\n"
"v_accvgpr_read_b32 v214, a6\n"
"v_mfma_f32_16x16x32_bf16 a[8:11], v[80:83], v[184:187], a[8:11]\n"
"s_waitcnt lgkmcnt(10)\n"
"v_accvgpr_read_b32 v215, a7\n"
"v_mfma_f32_16x16x32_bf16 a[16:19], v[80:83], v[160:163], a[16:19]\n"
"s_waitcnt lgkmcnt(8)\n"
"ds_read_b64_tr_b16 v[176:177], v247 offset:37120\n"
"v_mfma_f32_16x16x32_bf16 a[20:23], v[80:83], v[164:167], a[20:23]\n"
"s_mov_b32 s19, s77\n"
"s_waitcnt lgkmcnt(7)\n"
"v_mul_f32_dpp v212, v216, v212 row_newbcast:0 row_mask:0xf bank_mask:0xf\n"
"v_mfma_f32_16x16x32_bf16 a[68:71], v[84:87], v[164:167], a[68:71]\n"
"s_mov_b32 s80, 3\n"
"ds_read_b64_tr_b16 v[178:179], v247 offset:43264\n"
"v_mul_f32_dpp v213, v216, v213 row_newbcast:4 row_mask:0xf bank_mask:0xf\n"
"v_mfma_f32_16x16x32_bf16 a[64:67], v[84:87], v[160:163], a[64:67]\n"
"s_mov_b64 vcc, 0\n"
"v_readlane_b32 s27, v255, 30\n"
"v_mul_f32_dpp v214, v216, v214 row_newbcast:8 row_mask:0xf bank_mask:0xf\n"
"v_mfma_f32_16x16x32_bf16 a[112:115], v[88:91], v[160:163], a[112:115]\n"
"ds_read_b64_tr_b16 v[180:181], v248 offset:37120\n"
"v_mul_f32_dpp v215, v216, v215 row_newbcast:12 row_mask:0xf bank_mask:0xf\n"
"v_mfma_f32_16x16x32_bf16 a[116:119], v[88:91], v[164:167], a[116:119]\n"
"s_mov_b64 s[20:21], s[28:29]\n"
"v_cvt_pk_bf16_f32 v212, v212, v213\n"
"v_cvt_pk_bf16_f32 v213, v214, v215\n"
"v_mfma_f32_16x16x32_bf16 a[164:167], v[92:95], v[164:167], a[164:167]\n"
"s_mov_b64 s[22:23], s[30:31]\n"
"ds_read_b64_tr_b16 v[182:183], v248 offset:43264\n"
"ds_write_b64 v244, v[212:213] offset:13320\n"
"v_mfma_f32_16x16x32_bf16 a[160:163], v[92:95], v[160:163], a[160:163]\n"
"s_waitcnt lgkmcnt(9)\n"
"v_accvgpr_read_b32 v208, a0\n"
"v_accvgpr_read_b32 v209, a1\n"
"v_mfma_f32_16x16x32_bf16 a[168:171], v[92:95], v[168:171], a[168:171]\n"
"s_waitcnt lgkmcnt(5)\n"
"ds_read_b64_tr_b16 v[184:185], v239 offset:37120\n"
"v_accvgpr_read_b32 v210, a2\n"
"v_mfma_f32_16x16x32_bf16 a[172:175], v[92:95], v[172:175], a[172:175]\n"
"s_mov_b32 s70, 327680\n"
"v_accvgpr_read_b32 v211, a3\n"
"v_mul_f32_dpp v208, v216, v208 row_newbcast:0 row_mask:0xf bank_mask:0xf\n"
"v_mfma_f32_16x16x32_bf16 a[124:127], v[88:91], v[172:175], a[124:127]\n"
"s_mov_b32 s91, 0xf0000000\n"
"ds_read_b64_tr_b16 v[186:187], v239 offset:43264\n"
"v_min_u32 v252, 304, v251\n"
"v_mfma_f32_16x16x32_bf16 a[120:123], v[88:91], v[168:171], a[120:123]\n"
"v_mul_f32_dpp v209, v216, v209 row_newbcast:4 row_mask:0xf bank_mask:0xf\n"
"v_mul_f32_dpp v210, v216, v210 row_newbcast:8 row_mask:0xf bank_mask:0xf\n"
"v_mfma_f32_16x16x32_bf16 a[72:75], v[84:87], v[168:171], a[72:75]\n"
"ds_read_b64_tr_b16 v[188:189], v240 offset:37120\n"
"v_lshlrev_b32 v252, 3, v252\n"
"v_mfma_f32_16x16x32_bf16 a[76:79], v[84:87], v[172:175], a[76:79]\n"
"v_mul_f32_dpp v211, v216, v211 row_newbcast:12 row_mask:0xf bank_mask:0xf\n"
"v_cvt_pk_bf16_f32 v211, v210, v211\n"
"v_mfma_f32_16x16x32_bf16 a[28:31], v[80:83], v[172:175], a[28:31]\n"
"s_cmp_eq_u32 s95, 0\n"
"ds_read_b64_tr_b16 v[190:191], v240 offset:43264\n"
"v_cvt_pk_bf16_f32 v210, v208, v209\n"
"v_mfma_f32_16x16x32_bf16 a[24:27], v[80:83], v[168:171], a[24:27]\n"
"s_waitcnt lgkmcnt(7)\n"
"ds_write_b64 v244, v[210:211] offset:13312\n"
"v_mfma_f32_16x16x32_bf16 a[32:35], v[80:83], v[176:179], a[32:35]\n"
"s_waitcnt lgkmcnt(6)\n"
"s_waitcnt vmcnt(0)\n"
"ds_read_b128 a[192:195], v232 offset:0\n"
"v_mfma_f32_16x16x32_bf16 a[36:39], v[80:83], v[180:183], a[36:39]\n"
"s_cselect_b32 s27, 0xf0000000, s27\n"
"ds_read_b64_tr_b16 v[192:193], v250 offset:0\n"
"v_accvgpr_read_b32 v212, a52\n"
"v_mfma_f32_16x16x32_bf16 a[84:87], v[84:87], v[180:183], a[84:87]\n"
"s_cmp_eq_u32 s80, 3\n"
"ds_read_b64_tr_b16 v[194:195], v250 offset:1024\n"
"v_accvgpr_read_b32 v213, a53\n"
"v_mfma_f32_16x16x32_bf16 a[80:83], v[84:87], v[176:179], a[80:83]\n"
"s_cselect_b32 s91, s27, s91\n"
"ds_read_b128 a[208:211], v233 offset:0\n"
"v_accvgpr_read_b32 v214, a54\n"
"v_mfma_f32_16x16x32_bf16 a[128:131], v[88:91], v[176:179], a[128:131]\n"
"s_cmp_eq_u32 s80, 9\n"
"ds_read_b64_tr_b16 v[196:197], v250 offset:4096\n"
"v_accvgpr_read_b32 v215, a55\n"
"v_mfma_f32_16x16x32_bf16 a[132:135], v[88:91], v[180:183], a[132:135]\n"
"s_cselect_b32 s91, 0xf0000000, s91\n"
"ds_read_b64_tr_b16 v[198:199], v250 offset:5120\n"
"ds_read_b64_tr_b16 a[0:1], v221 offset:13312\n"
"v_mfma_f32_16x16x32_bf16 a[180:183], v[92:95], v[180:183], a[180:183]\n"
"ds_read_b128 a[212:215], v233 offset:2048\n"
"ds_read_b64_tr_b16 a[2:3], v221 offset:13320\n"
"v_mfma_f32_16x16x32_bf16 a[176:179], v[92:95], v[176:179], a[176:179]\n"
"s_waitcnt lgkmcnt(12)\n"
"ds_read_b128 a[196:199], v232 offset:2048\n"
"v_mul_f32_dpp v212, v218, v212 row_newbcast:0 row_mask:0xf bank_mask:0xf\n"
"v_mfma_f32_16x16x32_bf16 a[184:187], v[92:95], v[184:187], a[184:187]\n"
"s_waitcnt lgkmcnt(11)\n"
"ds_read_b128 a[200:203], v232 offset:4096\n"
"v_mul_f32_dpp v213, v218, v213 row_newbcast:4 row_mask:0xf bank_mask:0xf\n"
"v_mfma_f32_16x16x32_bf16 a[188:191], v[92:95], v[188:191], a[188:191]\n"
"ds_read_b128 a[216:219], v233 offset:4096\n"
"v_mul_f32_dpp v214, v218, v214 row_newbcast:8 row_mask:0xf bank_mask:0xf\n"
"v_mfma_f32_16x16x32_bf16 a[140:143], v[88:91], v[188:191], a[140:143]\n"
"ds_read_b128 a[220:223], v233 offset:6144\n"
"v_mul_f32_dpp v215, v218, v215 row_newbcast:12 row_mask:0xf bank_mask:0xf\n"
"v_mfma_f32_16x16x32_bf16 a[136:139], v[88:91], v[184:187], a[136:139]\n"
"ds_read_b128 a[204:207], v232 offset:6144\n"
"v_cvt_pk_bf16_f32 v212, v212, v213\n"
"v_mfma_f32_16x16x32_bf16 a[88:91], v[84:87], v[184:187], a[88:91]\n"
"s_waitcnt lgkmcnt(9)\n"
"ds_read_b64_tr_b16 v[200:201], v250 offset:8\n"
"v_cvt_pk_bf16_f32 v213, v214, v215\n"
"v_mfma_f32_16x16x32_bf16 a[92:95], v[84:87], v[188:191], a[92:95]\n"
"s_barrier\n"
"v_mfma_f32_16x16x32_bf16 a[44:47], v[80:83], v[188:191], a[44:47]\n"
"ds_read_b64_tr_b16 v[202:203], v250 offset:1032\n"
"ds_write_b64 v244, v[212:213] offset:13320\n"
"v_mfma_f32_16x16x32_bf16 a[40:43], v[80:83], v[184:187], a[40:43]\n"
"v_accvgpr_read_b32 v208, a48\n"
"v_mfma_f32_16x16x32_bf16 v[0:3], v[192:195], a[192:195], 0\n"
"s_waitcnt lgkmcnt(11)\n"
"ds_read_b64_tr_b16 v[204:205], v250 offset:4104\n"
"v_accvgpr_read_b32 v209, a49\n"
"v_mfma_f32_16x16x32_bf16 v[0:3], v[196:199], a[208:211], v[0:3]\n"
"s_waitcnt lgkmcnt(10)\n"
"v_accvgpr_read_b32 v210, a50\n"
"v_accvgpr_read_b32 v211, a51\n"
"v_mfma_f32_16x16x32_bf16 v[48:51], v[196:199], a[212:215], 0\n"
"s_waitcnt lgkmcnt(8)\n"
"buffer_store_dwordx4 a[0:3], v251, s[56:59], s76 offen offset:0 sc1\n"
"s_mov_b32 m0, s68\n"
"ds_read_b64_tr_b16 v[206:207], v250 offset:5128\n"
"v_mul_f32_dpp v208, v218, v208 row_newbcast:0 row_mask:0xf bank_mask:0xf\n"
"v_mfma_f32_16x16x32_bf16 v[48:51], v[192:195], a[196:199], v[48:51]\n"
"s_waitcnt lgkmcnt(8)\n"
"buffer_load_dwordx4 v228, s[52:55], s75 offen offset:0 lds\n"
"v_mfma_f32_16x16x32_bf16 v[96:99], v[192:195], a[200:203], 0\n"
"s_waitcnt lgkmcnt(7)\n"
"ds_read_b64_tr_b16 a[4:5], v250 offset:256\n"
"v_mul_f32_dpp v209, v218, v209 row_newbcast:4 row_mask:0xf bank_mask:0xf\n"
"v_mfma_f32_16x16x32_bf16 v[96:99], v[196:199], a[216:219], v[96:99]\n"
"s_waitcnt lgkmcnt(7)\n"
"v_mul_f32_dpp v210, v218, v210 row_newbcast:8 row_mask:0xf bank_mask:0xf\n"
"v_mul_f32_dpp v211, v218, v211 row_newbcast:12 row_mask:0xf bank_mask:0xf\n"
"v_mfma_f32_16x16x32_bf16 v[144:147], v[196:199], a[220:223], 0\n"
"s_waitcnt lgkmcnt(6)\n"
"ds_read_b64_tr_b16 a[6:7], v250 offset:1280\n"
"v_cvt_pk_bf16_f32 v211, v210, v211\n"
"v_cvt_pk_bf16_f32 v210, v208, v209\n"
"v_mfma_f32_16x16x32_bf16 v[144:147], v[192:195], a[204:207], v[144:147]\n"
"s_waitcnt lgkmcnt(5)\n"
"buffer_load_dwordx4 v229, s[52:55], s75 offen offset:1024 lds\n"
"v_mfma_f32_16x16x32_bf16 v[148:151], v[200:203], a[204:207], 0\n"
"s_waitcnt lgkmcnt(2)\n"
"ds_read_b64_tr_b16 a[52:53], v250 offset:4352\n"
"ds_write_b64 v244, v[210:211] offset:13312\n"
"v_mfma_f32_16x16x32_bf16 v[148:151], v[204:207], a[220:223], v[148:151]\n"
"v_accvgpr_read_b32 v212, a100\n"
"v_accvgpr_read_b32 v213, a101\n"
"v_mfma_f32_16x16x32_bf16 v[100:103], v[204:207], a[216:219], 0\n"
"ds_read_b64_tr_b16 a[54:55], v250 offset:5376\n"
"v_accvgpr_read_b32 v214, a102\n"
"v_mfma_f32_16x16x32_bf16 v[100:103], v[200:203], a[200:203], v[100:103]\n"
"buffer_load_dwordx4 v230, s[52:55], s75 offen offset:2048 lds\n"
"v_mfma_f32_16x16x32_bf16 v[52:55], v[200:203], a[196:199], 0\n"
"ds_read_b64_tr_b16 v[192:193], v250 offset:264\n"
"v_accvgpr_read_b32 v215, a103\n"
"v_mfma_f32_16x16x32_bf16 v[52:55], v[204:207], a[212:215], v[52:55]\n"
"ds_read_b64_tr_b16 a[48:49], v221 offset:13312\n"
"ds_read_b64_tr_b16 a[50:51], v221 offset:13320\n"
"v_mul_f32_dpp v212, v220, v212 row_newbcast:0 row_mask:0xf bank_mask:0xf\n"
"v_mfma_f32_16x16x32_bf16 v[4:7], v[204:207], a[208:211], 0\n"
"ds_read_b64_tr_b16 v[194:195], v250 offset:1288\n"
"v_mul_f32_dpp v213, v220, v213 row_newbcast:4 row_mask:0xf bank_mask:0xf\n"
"v_mfma_f32_16x16x32_bf16 v[4:7], v[200:203], a[192:195], v[4:7]\n"
"s_waitcnt lgkmcnt(7)\n"
"buffer_load_dwordx4 v231, s[52:55], s75 offen offset:3072 lds\n"
"v_mfma_f32_16x16x32_bf16 v[8:11], a[4:7], a[192:195], 0\n"
"s_waitcnt lgkmcnt(4)\n"
"ds_read_b64_tr_b16 v[196:197], v250 offset:4360\n"
"v_mul_f32_dpp v214, v220, v214 row_newbcast:8 row_mask:0xf bank_mask:0xf\n"
"v_mfma_f32_16x16x32_bf16 v[8:11], a[52:55], a[208:211], v[8:11]\n"
"s_add_u32 m0, s68, 4096\n"
"v_mul_f32_dpp v215, v220, v215 row_newbcast:12 row_mask:0xf bank_mask:0xf\n"
"v_cvt_pk_bf16_f32 v212, v212, v213\n"
"v_mfma_f32_16x16x32_bf16 v[56:59], a[52:55], a[212:215], 0\n"
"s_add_u32 s97, s75, s64\n"
"ds_read_b64_tr_b16 v[198:199], v250 offset:5384\n"
"v_cvt_pk_bf16_f32 v213, v214, v215\n"
"v_mfma_f32_16x16x32_bf16 v[56:59], a[4:7], a[196:199], v[56:59]\n"
"buffer_load_dwordx4 v228, s[52:55], s97 offen offset:0 lds\n"
"v_accvgpr_read_b32 v208, a96\n"
"v_mfma_f32_16x16x32_bf16 v[104:107], a[4:7], a[200:203], 0\n"
"ds_read_b64_tr_b16 v[200:201], v250 offset:8192\n"
"ds_write_b64 v244, v[212:213] offset:13320\n"
"v_mfma_f32_16x16x32_bf16 v[104:107], a[52:55], a[216:219], v[104:107]\n"
"v_accvgpr_read_b32 v209, a97\n"
"v_accvgpr_read_b32 v210, a98\n"
"v_mfma_f32_16x16x32_bf16 v[152:155], a[52:55], a[220:223], 0\n"
"s_waitcnt lgkmcnt(5)\n"
"buffer_store_dwordx4 a[48:51], v251, s[56:59], s76 offen offset:2048 sc1\n"
"ds_read_b64_tr_b16 v[202:203], v250 offset:9216\n"
"v_accvgpr_read_b32 v211, a99\n"
"v_mul_f32_dpp v208, v220, v208 row_newbcast:0 row_mask:0xf bank_mask:0xf\n"
"v_mfma_f32_16x16x32_bf16 v[152:155], a[4:7], a[204:207], v[152:155]\n"
"s_waitcnt lgkmcnt(5)\n"
"buffer_load_dwordx4 v229, s[52:55], s97 offen offset:1024 lds\n"
"v_mfma_f32_16x16x32_bf16 v[156:159], v[192:195], a[204:207], 0\n"
"s_waitcnt lgkmcnt(3)\n"
"ds_read_b64_tr_b16 v[204:205], v250 offset:12288\n"
"v_mul_f32_dpp v209, v220, v209 row_newbcast:4 row_mask:0xf bank_mask:0xf\n"
"v_mfma_f32_16x16x32_bf16 v[156:159], v[196:199], a[220:223], v[156:159]\n"
"v_mul_f32_dpp v210, v220, v210 row_newbcast:8 row_mask:0xf bank_mask:0xf\n"
"v_mul_f32_dpp v211, v220, v211 row_newbcast:12 row_mask:0xf bank_mask:0xf\n"
"v_mfma_f32_16x16x32_bf16 v[108:111], v[196:199], a[216:219], 0\n"
"ds_read_b64_tr_b16 v[206:207], v250 offset:13312\n"
"v_cvt_pk_bf16_f32 v211, v210, v211\n"
"v_cvt_pk_bf16_f32 v210, v208, v209\n"
"v_mfma_f32_16x16x32_bf16 v[108:111], v[192:195], a[200:203], v[108:111]\n"
"buffer_load_dwordx4 v230, s[52:55], s97 offen offset:2048 lds\n"
"v_mfma_f32_16x16x32_bf16 v[60:63], v[192:195], a[196:199], 0\n"
"ds_read_b64_tr_b16 a[4:5], v250 offset:8200\n"
"ds_write_b64 v244, v[210:211] offset:13312\n"
"v_mfma_f32_16x16x32_bf16 v[60:63], v[196:199], a[212:215], v[60:63]\n"
"v_accvgpr_read_b32 v212, a148\n"
"v_accvgpr_read_b32 v213, a149\n"
"v_mfma_f32_16x16x32_bf16 v[12:15], v[196:199], a[208:211], 0\n"
"ds_read_b64_tr_b16 a[6:7], v250 offset:9224\n"
"v_accvgpr_read_b32 v214, a150\n"
"v_mfma_f32_16x16x32_bf16 v[12:15], v[192:195], a[192:195], v[12:15]\n"
"s_waitcnt lgkmcnt(5)\n"
"buffer_load_dwordx4 v231, s[52:55], s97 offen offset:3072 lds\n"
"v_mfma_f32_16x16x32_bf16 v[16:19], v[200:203], a[192:195], 0\n"
"s_waitcnt lgkmcnt(3)\n"
"s_add_u32 s75, s75, 128\n"
"ds_read_b64_tr_b16 a[52:53], v250 offset:12296\n"
"v_mfma_f32_16x16x32_bf16 v[16:19], v[204:207], a[208:211], v[16:19]\n"
"s_barrier\n"
"v_accvgpr_read_b32 v215, a151\n"
"v_mfma_f32_16x16x32_bf16 v[64:67], v[204:207], a[212:215], 0\n"
"s_add_u32 m0, s79, 40960\n"
"ds_read_b64_tr_b16 a[54:55], v250 offset:13320\n"
"ds_read_b64_tr_b16 a[96:97], v221 offset:13312\n"
"v_mfma_f32_16x16x32_bf16 v[64:67], v[200:203], a[196:199], v[64:67]\n"
"buffer_load_dwordx4 v242, s[48:51], s74 offen offset:0 lds\n"
"v_mfma_f32_16x16x32_bf16 v[112:115], v[200:203], a[200:203], 0\n"
"ds_read_b64_tr_b16 v[192:193], v250 offset:8448\n"
"ds_read_b64_tr_b16 a[98:99], v221 offset:13320\n"
"v_mfma_f32_16x16x32_bf16 v[112:115], v[204:207], a[216:219], v[112:115]\n"
"buffer_load_dwordx4 v243, s[48:51], s74 offen offset:1024 lds\n"
"v_mfma_f32_16x16x32_bf16 v[160:163], v[204:207], a[220:223], 0\n"
"s_add_u32 s74, s74, 786176\n"
"ds_read_b64_tr_b16 v[194:195], v250 offset:9472\n"
"v_mul_f32_dpp v212, v222, v212 row_newbcast:0 row_mask:0xf bank_mask:0xf\n"
"v_mfma_f32_16x16x32_bf16 v[160:163], v[200:203], a[204:207], v[160:163]\n"
"s_waitcnt lgkmcnt(6)\n"
"v_mul_f32_dpp v213, v222, v213 row_newbcast:4 row_mask:0xf bank_mask:0xf\n"
"v_mul_f32_dpp v214, v222, v214 row_newbcast:8 row_mask:0xf bank_mask:0xf\n"
"v_mul_f32_dpp v215, v222, v215 row_newbcast:12 row_mask:0xf bank_mask:0xf\n"
"v_mfma_f32_16x16x32_bf16 v[164:167], a[4:7], a[204:207], 0\n"
"s_waitcnt lgkmcnt(4)\n"
"ds_read_b64_tr_b16 v[196:197], v250 offset:12544\n"
"v_cvt_pk_bf16_f32 v212, v212, v213\n"
"v_mfma_f32_16x16x32_bf16 v[164:167], a[52:55], a[220:223], v[164:167]\n"
"v_cvt_pk_bf16_f32 v213, v214, v215\n"
"v_accvgpr_read_b32 v208, a144\n"
"v_mfma_f32_16x16x32_bf16 v[116:119], a[52:55], a[216:219], 0\n"
"ds_read_b64_tr_b16 v[198:199], v250 offset:13568\n"
"ds_write_b64 v244, v[212:213] offset:13320\n"
"v_mfma_f32_16x16x32_bf16 v[116:119], a[4:7], a[200:203], v[116:119]\n"
"v_accvgpr_read_b32 v209, a145\n"
"v_accvgpr_read_b32 v210, a146\n"
"v_mfma_f32_16x16x32_bf16 v[68:71], a[4:7], a[196:199], 0\n"
"s_waitcnt lgkmcnt(4)\n"
"buffer_store_dwordx4 a[96:99], v253, s[56:59], s76 offen offset:0 sc1\n"
"ds_read_b64_tr_b16 v[200:201], v250 offset:8456\n"
"v_accvgpr_read_b32 v211, a147\n"
"v_mul_f32_dpp v208, v222, v208 row_newbcast:0 row_mask:0xf bank_mask:0xf\n"
"v_mfma_f32_16x16x32_bf16 v[68:71], a[52:55], a[212:215], v[68:71]\n"
"v_mul_f32_dpp v209, v222, v209 row_newbcast:4 row_mask:0xf bank_mask:0xf\n"
"v_mul_f32_dpp v210, v222, v210 row_newbcast:8 row_mask:0xf bank_mask:0xf\n"
"v_mfma_f32_16x16x32_bf16 v[20:23], a[52:55], a[208:211], 0\n"
"ds_read_b64_tr_b16 v[202:203], v250 offset:9480\n"
"v_mul_f32_dpp v211, v222, v211 row_newbcast:12 row_mask:0xf bank_mask:0xf\n"
"v_cvt_pk_bf16_f32 v211, v210, v211\n"
"v_mfma_f32_16x16x32_bf16 v[20:23], a[4:7], a[192:195], v[20:23]\n"
"s_waitcnt lgkmcnt(5)\n"
"v_cvt_pk_bf16_f32 v210, v208, v209\n"
"v_mfma_f32_16x16x32_bf16 v[24:27], v[192:195], a[192:195], 0\n"
"s_waitcnt lgkmcnt(3)\n"
"ds_read_b64_tr_b16 v[204:205], v250 offset:12552\n"
"ds_write_b64 v244, v[210:211] offset:13312\n"
"v_mfma_f32_16x16x32_bf16 v[24:27], v[196:199], a[208:211], v[24:27]\n"
"v_accvgpr_read_b32 v212, a12\n"
"v_accvgpr_read_b32 v213, a13\n"
"v_mfma_f32_16x16x32_bf16 v[72:75], v[196:199], a[212:215], 0\n"
"s_mov_b32 m0, s37\n"
"ds_read_b64_tr_b16 v[206:207], v250 offset:13576\n"
"v_accvgpr_read_b32 v214, a14\n"
"v_mfma_f32_16x16x32_bf16 v[72:75], v[192:195], a[196:199], v[72:75]\n"
"buffer_load_dwordx4 v228, s[52:55], s75 offen offset:0 lds\n"
"v_mfma_f32_16x16x32_bf16 v[120:123], v[192:195], a[200:203], 0\n"
"ds_read_b64_tr_b16 a[4:5], v250 offset:16384\n"
"v_accvgpr_read_b32 v215, a15\n"
"v_mfma_f32_16x16x32_bf16 v[120:123], v[196:199], a[216:219], v[120:123]\n"
"ds_read_b64_tr_b16 a[144:145], v221 offset:13312\n"
"ds_read_b64_tr_b16 a[146:147], v221 offset:13320\n"
"v_mfma_f32_16x16x32_bf16 v[168:171], v[196:199], a[220:223], 0\n"
"ds_read_b64_tr_b16 a[6:7], v250 offset:17408\n"
"v_mul_f32_dpp v212, v216, v212 row_newbcast:0 row_mask:0xf bank_mask:0xf\n"
"v_mul_f32_dpp v213, v216, v213 row_newbcast:4 row_mask:0xf bank_mask:0xf\n"
"v_mfma_f32_16x16x32_bf16 v[168:171], v[192:195], a[204:207], v[168:171]\n"
"s_waitcnt lgkmcnt(7)\n"
"buffer_load_dwordx4 v229, s[52:55], s75 offen offset:1024 lds\n"
"v_mfma_f32_16x16x32_bf16 v[172:175], v[200:203], a[204:207], 0\n"
"s_waitcnt lgkmcnt(4)\n"
"ds_read_b64_tr_b16 a[52:53], v250 offset:20480\n"
"v_mul_f32_dpp v214, v216, v214 row_newbcast:8 row_mask:0xf bank_mask:0xf\n"
"v_mfma_f32_16x16x32_bf16 v[172:175], v[204:207], a[220:223], v[172:175]\n"
"v_mul_f32_dpp v215, v216, v215 row_newbcast:12 row_mask:0xf bank_mask:0xf\n"
"v_cvt_pk_bf16_f32 v212, v212, v213\n"
"v_mfma_f32_16x16x32_bf16 v[124:127], v[204:207], a[216:219], 0\n"
"ds_read_b64_tr_b16 a[54:55], v250 offset:21504\n"
"v_cvt_pk_bf16_f32 v213, v214, v215\n"
"v_mfma_f32_16x16x32_bf16 v[124:127], v[200:203], a[200:203], v[124:127]\n"
"buffer_load_dwordx4 v230, s[52:55], s75 offen offset:2048 lds\n"
"v_mfma_f32_16x16x32_bf16 v[76:79], v[200:203], a[196:199], 0\n"
"ds_read_b64_tr_b16 v[192:193], v250 offset:16392\n"
"ds_write_b64 v244, v[212:213] offset:13320\n"
"v_mfma_f32_16x16x32_bf16 v[76:79], v[204:207], a[212:215], v[76:79]\n"
"v_accvgpr_read_b32 v208, a8\n"
"v_accvgpr_read_b32 v209, a9\n"
"v_accvgpr_read_b32 v210, a10\n"
"v_mfma_f32_16x16x32_bf16 v[28:31], v[204:207], a[208:211], 0\n"
"s_waitcnt lgkmcnt(5)\n"
"buffer_store_dwordx4 a[144:147], v253, s[56:59], s76 offen offset:2048 sc1\n"
"ds_read_b64_tr_b16 v[194:195], v250 offset:17416\n"
"v_accvgpr_read_b32 v211, a11\n"
"v_mul_f32_dpp v208, v216, v208 row_newbcast:0 row_mask:0xf bank_mask:0xf\n"
"v_mfma_f32_16x16x32_bf16 v[28:31], v[200:203], a[192:195], v[28:31]\n"
"s_waitcnt lgkmcnt(5)\n"
"buffer_load_dwordx4 v231, s[52:55], s75 offen offset:3072 lds\n"
"v_mfma_f32_16x16x32_bf16 v[32:35], a[4:7], a[192:195], 0\n"
"s_waitcnt lgkmcnt(3)\n"
"ds_read_b64_tr_b16 v[196:197], v250 offset:20488\n"
"v_mul_f32_dpp v209, v216, v209 row_newbcast:4 row_mask:0xf bank_mask:0xf\n"
"v_mfma_f32_16x16x32_bf16 v[32:35], a[52:55], a[208:211], v[32:35]\n"
"s_add_u32 m0, s37, 4096\n"
"v_mul_f32_dpp v210, v216, v210 row_newbcast:8 row_mask:0xf bank_mask:0xf\n"
"v_mul_f32_dpp v211, v216, v211 row_newbcast:12 row_mask:0xf bank_mask:0xf\n"
"v_mfma_f32_16x16x32_bf16 v[80:83], a[52:55], a[212:215], 0\n"
"s_add_u32 s97, s75, s64\n"
"ds_read_b64_tr_b16 v[198:199], v250 offset:21512\n"
"v_cvt_pk_bf16_f32 v211, v210, v211\n"
"v_mfma_f32_16x16x32_bf16 v[80:83], a[4:7], a[196:199], v[80:83]\n"
"buffer_load_dwordx4 v228, s[52:55], s97 offen offset:0 lds\n"
"v_cvt_pk_bf16_f32 v210, v208, v209\n"
"v_mfma_f32_16x16x32_bf16 v[128:131], a[4:7], a[200:203], 0\n"
"ds_read_b64_tr_b16 v[200:201], v250 offset:16640\n"
"ds_write_b64 v244, v[210:211] offset:13312\n"
"v_mfma_f32_16x16x32_bf16 v[128:131], a[52:55], a[216:219], v[128:131]\n"
"v_accvgpr_read_b32 v212, a60\n"
"v_accvgpr_read_b32 v213, a61\n"
"v_mfma_f32_16x16x32_bf16 v[176:179], a[52:55], a[220:223], 0\n"
"ds_read_b64_tr_b16 v[202:203], v250 offset:17664\n"
"v_accvgpr_read_b32 v214, a62\n"
"v_mfma_f32_16x16x32_bf16 v[176:179], a[4:7], a[204:207], v[176:179]\n"
"s_waitcnt lgkmcnt(5)\n"
"buffer_load_dwordx4 v229, s[52:55], s97 offen offset:1024 lds\n"
"v_mfma_f32_16x16x32_bf16 v[180:183], v[192:195], a[204:207], 0\n"
"s_waitcnt lgkmcnt(3)\n"
"ds_read_b64_tr_b16 v[204:205], v250 offset:20736\n"
"v_accvgpr_read_b32 v215, a63\n"
"v_mfma_f32_16x16x32_bf16 v[180:183], v[196:199], a[220:223], v[180:183]\n"
"ds_read_b64_tr_b16 a[8:9], v221 offset:13312\n"
"ds_read_b64_tr_b16 a[10:11], v221 offset:13320\n"
"v_mfma_f32_16x16x32_bf16 v[132:135], v[196:199], a[216:219], 0\n"
"ds_read_b64_tr_b16 v[206:207], v250 offset:21760\n"
"v_mul_f32_dpp v212, v218, v212 row_newbcast:0 row_mask:0xf bank_mask:0xf\n"
"v_mul_f32_dpp v213, v218, v213 row_newbcast:4 row_mask:0xf bank_mask:0xf\n"
"v_mfma_f32_16x16x32_bf16 v[132:135], v[192:195], a[200:203], v[132:135]\n"
"buffer_load_dwordx4 v230, s[52:55], s97 offen offset:2048 lds\n"
"v_mfma_f32_16x16x32_bf16 v[84:87], v[192:195], a[196:199], 0\n"
"ds_read_b64_tr_b16 a[4:5], v250 offset:16648\n"
"v_mul_f32_dpp v214, v218, v214 row_newbcast:8 row_mask:0xf bank_mask:0xf\n"
"v_mfma_f32_16x16x32_bf16 v[84:87], v[196:199], a[212:215], v[84:87]\n"
"v_mul_f32_dpp v215, v218, v215 row_newbcast:12 row_mask:0xf bank_mask:0xf\n"
"v_cvt_pk_bf16_f32 v212, v212, v213\n"
"v_mfma_f32_16x16x32_bf16 v[36:39], v[196:199], a[208:211], 0\n"
"ds_read_b64_tr_b16 a[6:7], v250 offset:17672\n"
"v_cvt_pk_bf16_f32 v213, v214, v215\n"
"v_mfma_f32_16x16x32_bf16 v[36:39], v[192:195], a[192:195], v[36:39]\n"
"s_waitcnt lgkmcnt(6)\n"
"buffer_load_dwordx4 v231, s[52:55], s97 offen offset:3072 lds\n"
"v_mfma_f32_16x16x32_bf16 v[40:43], v[200:203], a[192:195], 0\n"
"s_waitcnt lgkmcnt(2)\n"
"s_add_u32 s75, s75, 128\n"
"ds_read_b64_tr_b16 a[52:53], v250 offset:20744\n"
"ds_write_b64 v244, v[212:213] offset:13320\n"
"v_mfma_f32_16x16x32_bf16 v[40:43], v[204:207], a[208:211], v[40:43]\n"
"s_waitcnt vmcnt(12)\n"
"s_barrier\n"
"v_accvgpr_read_b32 v208, a56\n"
"v_mfma_f32_16x16x32_bf16 v[88:91], v[204:207], a[212:215], 0\n"
"s_mov_b32 m0, s79\n"
"ds_read_b64_tr_b16 a[54:55], v250 offset:21768\n"
"ds_read_b128 a[224:227], v217 offset:0\n"
"v_mfma_f32_16x16x32_bf16 v[88:91], v[200:203], a[196:199], v[88:91]\n"
"buffer_load_dwordx4 v242, s[48:51], s74 offen offset:0 lds\n"
"v_mfma_f32_16x16x32_bf16 v[136:139], v[200:203], a[200:203], 0\n"
"ds_read_b64_tr_b16 v[192:193], v250 offset:24576\n"
"ds_read_b128 a[240:243], v219 offset:0\n"
"v_mfma_f32_16x16x32_bf16 v[136:139], v[204:207], a[216:219], v[136:139]\n"
"buffer_load_dwordx4 v243, s[48:51], s74 offen offset:1024 lds\n"
"v_mfma_f32_16x16x32_bf16 v[184:187], v[204:207], a[220:223], 0\n"
"s_add_u32 s74, s74, 128\n"
"ds_read_b64_tr_b16 v[194:195], v250 offset:25600\n"
"ds_read_b128 a[244:247], v219 offset:2048\n"
"v_mfma_f32_16x16x32_bf16 v[184:187], v[200:203], a[204:207], v[184:187]\n"
"s_waitcnt lgkmcnt(8)\n"
"ds_read_b128 a[228:231], v217 offset:2048\n"
"ds_read_b128 a[232:235], v217 offset:4096\n"
"v_accvgpr_read_b32 v209, a57\n"
"v_mfma_f32_16x16x32_bf16 v[188:191], a[4:7], a[204:207], 0\n"
"s_waitcnt lgkmcnt(7)\n"
"buffer_store_dwordx4 a[8:11], v251, s[56:59], s76 offen offset:1024 sc1\n"
"ds_read_b64_tr_b16 v[196:197], v250 offset:28672\n"
"ds_read_b128 a[248:251], v219 offset:4096\n"
"v_accvgpr_read_b32 v210, a58\n"
"v_mfma_f32_16x16x32_bf16 v[188:191], a[52:55], a[220:223], v[188:191]\n"
"ds_read_b128 a[252:255], v219 offset:6144\n"
"ds_read_b128 a[236:239], v217 offset:6144\n"
"v_mfma_f32_16x16x32_bf16 v[140:143], a[52:55], a[216:219], 0\n"
"s_add_u32 m0, s79, 8192\n"
"ds_read_b64_tr_b16 v[198:199], v250 offset:29696\n"
"v_accvgpr_read_b32 v211, a59\n"
"v_mfma_f32_16x16x32_bf16 v[140:143], a[4:7], a[200:203], v[140:143]\n"
"buffer_load_dwordx4 v242, s[48:51], s74 offen offset:0 lds\n"
"v_mfma_f32_16x16x32_bf16 v[92:95], a[4:7], a[196:199], 0\n"
"ds_read_b64_tr_b16 v[200:201], v250 offset:24584\n"
"v_mul_f32_dpp v208, v218, v208 row_newbcast:0 row_mask:0xf bank_mask:0xf\n"
"v_mfma_f32_16x16x32_bf16 v[92:95], a[52:55], a[212:215], v[92:95]\n"
"buffer_load_dwordx4 v243, s[48:51], s74 offen offset:1024 lds\n"
"v_mfma_f32_16x16x32_bf16 v[44:47], a[52:55], a[208:211], 0\n"
"s_add_u32 s74, s74, 128\n"
"ds_read_b64_tr_b16 v[202:203], v250 offset:25608\n"
"v_mul_f32_dpp v209, v218, v209 row_newbcast:4 row_mask:0xf bank_mask:0xf\n"
"v_mul_f32_dpp v210, v218, v210 row_newbcast:8 row_mask:0xf bank_mask:0xf\n"
"v_mfma_f32_16x16x32_bf16 v[44:47], a[4:7], a[192:195], v[44:47]\n"
"s_waitcnt lgkmcnt(10)\n"
"v_mul_f32_dpp v211, v218, v211 row_newbcast:12 row_mask:0xf bank_mask:0xf\n"
"v_mfma_f32_16x16x32_bf16 v[0:3], v[192:195], a[224:227], v[0:3]\n"
"s_waitcnt lgkmcnt(2)\n"
"ds_read_b64_tr_b16 v[204:205], v250 offset:28680\n"
"v_cvt_pk_bf16_f32 v211, v210, v211\n"
"v_mfma_f32_16x16x32_bf16 v[0:3], v[196:199], a[240:243], v[0:3]\n"
"v_cvt_pk_bf16_f32 v210, v208, v209\n"
"v_accvgpr_read_b32 v212, a108\n"
"v_mfma_f32_16x16x32_bf16 v[48:51], v[196:199], a[244:247], v[48:51]\n"
"s_mov_b32 m0, s68\n"
"ds_read_b64_tr_b16 v[206:207], v250 offset:29704\n"
"ds_write_b64 v244, v[210:211] offset:13312\n"
"v_mfma_f32_16x16x32_bf16 v[48:51], v[192:195], a[228:231], v[48:51]\n"
"buffer_load_dwordx4 v228, s[52:55], s75 offen offset:0 lds\n"
"v_mfma_f32_16x16x32_bf16 v[96:99], v[192:195], a[232:235], v[96:99]\n"
"ds_read_b64_tr_b16 a[4:5], v250 offset:24832\n"
"v_accvgpr_read_b32 v213, a109\n"
"v_mfma_f32_16x16x32_bf16 v[96:99], v[196:199], a[248:251], v[96:99]\n"
"v_accvgpr_read_b32 v214, a110\n"
"v_accvgpr_read_b32 v215, a111\n"
"v_mfma_f32_16x16x32_bf16 v[144:147], v[196:199], a[252:255], v[144:147]\n"
"ds_read_b64_tr_b16 a[6:7], v250 offset:25856\n"
"ds_read_b64_tr_b16 a[56:57], v221 offset:13312\n"
"ds_read_b64_tr_b16 a[58:59], v221 offset:13320\n"
"v_mfma_f32_16x16x32_bf16 v[144:147], v[192:195], a[236:239], v[144:147]\n"
"s_waitcnt lgkmcnt(7)\n"
"buffer_load_dwordx4 v229, s[52:55], s75 offen offset:1024 lds\n"
"v_mfma_f32_16x16x32_bf16 v[148:151], v[200:203], a[236:239], v[148:151]\n"
"s_waitcnt lgkmcnt(5)\n"
"ds_read_b64_tr_b16 a[52:53], v250 offset:28928\n"
"v_mul_f32_dpp v212, v220, v212 row_newbcast:0 row_mask:0xf bank_mask:0xf\n"
"v_mfma_f32_16x16x32_bf16 v[148:151], v[204:207], a[252:255], v[148:151]\n"
"v_mul_f32_dpp v213, v220, v213 row_newbcast:4 row_mask:0xf bank_mask:0xf\n"
"v_mul_f32_dpp v214, v220, v214 row_newbcast:8 row_mask:0xf bank_mask:0xf\n"
"v_mfma_f32_16x16x32_bf16 v[100:103], v[204:207], a[248:251], v[100:103]\n"
"ds_read_b64_tr_b16 a[54:55], v250 offset:29952\n"
"v_mul_f32_dpp v215, v220, v215 row_newbcast:12 row_mask:0xf bank_mask:0xf\n"
"v_mfma_f32_16x16x32_bf16 v[100:103], v[200:203], a[232:235], v[100:103]\n"
"buffer_load_dwordx4 v230, s[52:55], s75 offen offset:2048 lds\n"
"v_mfma_f32_16x16x32_bf16 v[52:55], v[200:203], a[228:231], v[52:55]\n"
"ds_read_b64_tr_b16 v[192:193], v250 offset:24840\n"
"v_cvt_pk_bf16_f32 v212, v212, v213\n"
"v_mfma_f32_16x16x32_bf16 v[52:55], v[204:207], a[244:247], v[52:55]\n"
"v_cvt_pk_bf16_f32 v213, v214, v215\n"
"v_accvgpr_read_b32 v208, a104\n"
"v_accvgpr_read_b32 v209, a105\n"
"v_mfma_f32_16x16x32_bf16 v[4:7], v[204:207], a[240:243], v[4:7]\n"
"ds_read_b64_tr_b16 v[194:195], v250 offset:25864\n"
"ds_write_b64 v244, v[212:213] offset:13320\n"
"v_mfma_f32_16x16x32_bf16 v[4:7], v[200:203], a[224:227], v[4:7]\n"
"s_waitcnt lgkmcnt(7)\n"
"buffer_load_dwordx4 v231, s[52:55], s75 offen offset:3072 lds\n"
"v_mfma_f32_16x16x32_bf16 v[8:11], a[4:7], a[224:227], v[8:11]\n"
"s_waitcnt lgkmcnt(3)\n"
"buffer_store_dwordx4 a[56:59], v251, s[56:59], s76 offen offset:3072 sc1\n"
"ds_read_b64_tr_b16 v[196:197], v250 offset:28936\n"
"v_accvgpr_read_b32 v210, a106\n"
"v_accvgpr_read_b32 v211, a107\n"
"v_mfma_f32_16x16x32_bf16 v[8:11], a[52:55], a[240:243], v[8:11]\n"
"s_add_u32 m0, s68, 4096\n"
"v_mul_f32_dpp v208, v220, v208 row_newbcast:0 row_mask:0xf bank_mask:0xf\n"
"v_mul_f32_dpp v209, v220, v209 row_newbcast:4 row_mask:0xf bank_mask:0xf\n"
"v_mfma_f32_16x16x32_bf16 v[56:59], a[52:55], a[244:247], v[56:59]\n"
"s_add_u32 s97, s75, s64\n"
"ds_read_b64_tr_b16 v[198:199], v250 offset:29960\n"
"v_mul_f32_dpp v210, v220, v210 row_newbcast:8 row_mask:0xf bank_mask:0xf\n"
"v_mfma_f32_16x16x32_bf16 v[56:59], a[4:7], a[228:231], v[56:59]\n"
"buffer_load_dwordx4 v228, s[52:55], s97 offen offset:0 lds\n"
"v_mul_f32_dpp v211, v220, v211 row_newbcast:12 row_mask:0xf bank_mask:0xf\n"
"v_mfma_f32_16x16x32_bf16 v[104:107], a[4:7], a[232:235], v[104:107]\n"
"ds_read_b64_tr_b16 v[200:201], v250 offset:32768\n"
"v_cvt_pk_bf16_f32 v211, v210, v211\n"
"v_mfma_f32_16x16x32_bf16 v[104:107], a[52:55], a[248:251], v[104:107]\n"
"v_cvt_pk_bf16_f32 v210, v208, v209\n"
"v_accvgpr_read_b32 v212, a156\n"
"v_mfma_f32_16x16x32_bf16 v[152:155], a[52:55], a[252:255], v[152:155]\n"
"ds_read_b64_tr_b16 v[202:203], v250 offset:33792\n"
"ds_write_b64 v244, v[210:211] offset:13312\n"
"v_mfma_f32_16x16x32_bf16 v[152:155], a[4:7], a[236:239], v[152:155]\n"
"s_waitcnt lgkmcnt(6)\n"
"buffer_load_dwordx4 v229, s[52:55], s97 offen offset:1024 lds\n"
"v_mfma_f32_16x16x32_bf16 v[156:159], v[192:195], a[236:239], v[156:159]\n"
"s_waitcnt lgkmcnt(3)\n"
"ds_read_b64_tr_b16 v[204:205], v250 offset:36864\n"
"v_accvgpr_read_b32 v213, a157\n"
"v_mfma_f32_16x16x32_bf16 v[156:159], v[196:199], a[252:255], v[156:159]\n"
"v_accvgpr_read_b32 v214, a158\n"
"v_accvgpr_read_b32 v215, a159\n"
"v_mfma_f32_16x16x32_bf16 v[108:111], v[196:199], a[248:251], v[108:111]\n"
"ds_read_b64_tr_b16 v[206:207], v250 offset:37888\n"
"ds_read_b64_tr_b16 a[104:105], v221 offset:13312\n"
"ds_read_b64_tr_b16 a[106:107], v221 offset:13320\n"
"v_mfma_f32_16x16x32_bf16 v[108:111], v[192:195], a[232:235], v[108:111]\n"
"buffer_load_dwordx4 v230, s[52:55], s97 offen offset:2048 lds\n"
"v_mfma_f32_16x16x32_bf16 v[60:63], v[192:195], a[228:231], v[60:63]\n"
"ds_read_b64_tr_b16 a[4:5], v250 offset:32776\n"
"v_mul_f32_dpp v212, v222, v212 row_newbcast:0 row_mask:0xf bank_mask:0xf\n"
"v_mfma_f32_16x16x32_bf16 v[60:63], v[196:199], a[244:247], v[60:63]\n"
"v_mul_f32_dpp v213, v222, v213 row_newbcast:4 row_mask:0xf bank_mask:0xf\n"
"v_mul_f32_dpp v214, v222, v214 row_newbcast:8 row_mask:0xf bank_mask:0xf\n"
"v_mfma_f32_16x16x32_bf16 v[12:15], v[196:199], a[240:243], v[12:15]\n"
"ds_read_b64_tr_b16 a[6:7], v250 offset:33800\n"
"v_mul_f32_dpp v215, v222, v215 row_newbcast:12 row_mask:0xf bank_mask:0xf\n"
"v_mfma_f32_16x16x32_bf16 v[12:15], v[192:195], a[224:227], v[12:15]\n"
"s_waitcnt lgkmcnt(7)\n"
"buffer_load_dwordx4 v231, s[52:55], s97 offen offset:3072 lds\n"
"v_mfma_f32_16x16x32_bf16 v[16:19], v[200:203], a[224:227], v[16:19]\n"
"s_waitcnt lgkmcnt(4)\n"
"s_add_u32 s75, s75, 128\n"
"ds_read_b64_tr_b16 a[52:53], v250 offset:36872\n"
"v_mfma_f32_16x16x32_bf16 v[16:19], v[204:207], a[240:243], v[16:19]\n"
"s_waitcnt vmcnt(24)\n"
"s_barrier\n"
"v_cvt_pk_bf16_f32 v212, v212, v213\n"
"v_mfma_f32_16x16x32_bf16 v[64:67], v[204:207], a[244:247], v[64:67]\n"
"s_add_u32 m0, s79, 16384\n"
"ds_read_b64_tr_b16 a[54:55], v250 offset:37896\n"
"v_cvt_pk_bf16_f32 v213, v214, v215\n"
"v_mfma_f32_16x16x32_bf16 v[64:67], v[200:203], a[228:231], v[64:67]\n"
"buffer_load_dwordx4 v242, s[48:51], s74 offen offset:0 lds\n"
"v_mfma_f32_16x16x32_bf16 v[112:115], v[200:203], a[232:235], v[112:115]\n"
"ds_read_b64_tr_b16 v[192:193], v250 offset:33024\n"
"ds_write_b64 v244, v[212:213] offset:13320\n"
"v_mfma_f32_16x16x32_bf16 v[112:115], v[204:207], a[248:251], v[112:115]\n"
"buffer_load_dwordx4 v243, s[48:51], s74 offen offset:1024 lds\n"
"v_mfma_f32_16x16x32_bf16 v[160:163], v[204:207], a[252:255], v[160:163]\n"
"s_add_u32 s74, s74, 786176\n"
"ds_read_b64_tr_b16 v[194:195], v250 offset:34048\n"
"v_accvgpr_read_b32 v208, a152\n"
"v_mfma_f32_16x16x32_bf16 v[160:163], v[200:203], a[236:239], v[160:163]\n"
"s_waitcnt lgkmcnt(5)\n"
"v_accvgpr_read_b32 v209, a153\n"
"v_accvgpr_read_b32 v210, a154\n"
"v_accvgpr_read_b32 v211, a155\n"
"v_mfma_f32_16x16x32_bf16 v[164:167], a[4:7], a[236:239], v[164:167]\n"
"s_waitcnt lgkmcnt(3)\n"
"buffer_store_dwordx4 a[104:107], v253, s[56:59], s76 offen offset:1024 sc1\n"
"ds_read_b64_tr_b16 v[196:197], v250 offset:37120\n"
"v_mul_f32_dpp v208, v222, v208 row_newbcast:0 row_mask:0xf bank_mask:0xf\n"
"v_mul_f32_dpp v209, v222, v209 row_newbcast:4 row_mask:0xf bank_mask:0xf\n"
"v_mfma_f32_16x16x32_bf16 v[164:167], a[52:55], a[252:255], v[164:167]\n"
"v_mul_f32_dpp v210, v222, v210 row_newbcast:8 row_mask:0xf bank_mask:0xf\n"
"v_mul_f32_dpp v211, v222, v211 row_newbcast:12 row_mask:0xf bank_mask:0xf\n"
"v_mfma_f32_16x16x32_bf16 v[116:119], a[52:55], a[248:251], v[116:119]\n"
"ds_read_b64_tr_b16 v[198:199], v250 offset:38144\n"
"v_cvt_pk_bf16_f32 v211, v210, v211\n"
"v_mfma_f32_16x16x32_bf16 v[116:119], a[4:7], a[232:235], v[116:119]\n"
"v_cvt_pk_bf16_f32 v210, v208, v209\n"
"v_accvgpr_read_b32 v212, a20\n"
"v_mfma_f32_16x16x32_bf16 v[68:71], a[4:7], a[228:231], v[68:71]\n"
"ds_read_b64_tr_b16 v[200:201], v250 offset:33032\n"
"ds_write_b64 v244, v[210:211] offset:13312\n"
"v_mfma_f32_16x16x32_bf16 v[68:71], a[52:55], a[244:247], v[68:71]\n"
"v_accvgpr_read_b32 v213, a21\n"
"v_accvgpr_read_b32 v214, a22\n"
"v_mfma_f32_16x16x32_bf16 v[20:23], a[52:55], a[240:243], v[20:23]\n"
"ds_read_b64_tr_b16 v[202:203], v250 offset:34056\n"
"v_accvgpr_read_b32 v215, a23\n"
"v_mul_f32_dpp v212, v216, v212 row_newbcast:0 row_mask:0xf bank_mask:0xf\n"
"v_mfma_f32_16x16x32_bf16 v[20:23], a[4:7], a[224:227], v[20:23]\n"
"s_waitcnt lgkmcnt(5)\n"
"ds_read_b64_tr_b16 a[152:153], v221 offset:13312\n"
"v_mfma_f32_16x16x32_bf16 v[24:27], v[192:195], a[224:227], v[24:27]\n"
"s_waitcnt lgkmcnt(4)\n"
"ds_read_b64_tr_b16 v[204:205], v250 offset:37128\n"
"ds_read_b64_tr_b16 a[154:155], v221 offset:13320\n"
"v_mfma_f32_16x16x32_bf16 v[24:27], v[196:199], a[240:243], v[24:27]\n"
"v_mul_f32_dpp v213, v216, v213 row_newbcast:4 row_mask:0xf bank_mask:0xf\n"
"v_mul_f32_dpp v214, v216, v214 row_newbcast:8 row_mask:0xf bank_mask:0xf\n"
"v_mfma_f32_16x16x32_bf16 v[72:75], v[196:199], a[244:247], v[72:75]\n"
"s_mov_b32 m0, s25\n"
"ds_read_b64_tr_b16 v[206:207], v250 offset:38152\n"
"v_mul_f32_dpp v215, v216, v215 row_newbcast:12 row_mask:0xf bank_mask:0xf\n"
"v_mfma_f32_16x16x32_bf16 v[72:75], v[192:195], a[228:231], v[72:75]\n"
"buffer_load_dwordx4 v236, s[44:47], s24 offen offset:0 nt lds\n"
"v_mfma_f32_16x16x32_bf16 v[120:123], v[192:195], a[232:235], v[120:123]\n"
"ds_read_b64_tr_b16 a[4:5], v250 offset:40960\n"
"v_cvt_pk_bf16_f32 v212, v212, v213\n"
"v_mfma_f32_16x16x32_bf16 v[120:123], v[196:199], a[248:251], v[120:123]\n"
"v_cvt_pk_bf16_f32 v213, v214, v215\n"
"v_accvgpr_read_b32 v208, a16\n"
"v_mfma_f32_16x16x32_bf16 v[168:171], v[196:199], a[252:255], v[168:171]\n"
"ds_read_b64_tr_b16 a[6:7], v250 offset:41984\n"
"ds_write_b64 v244, v[212:213] offset:13320\n"
"v_accvgpr_read_b32 v209, a17\n"
"v_mfma_f32_16x16x32_bf16 v[168:171], v[192:195], a[236:239], v[168:171]\n"
"s_waitcnt lgkmcnt(7)\n"
"v_accvgpr_read_b32 v210, a18\n"
"v_accvgpr_read_b32 v211, a19\n"
"v_mfma_f32_16x16x32_bf16 v[172:175], v[200:203], a[236:239], v[172:175]\n"
"s_waitcnt lgkmcnt(3)\n"
"buffer_store_dwordx4 a[152:155], v253, s[56:59], s76 offen offset:3072 sc1\n"
"ds_read_b64_tr_b16 a[52:53], v250 offset:45056\n"
"s_add_u32 s76, s76, 524288\n"
"v_mul_f32_dpp v208, v216, v208 row_newbcast:0 row_mask:0xf bank_mask:0xf\n"
"v_mfma_f32_16x16x32_bf16 v[172:175], v[204:207], a[252:255], v[172:175]\n"
"v_mul_f32_dpp v209, v216, v209 row_newbcast:4 row_mask:0xf bank_mask:0xf\n"
"v_mul_f32_dpp v210, v216, v210 row_newbcast:8 row_mask:0xf bank_mask:0xf\n"
"v_mfma_f32_16x16x32_bf16 v[124:127], v[204:207], a[248:251], v[124:127]\n"
"ds_read_b64_tr_b16 a[54:55], v250 offset:46080\n"
"v_mul_f32_dpp v211, v216, v211 row_newbcast:12 row_mask:0xf bank_mask:0xf\n"
"v_mfma_f32_16x16x32_bf16 v[124:127], v[200:203], a[232:235], v[124:127]\n"
"buffer_load_dwordx4 v237, s[44:47], s24 offen offset:1024 nt lds\n"
"v_mfma_f32_16x16x32_bf16 v[76:79], v[200:203], a[228:231], v[76:79]\n"
"ds_read_b64_tr_b16 v[192:193], v250 offset:40968\n"
"v_cvt_pk_bf16_f32 v211, v210, v211\n"
"v_mfma_f32_16x16x32_bf16 v[76:79], v[204:207], a[244:247], v[76:79]\n"
"v_cvt_pk_bf16_f32 v210, v208, v209\n"
"v_accvgpr_read_b32 v212, a68\n"
"v_accvgpr_read_b32 v213, a69\n"
"v_mfma_f32_16x16x32_bf16 v[28:31], v[204:207], a[240:243], v[28:31]\n"
"ds_read_b64_tr_b16 v[194:195], v250 offset:41992\n"
"ds_write_b64 v244, v[210:211] offset:13312\n"
"v_mfma_f32_16x16x32_bf16 v[28:31], v[200:203], a[224:227], v[28:31]\n"
"s_waitcnt lgkmcnt(6)\n"
"v_accvgpr_read_b32 v214, a70\n"
"v_accvgpr_read_b32 v215, a71\n"
"v_mfma_f32_16x16x32_bf16 v[32:35], a[4:7], a[224:227], v[32:35]\n"
"s_waitcnt lgkmcnt(3)\n"
"ds_read_b64_tr_b16 v[196:197], v250 offset:45064\n"
"ds_read_b64_tr_b16 a[16:17], v221 offset:13312\n"
"v_mfma_f32_16x16x32_bf16 v[32:35], a[52:55], a[240:243], v[32:35]\n"
"ds_read_b64_tr_b16 a[18:19], v221 offset:13320\n"
"v_mul_f32_dpp v212, v218, v212 row_newbcast:0 row_mask:0xf bank_mask:0xf\n"
"v_mfma_f32_16x16x32_bf16 v[80:83], a[52:55], a[244:247], v[80:83]\n"
"ds_read_b64_tr_b16 v[198:199], v250 offset:46088\n"
"v_mul_f32_dpp v213, v218, v213 row_newbcast:4 row_mask:0xf bank_mask:0xf\n"
"v_mfma_f32_16x16x32_bf16 v[80:83], a[4:7], a[228:231], v[80:83]\n"
"buffer_load_dwordx4 v238, s[44:47], s24 offen offset:2048 nt lds\n"
"v_mul_f32_dpp v214, v218, v214 row_newbcast:8 row_mask:0xf bank_mask:0xf\n"
"v_mfma_f32_16x16x32_bf16 v[128:131], a[4:7], a[232:235], v[128:131]\n"
"ds_read_b64_tr_b16 v[200:201], v250 offset:41216\n"
"v_mul_f32_dpp v215, v218, v215 row_newbcast:12 row_mask:0xf bank_mask:0xf\n"
"v_mfma_f32_16x16x32_bf16 v[128:131], a[52:55], a[248:251], v[128:131]\n"
"v_cvt_pk_bf16_f32 v212, v212, v213\n"
"v_cvt_pk_bf16_f32 v213, v214, v215\n"
"v_mfma_f32_16x16x32_bf16 v[176:179], a[52:55], a[252:255], v[176:179]\n"
"ds_read_b64_tr_b16 v[202:203], v250 offset:42240\n"
"ds_write_b64 v244, v[212:213] offset:13320\n"
"v_mfma_f32_16x16x32_bf16 v[176:179], a[4:7], a[236:239], v[176:179]\n"
"s_waitcnt lgkmcnt(8)\n"
"v_accvgpr_read_b32 v208, a64\n"
"v_accvgpr_read_b32 v209, a65\n"
"v_mfma_f32_16x16x32_bf16 v[180:183], v[192:195], a[236:239], v[180:183]\n"
"s_waitcnt lgkmcnt(3)\n"
"buffer_store_dwordx4 a[16:19], v251, s[56:59], s76 offen offset:0 sc1\n"
"ds_read_b64_tr_b16 v[204:205], v250 offset:45312\n"
"v_accvgpr_read_b32 v210, a66\n"
"v_accvgpr_read_b32 v211, a67\n"
"v_mfma_f32_16x16x32_bf16 v[180:183], v[196:199], a[252:255], v[180:183]\n"
"v_mul_f32_dpp v208, v218, v208 row_newbcast:0 row_mask:0xf bank_mask:0xf\n"
"v_mul_f32_dpp v209, v218, v209 row_newbcast:4 row_mask:0xf bank_mask:0xf\n"
"v_mfma_f32_16x16x32_bf16 v[132:135], v[196:199], a[248:251], v[132:135]\n"
"ds_read_b64_tr_b16 v[206:207], v250 offset:46336\n"
"v_mul_f32_dpp v210, v218, v210 row_newbcast:8 row_mask:0xf bank_mask:0xf\n"
"v_mul_f32_dpp v211, v218, v211 row_newbcast:12 row_mask:0xf bank_mask:0xf\n"
"v_mfma_f32_16x16x32_bf16 v[132:135], v[192:195], a[232:235], v[132:135]\n"
"v_cvt_pk_bf16_f32 v211, v210, v211\n"
"v_cvt_pk_bf16_f32 v210, v208, v209\n"
"v_mfma_f32_16x16x32_bf16 v[84:87], v[192:195], a[228:231], v[84:87]\n"
"ds_read_b64_tr_b16 a[4:5], v250 offset:41224\n"
"ds_write_b64 v244, v[210:211] offset:13312\n"
"v_mfma_f32_16x16x32_bf16 v[84:87], v[196:199], a[244:247], v[84:87]\n"
"v_mfma_f32_16x16x32_bf16 v[36:39], v[196:199], a[240:243], v[36:39]\n"
"ds_read_b64_tr_b16 a[6:7], v250 offset:42248\n"
"ds_read_b64_tr_b16 a[64:65], v221 offset:13312\n"
"v_mfma_f32_16x16x32_bf16 v[36:39], v[192:195], a[224:227], v[36:39]\n"
"s_waitcnt lgkmcnt(7)\n"
"ds_read_b64_tr_b16 a[66:67], v221 offset:13320\n"
"v_mfma_f32_16x16x32_bf16 v[40:43], v[200:203], a[224:227], v[40:43]\n"
"s_waitcnt lgkmcnt(5)\n"
"ds_read_b64_tr_b16 a[52:53], v250 offset:45320\n"
"v_mfma_f32_16x16x32_bf16 v[40:43], v[204:207], a[240:243], v[40:43]\n"
"s_waitcnt vmcnt(17)\n"
"s_barrier\n"
"v_mfma_f32_16x16x32_bf16 v[88:91], v[204:207], a[244:247], v[88:91]\n"
"s_add_u32 m0, s79, 24576\n"
"ds_read_b64_tr_b16 a[54:55], v250 offset:46344\n"
"ds_read_b128 a[192:195], v232 offset:0\n"
"v_mfma_f32_16x16x32_bf16 v[88:91], v[200:203], a[228:231], v[88:91]\n"
"buffer_load_dwordx4 v242, s[48:51], s74 offen offset:0 lds\n"
"v_mfma_f32_16x16x32_bf16 v[136:139], v[200:203], a[232:235], v[136:139]\n"
"ds_read_b64_tr_b16 v[192:193], v250 offset:0\n"
"ds_read_b128 a[208:211], v233 offset:0\n"
"v_mfma_f32_16x16x32_bf16 v[136:139], v[204:207], a[248:251], v[136:139]\n"
"buffer_load_dwordx4 v243, s[48:51], s74 offen offset:1024 lds\n"
"v_mfma_f32_16x16x32_bf16 v[184:187], v[204:207], a[252:255], v[184:187]\n"
"s_waitcnt lgkmcnt(5)\n"
"buffer_store_dwordx4 a[64:67], v251, s[56:59], s76 offen offset:2048 sc1\n"
"s_add_u32 s74, s74, 128\n"
"ds_read_b64_tr_b16 v[194:195], v250 offset:1024\n"
"ds_read_b128 a[212:215], v233 offset:2048\n"
"v_mfma_f32_16x16x32_bf16 v[184:187], v[200:203], a[236:239], v[184:187]\n"
"ds_read_b128 a[196:199], v232 offset:2048\n"
"ds_read_b128 a[200:203], v232 offset:4096\n"
"v_mfma_f32_16x16x32_bf16 v[188:191], a[4:7], a[236:239], v[188:191]\n"
"s_waitcnt lgkmcnt(7)\n"
"ds_read_b64_tr_b16 v[196:197], v250 offset:4096\n"
"ds_read_b128 a[216:219], v233 offset:4096\n"
"v_mfma_f32_16x16x32_bf16 v[188:191], a[52:55], a[252:255], v[188:191]\n"
"ds_read_b128 a[220:223], v233 offset:6144\n"
"ds_read_b128 a[204:207], v232 offset:6144\n"
"v_mfma_f32_16x16x32_bf16 v[140:143], a[52:55], a[248:251], v[140:143]\n"
"s_add_u32 m0, s79, 32768\n"
"ds_read_b64_tr_b16 v[198:199], v250 offset:5120\n"
"v_mfma_f32_16x16x32_bf16 v[140:143], a[4:7], a[232:235], v[140:143]\n"
"buffer_load_dwordx4 v242, s[48:51], s74 offen offset:0 lds\n"
"v_mfma_f32_16x16x32_bf16 v[92:95], a[4:7], a[228:231], v[92:95]\n"
"ds_read_b64_tr_b16 v[200:201], v250 offset:8\n"
"v_mfma_f32_16x16x32_bf16 v[92:95], a[52:55], a[244:247], v[92:95]\n"
"buffer_load_dwordx4 v243, s[48:51], s74 offen offset:1024 lds\n"
"v_mfma_f32_16x16x32_bf16 v[44:47], a[52:55], a[240:243], v[44:47]\n"
"s_add_u32 s74, s74, 128\n"
"ds_read_b64_tr_b16 v[202:203], v250 offset:1032\n"
"v_mfma_f32_16x16x32_bf16 v[44:47], a[4:7], a[224:227], v[44:47]\n"
"s_branch .Lat_q\n"
".Lat_drainB:\n"
"v_xor_b32 v211, s13, v211\n"
"v_xor_b32 v208, s13, v208\n"
"v_xor_b32 v209, s13, v209\n"
"v_xor_b32 v210, s13, v210\n"
"v_or_b32 v211, v211, v208\n"
"v_or3_b32 v211, v211, v209, v210\n"
"v_cmp_ne_u32 vcc, 0, v211\n"
"v_readlane_b32 s92, v255, 2\n"
"v_readlane_b32 s61, v255, 27\n"
"s_nop 2\n"
"s_cbranch_vccz .Lat_h0ok\n"
"v_readlane_b32 s12, v255, 10\n"
"s_nop 1\n"
"v_mov_b32 v216, s12\n"
"s_mov_b32 s36, 0\n"
".Lat_pollh0:\n"
"global_load_dwordx4 v[208:211], v216, s[14:15] sc1\n"
"s_waitcnt vmcnt(0)\n"
"v_cmp_ne_u32 vcc, s13, v208\n"
"s_nop 1\n"
"s_mov_b32 s12, vcc_lo\n"
"v_cmp_ne_u32 vcc, s13, v209\n"
"s_nop 1\n"
"s_or_b32 s12, s12, vcc_lo\n"
"v_cmp_ne_u32 vcc, s13, v210\n"
"s_nop 1\n"
"s_or_b32 s12, s12, vcc_lo\n"
"v_cmp_ne_u32 vcc, s13, v211\n"
"s_nop 1\n"
"s_or_b32 s12, s12, vcc_lo\n"
"s_cmp_eq_u32 s12, 0\n"
"s_cbranch_scc1 .Lat_pdoneh0\n"
"s_add_u32 s36, s36, 1\n"
"s_sleep 1\n"
"s_cmp_lt_u32 s36, 1048576\n"
"s_cbranch_scc1 .Lat_pollh0\n"
".Lat_pdoneh0:\n"
".Lat_h0ok:\n"
"v_add_f32 v206, v206, v194\n"
"v_exp_f32 v195, v107\n"
"v_exp_f32 v196, v52\n"
"v_add_f32 v206, v206, v195\n"
"v_cvt_pk_bf16_f32 v89, v194, v195\n"
"v_add_f32 v206, v206, v196\n"
"v_exp_f32 v197, v53\n"
"v_exp_f32 v198, v54\n"
"s_waitcnt lgkmcnt(6)\n"
"v_mfma_f32_16x16x32_bf16 a[24:27], v[112:115], v[160:163], a[24:27]\n"
"s_waitcnt lgkmcnt(4)\n"
"s_add_u32 s72, s92, 0\n"
"ds_read_b64_tr_b16 v[176:177], v247 offset:12544\n"
"v_mfma_f32_16x16x32_bf16 a[24:27], v[32:35], v[164:167], a[24:27]\n"
"buffer_load_dwordx4 a[192:195], v251, s[56:59], s72 offen offset:0 sc1\n"
"v_mfma_f32_16x16x32_bf16 a[72:75], v[40:43], v[164:167], a[72:75]\n"
"ds_read_b64_tr_b16 v[178:179], v247 offset:18688\n"
"v_add_f32 v206, v206, v197\n"
"v_mfma_f32_16x16x32_bf16 a[72:75], v[116:119], v[160:163], a[72:75]\n"
"v_cvt_pk_bf16_f32 v90, v196, v197\n"
"v_add_f32 v206, v206, v198\n"
"v_mfma_f32_16x16x32_bf16 a[120:123], v[120:123], v[160:163], a[120:123]\n"
"ds_read_b64_tr_b16 v[180:181], v247 offset:24832\n"
"v_mfma_f32_16x16x32_bf16 a[120:123], v[48:51], v[164:167], a[120:123]\n"
"buffer_load_dwordx4 a[196:199], v251, s[56:59], s72 offen offset:1024 sc1\n"
"v_mfma_f32_16x16x32_bf16 a[168:171], v[56:59], v[164:167], a[168:171]\n"
"ds_read_b64_tr_b16 v[182:183], v247 offset:30976\n"
"v_mfma_f32_16x16x32_bf16 a[168:171], v[124:127], v[160:163], a[168:171]\n"
"s_waitcnt lgkmcnt(6)\n"
"v_exp_f32 v199, v55\n"
"v_mfma_f32_16x16x32_bf16 a[172:175], v[124:127], v[168:171], a[172:175]\n"
"s_waitcnt lgkmcnt(4)\n"
"ds_read_b64_tr_b16 v[184:185], v248 offset:12544\n"
"v_add_f32 v206, v206, v199\n"
"v_mfma_f32_16x16x32_bf16 a[172:175], v[56:59], v[172:175], a[172:175]\n"
"buffer_load_dwordx4 a[200:203], v251, s[56:59], s72 offen offset:2048 sc1\n"
"v_mfma_f32_16x16x32_bf16 a[124:127], v[48:51], v[172:175], a[124:127]\n"
"ds_read_b64_tr_b16 v[186:187], v248 offset:18688\n"
"v_exp_f32 v192, v108\n"
"v_mfma_f32_16x16x32_bf16 a[124:127], v[120:123], v[168:171], a[124:127]\n"
"v_cvt_pk_bf16_f32 v91, v198, v199\n"
"v_add_f32 v207, v203, v192\n"
"v_mfma_f32_16x16x32_bf16 a[76:79], v[116:119], v[168:171], a[76:79]\n"
"ds_read_b64_tr_b16 v[188:189], v248 offset:24832\n"
"v_mfma_f32_16x16x32_bf16 a[76:79], v[40:43], v[172:175], a[76:79]\n"
"buffer_load_dwordx4 a[204:207], v251, s[56:59], s72 offen offset:3072 sc1\n"
"v_mfma_f32_16x16x32_bf16 a[28:31], v[32:35], v[172:175], a[28:31]\n"
"ds_read_b64_tr_b16 v[190:191], v248 offset:30976\n"
"v_mfma_f32_16x16x32_bf16 a[28:31], v[112:115], v[168:171], a[28:31]\n"
"s_waitcnt lgkmcnt(6)\n"
"v_exp_f32 v193, v109\n"
"v_mfma_f32_16x16x32_bf16 a[32:35], v[112:115], v[176:179], a[32:35]\n"
"s_waitcnt lgkmcnt(4)\n"
"ds_read_b64_tr_b16 v[160:161], v239 offset:12544\n"
"v_mfma_f32_16x16x32_bf16 a[32:35], v[32:35], v[180:183], a[32:35]\n"
"buffer_load_dwordx4 a[208:211], v253, s[56:59], s72 offen offset:0 sc1\n"
"v_mfma_f32_16x16x32_bf16 a[80:83], v[40:43], v[180:183], a[80:83]\n"
"ds_read_b64_tr_b16 v[162:163], v239 offset:18688\n"
"v_mfma_f32_16x16x32_bf16 a[80:83], v[116:119], v[176:179], a[80:83]\n"
"v_exp_f32 v194, v110\n"
"v_mfma_f32_16x16x32_bf16 a[128:131], v[120:123], v[176:179], a[128:131]\n"
"ds_read_b64_tr_b16 v[164:165], v239 offset:24832\n"
"v_add_f32 v207, v207, v193\n"
"v_cvt_pk_bf16_f32 v92, v192, v193\n"
"v_mfma_f32_16x16x32_bf16 a[128:131], v[48:51], v[180:183], a[128:131]\n"
"buffer_load_dwordx4 a[212:215], v253, s[56:59], s72 offen offset:1024 sc1\n"
"v_mfma_f32_16x16x32_bf16 a[176:179], v[56:59], v[180:183], a[176:179]\n"
"ds_read_b64_tr_b16 v[166:167], v239 offset:30976\n"
"v_add_f32 v207, v207, v194\n"
"v_mfma_f32_16x16x32_bf16 a[176:179], v[124:127], v[176:179], a[176:179]\n"
"s_waitcnt lgkmcnt(6)\n"
"v_exp_f32 v195, v111\n"
"v_mfma_f32_16x16x32_bf16 a[180:183], v[124:127], v[184:187], a[180:183]\n"
"s_waitcnt lgkmcnt(4)\n"
"ds_read_b64_tr_b16 v[168:169], v240 offset:12544\n"
"v_mfma_f32_16x16x32_bf16 a[180:183], v[56:59], v[188:191], a[180:183]\n"
"buffer_load_dwordx4 a[216:219], v253, s[56:59], s72 offen offset:2048 sc1\n"
"v_mfma_f32_16x16x32_bf16 a[132:135], v[48:51], v[188:191], a[132:135]\n"
"ds_read_b64_tr_b16 v[170:171], v240 offset:18688\n"
"v_mfma_f32_16x16x32_bf16 a[132:135], v[120:123], v[184:187], a[132:135]\n"
"v_exp_f32 v196, v60\n"
"v_mfma_f32_16x16x32_bf16 a[84:87], v[116:119], v[184:187], a[84:87]\n"
"ds_read_b64_tr_b16 v[172:173], v240 offset:24832\n"
"v_add_f32 v207, v207, v195\n"
"v_mfma_f32_16x16x32_bf16 a[84:87], v[40:43], v[188:191], a[84:87]\n"
"buffer_load_dwordx4 a[220:223], v253, s[56:59], s72 offen offset:3072 sc1\n"
"v_mfma_f32_16x16x32_bf16 a[36:39], v[32:35], v[188:191], a[36:39]\n"
"ds_read_b64_tr_b16 v[174:175], v240 offset:30976\n"
"v_cvt_pk_bf16_f32 v93, v194, v195\n"
"v_add_f32 v207, v207, v196\n"
"v_mfma_f32_16x16x32_bf16 a[36:39], v[112:115], v[184:187], a[36:39]\n"
"s_waitcnt lgkmcnt(6)\n"
"v_exp_f32 v197, v61\n"
"v_mfma_f32_16x16x32_bf16 a[40:43], v[112:115], v[160:163], a[40:43]\n"
"s_waitcnt lgkmcnt(4)\n"
"v_add_f32 v207, v207, v197\n"
"v_mfma_f32_16x16x32_bf16 a[40:43], v[32:35], v[164:167], a[40:43]\n"
"v_exp_f32 v198, v62\n"
"v_mfma_f32_16x16x32_bf16 a[88:91], v[40:43], v[164:167], a[88:91]\n"
"v_cvt_pk_bf16_f32 v94, v196, v197\n"
"v_add_f32 v207, v207, v198\n"
"v_mfma_f32_16x16x32_bf16 a[88:91], v[116:119], v[160:163], a[88:91]\n"
"s_waitcnt vmcnt(13) lgkmcnt(0)\n"
"s_barrier\n"
"v_mfma_f32_16x16x32_bf16 a[136:139], v[120:123], v[160:163], a[136:139]\n"
"ds_read_b64_tr_b16 v[176:177], v247 offset:36864\n"
"v_mfma_f32_16x16x32_bf16 a[136:139], v[48:51], v[164:167], a[136:139]\n"
"ds_read_b64_tr_b16 v[178:179], v247 offset:43008\n"
"v_mfma_f32_16x16x32_bf16 a[184:187], v[56:59], v[164:167], a[184:187]\n"
"ds_read_b64_tr_b16 v[180:181], v248 offset:36864\n"
"v_mfma_f32_16x16x32_bf16 a[184:187], v[124:127], v[160:163], a[184:187]\n"
"ds_read_b64_tr_b16 v[182:183], v248 offset:43008\n"
"v_mfma_f32_16x16x32_bf16 a[188:191], v[124:127], v[168:171], a[188:191]\n"
"ds_read_b64_tr_b16 v[184:185], v239 offset:36864\n"
"v_exp_f32 v199, v63\n"
"v_mfma_f32_16x16x32_bf16 a[188:191], v[56:59], v[172:175], a[188:191]\n"
"v_add_f32 v207, v207, v199\n"
"v_cvt_pk_bf16_f32 v95, v198, v199\n"
"v_mfma_f32_16x16x32_bf16 a[140:143], v[48:51], v[172:175], a[140:143]\n"
"ds_read_b64_tr_b16 v[186:187], v239 offset:43008\n"
"v_add_f32 v212, v204, v205\n"
"v_mfma_f32_16x16x32_bf16 a[140:143], v[120:123], v[168:171], a[140:143]\n"
"v_add_f32 v213, v206, v207\n"
"v_add_f32 v212, v212, v213\n"
"v_mfma_f32_16x16x32_bf16 a[92:95], v[116:119], v[168:171], a[92:95]\n"
"ds_read_b64_tr_b16 v[188:189], v240 offset:36864\n"
"v_cmp_nge_f32 vcc, s90, v212\n"
"v_mfma_f32_16x16x32_bf16 a[92:95], v[40:43], v[172:175], a[92:95]\n"
"ds_read_b64_tr_b16 v[190:191], v240 offset:43008\n"
"v_mfma_f32_16x16x32_bf16 a[44:47], v[32:35], v[172:175], a[44:47]\n"
"v_mfma_f32_16x16x32_bf16 a[44:47], v[112:115], v[168:171], a[44:47]\n"
"s_nop 0\n"
"s_cbranch_vccnz .Lat_tr7\n"
".Lat_bk7:\n"
"s_waitcnt lgkmcnt(6)\n"
"v_mfma_f32_16x16x32_bf16 a[0:3], v[80:83], v[176:179], a[0:3]\n"
"s_waitcnt lgkmcnt(4)\n"
"s_add_u32 s72, s92, 524288\n"
"ds_read_b64_tr_b16 v[160:161], v247 offset:36992\n"
"v_mfma_f32_16x16x32_bf16 a[4:7], v[80:83], v[180:183], a[4:7]\n"
"buffer_load_dwordx4 a[224:227], v251, s[56:59], s72 offen offset:0 sc1\n"
"v_mfma_f32_16x16x32_bf16 a[52:55], v[84:87], v[180:183], a[52:55]\n"
"ds_read_b64_tr_b16 v[162:163], v247 offset:43136\n"
"v_permlane16_swap_b32 v204, v205\n"
"v_mfma_f32_16x16x32_bf16 a[48:51], v[84:87], v[176:179], a[48:51]\n"
"buffer_load_dwordx4 a[228:231], v251, s[56:59], s72 offen offset:1024 sc1\n"
"v_mfma_f32_16x16x32_bf16 a[96:99], v[88:91], v[176:179], a[96:99]\n"
"ds_read_b64_tr_b16 v[164:165], v248 offset:36992\n"
"v_permlane16_swap_b32 v206, v207\n"
"v_mfma_f32_16x16x32_bf16 a[100:103], v[88:91], v[180:183], a[100:103]\n"
"buffer_load_dwordx4 a[232:235], v251, s[56:59], s72 offen offset:2048 sc1\n"
"v_mfma_f32_16x16x32_bf16 a[148:151], v[92:95], v[180:183], a[148:151]\n"
"ds_read_b64_tr_b16 v[166:167], v248 offset:43136\n"
"v_add_f32 v204, v204, v205\n"
"v_mfma_f32_16x16x32_bf16 a[144:147], v[92:95], v[176:179], a[144:147]\n"
"s_waitcnt lgkmcnt(6)\n"
"v_add_f32 v206, v206, v207\n"
"v_lshrrev_b32 v205, 4, v251\n"
"v_mfma_f32_16x16x32_bf16 a[152:155], v[92:95], v[184:187], a[152:155]\n"
"s_waitcnt lgkmcnt(4)\n"
"ds_read_b64_tr_b16 v[168:169], v239 offset:36992\n"
"v_permlane32_swap_b32 v204, v206\n"
"v_mfma_f32_16x16x32_bf16 a[156:159], v[92:95], v[188:191], a[156:159]\n"
"buffer_load_dwordx4 a[236:239], v251, s[56:59], s72 offen offset:3072 sc1\n"
"v_mfma_f32_16x16x32_bf16 a[108:111], v[88:91], v[188:191], a[108:111]\n"
"ds_read_b64_tr_b16 v[170:171], v239 offset:43136\n"
"v_add_f32 v204, v204, v206\n"
"v_mfma_f32_16x16x32_bf16 a[104:107], v[88:91], v[184:187], a[104:107]\n"
"buffer_load_dwordx4 a[240:243], v253, s[56:59], s72 offen offset:0 sc1\n"
"v_mfma_f32_16x16x32_bf16 a[56:59], v[84:87], v[184:187], a[56:59]\n"
"ds_read_b64_tr_b16 v[172:173], v240 offset:36992\n"
"v_mfma_f32_16x16x32_bf16 a[60:63], v[84:87], v[188:191], a[60:63]\n"
"buffer_load_dwordx4 a[244:247], v253, s[56:59], s72 offen offset:1024 sc1\n"
"v_mfma_f32_16x16x32_bf16 a[12:15], v[80:83], v[188:191], a[12:15]\n"
"ds_read_b64_tr_b16 v[174:175], v240 offset:43136\n"
"v_mfma_f32_16x16x32_bf16 a[8:11], v[80:83], v[184:187], a[8:11]\n"
"s_waitcnt lgkmcnt(6)\n"
"v_rcp_f32 v204, v204\n"
"v_mfma_f32_16x16x32_bf16 a[16:19], v[80:83], v[160:163], a[16:19]\n"
"s_waitcnt lgkmcnt(4)\n"
"ds_read_b64_tr_b16 v[176:177], v247 offset:37120\n"
"ds_bpermute_b32 v216, v205, v204 offset:0\n"
"v_mfma_f32_16x16x32_bf16 a[20:23], v[80:83], v[164:167], a[20:23]\n"
"buffer_load_dwordx4 a[248:251], v253, s[56:59], s72 offen offset:2048 sc1\n"
"v_mfma_f32_16x16x32_bf16 a[68:71], v[84:87], v[164:167], a[68:71]\n"
"ds_read_b64_tr_b16 v[178:179], v247 offset:43264\n"
"v_accvgpr_read_b32 v156, a4\n"
"v_mfma_f32_16x16x32_bf16 a[64:67], v[84:87], v[160:163], a[64:67]\n"
"buffer_load_dwordx4 a[252:255], v253, s[56:59], s72 offen offset:3072 sc1\n"
"v_mfma_f32_16x16x32_bf16 a[112:115], v[88:91], v[160:163], a[112:115]\n"
"ds_read_b64_tr_b16 v[180:181], v248 offset:37120\n"
"v_add_u32 v228, 61440, v235\n"
"v_mfma_f32_16x16x32_bf16 a[116:119], v[88:91], v[164:167], a[116:119]\n"
"v_accvgpr_read_b32 v157, a5\n"
"v_accvgpr_read_b32 v158, a6\n"
"v_mfma_f32_16x16x32_bf16 a[164:167], v[92:95], v[164:167], a[164:167]\n"
"ds_read_b64_tr_b16 v[182:183], v248 offset:43264\n"
"v_add_u32 v229, 61440, v249\n"
"v_mfma_f32_16x16x32_bf16 a[160:163], v[92:95], v[160:163], a[160:163]\n"
"s_waitcnt lgkmcnt(7)\n"
"v_accvgpr_read_b32 v159, a7\n"
"v_mfma_f32_16x16x32_bf16 a[168:171], v[92:95], v[168:171], a[168:171]\n"
"s_waitcnt lgkmcnt(5)\n"
"ds_read_b64_tr_b16 v[184:185], v239 offset:37120\n"
"v_add_u32 v230, 122880, v235\n"
"v_mfma_f32_16x16x32_bf16 a[172:175], v[92:95], v[172:175], a[172:175]\n"
"s_waitcnt lgkmcnt(4)\n"
"v_mul_f32_dpp v156, v216, v156 row_newbcast:0 row_mask:0xf bank_mask:0xf\n"
"v_mul_f32_dpp v157, v216, v157 row_newbcast:4 row_mask:0xf bank_mask:0xf\n"
"v_mfma_f32_16x16x32_bf16 a[124:127], v[88:91], v[172:175], a[124:127]\n"
"ds_read_b64_tr_b16 v[186:187], v239 offset:43264\n"
"v_add_u32 v231, 122880, v249\n"
"v_mfma_f32_16x16x32_bf16 a[120:123], v[88:91], v[168:171], a[120:123]\n"
"v_mul_f32_dpp v158, v216, v158 row_newbcast:8 row_mask:0xf bank_mask:0xf\n"
"v_mul_f32_dpp v159, v216, v159 row_newbcast:12 row_mask:0xf bank_mask:0xf\n"
"v_mfma_f32_16x16x32_bf16 a[72:75], v[84:87], v[168:171], a[72:75]\n"
"ds_read_b64_tr_b16 v[188:189], v240 offset:37120\n"
"v_readlane_b32 s19, v255, 0\n"
"v_mfma_f32_16x16x32_bf16 a[76:79], v[84:87], v[172:175], a[76:79]\n"
"v_cvt_pk_bf16_f32 v156, v156, v157\n"
"v_cvt_pk_bf16_f32 v157, v158, v159\n"
"v_mfma_f32_16x16x32_bf16 a[28:31], v[80:83], v[172:175], a[28:31]\n"
"ds_read_b64_tr_b16 v[190:191], v240 offset:43264\n"
"v_readlane_b32 s79, v255, 26\n"
"v_mfma_f32_16x16x32_bf16 a[24:27], v[80:83], v[168:171], a[24:27]\n"
"s_waitcnt lgkmcnt(6)\n"
"ds_bpermute_b32 v218, v205, v204 offset:64\n"
"v_mfma_f32_16x16x32_bf16 a[32:35], v[80:83], v[176:179], a[32:35]\n"
"s_waitcnt lgkmcnt(5)\n"
"ds_write_b64 v244, v[156:157] offset:13320\n"
"v_mfma_f32_16x16x32_bf16 a[36:39], v[80:83], v[180:183], a[36:39]\n"
"v_accvgpr_read_b32 v152, a0\n"
"v_accvgpr_read_b32 v153, a1\n"
"v_mfma_f32_16x16x32_bf16 a[84:87], v[84:87], v[180:183], a[84:87]\n"
"v_accvgpr_read_b32 v154, a2\n"
"v_accvgpr_read_b32 v155, a3\n"
"v_mfma_f32_16x16x32_bf16 a[80:83], v[84:87], v[176:179], a[80:83]\n"
"ds_bpermute_b32 v220, v205, v204 offset:128\n"
"v_mfma_f32_16x16x32_bf16 a[128:131], v[88:91], v[176:179], a[128:131]\n"
"ds_read_b64_tr_b16 v[160:161], v228 offset:40960\n"
"v_mul_f32_dpp v152, v216, v152 row_newbcast:0 row_mask:0xf bank_mask:0xf\n"
"v_mfma_f32_16x16x32_bf16 a[132:135], v[88:91], v[180:183], a[132:135]\n"
"v_mul_f32_dpp v153, v216, v153 row_newbcast:4 row_mask:0xf bank_mask:0xf\n"
"v_mul_f32_dpp v154, v216, v154 row_newbcast:8 row_mask:0xf bank_mask:0xf\n"
"v_mfma_f32_16x16x32_bf16 a[180:183], v[92:95], v[180:183], a[180:183]\n"
"ds_read_b64_tr_b16 v[162:163], v228 offset:46080\n"
"ds_bpermute_b32 v222, v205, v204 offset:192\n"
"v_mfma_f32_16x16x32_bf16 a[176:179], v[92:95], v[176:179], a[176:179]\n"
"s_waitcnt lgkmcnt(8)\n"
"v_mul_f32_dpp v155, v216, v155 row_newbcast:12 row_mask:0xf bank_mask:0xf\n"
"v_cvt_pk_bf16_f32 v155, v154, v155\n"
"v_mfma_f32_16x16x32_bf16 a[184:187], v[92:95], v[184:187], a[184:187]\n"
"s_waitcnt lgkmcnt(6)\n"
"ds_read_b64_tr_b16 v[164:165], v228 offset:51200\n"
"v_cvt_pk_bf16_f32 v154, v152, v153\n"
"v_mfma_f32_16x16x32_bf16 a[188:191], v[92:95], v[188:191], a[188:191]\n"
"ds_write_b64 v244, v[154:155] offset:13312\n"
"v_mfma_f32_16x16x32_bf16 a[140:143], v[88:91], v[188:191], a[140:143]\n"
"ds_read_b64_tr_b16 v[166:167], v228 offset:56320\n"
"v_accvgpr_read_b32 v148, a52\n"
"v_mfma_f32_16x16x32_bf16 a[136:139], v[88:91], v[184:187], a[136:139]\n"
"v_accvgpr_read_b32 v149, a53\n"
"v_accvgpr_read_b32 v150, a54\n"
"v_mfma_f32_16x16x32_bf16 a[88:91], v[84:87], v[184:187], a[88:91]\n"
"ds_read_b64_tr_b16 v[168:169], v229 offset:40960\n"
"v_accvgpr_read_b32 v151, a55\n"
"v_mfma_f32_16x16x32_bf16 a[92:95], v[84:87], v[188:191], a[92:95]\n"
"s_waitcnt vmcnt(8)\n"
"ds_read_b64_tr_b16 a[0:1], v221 offset:13312\n"
"ds_read_b64_tr_b16 a[2:3], v221 offset:13320\n"
"v_mfma_f32_16x16x32_bf16 a[44:47], v[80:83], v[188:191], a[44:47]\n"
"ds_read_b64_tr_b16 v[170:171], v229 offset:46080\n"
"v_mfma_f32_16x16x32_bf16 a[40:43], v[80:83], v[184:187], a[40:43]\n"
"s_waitcnt lgkmcnt(12)\n"
"v_mul_f32_dpp v148, v218, v148 row_newbcast:0 row_mask:0xf bank_mask:0xf\n"
"s_waitcnt lgkmcnt(8)\n"
"v_mfma_f32_16x16x32_bf16 v[0:3], a[192:195], v[160:163], 0\n"
"s_waitcnt lgkmcnt(4)\n"
"ds_read_b64_tr_b16 v[172:173], v229 offset:51200\n"
"v_mul_f32_dpp v149, v218, v149 row_newbcast:4 row_mask:0xf bank_mask:0xf\n"
"v_mfma_f32_16x16x32_bf16 v[0:3], a[196:199], v[164:167], v[0:3]\n"
"v_readlane_b32 s18, v255, 1\n"
"v_mul_f32_dpp v150, v218, v150 row_newbcast:8 row_mask:0xf bank_mask:0xf\n"
"v_mfma_f32_16x16x32_bf16 v[40:43], a[204:207], v[164:167], 0\n"
"ds_read_b64_tr_b16 v[174:175], v229 offset:56320\n"
"v_mul_f32_dpp v151, v218, v151 row_newbcast:12 row_mask:0xf bank_mask:0xf\n"
"v_mfma_f32_16x16x32_bf16 v[40:43], a[200:203], v[160:163], v[40:43]\n"
"v_readlane_b32 s12, v255, 3\n"
"v_cvt_pk_bf16_f32 v148, v148, v149\n"
"v_mfma_f32_16x16x32_bf16 v[80:83], a[208:211], v[160:163], 0\n"
"ds_read_b64_tr_b16 v[200:201], v228 offset:41216\n"
"v_cvt_pk_bf16_f32 v149, v150, v151\n"
"v_mfma_f32_16x16x32_bf16 v[80:83], a[212:215], v[164:167], v[80:83]\n"
"s_add_u32 s18, s18, s19\n"
"ds_write_b64 v244, v[148:149] offset:13320\n"
"v_mfma_f32_16x16x32_bf16 v[120:123], a[220:223], v[164:167], 0\n"
"s_add_u32 s18, s18, 163840\n"
"ds_read_b64_tr_b16 v[202:203], v228 offset:46336\n"
"v_accvgpr_read_b32 v144, a48\n"
"v_mfma_f32_16x16x32_bf16 v[120:123], a[216:219], v[160:163], v[120:123]\n"
"s_waitcnt lgkmcnt(5)\n"
"v_accvgpr_read_b32 v145, a49\n"
"v_accvgpr_read_b32 v146, a50\n"
"v_mfma_f32_16x16x32_bf16 v[124:127], a[216:219], v[168:171], 0\n"
"s_waitcnt lgkmcnt(3)\n"
"buffer_store_dwordx4 a[0:3], v251, s[56:59], s76 offen offset:0 sc1\n"
"ds_read_b64_tr_b16 v[160:161], v228 offset:51456\n"
"v_accvgpr_read_b32 v147, a51\n"
"v_mul_f32_dpp v144, v218, v144 row_newbcast:0 row_mask:0xf bank_mask:0xf\n"
"v_mfma_f32_16x16x32_bf16 v[124:127], a[220:223], v[172:175], v[124:127]\n"
"s_cmp_le_u32 s12, 0\n"
"s_cselect_b32 s36, 15, 12\n"
"v_mul_f32_dpp v145, v218, v145 row_newbcast:4 row_mask:0xf bank_mask:0xf\n"
"v_mfma_f32_16x16x32_bf16 v[84:87], a[212:215], v[172:175], 0\n"
"s_mul_i32 s86, s36, 81920\n"
"ds_read_b64_tr_b16 v[162:163], v228 offset:56576\n"
"v_mul_f32_dpp v146, v218, v146 row_newbcast:8 row_mask:0xf bank_mask:0xf\n"
"v_mfma_f32_16x16x32_bf16 v[84:87], a[208:211], v[168:171], v[84:87]\n"
"s_mul_i32 s36, s36, 524288\n"
"v_mul_f32_dpp v147, v218, v147 row_newbcast:12 row_mask:0xf bank_mask:0xf\n"
"v_cvt_pk_bf16_f32 v147, v146, v147\n"
"v_mfma_f32_16x16x32_bf16 v[44:47], a[200:203], v[168:171], 0\n"
"s_add_u32 s86, s86, s19\n"
"ds_read_b64_tr_b16 v[164:165], v229 offset:41216\n"
"v_cvt_pk_bf16_f32 v146, v144, v145\n"
"v_mfma_f32_16x16x32_bf16 v[44:47], a[204:207], v[172:175], v[44:47]\n"
"s_add_u32 s53, s36, s92\n"
"ds_write_b64 v244, v[146:147] offset:13312\n"
"v_mfma_f32_16x16x32_bf16 v[4:7], a[196:199], v[172:175], 0\n"
"s_cmp_le_u32 s12, 1\n"
"s_cselect_b32 s36, 18, 15\n"
"ds_read_b64_tr_b16 v[166:167], v229 offset:46336\n"
"v_mfma_f32_16x16x32_bf16 v[4:7], a[192:195], v[168:171], v[4:7]\n"
"s_waitcnt lgkmcnt(5)\n"
"v_accvgpr_read_b32 v180, a100\n"
"v_accvgpr_read_b32 v181, a101\n"
"v_mfma_f32_16x16x32_bf16 v[8:11], a[192:195], v[200:203], 0\n"
"s_waitcnt lgkmcnt(3)\n"
"ds_read_b64_tr_b16 v[168:169], v229 offset:51456\n"
"v_accvgpr_read_b32 v182, a102\n"
"v_mfma_f32_16x16x32_bf16 v[8:11], a[196:199], v[160:163], v[8:11]\n"
"s_mul_i32 s87, s36, 81920\n"
"v_accvgpr_read_b32 v183, a103\n"
"v_mul_f32_dpp v180, v220, v180 row_newbcast:0 row_mask:0xf bank_mask:0xf\n"
"v_mfma_f32_16x16x32_bf16 v[48:51], a[204:207], v[160:163], 0\n"
"s_mul_i32 s36, s36, 524288\n"
"ds_read_b64_tr_b16 v[170:171], v229 offset:56576\n"
"ds_read_b64_tr_b16 a[48:49], v221 offset:13312\n"
"v_mfma_f32_16x16x32_bf16 v[48:51], a[200:203], v[200:203], v[48:51]\n"
"s_add_u32 s87, s87, s19\n"
"ds_read_b64_tr_b16 a[50:51], v221 offset:13320\n"
"v_mul_f32_dpp v181, v220, v181 row_newbcast:4 row_mask:0xf bank_mask:0xf\n"
"v_mfma_f32_16x16x32_bf16 v[88:91], a[208:211], v[200:203], 0\n"
"s_add_u32 s54, s36, s92\n"
"ds_read_b64_tr_b16 v[172:173], v228 offset:41472\n"
"v_mul_f32_dpp v182, v220, v182 row_newbcast:8 row_mask:0xf bank_mask:0xf\n"
"v_mfma_f32_16x16x32_bf16 v[88:91], a[212:215], v[160:163], v[88:91]\n"
"s_cmp_le_u32 s12, 2\n"
"s_cselect_b32 s36, 21, 18\n"
"v_mul_f32_dpp v183, v220, v183 row_newbcast:12 row_mask:0xf bank_mask:0xf\n"
"v_mfma_f32_16x16x32_bf16 v[128:131], a[220:223], v[160:163], 0\n"
"s_mul_i32 s88, s36, 81920\n"
"ds_read_b64_tr_b16 v[174:175], v228 offset:46592\n"
"v_cvt_pk_bf16_f32 v180, v180, v181\n"
"v_mfma_f32_16x16x32_bf16 v[128:131], a[216:219], v[200:203], v[128:131]\n"
"s_waitcnt lgkmcnt(6)\n"
"v_cvt_pk_bf16_f32 v181, v182, v183\n"
"v_accvgpr_read_b32 v176, a96\n"
"v_mfma_f32_16x16x32_bf16 v[132:135], a[216:219], v[164:167], 0\n"
"s_waitcnt lgkmcnt(4)\n"
"ds_read_b64_tr_b16 v[200:201], v228 offset:51712\n"
"ds_write_b64 v244, v[180:181] offset:13320\n"
"v_mfma_f32_16x16x32_bf16 v[132:135], a[220:223], v[168:171], v[132:135]\n"
"s_mul_i32 s36, s36, 524288\n"
"v_accvgpr_read_b32 v177, a97\n"
"v_accvgpr_read_b32 v178, a98\n"
"v_mfma_f32_16x16x32_bf16 v[92:95], a[212:215], v[168:171], 0\n"
"s_waitcnt lgkmcnt(4)\n"
"buffer_store_dwordx4 a[48:51], v251, s[56:59], s76 offen offset:2048 sc1\n"
"s_add_u32 s88, s88, s19\n"
"ds_read_b64_tr_b16 v[202:203], v228 offset:56832\n"
"v_accvgpr_read_b32 v179, a99\n"
"v_mfma_f32_16x16x32_bf16 v[92:95], a[208:211], v[164:167], v[92:95]\n"
"s_add_u32 s55, s36, s92\n"
"v_mul_f32_dpp v176, v220, v176 row_newbcast:0 row_mask:0xf bank_mask:0xf\n"
"v_mul_f32_dpp v177, v220, v177 row_newbcast:4 row_mask:0xf bank_mask:0xf\n"
"v_mfma_f32_16x16x32_bf16 v[52:55], a[200:203], v[164:167], 0\n"
"ds_read_b64_tr_b16 v[160:161], v229 offset:41472\n"
"v_mul_f32_dpp v178, v220, v178 row_newbcast:8 row_mask:0xf bank_mask:0xf\n"
"v_mfma_f32_16x16x32_bf16 v[52:55], a[204:207], v[168:171], v[52:55]\n"
"v_mul_f32_dpp v179, v220, v179 row_newbcast:12 row_mask:0xf bank_mask:0xf\n"
"v_cvt_pk_bf16_f32 v179, v178, v179\n"
"v_mfma_f32_16x16x32_bf16 v[12:15], a[196:199], v[168:171], 0\n"
"ds_read_b64_tr_b16 v[162:163], v229 offset:46592\n"
"v_cvt_pk_bf16_f32 v178, v176, v177\n"
"v_mfma_f32_16x16x32_bf16 v[12:15], a[192:195], v[164:167], v[12:15]\n"
"s_waitcnt lgkmcnt(5)\n"
"ds_write_b64 v244, v[178:179] offset:13312\n"
"v_mfma_f32_16x16x32_bf16 v[16:19], a[192:195], v[172:175], 0\n"
"s_waitcnt lgkmcnt(3)\n"
"ds_read_b64_tr_b16 v[164:165], v229 offset:51712\n"
"v_accvgpr_read_b32 v188, a148\n"
"v_mfma_f32_16x16x32_bf16 v[16:19], a[196:199], v[200:203], v[16:19]\n"
"s_barrier\n"
"v_mfma_f32_16x16x32_bf16 v[56:59], a[204:207], v[200:203], 0\n"
"ds_read_b64_tr_b16 v[166:167], v229 offset:56832\n"
"v_accvgpr_read_b32 v189, a149\n"
"v_mfma_f32_16x16x32_bf16 v[56:59], a[200:203], v[172:175], v[56:59]\n"
"v_accvgpr_read_b32 v190, a150\n"
"v_accvgpr_read_b32 v191, a151\n"
"v_mfma_f32_16x16x32_bf16 v[96:99], a[208:211], v[172:175], 0\n"
"ds_read_b64_tr_b16 v[168:169], v228 offset:41728\n"
"ds_read_b64_tr_b16 a[96:97], v221 offset:13312\n"
"v_mfma_f32_16x16x32_bf16 v[96:99], a[212:215], v[200:203], v[96:99]\n"
"s_add_u32 s74, s19, 163840\n"
"ds_read_b64_tr_b16 a[98:99], v221 offset:13320\n"
"v_mul_f32_dpp v188, v222, v188 row_newbcast:0 row_mask:0xf bank_mask:0xf\n"
"v_mfma_f32_16x16x32_bf16 v[136:139], a[220:223], v[200:203], 0\n"
"s_mov_b32 m0, s79\n"
"ds_read_b64_tr_b16 v[170:171], v228 offset:46848\n"
"v_mul_f32_dpp v189, v222, v189 row_newbcast:4 row_mask:0xf bank_mask:0xf\n"
"v_mfma_f32_16x16x32_bf16 v[136:139], a[216:219], v[172:175], v[136:139]\n"
"s_waitcnt lgkmcnt(7)\n"
"buffer_load_dwordx4 v224, s[28:31], s74 offen offset:0 lds\n"
"v_mfma_f32_16x16x32_bf16 v[140:143], a[216:219], v[160:163], 0\n"
"s_waitcnt lgkmcnt(4)\n"
"ds_read_b64_tr_b16 v[172:173], v228 offset:51968\n"
"v_mul_f32_dpp v190, v222, v190 row_newbcast:8 row_mask:0xf bank_mask:0xf\n"
"v_mfma_f32_16x16x32_bf16 v[140:143], a[220:223], v[164:167], v[140:143]\n"
"v_mul_f32_dpp v191, v222, v191 row_newbcast:12 row_mask:0xf bank_mask:0xf\n"
"v_cvt_pk_bf16_f32 v188, v188, v189\n"
"v_mfma_f32_16x16x32_bf16 v[100:103], a[212:215], v[164:167], 0\n"
"ds_read_b64_tr_b16 v[174:175], v228 offset:57088\n"
"v_cvt_pk_bf16_f32 v189, v190, v191\n"
"v_mfma_f32_16x16x32_bf16 v[100:103], a[208:211], v[160:163], v[100:103]\n"
"ds_write_b64 v244, v[188:189] offset:13320\n"
"v_mfma_f32_16x16x32_bf16 v[60:63], a[200:203], v[160:163], 0\n"
"ds_read_b64_tr_b16 v[200:201], v229 offset:41728\n"
"v_accvgpr_read_b32 v184, a144\n"
"v_mfma_f32_16x16x32_bf16 v[60:63], a[204:207], v[164:167], v[60:63]\n"
"v_accvgpr_read_b32 v185, a145\n"
"v_accvgpr_read_b32 v186, a146\n"
"v_mfma_f32_16x16x32_bf16 v[20:23], a[196:199], v[164:167], 0\n"
"ds_read_b64_tr_b16 v[202:203], v229 offset:46848\n"
"v_accvgpr_read_b32 v187, a147\n"
"v_mfma_f32_16x16x32_bf16 v[20:23], a[192:195], v[160:163], v[20:23]\n"
"s_waitcnt lgkmcnt(5)\n"
"buffer_load_dwordx4 v225, s[28:31], s74 offen offset:1024 lds\n"
"v_mfma_f32_16x16x32_bf16 v[24:27], a[192:195], v[168:171], 0\n"
"s_waitcnt lgkmcnt(3)\n"
"ds_read_b64_tr_b16 v[160:161], v229 offset:51968\n"
"v_mul_f32_dpp v184, v222, v184 row_newbcast:0 row_mask:0xf bank_mask:0xf\n"
"v_mfma_f32_16x16x32_bf16 v[24:27], a[196:199], v[172:175], v[24:27]\n"
"v_mul_f32_dpp v185, v222, v185 row_newbcast:4 row_mask:0xf bank_mask:0xf\n"
"v_mul_f32_dpp v186, v222, v186 row_newbcast:8 row_mask:0xf bank_mask:0xf\n"
"v_mfma_f32_16x16x32_bf16 v[64:67], a[204:207], v[172:175], 0\n"
"ds_read_b64_tr_b16 v[162:163], v229 offset:57088\n"
"v_mul_f32_dpp v187, v222, v187 row_newbcast:12 row_mask:0xf bank_mask:0xf\n"
"v_mfma_f32_16x16x32_bf16 v[64:67], a[200:203], v[168:171], v[64:67]\n"
"buffer_store_dwordx4 a[96:99], v253, s[56:59], s76 offen offset:0 sc1\n"
"v_cvt_pk_bf16_f32 v187, v186, v187\n"
"v_cvt_pk_bf16_f32 v186, v184, v185\n"
"v_accvgpr_read_b32 v180, a12\n"
"v_mfma_f32_16x16x32_bf16 v[104:107], a[208:211], v[168:171], 0\n"
"ds_read_b64_tr_b16 v[164:165], v228 offset:41984\n"
"ds_write_b64 v244, v[186:187] offset:13312\n"
"v_mfma_f32_16x16x32_bf16 v[104:107], a[212:215], v[172:175], v[104:107]\n"
"v_accvgpr_read_b32 v181, a13\n"
"v_accvgpr_read_b32 v182, a14\n"
"v_mfma_f32_16x16x32_bf16 v[144:147], a[220:223], v[172:175], 0\n"
"ds_read_b64_tr_b16 v[166:167], v228 offset:47104\n"
"v_accvgpr_read_b32 v183, a15\n"
"v_mfma_f32_16x16x32_bf16 v[144:147], a[216:219], v[168:171], v[144:147]\n"
"s_waitcnt lgkmcnt(5)\n"
"buffer_load_dwordx4 v226, s[28:31], s74 offen offset:2048 lds\n"
"v_mfma_f32_16x16x32_bf16 v[148:151], a[216:219], v[200:203], 0\n"
"s_waitcnt lgkmcnt(3)\n"
"ds_read_b64_tr_b16 v[168:169], v228 offset:52224\n"
"ds_read_b64_tr_b16 a[144:145], v221 offset:13312\n"
"v_mfma_f32_16x16x32_bf16 v[148:151], a[220:223], v[160:163], v[148:151]\n"
"ds_read_b64_tr_b16 a[146:147], v221 offset:13320\n"
"v_mul_f32_dpp v180, v216, v180 row_newbcast:0 row_mask:0xf bank_mask:0xf\n"
"v_mfma_f32_16x16x32_bf16 v[108:111], a[212:215], v[160:163], 0\n"
"ds_read_b64_tr_b16 v[170:171], v228 offset:57344\n"
"v_mul_f32_dpp v181, v216, v181 row_newbcast:4 row_mask:0xf bank_mask:0xf\n"
"v_mfma_f32_16x16x32_bf16 v[108:111], a[208:211], v[200:203], v[108:111]\n"
"v_mul_f32_dpp v182, v216, v182 row_newbcast:8 row_mask:0xf bank_mask:0xf\n"
"v_mul_f32_dpp v183, v216, v183 row_newbcast:12 row_mask:0xf bank_mask:0xf\n"
"v_mfma_f32_16x16x32_bf16 v[68:71], a[200:203], v[200:203], 0\n"
"ds_read_b64_tr_b16 v[172:173], v229 offset:41984\n"
"v_cvt_pk_bf16_f32 v180, v180, v181\n"
"v_mfma_f32_16x16x32_bf16 v[68:71], a[204:207], v[160:163], v[68:71]\n"
"v_cvt_pk_bf16_f32 v181, v182, v183\n"
"v_accvgpr_read_b32 v176, a8\n"
"v_mfma_f32_16x16x32_bf16 v[28:31], a[196:199], v[160:163], 0\n"
"ds_read_b64_tr_b16 v[174:175], v229 offset:47104\n"
"ds_write_b64 v244, v[180:181] offset:13320\n"
"v_mfma_f32_16x16x32_bf16 v[28:31], a[192:195], v[200:203], v[28:31]\n"
"s_waitcnt lgkmcnt(7)\n"
"buffer_load_dwordx4 v227, s[28:31], s74 offen offset:3072 lds\n"
"v_mfma_f32_16x16x32_bf16 v[32:35], a[192:195], v[164:167], 0\n"
"s_waitcnt lgkmcnt(3)\n"
"ds_read_b64_tr_b16 v[200:201], v229 offset:52224\n"
"v_accvgpr_read_b32 v177, a9\n"
"v_mfma_f32_16x16x32_bf16 v[32:35], a[196:199], v[168:171], v[32:35]\n"
"v_accvgpr_read_b32 v178, a10\n"
"v_accvgpr_read_b32 v179, a11\n"
"v_mfma_f32_16x16x32_bf16 v[72:75], a[204:207], v[168:171], 0\n"
"ds_read_b64_tr_b16 v[202:203], v229 offset:57344\n"
"v_mul_f32_dpp v176, v216, v176 row_newbcast:0 row_mask:0xf bank_mask:0xf\n"
"v_mfma_f32_16x16x32_bf16 v[72:75], a[200:203], v[164:167], v[72:75]\n"
"buffer_store_dwordx4 a[144:147], v253, s[56:59], s76 offen offset:2048 sc1\n"
"v_mul_f32_dpp v177, v216, v177 row_newbcast:4 row_mask:0xf bank_mask:0xf\n"
"v_mul_f32_dpp v178, v216, v178 row_newbcast:8 row_mask:0xf bank_mask:0xf\n"
"v_mul_f32_dpp v179, v216, v179 row_newbcast:12 row_mask:0xf bank_mask:0xf\n"
"v_mfma_f32_16x16x32_bf16 v[112:115], a[208:211], v[164:167], 0\n"
"ds_read_b64_tr_b16 v[160:161], v230 offset:0\n"
"v_cvt_pk_bf16_f32 v179, v178, v179\n"
"v_mfma_f32_16x16x32_bf16 v[112:115], a[212:215], v[168:171], v[112:115]\n"
"v_cvt_pk_bf16_f32 v178, v176, v177\n"
"v_accvgpr_read_b32 v188, a60\n"
"v_mfma_f32_16x16x32_bf16 v[152:155], a[220:223], v[168:171], 0\n"
"s_add_u32 m0, s79, 4096\n"
"ds_read_b64_tr_b16 v[162:163], v230 offset:5120\n"
"ds_write_b64 v244, v[178:179] offset:13312\n"
"v_mfma_f32_16x16x32_bf16 v[152:155], a[216:219], v[164:167], v[152:155]\n"
"s_waitcnt lgkmcnt(6)\n"
"buffer_load_dwordx4 v234, s[28:31], s74 offen offset:0 lds\n"
"v_mfma_f32_16x16x32_bf16 v[156:159], a[216:219], v[172:175], 0\n"
"s_waitcnt lgkmcnt(3)\n"
"ds_read_b64_tr_b16 v[164:165], v230 offset:10240\n"
"v_accvgpr_read_b32 v189, a61\n"
"v_mfma_f32_16x16x32_bf16 v[156:159], a[220:223], v[200:203], v[156:159]\n"
"v_accvgpr_read_b32 v190, a62\n"
"v_accvgpr_read_b32 v191, a63\n"
"v_mfma_f32_16x16x32_bf16 v[116:119], a[212:215], v[200:203], 0\n"
"ds_read_b64_tr_b16 v[166:167], v230 offset:15360\n"
"ds_read_b64_tr_b16 a[8:9], v221 offset:13312\n"
"v_mfma_f32_16x16x32_bf16 v[116:119], a[208:211], v[172:175], v[116:119]\n"
"ds_read_b64_tr_b16 a[10:11], v221 offset:13320\n"
"v_mul_f32_dpp v188, v218, v188 row_newbcast:0 row_mask:0xf bank_mask:0xf\n"
"v_mfma_f32_16x16x32_bf16 v[76:79], a[200:203], v[172:175], 0\n"
"ds_read_b64_tr_b16 v[168:169], v231 offset:0\n"
"v_mul_f32_dpp v189, v218, v189 row_newbcast:4 row_mask:0xf bank_mask:0xf\n"
"v_mfma_f32_16x16x32_bf16 v[76:79], a[204:207], v[200:203], v[76:79]\n"
"s_waitcnt vmcnt(9)\n"
"v_mul_f32_dpp v190, v218, v190 row_newbcast:8 row_mask:0xf bank_mask:0xf\n"
"v_mul_f32_dpp v191, v218, v191 row_newbcast:12 row_mask:0xf bank_mask:0xf\n"
"v_mfma_f32_16x16x32_bf16 v[36:39], a[196:199], v[200:203], 0\n"
"ds_read_b64_tr_b16 v[170:171], v231 offset:5120\n"
"v_cvt_pk_bf16_f32 v188, v188, v189\n"
"v_mfma_f32_16x16x32_bf16 v[36:39], a[192:195], v[172:175], v[36:39]\n"
"s_waitcnt lgkmcnt(7)\n"
"v_cvt_pk_bf16_f32 v189, v190, v191\n"
"v_mfma_f32_16x16x32_bf16 v[0:3], a[224:227], v[160:163], v[0:3]\n"
"s_waitcnt lgkmcnt(4)\n"
"ds_read_b64_tr_b16 v[172:173], v231 offset:10240\n"
"ds_write_b64 v244, v[188:189] offset:13320\n"
"v_mfma_f32_16x16x32_bf16 v[0:3], a[228:231], v[164:167], v[0:3]\n"
"v_accvgpr_read_b32 v184, a56\n"
"v_accvgpr_read_b32 v185, a57\n"
"v_mfma_f32_16x16x32_bf16 v[40:43], a[236:239], v[164:167], v[40:43]\n"
"s_add_u32 s72, s92, 1048576\n"
"ds_read_b64_tr_b16 v[174:175], v231 offset:15360\n"
"v_accvgpr_read_b32 v186, a58\n"
"v_mfma_f32_16x16x32_bf16 v[40:43], a[232:235], v[160:163], v[40:43]\n"
"buffer_load_dwordx4 a[192:195], v251, s[56:59], s72 offen offset:0 sc1\n"
"v_mfma_f32_16x16x32_bf16 v[80:83], a[240:243], v[160:163], v[80:83]\n"
"ds_read_b64_tr_b16 v[200:201], v230 offset:256\n"
"v_accvgpr_read_b32 v187, a59\n"
"v_mfma_f32_16x16x32_bf16 v[80:83], a[244:247], v[164:167], v[80:83]\n"
"v_mul_f32_dpp v184, v218, v184 row_newbcast:0 row_mask:0xf bank_mask:0xf\n"
"v_mul_f32_dpp v185, v218, v185 row_newbcast:4 row_mask:0xf bank_mask:0xf\n"
"v_mfma_f32_16x16x32_bf16 v[120:123], a[252:255], v[164:167], v[120:123]\n"
"ds_read_b64_tr_b16 v[202:203], v230 offset:5376\n"
"v_mul_f32_dpp v186, v218, v186 row_newbcast:8 row_mask:0xf bank_mask:0xf\n"
"v_mfma_f32_16x16x32_bf16 v[120:123], a[248:251], v[160:163], v[120:123]\n"
"s_waitcnt lgkmcnt(5)\n"
"v_mul_f32_dpp v187, v218, v187 row_newbcast:12 row_mask:0xf bank_mask:0xf\n"
"v_cvt_pk_bf16_f32 v187, v186, v187\n"
"v_mfma_f32_16x16x32_bf16 v[124:127], a[248:251], v[168:171], v[124:127]\n"
"s_waitcnt lgkmcnt(2)\n"
"ds_read_b64_tr_b16 v[160:161], v230 offset:10496\n"
"v_cvt_pk_bf16_f32 v186, v184, v185\n"
"v_mfma_f32_16x16x32_bf16 v[124:127], a[252:255], v[172:175], v[124:127]\n"
"buffer_store_dwordx4 a[8:11], v251, s[56:59], s76 offen offset:1024 sc1\n"
"v_accvgpr_read_b32 v180, a108\n"
"v_accvgpr_read_b32 v181, a109\n"
"v_accvgpr_read_b32 v182, a110\n"
"v_mfma_f32_16x16x32_bf16 v[84:87], a[244:247], v[172:175], v[84:87]\n"
"ds_read_b64_tr_b16 v[162:163], v230 offset:15616\n"
"ds_write_b64 v244, v[186:187] offset:13312\n"
"v_mfma_f32_16x16x32_bf16 v[84:87], a[240:243], v[168:171], v[84:87]\n"
"buffer_load_dwordx4 a[196:199], v251, s[56:59], s72 offen offset:1024 sc1\n"
"v_mfma_f32_16x16x32_bf16 v[44:47], a[232:235], v[168:171], v[44:47]\n"
"ds_read_b64_tr_b16 v[164:165], v231 offset:256\n"
"v_accvgpr_read_b32 v183, a111\n"
"v_mfma_f32_16x16x32_bf16 v[44:47], a[236:239], v[172:175], v[44:47]\n"
"ds_read_b64_tr_b16 a[56:57], v221 offset:13312\n"
"ds_read_b64_tr_b16 a[58:59], v221 offset:13320\n"
"v_mfma_f32_16x16x32_bf16 v[4:7], a[228:231], v[172:175], v[4:7]\n"
"ds_read_b64_tr_b16 v[166:167], v231 offset:5376\n"
"v_mul_f32_dpp v180, v220, v180 row_newbcast:0 row_mask:0xf bank_mask:0xf\n"
"v_mfma_f32_16x16x32_bf16 v[4:7], a[224:227], v[168:171], v[4:7]\n"
"s_waitcnt lgkmcnt(7)\n"
"v_mul_f32_dpp v181, v220, v181 row_newbcast:4 row_mask:0xf bank_mask:0xf\n"
"v_mul_f32_dpp v182, v220, v182 row_newbcast:8 row_mask:0xf bank_mask:0xf\n"
"v_mfma_f32_16x16x32_bf16 v[8:11], a[224:227], v[200:203], v[8:11]\n"
"s_waitcnt lgkmcnt(5)\n"
"ds_read_b64_tr_b16 v[168:169], v231 offset:10496\n"
"v_mul_f32_dpp v183, v220, v183 row_newbcast:12 row_mask:0xf bank_mask:0xf\n"
"v_mfma_f32_16x16x32_bf16 v[8:11], a[228:231], v[160:163], v[8:11]\n"
"v_cvt_pk_bf16_f32 v180, v180, v181\n"
"v_cvt_pk_bf16_f32 v181, v182, v183\n"
"v_mfma_f32_16x16x32_bf16 v[48:51], a[236:239], v[160:163], v[48:51]\n"
"ds_read_b64_tr_b16 v[170:171], v231 offset:15616\n"
"ds_write_b64 v244, v[180:181] offset:13320\n"
"v_mfma_f32_16x16x32_bf16 v[48:51], a[232:235], v[200:203], v[48:51]\n"
"buffer_load_dwordx4 a[200:203], v251, s[56:59], s72 offen offset:2048 sc1\n"
"v_mfma_f32_16x16x32_bf16 v[88:91], a[240:243], v[200:203], v[88:91]\n"
"ds_read_b64_tr_b16 v[172:173], v230 offset:512\n"
"v_accvgpr_read_b32 v176, a104\n"
"v_mfma_f32_16x16x32_bf16 v[88:91], a[244:247], v[160:163], v[88:91]\n"
"v_accvgpr_read_b32 v177, a105\n"
"v_accvgpr_read_b32 v178, a106\n"
"v_mfma_f32_16x16x32_bf16 v[128:131], a[252:255], v[160:163], v[128:131]\n"
"ds_read_b64_tr_b16 v[174:175], v230 offset:5632\n"
"v_accvgpr_read_b32 v179, a107\n"
"v_mfma_f32_16x16x32_bf16 v[128:131], a[248:251], v[200:203], v[128:131]\n"
"s_waitcnt lgkmcnt(5)\n"
"v_mul_f32_dpp v176, v220, v176 row_newbcast:0 row_mask:0xf bank_mask:0xf\n"
"v_mul_f32_dpp v177, v220, v177 row_newbcast:4 row_mask:0xf bank_mask:0xf\n"
"v_mfma_f32_16x16x32_bf16 v[132:135], a[248:251], v[164:167], v[132:135]\n"
"s_waitcnt lgkmcnt(3)\n"
"ds_read_b64_tr_b16 v[200:201], v230 offset:10752\n"
"v_mul_f32_dpp v178, v220, v178 row_newbcast:8 row_mask:0xf bank_mask:0xf\n"
"v_mfma_f32_16x16x32_bf16 v[132:135], a[252:255], v[168:171], v[132:135]\n"
"v_mul_f32_dpp v179, v220, v179 row_newbcast:12 row_mask:0xf bank_mask:0xf\n"
"v_cvt_pk_bf16_f32 v179, v178, v179\n"
"v_mfma_f32_16x16x32_bf16 v[92:95], a[244:247], v[168:171], v[92:95]\n"
"ds_read_b64_tr_b16 v[202:203], v230 offset:15872\n"
"v_cvt_pk_bf16_f32 v178, v176, v177\n"
"v_mfma_f32_16x16x32_bf16 v[92:95], a[240:243], v[164:167], v[92:95]\n"
"buffer_load_dwordx4 a[204:207], v251, s[56:59], s72 offen offset:3072 sc1\n"
"v_mfma_f32_16x16x32_bf16 v[52:55], a[232:235], v[164:167], v[52:55]\n"
"ds_read_b64_tr_b16 v[160:161], v231 offset:512\n"
"ds_write_b64 v244, v[178:179] offset:13312\n"
"v_mfma_f32_16x16x32_bf16 v[52:55], a[236:239], v[168:171], v[52:55]\n"
"v_accvgpr_read_b32 v188, a156\n"
"v_accvgpr_read_b32 v189, a157\n"
"v_mfma_f32_16x16x32_bf16 v[12:15], a[228:231], v[168:171], v[12:15]\n"
"ds_read_b64_tr_b16 v[162:163], v231 offset:5632\n"
"v_accvgpr_read_b32 v190, a158\n"
"v_mfma_f32_16x16x32_bf16 v[12:15], a[224:227], v[164:167], v[12:15]\n"
"s_waitcnt lgkmcnt(5)\n"
"v_accvgpr_read_b32 v191, a159\n"
"v_mul_f32_dpp v188, v222, v188 row_newbcast:0 row_mask:0xf bank_mask:0xf\n"
"v_mfma_f32_16x16x32_bf16 v[16:19], a[224:227], v[172:175], v[16:19]\n"
"s_waitcnt lgkmcnt(3)\n"
"ds_read_b64_tr_b16 v[164:165], v231 offset:10752\n"
"ds_read_b64_tr_b16 a[104:105], v221 offset:13312\n"
"v_mfma_f32_16x16x32_bf16 v[16:19], a[228:231], v[200:203], v[16:19]\n"
"s_barrier\n"
"v_mfma_f32_16x16x32_bf16 v[56:59], a[236:239], v[200:203], v[56:59]\n"
"ds_read_b64_tr_b16 v[166:167], v231 offset:15872\n"
"ds_read_b64_tr_b16 a[106:107], v221 offset:13320\n"
"v_mfma_f32_16x16x32_bf16 v[56:59], a[232:235], v[172:175], v[56:59]\n"
"buffer_load_dwordx4 a[208:211], v253, s[56:59], s72 offen offset:0 sc1\n"
"v_mfma_f32_16x16x32_bf16 v[96:99], a[240:243], v[172:175], v[96:99]\n"
"ds_read_b64_tr_b16 v[168:169], v230 offset:768\n"
"v_mul_f32_dpp v189, v222, v189 row_newbcast:4 row_mask:0xf bank_mask:0xf\n"
"v_mfma_f32_16x16x32_bf16 v[96:99], a[244:247], v[200:203], v[96:99]\n"
"s_add_u32 s74, s19, 245760\n"
"v_mul_f32_dpp v190, v222, v190 row_newbcast:8 row_mask:0xf bank_mask:0xf\n"
"v_mul_f32_dpp v191, v222, v191 row_newbcast:12 row_mask:0xf bank_mask:0xf\n"
"v_mfma_f32_16x16x32_bf16 v[136:139], a[252:255], v[200:203], v[136:139]\n"
"s_add_u32 m0, s79, 102400\n"
"ds_read_b64_tr_b16 v[170:171], v230 offset:5888\n"
"v_cvt_pk_bf16_f32 v188, v188, v189\n"
"v_mfma_f32_16x16x32_bf16 v[136:139], a[248:251], v[172:175], v[136:139]\n"
"s_waitcnt lgkmcnt(6)\n"
"buffer_load_dwordx4 v224, s[28:31], s74 offen offset:0 lds\n"
"v_mfma_f32_16x16x32_bf16 v[140:143], a[248:251], v[160:163], v[140:143]\n"
"s_waitcnt lgkmcnt(3)\n"
"ds_read_b64_tr_b16 v[172:173], v230 offset:11008\n"
"v_cvt_pk_bf16_f32 v189, v190, v191\n"
"v_mfma_f32_16x16x32_bf16 v[140:143], a[252:255], v[164:167], v[140:143]\n"
"ds_write_b64 v244, v[188:189] offset:13320\n"
"v_mfma_f32_16x16x32_bf16 v[100:103], a[244:247], v[164:167], v[100:103]\n"
"ds_read_b64_tr_b16 v[174:175], v230 offset:16128\n"
"v_accvgpr_read_b32 v184, a152\n"
"v_mfma_f32_16x16x32_bf16 v[100:103], a[240:243], v[160:163], v[100:103]\n"
"buffer_load_dwordx4 a[212:215], v253, s[56:59], s72 offen offset:1024 sc1\n"
"v_mfma_f32_16x16x32_bf16 v[60:63], a[232:235], v[160:163], v[60:63]\n"
"ds_read_b64_tr_b16 v[200:201], v231 offset:768\n"
"v_accvgpr_read_b32 v185, a153\n"
"v_mfma_f32_16x16x32_bf16 v[60:63], a[236:239], v[164:167], v[60:63]\n"
"v_accvgpr_read_b32 v186, a154\n"
"v_accvgpr_read_b32 v187, a155\n"
"v_mfma_f32_16x16x32_bf16 v[20:23], a[228:231], v[164:167], v[20:23]\n"
"ds_read_b64_tr_b16 v[202:203], v231 offset:5888\n"
"v_mul_f32_dpp v184, v222, v184 row_newbcast:0 row_mask:0xf bank_mask:0xf\n"
"v_mfma_f32_16x16x32_bf16 v[20:23], a[224:227], v[160:163], v[20:23]\n"
"s_waitcnt lgkmcnt(5)\n"
"buffer_load_dwordx4 v225, s[28:31], s74 offen offset:1024 lds\n"
"v_mfma_f32_16x16x32_bf16 v[24:27], a[224:227], v[168:171], v[24:27]\n"
"s_waitcnt lgkmcnt(2)\n"
"ds_read_b64_tr_b16 v[160:161], v231 offset:11008\n"
"v_mul_f32_dpp v185, v222, v185 row_newbcast:4 row_mask:0xf bank_mask:0xf\n"
"v_mfma_f32_16x16x32_bf16 v[24:27], a[228:231], v[172:175], v[24:27]\n"
"v_mul_f32_dpp v186, v222, v186 row_newbcast:8 row_mask:0xf bank_mask:0xf\n"
"v_mul_f32_dpp v187, v222, v187 row_newbcast:12 row_mask:0xf bank_mask:0xf\n"
"v_mfma_f32_16x16x32_bf16 v[64:67], a[236:239], v[172:175], v[64:67]\n"
"ds_read_b64_tr_b16 v[162:163], v231 offset:16128\n"
"v_cvt_pk_bf16_f32 v187, v186, v187\n"
"v_mfma_f32_16x16x32_bf16 v[64:67], a[232:235], v[168:171], v[64:67]\n"
"buffer_load_dwordx4 a[216:219], v253, s[56:59], s72 offen offset:2048 sc1\n"
"v_mfma_f32_16x16x32_bf16 v[104:107], a[240:243], v[168:171], v[104:107]\n"
"ds_read_b64_tr_b16 v[164:165], v230 offset:1024\n"
"v_cvt_pk_bf16_f32 v186, v184, v185\n"
"v_mfma_f32_16x16x32_bf16 v[104:107], a[244:247], v[172:175], v[104:107]\n"
"ds_write_b64 v244, v[186:187] offset:13312\n"
"v_mfma_f32_16x16x32_bf16 v[144:147], a[252:255], v[172:175], v[144:147]\n"
"ds_read_b64_tr_b16 v[166:167], v230 offset:6144\n"
"v_accvgpr_read_b32 v180, a20\n"
"v_mfma_f32_16x16x32_bf16 v[144:147], a[248:251], v[168:171], v[144:147]\n"
"s_waitcnt lgkmcnt(5)\n"
"buffer_load_dwordx4 v226, s[28:31], s74 offen offset:2048 lds\n"
"v_mfma_f32_16x16x32_bf16 v[148:151], a[248:251], v[200:203], v[148:151]\n"
"s_waitcnt lgkmcnt(3)\n"
"ds_read_b64_tr_b16 v[168:169], v230 offset:11264\n"
"v_accvgpr_read_b32 v181, a21\n"
"v_mfma_f32_16x16x32_bf16 v[148:151], a[252:255], v[160:163], v[148:151]\n"
"v_accvgpr_read_b32 v182, a22\n"
"v_accvgpr_read_b32 v183, a23\n"
"v_mfma_f32_16x16x32_bf16 v[108:111], a[244:247], v[160:163], v[108:111]\n"
"ds_read_b64_tr_b16 v[170:171], v230 offset:16384\n"
"ds_read_b64_tr_b16 a[152:153], v221 offset:13312\n"
"v_mfma_f32_16x16x32_bf16 v[108:111], a[240:243], v[200:203], v[108:111]\n"
"buffer_load_dwordx4 a[220:223], v253, s[56:59], s72 offen offset:3072 sc1\n"
"v_mfma_f32_16x16x32_bf16 v[68:71], a[232:235], v[200:203], v[68:71]\n"
"ds_read_b64_tr_b16 v[172:173], v231 offset:1024\n"
"ds_read_b64_tr_b16 a[154:155], v221 offset:13320\n"
"v_mfma_f32_16x16x32_bf16 v[68:71], a[236:239], v[160:163], v[68:71]\n"
"v_mul_f32_dpp v180, v216, v180 row_newbcast:0 row_mask:0xf bank_mask:0xf\n"
"v_mul_f32_dpp v181, v216, v181 row_newbcast:4 row_mask:0xf bank_mask:0xf\n"
"v_mfma_f32_16x16x32_bf16 v[28:31], a[228:231], v[160:163], v[28:31]\n"
"ds_read_b64_tr_b16 v[174:175], v231 offset:6144\n"
"v_mul_f32_dpp v182, v216, v182 row_newbcast:8 row_mask:0xf bank_mask:0xf\n"
"v_mfma_f32_16x16x32_bf16 v[28:31], a[224:227], v[200:203], v[28:31]\n"
"s_waitcnt lgkmcnt(6)\n"
"buffer_load_dwordx4 v227, s[28:31], s74 offen offset:3072 lds\n"
"v_mfma_f32_16x16x32_bf16 v[32:35], a[224:227], v[164:167], v[32:35]\n"
"s_waitcnt lgkmcnt(4)\n"
"ds_read_b64_tr_b16 v[200:201], v231 offset:11264\n"
"v_mul_f32_dpp v183, v216, v183 row_newbcast:12 row_mask:0xf bank_mask:0xf\n"
"v_mfma_f32_16x16x32_bf16 v[32:35], a[228:231], v[168:171], v[32:35]\n"
"v_cvt_pk_bf16_f32 v180, v180, v181\n"
"v_cvt_pk_bf16_f32 v181, v182, v183\n"
"v_mfma_f32_16x16x32_bf16 v[72:75], a[236:239], v[168:171], v[72:75]\n"
"ds_read_b64_tr_b16 v[202:203], v231 offset:16384\n"
"ds_write_b64 v244, v[180:181] offset:13320\n"
"v_mfma_f32_16x16x32_bf16 v[72:75], a[232:235], v[164:167], v[72:75]\n"
"buffer_store_dwordx4 a[56:59], v251, s[56:59], s76 offen offset:3072 sc1\n"
"v_accvgpr_read_b32 v176, a16\n"
"v_accvgpr_read_b32 v177, a17\n"
"v_accvgpr_read_b32 v178, a18\n"
"v_mfma_f32_16x16x32_bf16 v[112:115], a[240:243], v[164:167], v[112:115]\n"
"ds_read_b64_tr_b16 v[160:161], v230 offset:20480\n"
"v_accvgpr_read_b32 v179, a19\n"
"v_mfma_f32_16x16x32_bf16 v[112:115], a[244:247], v[168:171], v[112:115]\n"
"v_mul_f32_dpp v176, v216, v176 row_newbcast:0 row_mask:0xf bank_mask:0xf\n"
"v_mul_f32_dpp v177, v216, v177 row_newbcast:4 row_mask:0xf bank_mask:0xf\n"
"v_mfma_f32_16x16x32_bf16 v[152:155], a[252:255], v[168:171], v[152:155]\n"
"s_add_u32 m0, s79, 106496\n"
"ds_read_b64_tr_b16 v[162:163], v230 offset:25600\n"
"v_mul_f32_dpp v178, v216, v178 row_newbcast:8 row_mask:0xf bank_mask:0xf\n"
"v_mfma_f32_16x16x32_bf16 v[152:155], a[248:251], v[164:167], v[152:155]\n"
"s_waitcnt lgkmcnt(5)\n"
"buffer_load_dwordx4 v234, s[28:31], s74 offen offset:0 lds\n"
"v_mfma_f32_16x16x32_bf16 v[156:159], a[248:251], v[172:175], v[156:159]\n"
"s_waitcnt lgkmcnt(3)\n"
"ds_read_b64_tr_b16 v[164:165], v230 offset:30720\n"
"v_mul_f32_dpp v179, v216, v179 row_newbcast:12 row_mask:0xf bank_mask:0xf\n"
"v_mfma_f32_16x16x32_bf16 v[156:159], a[252:255], v[200:203], v[156:159]\n"
"v_cvt_pk_bf16_f32 v179, v178, v179\n"
"v_cvt_pk_bf16_f32 v178, v176, v177\n"
"v_mfma_f32_16x16x32_bf16 v[116:119], a[244:247], v[200:203], v[116:119]\n"
"ds_read_b64_tr_b16 v[166:167], v230 offset:35840\n"
"ds_write_b64 v244, v[178:179] offset:13312\n"
"v_mfma_f32_16x16x32_bf16 v[116:119], a[240:243], v[172:175], v[116:119]\n"
"buffer_store_dwordx4 a[104:107], v253, s[56:59], s76 offen offset:1024 sc1\n"
"v_accvgpr_read_b32 v188, a68\n"
"v_accvgpr_read_b32 v189, a69\n"
"v_accvgpr_read_b32 v190, a70\n"
"v_mfma_f32_16x16x32_bf16 v[76:79], a[232:235], v[172:175], v[76:79]\n"
"ds_read_b64_tr_b16 v[168:169], v231 offset:20480\n"
"v_accvgpr_read_b32 v191, a71\n"
"v_mfma_f32_16x16x32_bf16 v[76:79], a[236:239], v[200:203], v[76:79]\n"
"ds_read_b64_tr_b16 a[16:17], v221 offset:13312\n"
"ds_read_b64_tr_b16 a[18:19], v221 offset:13320\n"
"v_mfma_f32_16x16x32_bf16 v[36:39], a[228:231], v[200:203], v[36:39]\n"
"ds_read_b64_tr_b16 v[170:171], v231 offset:25600\n"
"v_mul_f32_dpp v188, v218, v188 row_newbcast:0 row_mask:0xf bank_mask:0xf\n"
"v_mfma_f32_16x16x32_bf16 v[36:39], a[224:227], v[172:175], v[36:39]\n"
"s_waitcnt lgkmcnt(7)\n"
"v_mul_f32_dpp v189, v218, v189 row_newbcast:4 row_mask:0xf bank_mask:0xf\n"
"v_mfma_f32_16x16x32_bf16 v[0:3], a[0:3], v[160:163], v[0:3]\n"
"s_waitcnt lgkmcnt(5)\n"
"ds_read_b64_tr_b16 v[172:173], v231 offset:30720\n"
"v_mul_f32_dpp v190, v218, v190 row_newbcast:8 row_mask:0xf bank_mask:0xf\n"
"v_mfma_f32_16x16x32_bf16 v[0:3], a[8:11], v[164:167], v[0:3]\n"
"v_readlane_b32 s40, v255, 16\n"
"v_mul_f32_dpp v191, v218, v191 row_newbcast:12 row_mask:0xf bank_mask:0xf\n"
"v_mfma_f32_16x16x32_bf16 v[40:43], a[56:59], v[164:167], v[40:43]\n"
"s_add_u32 s72, s92, 1572864\n"
"ds_read_b64_tr_b16 v[174:175], v231 offset:35840\n"
"v_cvt_pk_bf16_f32 v188, v188, v189\n"
"v_mfma_f32_16x16x32_bf16 v[40:43], a[48:51], v[160:163], v[40:43]\n"
"buffer_load_dwordx4 a[224:227], v251, s[56:59], s72 offen offset:0 sc1\n"
"v_mfma_f32_16x16x32_bf16 v[80:83], a[96:99], v[160:163], v[80:83]\n"
"ds_read_b64_tr_b16 v[200:201], v230 offset:20736\n"
"v_readlane_b32 s41, v255, 17\n"
"v_mfma_f32_16x16x32_bf16 v[80:83], a[104:107], v[164:167], v[80:83]\n"
"v_cvt_pk_bf16_f32 v189, v190, v191\n"
"v_accvgpr_read_b32 v184, a64\n"
"v_mfma_f32_16x16x32_bf16 v[120:123], a[152:155], v[164:167], v[120:123]\n"
"ds_read_b64_tr_b16 v[202:203], v230 offset:25856\n"
"v_readlane_b32 s44, v255, 20\n"
"v_mfma_f32_16x16x32_bf16 v[120:123], a[144:147], v[160:163], v[120:123]\n"
"s_waitcnt lgkmcnt(4)\n"
"ds_write_b64 v244, v[188:189] offset:13320\n"
"v_mfma_f32_16x16x32_bf16 v[124:127], a[144:147], v[168:171], v[124:127]\n"
"s_waitcnt lgkmcnt(3)\n"
"ds_read_b64_tr_b16 v[160:161], v230 offset:30976\n"
"v_readlane_b32 s45, v255, 21\n"
"v_mfma_f32_16x16x32_bf16 v[124:127], a[152:155], v[172:175], v[124:127]\n"
"v_accvgpr_read_b32 v185, a65\n"
"v_accvgpr_read_b32 v186, a66\n"
"v_mfma_f32_16x16x32_bf16 v[84:87], a[104:107], v[172:175], v[84:87]\n"
"s_mov_b32 s42, 20971520\n"
"ds_read_b64_tr_b16 v[162:163], v230 offset:36096\n"
"v_accvgpr_read_b32 v187, a67\n"
"v_mfma_f32_16x16x32_bf16 v[84:87], a[96:99], v[168:171], v[84:87]\n"
"buffer_load_dwordx4 a[228:231], v251, s[56:59], s72 offen offset:1024 sc1\n"
"v_mfma_f32_16x16x32_bf16 v[44:47], a[48:51], v[168:171], v[44:47]\n"
"s_mov_b32 s46, 5242880\n"
"ds_read_b64_tr_b16 v[164:165], v231 offset:20736\n"
"v_readlane_b32 s73, v255, 4\n"
"v_mfma_f32_16x16x32_bf16 v[44:47], a[56:59], v[172:175], v[44:47]\n"
"v_mul_f32_dpp v184, v218, v184 row_newbcast:0 row_mask:0xf bank_mask:0xf\n"
"v_mul_f32_dpp v185, v218, v185 row_newbcast:4 row_mask:0xf bank_mask:0xf\n"
"v_mfma_f32_16x16x32_bf16 v[4:7], a[8:11], v[172:175], v[4:7]\n"
"ds_read_b64_tr_b16 v[166:167], v231 offset:25856\n"
"v_readlane_b32 s75, v255, 5\n"
"v_mfma_f32_16x16x32_bf16 v[4:7], a[0:3], v[168:171], v[4:7]\n"
"s_waitcnt lgkmcnt(5)\n"
"v_mul_f32_dpp v186, v218, v186 row_newbcast:8 row_mask:0xf bank_mask:0xf\n"
"v_mul_f32_dpp v187, v218, v187 row_newbcast:12 row_mask:0xf bank_mask:0xf\n"
"v_mfma_f32_16x16x32_bf16 v[8:11], a[0:3], v[200:203], v[8:11]\n"
"s_waitcnt lgkmcnt(2)\n"
"ds_read_b64_tr_b16 v[168:169], v231 offset:30976\n"
"v_readlane_b32 s60, v255, 6\n"
"v_mfma_f32_16x16x32_bf16 v[8:11], a[8:11], v[160:163], v[8:11]\n"
"v_cvt_pk_bf16_f32 v187, v186, v187\n"
"v_cvt_pk_bf16_f32 v186, v184, v185\n"
"v_mfma_f32_16x16x32_bf16 v[48:51], a[56:59], v[160:163], v[48:51]\n"
"ds_read_b64_tr_b16 v[170:171], v231 offset:36096\n"
"v_readlane_b32 s12, v255, 9\n"
"v_mfma_f32_16x16x32_bf16 v[48:51], a[48:51], v[200:203], v[48:51]\n"
"buffer_load_dwordx4 a[232:235], v251, s[56:59], s72 offen offset:2048 sc1\n"
"v_mfma_f32_16x16x32_bf16 v[88:91], a[96:99], v[200:203], v[88:91]\n"
"ds_read_b64_tr_b16 v[172:173], v230 offset:20992\n"
"v_readlane_b32 s36, v255, 11\n"
"v_mfma_f32_16x16x32_bf16 v[88:91], a[104:107], v[160:163], v[88:91]\n"
"ds_write_b64 v244, v[186:187] offset:13312\n"
"v_mfma_f32_16x16x32_bf16 v[128:131], a[152:155], v[160:163], v[128:131]\n"
"ds_read_b64_tr_b16 v[174:175], v230 offset:26112\n"
"v_mov_b32 v208, s12\n"
"v_mfma_f32_16x16x32_bf16 v[128:131], a[144:147], v[200:203], v[128:131]\n"
"s_waitcnt lgkmcnt(5)\n"
"v_accvgpr_read_b32 v180, a116\n"
"v_accvgpr_read_b32 v181, a117\n"
"v_mfma_f32_16x16x32_bf16 v[132:135], a[144:147], v[164:167], v[132:135]\n"
"s_waitcnt lgkmcnt(3)\n"
"ds_read_b64_tr_b16 v[200:201], v230 offset:31232\n"
"v_mov_b32 v209, s13\n"
"v_mfma_f32_16x16x32_bf16 v[132:135], a[152:155], v[168:171], v[132:135]\n"
"v_accvgpr_read_b32 v182, a118\n"
"v_accvgpr_read_b32 v183, a119\n"
"v_mfma_f32_16x16x32_bf16 v[92:95], a[104:107], v[168:171], v[92:95]\n"
"ds_read_b64_tr_b16 v[202:203], v230 offset:36352\n"
"v_mov_b32 v210, s36\n"
"v_mfma_f32_16x16x32_bf16 v[92:95], a[96:99], v[164:167], v[92:95]\n"
"buffer_load_dwordx4 a[236:239], v251, s[56:59], s72 offen offset:3072 sc1\n"
"v_mfma_f32_16x16x32_bf16 v[52:55], a[48:51], v[164:167], v[52:55]\n"
"s_xor_b32 s97, s61, 0\n"
"ds_read_b64_tr_b16 v[160:161], v231 offset:20992\n"
"ds_read_b64_tr_b16 a[64:65], v221 offset:13312\n"
"v_mfma_f32_16x16x32_bf16 v[52:55], a[56:59], v[168:171], v[52:55]\n"
"s_mul_i32 s62, s97, 5120\n"
"ds_read_b64_tr_b16 a[66:67], v221 offset:13320\n"
"v_mul_f32_dpp v180, v220, v180 row_newbcast:0 row_mask:0xf bank_mask:0xf\n"
"v_mfma_f32_16x16x32_bf16 v[12:15], a[8:11], v[168:171], v[12:15]\n"
"s_xor_b32 s97, s61, 1\n"
"ds_read_b64_tr_b16 v[162:163], v231 offset:26112\n"
"v_mul_f32_dpp v181, v220, v181 row_newbcast:4 row_mask:0xf bank_mask:0xf\n"
"v_mfma_f32_16x16x32_bf16 v[12:15], a[0:3], v[164:167], v[12:15]\n"
"s_waitcnt lgkmcnt(6)\n"
"v_mul_f32_dpp v182, v220, v182 row_newbcast:8 row_mask:0xf bank_mask:0xf\n"
"v_mul_f32_dpp v183, v220, v183 row_newbcast:12 row_mask:0xf bank_mask:0xf\n"
"v_mfma_f32_16x16x32_bf16 v[16:19], a[0:3], v[172:175], v[16:19]\n"
"s_waitcnt lgkmcnt(4)\n"
"ds_read_b64_tr_b16 v[164:165], v231 offset:31232\n"
"v_cvt_pk_bf16_f32 v180, v180, v181\n"
"v_mfma_f32_16x16x32_bf16 v[16:19], a[8:11], v[200:203], v[16:19]\n"
"s_waitcnt vmcnt(20)\n"
"s_barrier\n"
"v_mfma_f32_16x16x32_bf16 v[56:59], a[56:59], v[200:203], v[56:59]\n"
"s_mul_i32 s63, s97, 5120\n"
"ds_read_b64_tr_b16 v[166:167], v231 offset:36352\n"
"v_cvt_pk_bf16_f32 v181, v182, v183\n"
"v_mfma_f32_16x16x32_bf16 v[56:59], a[48:51], v[172:175], v[56:59]\n"
"buffer_load_dwordx4 a[240:243], v253, s[56:59], s72 offen offset:0 sc1\n"
"v_mfma_f32_16x16x32_bf16 v[96:99], a[96:99], v[172:175], v[96:99]\n"
"s_mul_i32 s69, s97, 5242880\n"
"ds_read_b64_tr_b16 v[168:169], v230 offset:21248\n"
"ds_write_b64 v244, v[180:181] offset:13320\n"
"v_mfma_f32_16x16x32_bf16 v[96:99], a[104:107], v[200:203], v[96:99]\n"
"s_add_u32 s74, s18, 81920\n"
"v_accvgpr_read_b32 v176, a112\n"
"v_accvgpr_read_b32 v177, a113\n"
"v_mfma_f32_16x16x32_bf16 v[136:139], a[152:155], v[200:203], v[136:139]\n"
"s_add_u32 m0, s79, 122880\n"
"ds_read_b64_tr_b16 v[170:171], v230 offset:26368\n"
"v_accvgpr_read_b32 v178, a114\n"
"v_mfma_f32_16x16x32_bf16 v[136:139], a[144:147], v[172:175], v[136:139]\n"
"s_waitcnt lgkmcnt(5)\n"
"buffer_load_dwordx4 v224, s[28:31], s74 offen offset:0 lds\n"
"v_mfma_f32_16x16x32_bf16 v[140:143], a[144:147], v[160:163], v[140:143]\n"
"s_waitcnt lgkmcnt(3)\n"
"ds_read_b64_tr_b16 v[172:173], v230 offset:31488\n"
"v_accvgpr_read_b32 v179, a115\n"
"v_mfma_f32_16x16x32_bf16 v[140:143], a[152:155], v[164:167], v[140:143]\n"
"s_xor_b32 s97, s61, 2\n"
"v_mul_f32_dpp v176, v220, v176 row_newbcast:0 row_mask:0xf bank_mask:0xf\n"
"v_mul_f32_dpp v177, v220, v177 row_newbcast:4 row_mask:0xf bank_mask:0xf\n"
"v_mfma_f32_16x16x32_bf16 v[100:103], a[104:107], v[164:167], v[100:103]\n"
"s_mul_i32 s64, s97, 5120\n"
"ds_read_b64_tr_b16 v[174:175], v230 offset:36608\n"
"v_mul_f32_dpp v178, v220, v178 row_newbcast:8 row_mask:0xf bank_mask:0xf\n"
"v_mfma_f32_16x16x32_bf16 v[100:103], a[96:99], v[160:163], v[100:103]\n"
"buffer_load_dwordx4 a[244:247], v253, s[56:59], s72 offen offset:1024 sc1\n"
"v_mfma_f32_16x16x32_bf16 v[60:63], a[48:51], v[160:163], v[60:63]\n"
"s_mul_i32 s68, s97, 5242880\n"
"ds_read_b64_tr_b16 v[200:201], v231 offset:21248\n"
"v_mul_f32_dpp v179, v220, v179 row_newbcast:12 row_mask:0xf bank_mask:0xf\n"
"v_mfma_f32_16x16x32_bf16 v[60:63], a[56:59], v[164:167], v[60:63]\n"
"s_xor_b32 s97, s61, 3\n"
"v_cvt_pk_bf16_f32 v179, v178, v179\n"
"v_cvt_pk_bf16_f32 v178, v176, v177\n"
"v_mfma_f32_16x16x32_bf16 v[20:23], a[8:11], v[164:167], v[20:23]\n"
"s_mul_i32 s65, s97, 5120\n"
"ds_read_b64_tr_b16 v[202:203], v231 offset:26368\n"
"ds_write_b64 v244, v[178:179] offset:13312\n"
"v_mfma_f32_16x16x32_bf16 v[20:23], a[0:3], v[160:163], v[20:23]\n"
"s_waitcnt lgkmcnt(5)\n"
"buffer_load_dwordx4 v225, s[28:31], s74 offen offset:1024 lds\n"
"v_mfma_f32_16x16x32_bf16 v[24:27], a[0:3], v[168:171], v[24:27]\n"
"s_waitcnt lgkmcnt(3)\n"
"ds_read_b64_tr_b16 v[160:161], v231 offset:31488\n"
"v_accvgpr_read_b32 v188, a164\n"
"v_mfma_f32_16x16x32_bf16 v[24:27], a[8:11], v[172:175], v[24:27]\n"
"s_mul_i32 s67, s97, 5242880\n"
"v_accvgpr_read_b32 v189, a165\n"
"v_accvgpr_read_b32 v190, a166\n"
"v_mfma_f32_16x16x32_bf16 v[64:67], a[56:59], v[172:175], v[64:67]\n"
"ds_read_b64_tr_b16 v[162:163], v231 offset:36608\n"
"v_accvgpr_read_b32 v191, a167\n"
"v_mfma_f32_16x16x32_bf16 v[64:67], a[48:51], v[168:171], v[64:67]\n"
"buffer_load_dwordx4 a[248:251], v253, s[56:59], s72 offen offset:2048 sc1\n"
"v_mfma_f32_16x16x32_bf16 v[104:107], a[96:99], v[168:171], v[104:107]\n"
"ds_read_b64_tr_b16 v[164:165], v230 offset:21504\n"
"ds_read_b64_tr_b16 a[112:113], v221 offset:13312\n"
"v_mfma_f32_16x16x32_bf16 v[104:107], a[104:107], v[172:175], v[104:107]\n"
"ds_read_b64_tr_b16 a[114:115], v221 offset:13320\n"
"v_mul_f32_dpp v188, v222, v188 row_newbcast:0 row_mask:0xf bank_mask:0xf\n"
"v_mfma_f32_16x16x32_bf16 v[144:147], a[152:155], v[172:175], v[144:147]\n"
"ds_read_b64_tr_b16 v[166:167], v230 offset:26624\n"
"v_mul_f32_dpp v189, v222, v189 row_newbcast:4 row_mask:0xf bank_mask:0xf\n"
"v_mfma_f32_16x16x32_bf16 v[144:147], a[144:147], v[168:171], v[144:147]\n"
"s_waitcnt lgkmcnt(7)\n"
"buffer_load_dwordx4 v226, s[28:31], s74 offen offset:2048 lds\n"
"v_mfma_f32_16x16x32_bf16 v[148:151], a[144:147], v[200:203], v[148:151]\n"
"s_waitcnt lgkmcnt(4)\n"
"ds_read_b64_tr_b16 v[168:169], v230 offset:31744\n"
"v_mul_f32_dpp v190, v222, v190 row_newbcast:8 row_mask:0xf bank_mask:0xf\n"
"v_mfma_f32_16x16x32_bf16 v[148:151], a[152:155], v[160:163], v[148:151]\n"
"v_mul_f32_dpp v191, v222, v191 row_newbcast:12 row_mask:0xf bank_mask:0xf\n"
"v_cvt_pk_bf16_f32 v188, v188, v189\n"
"v_mfma_f32_16x16x32_bf16 v[108:111], a[104:107], v[160:163], v[108:111]\n"
"ds_read_b64_tr_b16 v[170:171], v230 offset:36864\n"
"v_cvt_pk_bf16_f32 v189, v190, v191\n"
"v_mfma_f32_16x16x32_bf16 v[108:111], a[96:99], v[200:203], v[108:111]\n"
"buffer_load_dwordx4 a[252:255], v253, s[56:59], s72 offen offset:3072 sc1\n"
"v_mfma_f32_16x16x32_bf16 v[68:71], a[48:51], v[200:203], v[68:71]\n"
"ds_read_b64_tr_b16 v[172:173], v231 offset:21504\n"
"ds_write_b64 v244, v[188:189] offset:13320\n"
"v_mfma_f32_16x16x32_bf16 v[68:71], a[56:59], v[160:163], v[68:71]\n"
"v_accvgpr_read_b32 v184, a160\n"
"v_accvgpr_read_b32 v185, a161\n"
"v_mfma_f32_16x16x32_bf16 v[28:31], a[8:11], v[160:163], v[28:31]\n"
"ds_read_b64_tr_b16 v[174:175], v231 offset:26624\n"
"v_accvgpr_read_b32 v186, a162\n"
"v_mfma_f32_16x16x32_bf16 v[28:31], a[0:3], v[200:203], v[28:31]\n"
"s_waitcnt lgkmcnt(5)\n"
"buffer_load_dwordx4 v227, s[28:31], s74 offen offset:3072 lds\n"
"v_mfma_f32_16x16x32_bf16 v[32:35], a[0:3], v[164:167], v[32:35]\n"
"s_waitcnt lgkmcnt(3)\n"
"ds_read_b64_tr_b16 v[200:201], v231 offset:31744\n"
"v_accvgpr_read_b32 v187, a163\n"
"v_mfma_f32_16x16x32_bf16 v[32:35], a[8:11], v[168:171], v[32:35]\n"
"v_mul_f32_dpp v184, v222, v184 row_newbcast:0 row_mask:0xf bank_mask:0xf\n"
"v_mul_f32_dpp v185, v222, v185 row_newbcast:4 row_mask:0xf bank_mask:0xf\n"
"v_mfma_f32_16x16x32_bf16 v[72:75], a[56:59], v[168:171], v[72:75]\n"
"ds_read_b64_tr_b16 v[202:203], v231 offset:36864\n"
"v_mul_f32_dpp v186, v222, v186 row_newbcast:8 row_mask:0xf bank_mask:0xf\n"
"v_mfma_f32_16x16x32_bf16 v[72:75], a[48:51], v[164:167], v[72:75]\n"
"buffer_store_dwordx4 a[152:155], v253, s[56:59], s76 offen offset:3072 sc1\n"
"v_mul_f32_dpp v187, v222, v187 row_newbcast:12 row_mask:0xf bank_mask:0xf\n"
"v_cvt_pk_bf16_f32 v187, v186, v187\n"
"v_cvt_pk_bf16_f32 v186, v184, v185\n"
"v_mfma_f32_16x16x32_bf16 v[112:115], a[96:99], v[164:167], v[112:115]\n"
"s_add_u32 s76, s76, 524288\n"
"ds_read_b64_tr_b16 v[160:161], v235 offset:0\n"
"ds_write_b64 v244, v[186:187] offset:13312\n"
"v_mfma_f32_16x16x32_bf16 v[112:115], a[104:107], v[168:171], v[112:115]\n"
"v_accvgpr_read_b32 v180, a28\n"
"v_accvgpr_read_b32 v181, a29\n"
"v_mfma_f32_16x16x32_bf16 v[152:155], a[152:155], v[168:171], v[152:155]\n"
"s_add_u32 m0, s79, 126976\n"
"ds_read_b64_tr_b16 v[162:163], v235 offset:5120\n"
"v_accvgpr_read_b32 v182, a30\n"
"v_mfma_f32_16x16x32_bf16 v[152:155], a[144:147], v[164:167], v[152:155]\n"
"s_waitcnt lgkmcnt(5)\n"
"buffer_load_dwordx4 v234, s[28:31], s74 offen offset:0 lds\n"
"v_mfma_f32_16x16x32_bf16 v[156:159], a[144:147], v[172:175], v[156:159]\n"
"s_waitcnt lgkmcnt(3)\n"
"ds_read_b64_tr_b16 v[164:165], v235 offset:10240\n"
"v_accvgpr_read_b32 v183, a31\n"
"v_mfma_f32_16x16x32_bf16 v[156:159], a[152:155], v[200:203], v[156:159]\n"
"ds_read_b64_tr_b16 a[160:161], v221 offset:13312\n"
"ds_read_b64_tr_b16 a[162:163], v221 offset:13320\n"
"v_mfma_f32_16x16x32_bf16 v[116:119], a[104:107], v[200:203], v[116:119]\n"
"ds_read_b64_tr_b16 v[166:167], v235 offset:15360\n"
"v_mul_f32_dpp v180, v216, v180 row_newbcast:0 row_mask:0xf bank_mask:0xf\n"
"v_mfma_f32_16x16x32_bf16 v[116:119], a[96:99], v[172:175], v[116:119]\n"
"buffer_store_dwordx4 a[16:19], v251, s[56:59], s76 offen offset:0 sc1\n"
"v_mul_f32_dpp v181, v216, v181 row_newbcast:4 row_mask:0xf bank_mask:0xf\n"
"v_mul_f32_dpp v182, v216, v182 row_newbcast:8 row_mask:0xf bank_mask:0xf\n"
"v_mul_f32_dpp v183, v216, v183 row_newbcast:12 row_mask:0xf bank_mask:0xf\n"
"v_mfma_f32_16x16x32_bf16 v[76:79], a[48:51], v[172:175], v[76:79]\n"
"ds_read_b64_tr_b16 v[168:169], v249 offset:0\n"
"v_cvt_pk_bf16_f32 v180, v180, v181\n"
"v_mfma_f32_16x16x32_bf16 v[76:79], a[56:59], v[200:203], v[76:79]\n"
"s_waitcnt vmcnt(19)\n"
"v_cvt_pk_bf16_f32 v181, v182, v183\n"
"v_accvgpr_read_b32 v176, a24\n"
"v_mfma_f32_16x16x32_bf16 v[36:39], a[8:11], v[200:203], v[36:39]\n"
"ds_read_b64_tr_b16 v[170:171], v249 offset:5120\n"
"ds_write_b64 v244, v[180:181] offset:13320\n"
"v_mfma_f32_16x16x32_bf16 v[36:39], a[0:3], v[172:175], v[36:39]\n"
"s_waitcnt lgkmcnt(7)\n"
"v_accvgpr_read_b32 v177, a25\n"
"v_mfma_f32_16x16x32_bf16 v[0:3], a[192:195], v[160:163], v[0:3]\n"
"s_waitcnt lgkmcnt(3)\n"
"ds_read_b64_tr_b16 v[172:173], v249 offset:10240\n"
"v_accvgpr_read_b32 v178, a26\n"
"v_mfma_f32_16x16x32_bf16 v[0:3], a[196:199], v[164:167], v[0:3]\n"
"v_accvgpr_read_b32 v179, a27\n"
"v_mul_f32_dpp v176, v216, v176 row_newbcast:0 row_mask:0xf bank_mask:0xf\n"
"v_mfma_f32_16x16x32_bf16 v[40:43], a[204:207], v[164:167], v[40:43]\n"
"ds_read_b64_tr_b16 v[174:175], v249 offset:15360\n"
"v_mul_f32_dpp v177, v216, v177 row_newbcast:4 row_mask:0xf bank_mask:0xf\n"
"v_mfma_f32_16x16x32_bf16 v[40:43], a[200:203], v[160:163], v[40:43]\n"
"buffer_store_dwordx4 a[64:67], v251, s[56:59], s76 offen offset:2048 sc1\n"
"v_mul_f32_dpp v178, v216, v178 row_newbcast:8 row_mask:0xf bank_mask:0xf\n"
"v_mul_f32_dpp v179, v216, v179 row_newbcast:12 row_mask:0xf bank_mask:0xf\n"
"v_cvt_pk_bf16_f32 v179, v178, v179\n"
"v_mfma_f32_16x16x32_bf16 v[80:83], a[208:211], v[160:163], v[80:83]\n"
"ds_read_b64_tr_b16 v[200:201], v235 offset:256\n"
"v_cvt_pk_bf16_f32 v178, v176, v177\n"
"v_mfma_f32_16x16x32_bf16 v[80:83], a[212:215], v[164:167], v[80:83]\n"
"ds_write_b64 v244, v[178:179] offset:13312\n"
"v_mfma_f32_16x16x32_bf16 v[120:123], a[220:223], v[164:167], v[120:123]\n"
"ds_read_b64_tr_b16 v[202:203], v235 offset:5376\n"
"v_accvgpr_read_b32 v188, a76\n"
"v_mfma_f32_16x16x32_bf16 v[120:123], a[216:219], v[160:163], v[120:123]\n"
"s_waitcnt lgkmcnt(6)\n"
"v_accvgpr_read_b32 v189, a77\n"
"v_accvgpr_read_b32 v190, a78\n"
"v_mfma_f32_16x16x32_bf16 v[124:127], a[216:219], v[168:171], v[124:127]\n"
"s_waitcnt lgkmcnt(3)\n"
"ds_read_b64_tr_b16 v[160:161], v235 offset:10496\n"
"v_accvgpr_read_b32 v191, a79\n"
"v_mfma_f32_16x16x32_bf16 v[124:127], a[220:223], v[172:175], v[124:127]\n"
"ds_read_b64_tr_b16 a[24:25], v221 offset:13312\n"
"ds_read_b64_tr_b16 a[26:27], v221 offset:13320\n"
"v_mfma_f32_16x16x32_bf16 v[84:87], a[212:215], v[172:175], v[84:87]\n"
"ds_read_b64_tr_b16 v[162:163], v235 offset:15616\n"
"v_mul_f32_dpp v188, v218, v188 row_newbcast:0 row_mask:0xf bank_mask:0xf\n"
"v_mfma_f32_16x16x32_bf16 v[84:87], a[208:211], v[168:171], v[84:87]\n"
"v_mul_f32_dpp v189, v218, v189 row_newbcast:4 row_mask:0xf bank_mask:0xf\n"
"v_mul_f32_dpp v190, v218, v190 row_newbcast:8 row_mask:0xf bank_mask:0xf\n"
"v_mfma_f32_16x16x32_bf16 v[44:47], a[200:203], v[168:171], v[44:47]\n"
"ds_read_b64_tr_b16 v[164:165], v249 offset:256\n"
"v_mul_f32_dpp v191, v218, v191 row_newbcast:12 row_mask:0xf bank_mask:0xf\n"
"v_mfma_f32_16x16x32_bf16 v[44:47], a[204:207], v[172:175], v[44:47]\n"
"v_cvt_pk_bf16_f32 v188, v188, v189\n"
"v_cvt_pk_bf16_f32 v189, v190, v191\n"
"v_mfma_f32_16x16x32_bf16 v[4:7], a[196:199], v[172:175], v[4:7]\n"
"ds_read_b64_tr_b16 v[166:167], v249 offset:5376\n"
"ds_write_b64 v244, v[188:189] offset:13320\n"
"v_mfma_f32_16x16x32_bf16 v[4:7], a[192:195], v[168:171], v[4:7]\n"
"s_waitcnt lgkmcnt(7)\n"
"v_accvgpr_read_b32 v184, a72\n"
"v_accvgpr_read_b32 v185, a73\n"
"v_mfma_f32_16x16x32_bf16 v[8:11], a[192:195], v[200:203], v[8:11]\n"
"s_waitcnt lgkmcnt(3)\n"
"ds_read_b64_tr_b16 v[168:169], v249 offset:10496\n"
"v_accvgpr_read_b32 v186, a74\n"
"v_mfma_f32_16x16x32_bf16 v[8:11], a[196:199], v[160:163], v[8:11]\n"
"v_accvgpr_read_b32 v187, a75\n"
"v_mul_f32_dpp v184, v218, v184 row_newbcast:0 row_mask:0xf bank_mask:0xf\n"
"v_mfma_f32_16x16x32_bf16 v[48:51], a[204:207], v[160:163], v[48:51]\n"
"ds_read_b64_tr_b16 v[170:171], v249 offset:15616\n"
"v_mul_f32_dpp v185, v218, v185 row_newbcast:4 row_mask:0xf bank_mask:0xf\n"
"v_mfma_f32_16x16x32_bf16 v[48:51], a[200:203], v[200:203], v[48:51]\n"
"buffer_store_dwordx4 a[112:115], v253, s[56:59], s76 offen offset:0 sc1\n"
"v_mul_f32_dpp v186, v218, v186 row_newbcast:8 row_mask:0xf bank_mask:0xf\n"
"v_mul_f32_dpp v187, v218, v187 row_newbcast:12 row_mask:0xf bank_mask:0xf\n"
"v_cvt_pk_bf16_f32 v187, v186, v187\n"
"v_mfma_f32_16x16x32_bf16 v[88:91], a[208:211], v[200:203], v[88:91]\n"
"ds_read_b64_tr_b16 v[172:173], v235 offset:512\n"
"v_cvt_pk_bf16_f32 v186, v184, v185\n"
"v_mfma_f32_16x16x32_bf16 v[88:91], a[212:215], v[160:163], v[88:91]\n"
"ds_write_b64 v244, v[186:187] offset:13312\n"
"v_mfma_f32_16x16x32_bf16 v[128:131], a[220:223], v[160:163], v[128:131]\n"
"ds_read_b64_tr_b16 v[174:175], v235 offset:5632\n"
"v_accvgpr_read_b32 v180, a124\n"
"v_mfma_f32_16x16x32_bf16 v[128:131], a[216:219], v[200:203], v[128:131]\n"
"s_waitcnt lgkmcnt(6)\n"
"v_accvgpr_read_b32 v181, a125\n"
"v_accvgpr_read_b32 v182, a126\n"
"v_mfma_f32_16x16x32_bf16 v[132:135], a[216:219], v[164:167], v[132:135]\n"
"s_waitcnt lgkmcnt(3)\n"
"ds_read_b64_tr_b16 v[200:201], v235 offset:10752\n"
"v_accvgpr_read_b32 v183, a127\n"
"v_mfma_f32_16x16x32_bf16 v[132:135], a[220:223], v[168:171], v[132:135]\n"
"ds_read_b64_tr_b16 a[72:73], v221 offset:13312\n"
"ds_read_b64_tr_b16 a[74:75], v221 offset:13320\n"
"v_mfma_f32_16x16x32_bf16 v[92:95], a[212:215], v[168:171], v[92:95]\n"
"ds_read_b64_tr_b16 v[202:203], v235 offset:15872\n"
"v_mul_f32_dpp v180, v220, v180 row_newbcast:0 row_mask:0xf bank_mask:0xf\n"
"v_mfma_f32_16x16x32_bf16 v[92:95], a[208:211], v[164:167], v[92:95]\n"
"v_mul_f32_dpp v181, v220, v181 row_newbcast:4 row_mask:0xf bank_mask:0xf\n"
"v_mul_f32_dpp v182, v220, v182 row_newbcast:8 row_mask:0xf bank_mask:0xf\n"
"v_mfma_f32_16x16x32_bf16 v[52:55], a[200:203], v[164:167], v[52:55]\n"
"ds_read_b64_tr_b16 v[160:161], v249 offset:512\n"
"v_mul_f32_dpp v183, v220, v183 row_newbcast:12 row_mask:0xf bank_mask:0xf\n"
"v_mfma_f32_16x16x32_bf16 v[52:55], a[204:207], v[168:171], v[52:55]\n"
"v_cvt_pk_bf16_f32 v180, v180, v181\n"
"v_cvt_pk_bf16_f32 v181, v182, v183\n"
"v_mfma_f32_16x16x32_bf16 v[12:15], a[196:199], v[168:171], v[12:15]\n"
"ds_read_b64_tr_b16 v[162:163], v249 offset:5632\n"
"ds_write_b64 v244, v[180:181] offset:13320\n"
"v_mfma_f32_16x16x32_bf16 v[12:15], a[192:195], v[164:167], v[12:15]\n"
"s_waitcnt lgkmcnt(7)\n"
"v_accvgpr_read_b32 v176, a120\n"
"v_accvgpr_read_b32 v177, a121\n"
"v_mfma_f32_16x16x32_bf16 v[16:19], a[192:195], v[172:175], v[16:19]\n"
"s_waitcnt lgkmcnt(3)\n"
"ds_read_b64_tr_b16 v[164:165], v249 offset:10752\n"
"v_accvgpr_read_b32 v178, a122\n"
"v_mfma_f32_16x16x32_bf16 v[16:19], a[196:199], v[200:203], v[16:19]\n"
"s_waitcnt vmcnt(18)\n"
"s_barrier\n"
"v_mfma_f32_16x16x32_bf16 v[56:59], a[204:207], v[200:203], v[56:59]\n"
"s_add_u32 s72, s92, 2097152\n"
"ds_read_b64_tr_b16 v[166:167], v249 offset:15872\n"
"v_accvgpr_read_b32 v179, a123\n"
"v_mfma_f32_16x16x32_bf16 v[56:59], a[200:203], v[172:175], v[56:59]\n"
"buffer_load_dwordx4 a[4:7], v251, s[56:59], s72 offen offset:0 sc1\n"
"v_mfma_f32_16x16x32_bf16 v[96:99], a[208:211], v[172:175], v[96:99]\n"
"ds_read_b64_tr_b16 v[168:169], v235 offset:768\n"
"v_mul_f32_dpp v176, v220, v176 row_newbcast:0 row_mask:0xf bank_mask:0xf\n"
"v_mfma_f32_16x16x32_bf16 v[96:99], a[212:215], v[200:203], v[96:99]\n"
"s_add_u32 s74, s19, 327680\n"
"v_mul_f32_dpp v177, v220, v177 row_newbcast:4 row_mask:0xf bank_mask:0xf\n"
"v_mul_f32_dpp v178, v220, v178 row_newbcast:8 row_mask:0xf bank_mask:0xf\n"
"v_mfma_f32_16x16x32_bf16 v[136:139], a[220:223], v[200:203], v[136:139]\n"
"s_add_u32 m0, s79, 143360\n"
"ds_read_b64_tr_b16 v[170:171], v235 offset:5888\n"
"v_mul_f32_dpp v179, v220, v179 row_newbcast:12 row_mask:0xf bank_mask:0xf\n"
"v_mfma_f32_16x16x32_bf16 v[136:139], a[216:219], v[172:175], v[136:139]\n"
"s_waitcnt lgkmcnt(5)\n"
"buffer_load_dwordx4 v224, s[28:31], s74 offen offset:0 lds\n"
"v_mfma_f32_16x16x32_bf16 v[140:143], a[216:219], v[160:163], v[140:143]\n"
"s_waitcnt lgkmcnt(2)\n"
"ds_read_b64_tr_b16 v[172:173], v235 offset:11008\n"
"v_cvt_pk_bf16_f32 v179, v178, v179\n"
"v_mfma_f32_16x16x32_bf16 v[140:143], a[220:223], v[164:167], v[140:143]\n"
"v_cvt_pk_bf16_f32 v178, v176, v177\n"
"v_accvgpr_read_b32 v188, a172\n"
"v_mfma_f32_16x16x32_bf16 v[100:103], a[212:215], v[164:167], v[100:103]\n"
"ds_read_b64_tr_b16 v[174:175], v235 offset:16128\n"
"ds_write_b64 v244, v[178:179] offset:13312\n"
"v_mfma_f32_16x16x32_bf16 v[100:103], a[208:211], v[160:163], v[100:103]\n"
"buffer_load_dwordx4 a[12:15], v251, s[56:59], s72 offen offset:1024 sc1\n"
"v_mfma_f32_16x16x32_bf16 v[60:63], a[200:203], v[160:163], v[60:63]\n"
"ds_read_b64_tr_b16 v[200:201], v249 offset:768\n"
"v_accvgpr_read_b32 v189, a173\n"
"v_mfma_f32_16x16x32_bf16 v[60:63], a[204:207], v[164:167], v[60:63]\n"
"v_accvgpr_read_b32 v190, a174\n"
"v_accvgpr_read_b32 v191, a175\n"
"v_mfma_f32_16x16x32_bf16 v[20:23], a[196:199], v[164:167], v[20:23]\n"
"ds_read_b64_tr_b16 v[202:203], v249 offset:5888\n"
"ds_read_b64_tr_b16 a[120:121], v221 offset:13312\n"
"v_mfma_f32_16x16x32_bf16 v[20:23], a[192:195], v[160:163], v[20:23]\n"
"s_waitcnt lgkmcnt(6)\n"
"buffer_load_dwordx4 v225, s[28:31], s74 offen offset:1024 lds\n"
"v_mfma_f32_16x16x32_bf16 v[24:27], a[192:195], v[168:171], v[24:27]\n"
"s_waitcnt lgkmcnt(4)\n"
"ds_read_b64_tr_b16 v[160:161], v249 offset:11008\n"
"ds_read_b64_tr_b16 a[122:123], v221 offset:13320\n"
"v_mfma_f32_16x16x32_bf16 v[24:27], a[196:199], v[172:175], v[24:27]\n"
"v_mul_f32_dpp v188, v222, v188 row_newbcast:0 row_mask:0xf bank_mask:0xf\n"
"v_mul_f32_dpp v189, v222, v189 row_newbcast:4 row_mask:0xf bank_mask:0xf\n"
"v_mfma_f32_16x16x32_bf16 v[64:67], a[204:207], v[172:175], v[64:67]\n"
"ds_read_b64_tr_b16 v[162:163], v249 offset:16128\n"
"v_mul_f32_dpp v190, v222, v190 row_newbcast:8 row_mask:0xf bank_mask:0xf\n"
"v_mfma_f32_16x16x32_bf16 v[64:67], a[200:203], v[168:171], v[64:67]\n"
"buffer_load_dwordx4 a[52:55], v251, s[56:59], s72 offen offset:2048 sc1\n"
"v_mfma_f32_16x16x32_bf16 v[104:107], a[208:211], v[168:171], v[104:107]\n"
"ds_read_b64_tr_b16 v[164:165], v235 offset:1024\n"
"v_mul_f32_dpp v191, v222, v191 row_newbcast:12 row_mask:0xf bank_mask:0xf\n"
"v_mfma_f32_16x16x32_bf16 v[104:107], a[212:215], v[172:175], v[104:107]\n"
"v_cvt_pk_bf16_f32 v188, v188, v189\n"
"v_cvt_pk_bf16_f32 v189, v190, v191\n"
"v_mfma_f32_16x16x32_bf16 v[144:147], a[220:223], v[172:175], v[144:147]\n"
"ds_read_b64_tr_b16 v[166:167], v235 offset:6144\n"
"ds_write_b64 v244, v[188:189] offset:13320\n"
"v_mfma_f32_16x16x32_bf16 v[144:147], a[216:219], v[168:171], v[144:147]\n"
"s_waitcnt lgkmcnt(7)\n"
"buffer_load_dwordx4 v226, s[28:31], s74 offen offset:2048 lds\n"
"v_mfma_f32_16x16x32_bf16 v[148:151], a[216:219], v[200:203], v[148:151]\n"
"s_waitcnt lgkmcnt(3)\n"
"ds_read_b64_tr_b16 v[168:169], v235 offset:11264\n"
"v_accvgpr_read_b32 v184, a168\n"
"v_mfma_f32_16x16x32_bf16 v[148:151], a[220:223], v[160:163], v[148:151]\n"
"v_accvgpr_read_b32 v185, a169\n"
"v_accvgpr_read_b32 v186, a170\n"
"v_mfma_f32_16x16x32_bf16 v[108:111], a[212:215], v[160:163], v[108:111]\n"
"ds_read_b64_tr_b16 v[170:171], v235 offset:16384\n"
"v_accvgpr_read_b32 v187, a171\n"
"v_mfma_f32_16x16x32_bf16 v[108:111], a[208:211], v[200:203], v[108:111]\n"
"buffer_load_dwordx4 a[60:63], v251, s[56:59], s72 offen offset:3072 sc1\n"
"v_mfma_f32_16x16x32_bf16 v[68:71], a[200:203], v[200:203], v[68:71]\n"
"ds_read_b64_tr_b16 v[172:173], v249 offset:1024\n"
"v_mul_f32_dpp v184, v222, v184 row_newbcast:0 row_mask:0xf bank_mask:0xf\n"
"v_mfma_f32_16x16x32_bf16 v[68:71], a[204:207], v[160:163], v[68:71]\n"
"v_mul_f32_dpp v185, v222, v185 row_newbcast:4 row_mask:0xf bank_mask:0xf\n"
"v_mul_f32_dpp v186, v222, v186 row_newbcast:8 row_mask:0xf bank_mask:0xf\n"
"v_mfma_f32_16x16x32_bf16 v[28:31], a[196:199], v[160:163], v[28:31]\n"
"ds_read_b64_tr_b16 v[174:175], v249 offset:6144\n"
"v_mul_f32_dpp v187, v222, v187 row_newbcast:12 row_mask:0xf bank_mask:0xf\n"
"v_mfma_f32_16x16x32_bf16 v[28:31], a[192:195], v[200:203], v[28:31]\n"
"s_waitcnt lgkmcnt(5)\n"
"buffer_load_dwordx4 v227, s[28:31], s74 offen offset:3072 lds\n"
"v_mfma_f32_16x16x32_bf16 v[32:35], a[192:195], v[164:167], v[32:35]\n"
"s_waitcnt lgkmcnt(2)\n"
"ds_read_b64_tr_b16 v[200:201], v249 offset:11264\n"
"v_cvt_pk_bf16_f32 v187, v186, v187\n"
"v_mfma_f32_16x16x32_bf16 v[32:35], a[196:199], v[168:171], v[32:35]\n"
"v_cvt_pk_bf16_f32 v186, v184, v185\n"
"v_accvgpr_read_b32 v180, a36\n"
"v_mfma_f32_16x16x32_bf16 v[72:75], a[204:207], v[168:171], v[72:75]\n"
"ds_read_b64_tr_b16 v[202:203], v249 offset:16384\n"
"ds_write_b64 v244, v[186:187] offset:13312\n"
"v_mfma_f32_16x16x32_bf16 v[72:75], a[200:203], v[164:167], v[72:75]\n"
"buffer_store_dwordx4 a[160:163], v253, s[56:59], s76 offen offset:2048 sc1\n"
"v_accvgpr_read_b32 v181, a37\n"
"v_accvgpr_read_b32 v182, a38\n"
"v_accvgpr_read_b32 v183, a39\n"
"v_mfma_f32_16x16x32_bf16 v[112:115], a[208:211], v[164:167], v[112:115]\n"
"ds_read_b64_tr_b16 v[160:161], v228 offset:40960\n"
"ds_read_b64_tr_b16 a[168:169], v221 offset:13312\n"
"v_mfma_f32_16x16x32_bf16 v[112:115], a[212:215], v[168:171], v[112:115]\n"
"ds_read_b64_tr_b16 a[170:171], v221 offset:13320\n"
"v_mul_f32_dpp v180, v216, v180 row_newbcast:0 row_mask:0xf bank_mask:0xf\n"
"v_mfma_f32_16x16x32_bf16 v[152:155], a[220:223], v[168:171], v[152:155]\n"
"s_add_u32 m0, s79, 147456\n"
"ds_read_b64_tr_b16 v[162:163], v228 offset:46080\n"
"v_mul_f32_dpp v181, v216, v181 row_newbcast:4 row_mask:0xf bank_mask:0xf\n"
"v_mfma_f32_16x16x32_bf16 v[152:155], a[216:219], v[164:167], v[152:155]\n"
"s_waitcnt lgkmcnt(7)\n"
"buffer_load_dwordx4 v234, s[28:31], s74 offen offset:0 lds\n"
"v_mfma_f32_16x16x32_bf16 v[156:159], a[216:219], v[172:175], v[156:159]\n"
"s_waitcnt lgkmcnt(5)\n"
"ds_read_b64_tr_b16 v[164:165], v228 offset:51200\n"
"v_mul_f32_dpp v182, v216, v182 row_newbcast:8 row_mask:0xf bank_mask:0xf\n"
"v_mfma_f32_16x16x32_bf16 v[156:159], a[220:223], v[200:203], v[156:159]\n"
"v_mul_f32_dpp v183, v216, v183 row_newbcast:12 row_mask:0xf bank_mask:0xf\n"
"v_cvt_pk_bf16_f32 v180, v180, v181\n"
"v_mfma_f32_16x16x32_bf16 v[116:119], a[212:215], v[200:203], v[116:119]\n"
"ds_read_b64_tr_b16 v[166:167], v228 offset:56320\n"
"v_cvt_pk_bf16_f32 v181, v182, v183\n"
"v_mfma_f32_16x16x32_bf16 v[116:119], a[208:211], v[172:175], v[116:119]\n"
"ds_write_b64 v244, v[180:181] offset:13320\n"
"v_mfma_f32_16x16x32_bf16 v[76:79], a[200:203], v[172:175], v[76:79]\n"
"ds_read_b64_tr_b16 v[168:169], v229 offset:40960\n"
"v_accvgpr_read_b32 v176, a32\n"
"v_mfma_f32_16x16x32_bf16 v[76:79], a[204:207], v[200:203], v[76:79]\n"
"s_waitcnt vmcnt(16)\n"
"v_accvgpr_read_b32 v177, a33\n"
"v_accvgpr_read_b32 v178, a34\n"
"v_mfma_f32_16x16x32_bf16 v[36:39], a[196:199], v[200:203], v[36:39]\n"
"ds_read_b64_tr_b16 v[170:171], v229 offset:46080\n"
"v_accvgpr_read_b32 v179, a35\n"
"v_mfma_f32_16x16x32_bf16 v[36:39], a[192:195], v[172:175], v[36:39]\n"
"s_waitcnt lgkmcnt(5)\n"
"v_mul_f32_dpp v176, v216, v176 row_newbcast:0 row_mask:0xf bank_mask:0xf\n"
"v_mfma_f32_16x16x32_bf16 v[0:3], a[224:227], v[160:163], v[0:3]\n"
"s_waitcnt lgkmcnt(3)\n"
"ds_read_b64_tr_b16 v[172:173], v229 offset:51200\n"
"v_mul_f32_dpp v177, v216, v177 row_newbcast:4 row_mask:0xf bank_mask:0xf\n"
"v_mfma_f32_16x16x32_bf16 v[0:3], a[228:231], v[164:167], v[0:3]\n"
"v_mul_f32_dpp v178, v216, v178 row_newbcast:8 row_mask:0xf bank_mask:0xf\n"
"v_mul_f32_dpp v179, v216, v179 row_newbcast:12 row_mask:0xf bank_mask:0xf\n"
"v_mfma_f32_16x16x32_bf16 v[40:43], a[236:239], v[164:167], v[40:43]\n"
"ds_read_b64_tr_b16 v[174:175], v229 offset:56320\n"
"v_cvt_pk_bf16_f32 v179, v178, v179\n"
"v_mfma_f32_16x16x32_bf16 v[40:43], a[232:235], v[160:163], v[40:43]\n"
"buffer_store_dwordx4 a[24:27], v251, s[56:59], s76 offen offset:1024 sc1\n"
"v_cvt_pk_bf16_f32 v178, v176, v177\n"
"v_accvgpr_read_b32 v188, a84\n"
"v_accvgpr_read_b32 v189, a85\n"
"v_mfma_f32_16x16x32_bf16 v[80:83], a[240:243], v[160:163], v[80:83]\n"
"ds_read_b64_tr_b16 v[200:201], v228 offset:41216\n"
"ds_write_b64 v244, v[178:179] offset:13312\n"
"v_mfma_f32_16x16x32_bf16 v[80:83], a[244:247], v[164:167], v[80:83]\n"
"v_accvgpr_read_b32 v190, a86\n"
"v_accvgpr_read_b32 v191, a87\n"
"v_mfma_f32_16x16x32_bf16 v[120:123], a[252:255], v[164:167], v[120:123]\n"
"ds_read_b64_tr_b16 v[202:203], v228 offset:46336\n"
"ds_read_b64_tr_b16 a[32:33], v221 offset:13312\n"
"v_mfma_f32_16x16x32_bf16 v[120:123], a[248:251], v[160:163], v[120:123]\n"
"s_waitcnt lgkmcnt(6)\n"
"ds_read_b64_tr_b16 a[34:35], v221 offset:13320\n"
"v_mul_f32_dpp v188, v218, v188 row_newbcast:0 row_mask:0xf bank_mask:0xf\n"
"v_mfma_f32_16x16x32_bf16 v[124:127], a[248:251], v[168:171], v[124:127]\n"
"s_waitcnt lgkmcnt(5)\n"
"ds_read_b64_tr_b16 v[160:161], v228 offset:51456\n"
"v_mul_f32_dpp v189, v218, v189 row_newbcast:4 row_mask:0xf bank_mask:0xf\n"
"v_mfma_f32_16x16x32_bf16 v[124:127], a[252:255], v[172:175], v[124:127]\n"
"v_mul_f32_dpp v190, v218, v190 row_newbcast:8 row_mask:0xf bank_mask:0xf\n"
"v_mul_f32_dpp v191, v218, v191 row_newbcast:12 row_mask:0xf bank_mask:0xf\n"
"v_mfma_f32_16x16x32_bf16 v[84:87], a[244:247], v[172:175], v[84:87]\n"
"ds_read_b64_tr_b16 v[162:163], v228 offset:56576\n"
"v_cvt_pk_bf16_f32 v188, v188, v189\n"
"v_mfma_f32_16x16x32_bf16 v[84:87], a[240:243], v[168:171], v[84:87]\n"
"v_cvt_pk_bf16_f32 v189, v190, v191\n"
"v_accvgpr_read_b32 v184, a80\n"
"v_mfma_f32_16x16x32_bf16 v[44:47], a[232:235], v[168:171], v[44:47]\n"
"ds_read_b64_tr_b16 v[164:165], v229 offset:41216\n"
"ds_write_b64 v244, v[188:189] offset:13320\n"
"v_mfma_f32_16x16x32_bf16 v[44:47], a[236:239], v[172:175], v[44:47]\n"
"v_accvgpr_read_b32 v185, a81\n"
"v_accvgpr_read_b32 v186, a82\n"
"v_mfma_f32_16x16x32_bf16 v[4:7], a[228:231], v[172:175], v[4:7]\n"
"ds_read_b64_tr_b16 v[166:167], v229 offset:46336\n"
"v_accvgpr_read_b32 v187, a83\n"
"v_mfma_f32_16x16x32_bf16 v[4:7], a[224:227], v[168:171], v[4:7]\n"
"s_waitcnt lgkmcnt(7)\n"
"v_mul_f32_dpp v184, v218, v184 row_newbcast:0 row_mask:0xf bank_mask:0xf\n"
"v_mul_f32_dpp v185, v218, v185 row_newbcast:4 row_mask:0xf bank_mask:0xf\n"
"v_mfma_f32_16x16x32_bf16 v[8:11], a[224:227], v[200:203], v[8:11]\n"
"s_waitcnt lgkmcnt(3)\n"
"ds_read_b64_tr_b16 v[168:169], v229 offset:51456\n"
"v_mul_f32_dpp v186, v218, v186 row_newbcast:8 row_mask:0xf bank_mask:0xf\n"
"v_mfma_f32_16x16x32_bf16 v[8:11], a[228:231], v[160:163], v[8:11]\n"
"v_mul_f32_dpp v187, v218, v187 row_newbcast:12 row_mask:0xf bank_mask:0xf\n"
"v_cvt_pk_bf16_f32 v187, v186, v187\n"
"v_mfma_f32_16x16x32_bf16 v[48:51], a[236:239], v[160:163], v[48:51]\n"
"ds_read_b64_tr_b16 v[170:171], v229 offset:56576\n"
"v_cvt_pk_bf16_f32 v186, v184, v185\n"
"v_mfma_f32_16x16x32_bf16 v[48:51], a[232:235], v[200:203], v[48:51]\n"
"buffer_store_dwordx4 a[72:75], v251, s[56:59], s76 offen offset:3072 sc1\n"
"v_accvgpr_read_b32 v180, a132\n"
"v_accvgpr_read_b32 v181, a133\n"
"v_accvgpr_read_b32 v182, a134\n"
"v_mfma_f32_16x16x32_bf16 v[88:91], a[240:243], v[200:203], v[88:91]\n"
"ds_read_b64_tr_b16 v[172:173], v228 offset:41472\n"
"ds_write_b64 v244, v[186:187] offset:13312\n"
"v_mfma_f32_16x16x32_bf16 v[88:91], a[244:247], v[160:163], v[88:91]\n"
"v_accvgpr_read_b32 v183, a135\n"
"v_mul_f32_dpp v180, v220, v180 row_newbcast:0 row_mask:0xf bank_mask:0xf\n"
"v_mfma_f32_16x16x32_bf16 v[128:131], a[252:255], v[160:163], v[128:131]\n"
"ds_read_b64_tr_b16 v[174:175], v228 offset:46592\n"
"ds_read_b64_tr_b16 a[80:81], v221 offset:13312\n"
"v_mfma_f32_16x16x32_bf16 v[128:131], a[248:251], v[200:203], v[128:131]\n"
"s_waitcnt lgkmcnt(6)\n"
"ds_read_b64_tr_b16 a[82:83], v221 offset:13320\n"
"v_mul_f32_dpp v181, v220, v181 row_newbcast:4 row_mask:0xf bank_mask:0xf\n"
"v_mfma_f32_16x16x32_bf16 v[132:135], a[248:251], v[164:167], v[132:135]\n"
"s_waitcnt lgkmcnt(5)\n"
"ds_read_b64_tr_b16 v[200:201], v228 offset:51712\n"
"v_mul_f32_dpp v182, v220, v182 row_newbcast:8 row_mask:0xf bank_mask:0xf\n"
"v_mfma_f32_16x16x32_bf16 v[132:135], a[252:255], v[168:171], v[132:135]\n"
"v_mul_f32_dpp v183, v220, v183 row_newbcast:12 row_mask:0xf bank_mask:0xf\n"
"v_cvt_pk_bf16_f32 v180, v180, v181\n"
"v_mfma_f32_16x16x32_bf16 v[92:95], a[244:247], v[168:171], v[92:95]\n"
"ds_read_b64_tr_b16 v[202:203], v228 offset:56832\n"
"v_cvt_pk_bf16_f32 v181, v182, v183\n"
"v_mfma_f32_16x16x32_bf16 v[92:95], a[240:243], v[164:167], v[92:95]\n"
"ds_write_b64 v244, v[180:181] offset:13320\n"
"v_mfma_f32_16x16x32_bf16 v[52:55], a[232:235], v[164:167], v[52:55]\n"
"ds_read_b64_tr_b16 v[160:161], v229 offset:41472\n"
"v_accvgpr_read_b32 v176, a128\n"
"v_mfma_f32_16x16x32_bf16 v[52:55], a[236:239], v[168:171], v[52:55]\n"
"v_accvgpr_read_b32 v177, a129\n"
"v_accvgpr_read_b32 v178, a130\n"
"v_mfma_f32_16x16x32_bf16 v[12:15], a[228:231], v[168:171], v[12:15]\n"
"ds_read_b64_tr_b16 v[162:163], v229 offset:46592\n"
"v_accvgpr_read_b32 v179, a131\n"
"v_mfma_f32_16x16x32_bf16 v[12:15], a[224:227], v[164:167], v[12:15]\n"
"s_waitcnt lgkmcnt(7)\n"
"v_mul_f32_dpp v176, v220, v176 row_newbcast:0 row_mask:0xf bank_mask:0xf\n"
"v_mul_f32_dpp v177, v220, v177 row_newbcast:4 row_mask:0xf bank_mask:0xf\n"
"v_mfma_f32_16x16x32_bf16 v[16:19], a[224:227], v[172:175], v[16:19]\n"
"s_waitcnt lgkmcnt(3)\n"
"ds_read_b64_tr_b16 v[164:165], v229 offset:51712\n"
"v_mul_f32_dpp v178, v220, v178 row_newbcast:8 row_mask:0xf bank_mask:0xf\n"
"v_mfma_f32_16x16x32_bf16 v[16:19], a[228:231], v[200:203], v[16:19]\n"
"s_waitcnt vmcnt(15)\n"
"s_barrier\n"
"v_mfma_f32_16x16x32_bf16 v[56:59], a[236:239], v[200:203], v[56:59]\n"
"ds_read_b64_tr_b16 v[166:167], v229 offset:56832\n"
"v_mul_f32_dpp v179, v220, v179 row_newbcast:12 row_mask:0xf bank_mask:0xf\n"
"v_mfma_f32_16x16x32_bf16 v[56:59], a[232:235], v[172:175], v[56:59]\n"
"buffer_load_dwordx4 a[100:103], v253, s[56:59], s72 offen offset:0 sc1\n"
"v_mfma_f32_16x16x32_bf16 v[96:99], a[240:243], v[172:175], v[96:99]\n"
"ds_read_b64_tr_b16 v[168:169], v228 offset:41728\n"
"v_cvt_pk_bf16_f32 v179, v178, v179\n"
"v_mfma_f32_16x16x32_bf16 v[96:99], a[244:247], v[200:203], v[96:99]\n"
"s_add_u32 s74, s18, 163840\n"
"v_cvt_pk_bf16_f32 v178, v176, v177\n"
"v_accvgpr_read_b32 v188, a180\n"
"v_mfma_f32_16x16x32_bf16 v[136:139], a[252:255], v[200:203], v[136:139]\n"
"s_mov_b32 m0, s79\n"
"ds_read_b64_tr_b16 v[170:171], v228 offset:46848\n"
"ds_write_b64 v244, v[178:179] offset:13312\n"
"v_mfma_f32_16x16x32_bf16 v[136:139], a[248:251], v[172:175], v[136:139]\n"
"s_waitcnt lgkmcnt(5)\n"
"buffer_load_dwordx4 v224, s[28:31], s74 offen offset:0 lds\n"
"v_mfma_f32_16x16x32_bf16 v[140:143], a[248:251], v[160:163], v[140:143]\n"
"s_waitcnt lgkmcnt(3)\n"
"ds_read_b64_tr_b16 v[172:173], v228 offset:51968\n"
"v_accvgpr_read_b32 v189, a181\n"
"v_mfma_f32_16x16x32_bf16 v[140:143], a[252:255], v[164:167], v[140:143]\n"
"v_accvgpr_read_b32 v190, a182\n"
"v_accvgpr_read_b32 v191, a183\n"
"v_mfma_f32_16x16x32_bf16 v[100:103], a[244:247], v[164:167], v[100:103]\n"
"ds_read_b64_tr_b16 v[174:175], v228 offset:57088\n"
"ds_read_b64_tr_b16 a[128:129], v221 offset:13312\n"
"v_mfma_f32_16x16x32_bf16 v[100:103], a[240:243], v[160:163], v[100:103]\n"
"buffer_load_dwordx4 a[108:111], v253, s[56:59], s72 offen offset:1024 sc1\n"
"v_mfma_f32_16x16x32_bf16 v[60:63], a[232:235], v[160:163], v[60:63]\n"
"ds_read_b64_tr_b16 v[200:201], v229 offset:41728\n"
"ds_read_b64_tr_b16 a[130:131], v221 offset:13320\n"
"v_mfma_f32_16x16x32_bf16 v[60:63], a[236:239], v[164:167], v[60:63]\n"
"v_mul_f32_dpp v188, v222, v188 row_newbcast:0 row_mask:0xf bank_mask:0xf\n"
"v_mul_f32_dpp v189, v222, v189 row_newbcast:4 row_mask:0xf bank_mask:0xf\n"
"v_mfma_f32_16x16x32_bf16 v[20:23], a[228:231], v[164:167], v[20:23]\n"
"ds_read_b64_tr_b16 v[202:203], v229 offset:46848\n"
"v_mul_f32_dpp v190, v222, v190 row_newbcast:8 row_mask:0xf bank_mask:0xf\n"
"v_mfma_f32_16x16x32_bf16 v[20:23], a[224:227], v[160:163], v[20:23]\n"
"s_waitcnt lgkmcnt(7)\n"
"buffer_load_dwordx4 v225, s[28:31], s74 offen offset:1024 lds\n"
"v_mfma_f32_16x16x32_bf16 v[24:27], a[224:227], v[168:171], v[24:27]\n"
"s_waitcnt lgkmcnt(4)\n"
"ds_read_b64_tr_b16 v[160:161], v229 offset:51968\n"
"v_mul_f32_dpp v191, v222, v191 row_newbcast:12 row_mask:0xf bank_mask:0xf\n"
"v_mfma_f32_16x16x32_bf16 v[24:27], a[228:231], v[172:175], v[24:27]\n"
"v_cvt_pk_bf16_f32 v188, v188, v189\n"
"v_cvt_pk_bf16_f32 v189, v190, v191\n"
"v_mfma_f32_16x16x32_bf16 v[64:67], a[236:239], v[172:175], v[64:67]\n"
"ds_read_b64_tr_b16 v[162:163], v229 offset:57088\n"
"ds_write_b64 v244, v[188:189] offset:13320\n"
"v_mfma_f32_16x16x32_bf16 v[64:67], a[232:235], v[168:171], v[64:67]\n"
"buffer_load_dwordx4 a[148:151], v253, s[56:59], s72 offen offset:2048 sc1\n"
"v_mfma_f32_16x16x32_bf16 v[104:107], a[240:243], v[168:171], v[104:107]\n"
"ds_read_b64_tr_b16 v[164:165], v228 offset:41984\n"
"v_accvgpr_read_b32 v184, a176\n"
"v_mfma_f32_16x16x32_bf16 v[104:107], a[244:247], v[172:175], v[104:107]\n"
"v_accvgpr_read_b32 v185, a177\n"
"v_accvgpr_read_b32 v186, a178\n"
"v_mfma_f32_16x16x32_bf16 v[144:147], a[252:255], v[172:175], v[144:147]\n"
"ds_read_b64_tr_b16 v[166:167], v228 offset:47104\n"
"v_accvgpr_read_b32 v187, a179\n"
"v_mfma_f32_16x16x32_bf16 v[144:147], a[248:251], v[168:171], v[144:147]\n"
"s_waitcnt lgkmcnt(5)\n"
"buffer_load_dwordx4 v226, s[28:31], s74 offen offset:2048 lds\n"
"v_mfma_f32_16x16x32_bf16 v[148:151], a[248:251], v[200:203], v[148:151]\n"
"s_waitcnt lgkmcnt(3)\n"
"ds_read_b64_tr_b16 v[168:169], v228 offset:52224\n"
"v_mul_f32_dpp v184, v222, v184 row_newbcast:0 row_mask:0xf bank_mask:0xf\n"
"v_mfma_f32_16x16x32_bf16 v[148:151], a[252:255], v[160:163], v[148:151]\n"
"v_mul_f32_dpp v185, v222, v185 row_newbcast:4 row_mask:0xf bank_mask:0xf\n"
"v_mul_f32_dpp v186, v222, v186 row_newbcast:8 row_mask:0xf bank_mask:0xf\n"
"v_mfma_f32_16x16x32_bf16 v[108:111], a[244:247], v[160:163], v[108:111]\n"
"ds_read_b64_tr_b16 v[170:171], v228 offset:57344\n"
"v_mul_f32_dpp v187, v222, v187 row_newbcast:12 row_mask:0xf bank_mask:0xf\n"
"v_mfma_f32_16x16x32_bf16 v[108:111], a[240:243], v[200:203], v[108:111]\n"
"buffer_load_dwordx4 a[156:159], v253, s[56:59], s72 offen offset:3072 sc1\n"
"v_mfma_f32_16x16x32_bf16 v[68:71], a[232:235], v[200:203], v[68:71]\n"
"ds_read_b64_tr_b16 v[172:173], v229 offset:41984\n"
"v_cvt_pk_bf16_f32 v187, v186, v187\n"
"v_mfma_f32_16x16x32_bf16 v[68:71], a[236:239], v[160:163], v[68:71]\n"
"v_cvt_pk_bf16_f32 v186, v184, v185\n"
"v_accvgpr_read_b32 v180, a44\n"
"v_mfma_f32_16x16x32_bf16 v[28:31], a[228:231], v[160:163], v[28:31]\n"
"ds_read_b64_tr_b16 v[174:175], v229 offset:47104\n"
"ds_write_b64 v244, v[186:187] offset:13312\n"
"v_mfma_f32_16x16x32_bf16 v[28:31], a[224:227], v[200:203], v[28:31]\n"
"s_waitcnt lgkmcnt(5)\n"
"buffer_load_dwordx4 v227, s[28:31], s74 offen offset:3072 lds\n"
"v_mfma_f32_16x16x32_bf16 v[32:35], a[224:227], v[164:167], v[32:35]\n"
"s_waitcnt lgkmcnt(3)\n"
"ds_read_b64_tr_b16 v[200:201], v229 offset:52224\n"
"v_accvgpr_read_b32 v181, a45\n"
"v_mfma_f32_16x16x32_bf16 v[32:35], a[228:231], v[168:171], v[32:35]\n"
"v_accvgpr_read_b32 v182, a46\n"
"v_accvgpr_read_b32 v183, a47\n"
"v_mfma_f32_16x16x32_bf16 v[72:75], a[236:239], v[168:171], v[72:75]\n"
"ds_read_b64_tr_b16 v[202:203], v229 offset:57344\n"
"ds_read_b64_tr_b16 a[176:177], v221 offset:13312\n"
"v_mfma_f32_16x16x32_bf16 v[72:75], a[232:235], v[164:167], v[72:75]\n"
"buffer_store_dwordx4 a[120:123], v253, s[56:59], s76 offen offset:1024 sc1\n"
"ds_read_b64_tr_b16 a[178:179], v221 offset:13320\n"
"v_mul_f32_dpp v180, v216, v180 row_newbcast:0 row_mask:0xf bank_mask:0xf\n"
"v_mul_f32_dpp v181, v216, v181 row_newbcast:4 row_mask:0xf bank_mask:0xf\n"
"v_mfma_f32_16x16x32_bf16 v[112:115], a[240:243], v[164:167], v[112:115]\n"
"ds_read_b64_tr_b16 v[160:161], v230 offset:0\n"
"v_mul_f32_dpp v182, v216, v182 row_newbcast:8 row_mask:0xf bank_mask:0xf\n"
"v_mfma_f32_16x16x32_bf16 v[112:115], a[244:247], v[168:171], v[112:115]\n"
"v_mul_f32_dpp v183, v216, v183 row_newbcast:12 row_mask:0xf bank_mask:0xf\n"
"v_cvt_pk_bf16_f32 v180, v180, v181\n"
"v_mfma_f32_16x16x32_bf16 v[152:155], a[252:255], v[168:171], v[152:155]\n"
"s_add_u32 m0, s79, 4096\n"
"ds_read_b64_tr_b16 v[162:163], v230 offset:5120\n"
"v_cvt_pk_bf16_f32 v181, v182, v183\n"
"v_mfma_f32_16x16x32_bf16 v[152:155], a[248:251], v[164:167], v[152:155]\n"
"s_waitcnt lgkmcnt(7)\n"
"buffer_load_dwordx4 v234, s[28:31], s74 offen offset:0 lds\n"
"v_mfma_f32_16x16x32_bf16 v[156:159], a[248:251], v[172:175], v[156:159]\n"
"s_waitcnt lgkmcnt(4)\n"
"ds_read_b64_tr_b16 v[164:165], v230 offset:10240\n"
"ds_write_b64 v244, v[180:181] offset:13320\n"
"v_mfma_f32_16x16x32_bf16 v[156:159], a[252:255], v[200:203], v[156:159]\n"
"v_accvgpr_read_b32 v176, a40\n"
"v_accvgpr_read_b32 v177, a41\n"
"v_mfma_f32_16x16x32_bf16 v[116:119], a[244:247], v[200:203], v[116:119]\n"
"ds_read_b64_tr_b16 v[166:167], v230 offset:15360\n"
"v_accvgpr_read_b32 v178, a42\n"
"v_mfma_f32_16x16x32_bf16 v[116:119], a[240:243], v[172:175], v[116:119]\n"
"v_accvgpr_read_b32 v179, a43\n"
"v_mul_f32_dpp v176, v216, v176 row_newbcast:0 row_mask:0xf bank_mask:0xf\n"
"v_mfma_f32_16x16x32_bf16 v[76:79], a[232:235], v[172:175], v[76:79]\n"
"ds_read_b64_tr_b16 v[168:169], v231 offset:0\n"
"v_mul_f32_dpp v177, v216, v177 row_newbcast:4 row_mask:0xf bank_mask:0xf\n"
"v_mfma_f32_16x16x32_bf16 v[76:79], a[236:239], v[200:203], v[76:79]\n"
"v_mul_f32_dpp v178, v216, v178 row_newbcast:8 row_mask:0xf bank_mask:0xf\n"
"v_mul_f32_dpp v179, v216, v179 row_newbcast:12 row_mask:0xf bank_mask:0xf\n"
"v_mfma_f32_16x16x32_bf16 v[36:39], a[228:231], v[200:203], v[36:39]\n"
"ds_read_b64_tr_b16 v[170:171], v231 offset:5120\n"
"v_cvt_pk_bf16_f32 v179, v178, v179\n"
"v_mfma_f32_16x16x32_bf16 v[36:39], a[224:227], v[172:175], v[36:39]\n"
"s_waitcnt lgkmcnt(5)\n"
"v_cvt_pk_bf16_f32 v178, v176, v177\n"
"v_mfma_f32_16x16x32_bf16 v[0:3], a[16:19], v[160:163], v[0:3]\n"
"s_waitcnt lgkmcnt(2)\n"
"ds_read_b64_tr_b16 v[172:173], v231 offset:10240\n"
"ds_write_b64 v244, v[178:179] offset:13312\n"
"v_mfma_f32_16x16x32_bf16 v[0:3], a[24:27], v[164:167], v[0:3]\n"
"v_accvgpr_read_b32 v188, a92\n"
"v_accvgpr_read_b32 v189, a93\n"
"v_mfma_f32_16x16x32_bf16 v[40:43], a[72:75], v[164:167], v[40:43]\n"
"ds_read_b64_tr_b16 v[174:175], v231 offset:15360\n"
"v_accvgpr_read_b32 v190, a94\n"
"v_mfma_f32_16x16x32_bf16 v[40:43], a[64:67], v[160:163], v[40:43]\n"
"buffer_store_dwordx4 a[168:171], v253, s[56:59], s76 offen offset:3072 sc1\n"
"v_accvgpr_read_b32 v191, a95\n"
"v_mul_f32_dpp v188, v218, v188 row_newbcast:0 row_mask:0xf bank_mask:0xf\n"
"v_mul_f32_dpp v189, v218, v189 row_newbcast:4 row_mask:0xf bank_mask:0xf\n"
"v_mfma_f32_16x16x32_bf16 v[80:83], a[112:115], v[160:163], v[80:83]\n"
"s_add_u32 s76, s76, 524288\n"
"ds_read_b64_tr_b16 v[200:201], v230 offset:256\n"
"ds_read_b64_tr_b16 a[40:41], v221 offset:13312\n"
"v_mfma_f32_16x16x32_bf16 v[80:83], a[120:123], v[164:167], v[80:83]\n"
"ds_read_b64_tr_b16 a[42:43], v221 offset:13320\n"
"v_mul_f32_dpp v190, v218, v190 row_newbcast:8 row_mask:0xf bank_mask:0xf\n"
"v_mfma_f32_16x16x32_bf16 v[120:123], a[168:171], v[164:167], v[120:123]\n"
"ds_read_b64_tr_b16 v[202:203], v230 offset:5376\n"
"v_mul_f32_dpp v191, v218, v191 row_newbcast:12 row_mask:0xf bank_mask:0xf\n"
"v_mfma_f32_16x16x32_bf16 v[120:123], a[160:163], v[160:163], v[120:123]\n"
"s_waitcnt lgkmcnt(7)\n"
"v_cvt_pk_bf16_f32 v188, v188, v189\n"
"v_cvt_pk_bf16_f32 v189, v190, v191\n"
"v_mfma_f32_16x16x32_bf16 v[124:127], a[160:163], v[168:171], v[124:127]\n"
"s_waitcnt lgkmcnt(4)\n"
"ds_read_b64_tr_b16 v[160:161], v230 offset:10496\n"
"ds_write_b64 v244, v[188:189] offset:13320\n"
"v_mfma_f32_16x16x32_bf16 v[124:127], a[168:171], v[172:175], v[124:127]\n"
"v_accvgpr_read_b32 v184, a88\n"
"v_accvgpr_read_b32 v185, a89\n"
"v_mfma_f32_16x16x32_bf16 v[84:87], a[120:123], v[172:175], v[84:87]\n"
"ds_read_b64_tr_b16 v[162:163], v230 offset:15616\n"
"v_accvgpr_read_b32 v186, a90\n"
"v_mfma_f32_16x16x32_bf16 v[84:87], a[112:115], v[168:171], v[84:87]\n"
"v_accvgpr_read_b32 v187, a91\n"
"v_mul_f32_dpp v184, v218, v184 row_newbcast:0 row_mask:0xf bank_mask:0xf\n"
"v_mfma_f32_16x16x32_bf16 v[44:47], a[64:67], v[168:171], v[44:47]\n"
"ds_read_b64_tr_b16 v[164:165], v231 offset:256\n"
"v_mul_f32_dpp v185, v218, v185 row_newbcast:4 row_mask:0xf bank_mask:0xf\n"
"v_mfma_f32_16x16x32_bf16 v[44:47], a[72:75], v[172:175], v[44:47]\n"
"v_mul_f32_dpp v186, v218, v186 row_newbcast:8 row_mask:0xf bank_mask:0xf\n"
"v_mul_f32_dpp v187, v218, v187 row_newbcast:12 row_mask:0xf bank_mask:0xf\n"
"v_mfma_f32_16x16x32_bf16 v[4:7], a[24:27], v[172:175], v[4:7]\n"
"ds_read_b64_tr_b16 v[166:167], v231 offset:5376\n"
"v_cvt_pk_bf16_f32 v187, v186, v187\n"
"v_mfma_f32_16x16x32_bf16 v[4:7], a[16:19], v[168:171], v[4:7]\n"
"s_waitcnt lgkmcnt(5)\n"
"v_cvt_pk_bf16_f32 v186, v184, v185\n"
"v_accvgpr_read_b32 v180, a140\n"
"v_mfma_f32_16x16x32_bf16 v[8:11], a[16:19], v[200:203], v[8:11]\n"
"s_waitcnt lgkmcnt(2)\n"
"ds_read_b64_tr_b16 v[168:169], v231 offset:10496\n"
"ds_write_b64 v244, v[186:187] offset:13312\n"
"v_mfma_f32_16x16x32_bf16 v[8:11], a[24:27], v[160:163], v[8:11]\n"
"v_accvgpr_read_b32 v181, a141\n"
"v_accvgpr_read_b32 v182, a142\n"
"v_mfma_f32_16x16x32_bf16 v[48:51], a[72:75], v[160:163], v[48:51]\n"
"ds_read_b64_tr_b16 v[170:171], v231 offset:15616\n"
"v_accvgpr_read_b32 v183, a143\n"
"v_mfma_f32_16x16x32_bf16 v[48:51], a[64:67], v[200:203], v[48:51]\n"
"buffer_store_dwordx4 a[32:35], v251, s[56:59], s76 offen offset:0 sc1\n"
"ds_read_b64_tr_b16 a[88:89], v221 offset:13312\n"
"ds_read_b64_tr_b16 a[90:91], v221 offset:13320\n"
"v_mul_f32_dpp v180, v220, v180 row_newbcast:0 row_mask:0xf bank_mask:0xf\n"
"v_mfma_f32_16x16x32_bf16 v[88:91], a[112:115], v[200:203], v[88:91]\n"
"ds_read_b64_tr_b16 v[172:173], v230 offset:512\n"
"v_mul_f32_dpp v181, v220, v181 row_newbcast:4 row_mask:0xf bank_mask:0xf\n"
"v_mfma_f32_16x16x32_bf16 v[88:91], a[120:123], v[160:163], v[88:91]\n"
"v_mul_f32_dpp v182, v220, v182 row_newbcast:8 row_mask:0xf bank_mask:0xf\n"
"v_mul_f32_dpp v183, v220, v183 row_newbcast:12 row_mask:0xf bank_mask:0xf\n"
"v_mfma_f32_16x16x32_bf16 v[128:131], a[168:171], v[160:163], v[128:131]\n"
"ds_read_b64_tr_b16 v[174:175], v230 offset:5632\n"
"v_cvt_pk_bf16_f32 v180, v180, v181\n"
"v_mfma_f32_16x16x32_bf16 v[128:131], a[160:163], v[200:203], v[128:131]\n"
"s_waitcnt lgkmcnt(7)\n"
"v_cvt_pk_bf16_f32 v181, v182, v183\n"
"v_accvgpr_read_b32 v176, a136\n"
"v_mfma_f32_16x16x32_bf16 v[132:135], a[160:163], v[164:167], v[132:135]\n"
"s_waitcnt lgkmcnt(4)\n"
"ds_read_b64_tr_b16 v[200:201], v230 offset:10752\n"
"ds_write_b64 v244, v[180:181] offset:13320\n"
"v_mfma_f32_16x16x32_bf16 v[132:135], a[168:171], v[168:171], v[132:135]\n"
"v_accvgpr_read_b32 v177, a137\n"
"v_accvgpr_read_b32 v178, a138\n"
"v_mfma_f32_16x16x32_bf16 v[92:95], a[120:123], v[168:171], v[92:95]\n"
"ds_read_b64_tr_b16 v[202:203], v230 offset:15872\n"
"v_accvgpr_read_b32 v179, a139\n"
"v_mfma_f32_16x16x32_bf16 v[92:95], a[112:115], v[164:167], v[92:95]\n"
"global_store_dword v208, v209, s[14:15] sc1\n"
"v_mul_f32_dpp v176, v220, v176 row_newbcast:0 row_mask:0xf bank_mask:0xf\n"
"v_mfma_f32_16x16x32_bf16 v[52:55], a[64:67], v[164:167], v[52:55]\n"
"ds_read_b64_tr_b16 v[160:161], v231 offset:512\n"
"v_add_u32 v208, 16, v208\n"
"v_mfma_f32_16x16x32_bf16 v[52:55], a[72:75], v[168:171], v[52:55]\n"
"v_mul_f32_dpp v177, v220, v177 row_newbcast:4 row_mask:0xf bank_mask:0xf\n"
"v_mul_f32_dpp v178, v220, v178 row_newbcast:8 row_mask:0xf bank_mask:0xf\n"
"v_mfma_f32_16x16x32_bf16 v[12:15], a[24:27], v[168:171], v[12:15]\n"
"ds_read_b64_tr_b16 v[162:163], v231 offset:5632\n"
"v_mul_f32_dpp v179, v220, v179 row_newbcast:12 row_mask:0xf bank_mask:0xf\n"
"v_mfma_f32_16x16x32_bf16 v[12:15], a[16:19], v[164:167], v[12:15]\n"
"s_waitcnt lgkmcnt(5)\n"
"v_cvt_pk_bf16_f32 v179, v178, v179\n"
"v_cvt_pk_bf16_f32 v178, v176, v177\n"
"v_mfma_f32_16x16x32_bf16 v[16:19], a[16:19], v[172:175], v[16:19]\n"
"s_waitcnt lgkmcnt(2)\n"
"ds_read_b64_tr_b16 v[164:165], v231 offset:10752\n"
"ds_write_b64 v244, v[178:179] offset:13312\n"
"v_mfma_f32_16x16x32_bf16 v[16:19], a[24:27], v[200:203], v[16:19]\n"
"s_waitcnt vmcnt(15)\n"
"s_barrier\n"
"v_mfma_f32_16x16x32_bf16 v[56:59], a[72:75], v[200:203], v[56:59]\n"
"s_add_u32 s72, s92, 2621440\n"
"ds_read_b64_tr_b16 v[166:167], v231 offset:15872\n"
"v_accvgpr_read_b32 v188, a188\n"
"v_mfma_f32_16x16x32_bf16 v[56:59], a[64:67], v[172:175], v[56:59]\n"
"buffer_load_dwordx4 a[192:195], v251, s[56:59], s72 offen offset:0 sc1\n"
"v_mfma_f32_16x16x32_bf16 v[96:99], a[112:115], v[172:175], v[96:99]\n"
"ds_read_b64_tr_b16 v[168:169], v230 offset:768\n"
"v_accvgpr_read_b32 v189, a189\n"
"v_mfma_f32_16x16x32_bf16 v[96:99], a[120:123], v[200:203], v[96:99]\n"
"s_add_u32 s74, s19, 409600\n"
"v_accvgpr_read_b32 v190, a190\n"
"v_accvgpr_read_b32 v191, a191\n"
"v_mfma_f32_16x16x32_bf16 v[136:139], a[168:171], v[200:203], v[136:139]\n"
"s_add_u32 m0, s79, 102400\n"
"ds_read_b64_tr_b16 v[170:171], v230 offset:5888\n"
"ds_read_b64_tr_b16 a[136:137], v221 offset:13312\n"
"v_mfma_f32_16x16x32_bf16 v[136:139], a[160:163], v[172:175], v[136:139]\n"
"s_waitcnt lgkmcnt(6)\n"
"buffer_load_dwordx4 v224, s[28:31], s74 offen offset:0 lds\n"
"v_mfma_f32_16x16x32_bf16 v[140:143], a[160:163], v[160:163], v[140:143]\n"
"s_waitcnt lgkmcnt(3)\n"
"ds_read_b64_tr_b16 v[172:173], v230 offset:11008\n"
"ds_read_b64_tr_b16 a[138:139], v221 offset:13320\n"
"v_mfma_f32_16x16x32_bf16 v[140:143], a[168:171], v[164:167], v[140:143]\n"
"v_mul_f32_dpp v188, v222, v188 row_newbcast:0 row_mask:0xf bank_mask:0xf\n"
"v_mul_f32_dpp v189, v222, v189 row_newbcast:4 row_mask:0xf bank_mask:0xf\n"
"v_mfma_f32_16x16x32_bf16 v[100:103], a[120:123], v[164:167], v[100:103]\n"
"ds_read_b64_tr_b16 v[174:175], v230 offset:16128\n"
"v_mul_f32_dpp v190, v222, v190 row_newbcast:8 row_mask:0xf bank_mask:0xf\n"
"v_mfma_f32_16x16x32_bf16 v[100:103], a[112:115], v[160:163], v[100:103]\n"
"buffer_load_dwordx4 a[196:199], v251, s[56:59], s72 offen offset:1024 sc1\n"
"v_mfma_f32_16x16x32_bf16 v[60:63], a[64:67], v[160:163], v[60:63]\n"
"ds_read_b64_tr_b16 v[200:201], v231 offset:768\n"
"v_mul_f32_dpp v191, v222, v191 row_newbcast:12 row_mask:0xf bank_mask:0xf\n"
"v_mfma_f32_16x16x32_bf16 v[60:63], a[72:75], v[164:167], v[60:63]\n"
"v_cvt_pk_bf16_f32 v188, v188, v189\n"
"v_cvt_pk_bf16_f32 v189, v190, v191\n"
"v_mfma_f32_16x16x32_bf16 v[20:23], a[24:27], v[164:167], v[20:23]\n"
"ds_read_b64_tr_b16 v[202:203], v231 offset:5888\n"
"ds_write_b64 v244, v[188:189] offset:13320\n"
"v_mfma_f32_16x16x32_bf16 v[20:23], a[16:19], v[160:163], v[20:23]\n"
"s_waitcnt lgkmcnt(7)\n"
"buffer_load_dwordx4 v225, s[28:31], s74 offen offset:1024 lds\n"
"v_mfma_f32_16x16x32_bf16 v[24:27], a[16:19], v[168:171], v[24:27]\n"
"s_waitcnt lgkmcnt(3)\n"
"ds_read_b64_tr_b16 v[160:161], v231 offset:11008\n"
"v_accvgpr_read_b32 v184, a184\n"
"v_mfma_f32_16x16x32_bf16 v[24:27], a[24:27], v[172:175], v[24:27]\n"
"v_accvgpr_read_b32 v185, a185\n"
"v_accvgpr_read_b32 v186, a186\n"
"v_mfma_f32_16x16x32_bf16 v[64:67], a[72:75], v[172:175], v[64:67]\n"
"ds_read_b64_tr_b16 v[162:163], v231 offset:16128\n"
"v_accvgpr_read_b32 v187, a187\n"
"v_mfma_f32_16x16x32_bf16 v[64:67], a[64:67], v[168:171], v[64:67]\n"
"buffer_load_dwordx4 a[200:203], v251, s[56:59], s72 offen offset:2048 sc1\n"
"v_mfma_f32_16x16x32_bf16 v[104:107], a[112:115], v[168:171], v[104:107]\n"
"ds_read_b64_tr_b16 v[164:165], v230 offset:1024\n"
"v_mul_f32_dpp v184, v222, v184 row_newbcast:0 row_mask:0xf bank_mask:0xf\n"
"v_mfma_f32_16x16x32_bf16 v[104:107], a[120:123], v[172:175], v[104:107]\n"
"v_mul_f32_dpp v185, v222, v185 row_newbcast:4 row_mask:0xf bank_mask:0xf\n"
"v_mul_f32_dpp v186, v222, v186 row_newbcast:8 row_mask:0xf bank_mask:0xf\n"
"v_mfma_f32_16x16x32_bf16 v[144:147], a[168:171], v[172:175], v[144:147]\n"
"ds_read_b64_tr_b16 v[166:167], v230 offset:6144\n"
"v_mul_f32_dpp v187, v222, v187 row_newbcast:12 row_mask:0xf bank_mask:0xf\n"
"v_mfma_f32_16x16x32_bf16 v[144:147], a[160:163], v[168:171], v[144:147]\n"
"s_waitcnt lgkmcnt(5)\n"
"buffer_load_dwordx4 v226, s[28:31], s74 offen offset:2048 lds\n"
"v_mfma_f32_16x16x32_bf16 v[148:151], a[160:163], v[200:203], v[148:151]\n"
"s_waitcnt lgkmcnt(2)\n"
"ds_read_b64_tr_b16 v[168:169], v230 offset:11264\n"
"v_cvt_pk_bf16_f32 v187, v186, v187\n"
"v_mfma_f32_16x16x32_bf16 v[148:151], a[168:171], v[160:163], v[148:151]\n"
"v_cvt_pk_bf16_f32 v186, v184, v185\n"
"v_mfma_f32_16x16x32_bf16 v[108:111], a[120:123], v[160:163], v[108:111]\n"
"ds_read_b64_tr_b16 v[170:171], v230 offset:16384\n"
"ds_write_b64 v244, v[186:187] offset:13312\n"
"v_mfma_f32_16x16x32_bf16 v[108:111], a[112:115], v[200:203], v[108:111]\n"
"buffer_load_dwordx4 a[204:207], v251, s[56:59], s72 offen offset:3072 sc1\n"
"v_mfma_f32_16x16x32_bf16 v[68:71], a[64:67], v[200:203], v[68:71]\n"
"ds_read_b64_tr_b16 v[172:173], v231 offset:1024\n"
"ds_read_b64_tr_b16 a[184:185], v221 offset:13312\n"
"v_mfma_f32_16x16x32_bf16 v[68:71], a[72:75], v[160:163], v[68:71]\n"
"ds_read_b64_tr_b16 a[186:187], v221 offset:13320\n"
"v_mfma_f32_16x16x32_bf16 v[28:31], a[24:27], v[160:163], v[28:31]\n"
"ds_read_b64_tr_b16 v[174:175], v231 offset:6144\n"
"v_mfma_f32_16x16x32_bf16 v[28:31], a[16:19], v[200:203], v[28:31]\n"
"s_waitcnt lgkmcnt(7)\n"
"buffer_load_dwordx4 v227, s[28:31], s74 offen offset:3072 lds\n"
"v_mfma_f32_16x16x32_bf16 v[32:35], a[16:19], v[164:167], v[32:35]\n"
"s_waitcnt lgkmcnt(5)\n"
"ds_read_b64_tr_b16 v[200:201], v231 offset:11264\n"
"v_mfma_f32_16x16x32_bf16 v[32:35], a[24:27], v[168:171], v[32:35]\n"
"v_mfma_f32_16x16x32_bf16 v[72:75], a[72:75], v[168:171], v[72:75]\n"
"ds_read_b64_tr_b16 v[202:203], v231 offset:16384\n"
"v_mfma_f32_16x16x32_bf16 v[72:75], a[64:67], v[164:167], v[72:75]\n"
"buffer_store_dwordx4 a[80:83], v251, s[56:59], s76 offen offset:2048 sc1\n"
"v_mfma_f32_16x16x32_bf16 v[112:115], a[112:115], v[164:167], v[112:115]\n"
"ds_read_b64_tr_b16 v[160:161], v230 offset:20480\n"
"v_mfma_f32_16x16x32_bf16 v[112:115], a[120:123], v[168:171], v[112:115]\n"
"v_mfma_f32_16x16x32_bf16 v[152:155], a[168:171], v[168:171], v[152:155]\n"
"s_add_u32 m0, s79, 106496\n"
"ds_read_b64_tr_b16 v[162:163], v230 offset:25600\n"
"v_mfma_f32_16x16x32_bf16 v[152:155], a[160:163], v[164:167], v[152:155]\n"
"s_waitcnt lgkmcnt(4)\n"
"buffer_load_dwordx4 v234, s[28:31], s74 offen offset:0 lds\n"
"v_mfma_f32_16x16x32_bf16 v[156:159], a[160:163], v[172:175], v[156:159]\n"
"s_waitcnt lgkmcnt(2)\n"
"ds_read_b64_tr_b16 v[164:165], v230 offset:30720\n"
"v_mfma_f32_16x16x32_bf16 v[156:159], a[168:171], v[200:203], v[156:159]\n"
"v_mfma_f32_16x16x32_bf16 v[116:119], a[120:123], v[200:203], v[116:119]\n"
"ds_read_b64_tr_b16 v[166:167], v230 offset:35840\n"
"v_mfma_f32_16x16x32_bf16 v[116:119], a[112:115], v[172:175], v[116:119]\n"
"v_mfma_f32_16x16x32_bf16 v[76:79], a[64:67], v[172:175], v[76:79]\n"
"ds_read_b64_tr_b16 v[168:169], v231 offset:20480\n"
"v_mfma_f32_16x16x32_bf16 v[76:79], a[72:75], v[200:203], v[76:79]\n"
"s_waitcnt vmcnt(16)\n"
"v_mfma_f32_16x16x32_bf16 v[36:39], a[24:27], v[200:203], v[36:39]\n"
"ds_read_b64_tr_b16 v[170:171], v231 offset:25600\n"
"v_mfma_f32_16x16x32_bf16 v[36:39], a[16:19], v[172:175], v[36:39]\n"
"s_waitcnt lgkmcnt(4)\n"
"v_mfma_f32_16x16x32_bf16 v[0:3], a[4:7], v[160:163], v[0:3]\n"
"s_waitcnt lgkmcnt(2)\n"
"ds_read_b64_tr_b16 v[172:173], v231 offset:30720\n"
"v_mfma_f32_16x16x32_bf16 v[0:3], a[12:15], v[164:167], v[0:3]\n"
"s_add_u32 s48, s92, 3670016\n"
"v_mfma_f32_16x16x32_bf16 v[40:43], a[60:63], v[164:167], v[40:43]\n"
"s_add_u32 s49, s92, 4194304\n"
"ds_read_b64_tr_b16 v[174:175], v231 offset:35840\n"
"v_mfma_f32_16x16x32_bf16 v[40:43], a[52:55], v[160:163], v[40:43]\n"
"buffer_store_dwordx4 a[128:131], v253, s[56:59], s76 offen offset:0 sc1\n"
"v_mfma_f32_16x16x32_bf16 v[80:83], a[100:103], v[160:163], v[80:83]\n"
"s_add_u32 s50, s92, 4718592\n"
"ds_read_b64_tr_b16 v[200:201], v230 offset:20736\n"
"v_mfma_f32_16x16x32_bf16 v[80:83], a[108:111], v[164:167], v[80:83]\n"
"s_add_u32 s51, s92, 5242880\n"
"v_mfma_f32_16x16x32_bf16 v[120:123], a[156:159], v[164:167], v[120:123]\n"
"s_add_u32 s82, s19, 655360\n"
"ds_read_b64_tr_b16 v[202:203], v230 offset:25856\n"
"v_mfma_f32_16x16x32_bf16 v[120:123], a[148:151], v[160:163], v[120:123]\n"
"s_waitcnt lgkmcnt(4)\n"
"v_mfma_f32_16x16x32_bf16 v[124:127], a[148:151], v[168:171], v[124:127]\n"
"s_waitcnt lgkmcnt(2)\n"
"ds_read_b64_tr_b16 v[160:161], v230 offset:30976\n"
"v_mfma_f32_16x16x32_bf16 v[124:127], a[156:159], v[172:175], v[124:127]\n"
"s_add_u32 s83, s19, 737280\n"
"v_mfma_f32_16x16x32_bf16 v[84:87], a[108:111], v[172:175], v[84:87]\n"
"s_add_u32 s84, s19, 819200\n"
"ds_read_b64_tr_b16 v[162:163], v230 offset:36096\n"
"v_mfma_f32_16x16x32_bf16 v[84:87], a[100:103], v[168:171], v[84:87]\n"
"s_add_u32 s85, s19, 901120\n"
"v_mfma_f32_16x16x32_bf16 v[44:47], a[52:55], v[168:171], v[44:47]\n"
"s_add_u32 s52, s92, 5767168\n"
"ds_read_b64_tr_b16 v[164:165], v231 offset:20736\n"
"v_mfma_f32_16x16x32_bf16 v[44:47], a[60:63], v[172:175], v[44:47]\n"
"s_add_u32 s89, s86, 81920\n"
"v_mfma_f32_16x16x32_bf16 v[4:7], a[12:15], v[172:175], v[4:7]\n"
"s_add_u32 s16, s87, 81920\n"
"ds_read_b64_tr_b16 v[166:167], v231 offset:25856\n"
"v_mfma_f32_16x16x32_bf16 v[4:7], a[4:7], v[168:171], v[4:7]\n"
"s_waitcnt lgkmcnt(4)\n"
"v_mfma_f32_16x16x32_bf16 v[8:11], a[4:7], v[200:203], v[8:11]\n"
"s_waitcnt lgkmcnt(2)\n"
"ds_read_b64_tr_b16 v[168:169], v231 offset:30976\n"
"v_mfma_f32_16x16x32_bf16 v[8:11], a[12:15], v[160:163], v[8:11]\n"
"s_add_u32 s17, s79, 20480\n"
"v_mfma_f32_16x16x32_bf16 v[48:51], a[60:63], v[160:163], v[48:51]\n"
"s_mov_b32 s80, 2\n"
"ds_read_b64_tr_b16 v[170:171], v231 offset:36096\n"
"v_mfma_f32_16x16x32_bf16 v[48:51], a[52:55], v[200:203], v[48:51]\n"
"buffer_store_dwordx4 a[176:179], v253, s[56:59], s76 offen offset:2048 sc1\n"
"v_mfma_f32_16x16x32_bf16 v[88:91], a[100:103], v[200:203], v[88:91]\n"
"ds_read_b64_tr_b16 v[172:173], v230 offset:20992\n"
"v_mfma_f32_16x16x32_bf16 v[88:91], a[108:111], v[160:163], v[88:91]\n"
"v_mfma_f32_16x16x32_bf16 v[128:131], a[156:159], v[160:163], v[128:131]\n"
"ds_read_b64_tr_b16 v[174:175], v230 offset:26112\n"
"v_mfma_f32_16x16x32_bf16 v[128:131], a[148:151], v[200:203], v[128:131]\n"
"s_waitcnt lgkmcnt(4)\n"
"v_mfma_f32_16x16x32_bf16 v[132:135], a[148:151], v[164:167], v[132:135]\n"
"s_waitcnt lgkmcnt(2)\n"
"ds_read_b64_tr_b16 v[200:201], v230 offset:31232\n"
"v_mfma_f32_16x16x32_bf16 v[132:135], a[156:159], v[168:171], v[132:135]\n"
"v_mfma_f32_16x16x32_bf16 v[92:95], a[108:111], v[168:171], v[92:95]\n"
"ds_read_b64_tr_b16 v[202:203], v230 offset:36352\n"
"v_mfma_f32_16x16x32_bf16 v[92:95], a[100:103], v[164:167], v[92:95]\n"
"v_mfma_f32_16x16x32_bf16 v[52:55], a[52:55], v[164:167], v[52:55]\n"
"ds_read_b64_tr_b16 v[160:161], v231 offset:20992\n"
"v_mfma_f32_16x16x32_bf16 v[52:55], a[60:63], v[168:171], v[52:55]\n"
"v_mfma_f32_16x16x32_bf16 v[12:15], a[12:15], v[168:171], v[12:15]\n"
"ds_read_b64_tr_b16 v[162:163], v231 offset:26112\n"
"v_mfma_f32_16x16x32_bf16 v[12:15], a[4:7], v[164:167], v[12:15]\n"
"s_waitcnt lgkmcnt(4)\n"
"v_mfma_f32_16x16x32_bf16 v[16:19], a[4:7], v[172:175], v[16:19]\n"
"s_waitcnt lgkmcnt(2)\n"
"ds_read_b64_tr_b16 v[164:165], v231 offset:31232\n"
"v_mfma_f32_16x16x32_bf16 v[16:19], a[12:15], v[200:203], v[16:19]\n"
"s_waitcnt vmcnt(15)\n"
"s_barrier\n"
"v_mfma_f32_16x16x32_bf16 v[56:59], a[60:63], v[200:203], v[56:59]\n"
"ds_read_b64_tr_b16 v[166:167], v231 offset:36352\n"
"v_mfma_f32_16x16x32_bf16 v[56:59], a[52:55], v[172:175], v[56:59]\n"
"buffer_load_dwordx4 a[208:211], v253, s[56:59], s72 offen offset:0 sc1\n"
"v_mfma_f32_16x16x32_bf16 v[96:99], a[100:103], v[172:175], v[96:99]\n"
"ds_read_b64_tr_b16 v[168:169], v230 offset:21248\n"
"v_mfma_f32_16x16x32_bf16 v[96:99], a[108:111], v[200:203], v[96:99]\n"
"s_add_u32 s74, s19, 491520\n"
"v_mfma_f32_16x16x32_bf16 v[136:139], a[156:159], v[200:203], v[136:139]\n"
"s_add_u32 m0, s79, 122880\n"
"ds_read_b64_tr_b16 v[170:171], v230 offset:26368\n"
"v_mfma_f32_16x16x32_bf16 v[136:139], a[148:151], v[172:175], v[136:139]\n"
"s_waitcnt lgkmcnt(4)\n"
"buffer_load_dwordx4 v224, s[28:31], s74 offen offset:0 lds\n"
"v_mfma_f32_16x16x32_bf16 v[140:143], a[148:151], v[160:163], v[140:143]\n"
"s_waitcnt lgkmcnt(2)\n"
"ds_read_b64_tr_b16 v[172:173], v230 offset:31488\n"
"v_mfma_f32_16x16x32_bf16 v[140:143], a[156:159], v[164:167], v[140:143]\n"
"v_mfma_f32_16x16x32_bf16 v[100:103], a[108:111], v[164:167], v[100:103]\n"
"ds_read_b64_tr_b16 v[174:175], v230 offset:36608\n"
"v_mfma_f32_16x16x32_bf16 v[100:103], a[100:103], v[160:163], v[100:103]\n"
"buffer_load_dwordx4 a[212:215], v253, s[56:59], s72 offen offset:1024 sc1\n"
"v_mfma_f32_16x16x32_bf16 v[60:63], a[52:55], v[160:163], v[60:63]\n"
"ds_read_b64_tr_b16 v[200:201], v231 offset:21248\n"
"v_mfma_f32_16x16x32_bf16 v[60:63], a[60:63], v[164:167], v[60:63]\n"
"v_mfma_f32_16x16x32_bf16 v[20:23], a[12:15], v[164:167], v[20:23]\n"
"ds_read_b64_tr_b16 v[202:203], v231 offset:26368\n"
"v_mfma_f32_16x16x32_bf16 v[20:23], a[4:7], v[160:163], v[20:23]\n"
"s_waitcnt lgkmcnt(4)\n"
"buffer_load_dwordx4 v225, s[28:31], s74 offen offset:1024 lds\n"
"v_mfma_f32_16x16x32_bf16 v[24:27], a[4:7], v[168:171], v[24:27]\n"
"s_waitcnt lgkmcnt(2)\n"
"ds_read_b64_tr_b16 v[160:161], v231 offset:31488\n"
"v_mfma_f32_16x16x32_bf16 v[24:27], a[12:15], v[172:175], v[24:27]\n"
"v_mfma_f32_16x16x32_bf16 v[64:67], a[60:63], v[172:175], v[64:67]\n"
"ds_read_b64_tr_b16 v[162:163], v231 offset:36608\n"
"v_mfma_f32_16x16x32_bf16 v[64:67], a[52:55], v[168:171], v[64:67]\n"
"buffer_load_dwordx4 a[216:219], v253, s[56:59], s72 offen offset:2048 sc1\n"
"v_mfma_f32_16x16x32_bf16 v[104:107], a[100:103], v[168:171], v[104:107]\n"
"ds_read_b64_tr_b16 v[164:165], v230 offset:21504\n"
"v_mfma_f32_16x16x32_bf16 v[104:107], a[108:111], v[172:175], v[104:107]\n"
"v_mfma_f32_16x16x32_bf16 v[144:147], a[156:159], v[172:175], v[144:147]\n"
"ds_read_b64_tr_b16 v[166:167], v230 offset:26624\n"
"v_mfma_f32_16x16x32_bf16 v[144:147], a[148:151], v[168:171], v[144:147]\n"
"s_waitcnt lgkmcnt(4)\n"
"buffer_load_dwordx4 v226, s[28:31], s74 offen offset:2048 lds\n"
"v_mfma_f32_16x16x32_bf16 v[148:151], a[148:151], v[200:203], v[148:151]\n"
"s_waitcnt lgkmcnt(2)\n"
"ds_read_b64_tr_b16 v[168:169], v230 offset:31744\n"
"v_mfma_f32_16x16x32_bf16 v[148:151], a[156:159], v[160:163], v[148:151]\n"
"v_mfma_f32_16x16x32_bf16 v[108:111], a[108:111], v[160:163], v[108:111]\n"
"ds_read_b64_tr_b16 v[170:171], v230 offset:36864\n"
"v_mfma_f32_16x16x32_bf16 v[108:111], a[100:103], v[200:203], v[108:111]\n"
"buffer_load_dwordx4 a[220:223], v253, s[56:59], s72 offen offset:3072 sc1\n"
"v_mfma_f32_16x16x32_bf16 v[68:71], a[52:55], v[200:203], v[68:71]\n"
"ds_read_b64_tr_b16 v[172:173], v231 offset:21504\n"
"v_mfma_f32_16x16x32_bf16 v[68:71], a[60:63], v[160:163], v[68:71]\n"
"v_mfma_f32_16x16x32_bf16 v[28:31], a[12:15], v[160:163], v[28:31]\n"
"ds_read_b64_tr_b16 v[174:175], v231 offset:26624\n"
"v_mfma_f32_16x16x32_bf16 v[28:31], a[4:7], v[200:203], v[28:31]\n"
"s_waitcnt lgkmcnt(4)\n"
"buffer_load_dwordx4 v227, s[28:31], s74 offen offset:3072 lds\n"
"v_mfma_f32_16x16x32_bf16 v[32:35], a[4:7], v[164:167], v[32:35]\n"
"s_waitcnt lgkmcnt(2)\n"
"ds_read_b64_tr_b16 v[200:201], v231 offset:31744\n"
"v_mfma_f32_16x16x32_bf16 v[32:35], a[12:15], v[168:171], v[32:35]\n"
"v_mfma_f32_16x16x32_bf16 v[72:75], a[60:63], v[168:171], v[72:75]\n"
"ds_read_b64_tr_b16 v[202:203], v231 offset:36864\n"
"v_mfma_f32_16x16x32_bf16 v[72:75], a[52:55], v[164:167], v[72:75]\n"
"buffer_store_dwordx4 a[40:43], v251, s[56:59], s76 offen offset:1024 sc1\n"
"v_mfma_f32_16x16x32_bf16 v[112:115], a[100:103], v[164:167], v[112:115]\n"
"ds_read_b64_tr_b16 v[160:161], v235 offset:0\n"
"v_mfma_f32_16x16x32_bf16 v[112:115], a[108:111], v[168:171], v[112:115]\n"
"v_mfma_f32_16x16x32_bf16 v[152:155], a[156:159], v[168:171], v[152:155]\n"
"s_add_u32 m0, s79, 126976\n"
"ds_read_b64_tr_b16 v[162:163], v235 offset:5120\n"
"v_mfma_f32_16x16x32_bf16 v[152:155], a[148:151], v[164:167], v[152:155]\n"
"s_waitcnt lgkmcnt(4)\n"
"buffer_load_dwordx4 v234, s[28:31], s74 offen offset:0 lds\n"
"v_mfma_f32_16x16x32_bf16 v[156:159], a[148:151], v[172:175], v[156:159]\n"
"s_waitcnt lgkmcnt(2)\n"
"ds_read_b64_tr_b16 v[164:165], v235 offset:10240\n"
"v_mfma_f32_16x16x32_bf16 v[156:159], a[156:159], v[200:203], v[156:159]\n"
"v_mfma_f32_16x16x32_bf16 v[116:119], a[108:111], v[200:203], v[116:119]\n"
"ds_read_b64_tr_b16 v[166:167], v235 offset:15360\n"
"v_mfma_f32_16x16x32_bf16 v[116:119], a[100:103], v[172:175], v[116:119]\n"
"buffer_store_dwordx4 a[88:91], v251, s[56:59], s76 offen offset:3072 sc1\n"
"v_mfma_f32_16x16x32_bf16 v[76:79], a[52:55], v[172:175], v[76:79]\n"
"ds_read_b64_tr_b16 v[168:169], v249 offset:0\n"
"v_mfma_f32_16x16x32_bf16 v[76:79], a[60:63], v[200:203], v[76:79]\n"
"v_mfma_f32_16x16x32_bf16 v[36:39], a[12:15], v[200:203], v[36:39]\n"
"ds_read_b64_tr_b16 v[170:171], v249 offset:5120\n"
"v_mfma_f32_16x16x32_bf16 v[36:39], a[4:7], v[172:175], v[36:39]\n"
"s_waitcnt lgkmcnt(4)\n"
"v_mfma_f32_16x16x32_bf16 v[0:3], a[32:35], v[160:163], v[0:3]\n"
"s_waitcnt lgkmcnt(2)\n"
"ds_read_b64_tr_b16 v[172:173], v249 offset:10240\n"
"v_mfma_f32_16x16x32_bf16 v[0:3], a[40:43], v[164:167], v[0:3]\n"
"v_mfma_f32_16x16x32_bf16 v[40:43], a[88:91], v[164:167], v[40:43]\n"
"s_add_u32 s72, s92, 3145728\n"
"ds_read_b64_tr_b16 v[174:175], v249 offset:15360\n"
"v_mfma_f32_16x16x32_bf16 v[40:43], a[80:83], v[160:163], v[40:43]\n"
"buffer_load_dwordx4 a[224:227], v251, s[56:59], s72 offen offset:0 sc1\n"
"v_mfma_f32_16x16x32_bf16 v[80:83], a[128:131], v[160:163], v[80:83]\n"
"ds_read_b64_tr_b16 v[200:201], v235 offset:256\n"
"v_mfma_f32_16x16x32_bf16 v[80:83], a[136:139], v[164:167], v[80:83]\n"
"v_mfma_f32_16x16x32_bf16 v[120:123], a[184:187], v[164:167], v[120:123]\n"
"ds_read_b64_tr_b16 v[202:203], v235 offset:5376\n"
"v_mfma_f32_16x16x32_bf16 v[120:123], a[176:179], v[160:163], v[120:123]\n"
"s_waitcnt lgkmcnt(4)\n"
"v_mfma_f32_16x16x32_bf16 v[124:127], a[176:179], v[168:171], v[124:127]\n"
"s_waitcnt lgkmcnt(2)\n"
"ds_read_b64_tr_b16 v[160:161], v235 offset:10496\n"
"v_mfma_f32_16x16x32_bf16 v[124:127], a[184:187], v[172:175], v[124:127]\n"
"v_mfma_f32_16x16x32_bf16 v[84:87], a[136:139], v[172:175], v[84:87]\n"
"ds_read_b64_tr_b16 v[162:163], v235 offset:15616\n"
"v_mfma_f32_16x16x32_bf16 v[84:87], a[128:131], v[168:171], v[84:87]\n"
"buffer_load_dwordx4 a[228:231], v251, s[56:59], s72 offen offset:1024 sc1\n"
"v_mfma_f32_16x16x32_bf16 v[44:47], a[80:83], v[168:171], v[44:47]\n"
"ds_read_b64_tr_b16 v[164:165], v249 offset:256\n"
"v_mfma_f32_16x16x32_bf16 v[44:47], a[88:91], v[172:175], v[44:47]\n"
"v_mfma_f32_16x16x32_bf16 v[4:7], a[40:43], v[172:175], v[4:7]\n"
"ds_read_b64_tr_b16 v[166:167], v249 offset:5376\n"
"v_mfma_f32_16x16x32_bf16 v[4:7], a[32:35], v[168:171], v[4:7]\n"
"s_waitcnt lgkmcnt(4)\n"
"v_mfma_f32_16x16x32_bf16 v[8:11], a[32:35], v[200:203], v[8:11]\n"
"s_waitcnt lgkmcnt(2)\n"
"ds_read_b64_tr_b16 v[168:169], v249 offset:10496\n"
"v_mfma_f32_16x16x32_bf16 v[8:11], a[40:43], v[160:163], v[8:11]\n"
"v_mfma_f32_16x16x32_bf16 v[48:51], a[88:91], v[160:163], v[48:51]\n"
"ds_read_b64_tr_b16 v[170:171], v249 offset:15616\n"
"v_mfma_f32_16x16x32_bf16 v[48:51], a[80:83], v[200:203], v[48:51]\n"
"buffer_load_dwordx4 a[232:235], v251, s[56:59], s72 offen offset:2048 sc1\n"
"v_mfma_f32_16x16x32_bf16 v[88:91], a[128:131], v[200:203], v[88:91]\n"
"ds_read_b64_tr_b16 v[172:173], v235 offset:512\n"
"v_mfma_f32_16x16x32_bf16 v[88:91], a[136:139], v[160:163], v[88:91]\n"
"v_mfma_f32_16x16x32_bf16 v[128:131], a[184:187], v[160:163], v[128:131]\n"
"ds_read_b64_tr_b16 v[174:175], v235 offset:5632\n"
"v_mfma_f32_16x16x32_bf16 v[128:131], a[176:179], v[200:203], v[128:131]\n"
"s_waitcnt lgkmcnt(4)\n"
"v_mfma_f32_16x16x32_bf16 v[132:135], a[176:179], v[164:167], v[132:135]\n"
"s_waitcnt lgkmcnt(2)\n"
"ds_read_b64_tr_b16 v[200:201], v235 offset:10752\n"
"v_mfma_f32_16x16x32_bf16 v[132:135], a[184:187], v[168:171], v[132:135]\n"
"v_mfma_f32_16x16x32_bf16 v[92:95], a[136:139], v[168:171], v[92:95]\n"
"ds_read_b64_tr_b16 v[202:203], v235 offset:15872\n"
"v_mfma_f32_16x16x32_bf16 v[92:95], a[128:131], v[164:167], v[92:95]\n"
"buffer_load_dwordx4 a[236:239], v251, s[56:59], s72 offen offset:3072 sc1\n"
"v_mfma_f32_16x16x32_bf16 v[52:55], a[80:83], v[164:167], v[52:55]\n"
"ds_read_b64_tr_b16 v[160:161], v249 offset:512\n"
"v_mfma_f32_16x16x32_bf16 v[52:55], a[88:91], v[168:171], v[52:55]\n"
"v_mfma_f32_16x16x32_bf16 v[12:15], a[40:43], v[168:171], v[12:15]\n"
"ds_read_b64_tr_b16 v[162:163], v249 offset:5632\n"
"v_mfma_f32_16x16x32_bf16 v[12:15], a[32:35], v[164:167], v[12:15]\n"
"s_waitcnt lgkmcnt(4)\n"
"v_mfma_f32_16x16x32_bf16 v[16:19], a[32:35], v[172:175], v[16:19]\n"
"s_waitcnt lgkmcnt(2)\n"
"ds_read_b64_tr_b16 v[164:165], v249 offset:10752\n"
"v_mfma_f32_16x16x32_bf16 v[16:19], a[40:43], v[200:203], v[16:19]\n"
"s_waitcnt vmcnt(17)\n"
"s_barrier\n"
"v_mfma_f32_16x16x32_bf16 v[56:59], a[88:91], v[200:203], v[56:59]\n"
"ds_read_b64_tr_b16 v[166:167], v249 offset:15872\n"
"v_mfma_f32_16x16x32_bf16 v[56:59], a[80:83], v[172:175], v[56:59]\n"
"buffer_load_dwordx4 a[240:243], v253, s[56:59], s72 offen offset:0 sc1\n"
"v_mfma_f32_16x16x32_bf16 v[96:99], a[128:131], v[172:175], v[96:99]\n"
"ds_read_b64_tr_b16 v[168:169], v235 offset:768\n"
"v_mfma_f32_16x16x32_bf16 v[96:99], a[136:139], v[200:203], v[96:99]\n"
"s_add_u32 s74, s19, 573440\n"
"v_mfma_f32_16x16x32_bf16 v[136:139], a[184:187], v[200:203], v[136:139]\n"
"s_add_u32 m0, s79, 143360\n"
"ds_read_b64_tr_b16 v[170:171], v235 offset:5888\n"
"v_mfma_f32_16x16x32_bf16 v[136:139], a[176:179], v[172:175], v[136:139]\n"
"s_waitcnt lgkmcnt(4)\n"
"buffer_load_dwordx4 v224, s[28:31], s74 offen offset:0 lds\n"
"v_mfma_f32_16x16x32_bf16 v[140:143], a[176:179], v[160:163], v[140:143]\n"
"s_waitcnt lgkmcnt(2)\n"
"ds_read_b64_tr_b16 v[172:173], v235 offset:11008\n"
"v_mfma_f32_16x16x32_bf16 v[140:143], a[184:187], v[164:167], v[140:143]\n"
"v_mfma_f32_16x16x32_bf16 v[100:103], a[136:139], v[164:167], v[100:103]\n"
"ds_read_b64_tr_b16 v[174:175], v235 offset:16128\n"
"v_mfma_f32_16x16x32_bf16 v[100:103], a[128:131], v[160:163], v[100:103]\n"
"buffer_load_dwordx4 a[244:247], v253, s[56:59], s72 offen offset:1024 sc1\n"
"v_mfma_f32_16x16x32_bf16 v[60:63], a[80:83], v[160:163], v[60:63]\n"
"ds_read_b64_tr_b16 v[200:201], v249 offset:768\n"
"v_mfma_f32_16x16x32_bf16 v[60:63], a[88:91], v[164:167], v[60:63]\n"
"v_mfma_f32_16x16x32_bf16 v[20:23], a[40:43], v[164:167], v[20:23]\n"
"ds_read_b64_tr_b16 v[202:203], v249 offset:5888\n"
"v_mfma_f32_16x16x32_bf16 v[20:23], a[32:35], v[160:163], v[20:23]\n"
"s_waitcnt lgkmcnt(4)\n"
"buffer_load_dwordx4 v225, s[28:31], s74 offen offset:1024 lds\n"
"v_mfma_f32_16x16x32_bf16 v[24:27], a[32:35], v[168:171], v[24:27]\n"
"s_waitcnt lgkmcnt(2)\n"
"ds_read_b64_tr_b16 v[160:161], v249 offset:11008\n"
"v_mfma_f32_16x16x32_bf16 v[24:27], a[40:43], v[172:175], v[24:27]\n"
"v_mfma_f32_16x16x32_bf16 v[64:67], a[88:91], v[172:175], v[64:67]\n"
"ds_read_b64_tr_b16 v[162:163], v249 offset:16128\n"
"v_mfma_f32_16x16x32_bf16 v[64:67], a[80:83], v[168:171], v[64:67]\n"
"buffer_load_dwordx4 a[248:251], v253, s[56:59], s72 offen offset:2048 sc1\n"
"v_mfma_f32_16x16x32_bf16 v[104:107], a[128:131], v[168:171], v[104:107]\n"
"ds_read_b64_tr_b16 v[164:165], v235 offset:1024\n"
"v_mfma_f32_16x16x32_bf16 v[104:107], a[136:139], v[172:175], v[104:107]\n"
"v_mfma_f32_16x16x32_bf16 v[144:147], a[184:187], v[172:175], v[144:147]\n"
"ds_read_b64_tr_b16 v[166:167], v235 offset:6144\n"
"v_mfma_f32_16x16x32_bf16 v[144:147], a[176:179], v[168:171], v[144:147]\n"
"s_waitcnt lgkmcnt(4)\n"
"buffer_load_dwordx4 v226, s[28:31], s74 offen offset:2048 lds\n"
"v_mfma_f32_16x16x32_bf16 v[148:151], a[176:179], v[200:203], v[148:151]\n"
"s_waitcnt lgkmcnt(2)\n"
"ds_read_b64_tr_b16 v[168:169], v235 offset:11264\n"
"v_mfma_f32_16x16x32_bf16 v[148:151], a[184:187], v[160:163], v[148:151]\n"
"v_mfma_f32_16x16x32_bf16 v[108:111], a[136:139], v[160:163], v[108:111]\n"
"ds_read_b64_tr_b16 v[170:171], v235 offset:16384\n"
"v_mfma_f32_16x16x32_bf16 v[108:111], a[128:131], v[200:203], v[108:111]\n"
"buffer_load_dwordx4 a[252:255], v253, s[56:59], s72 offen offset:3072 sc1\n"
"v_mfma_f32_16x16x32_bf16 v[68:71], a[80:83], v[200:203], v[68:71]\n"
"ds_read_b64_tr_b16 v[172:173], v249 offset:1024\n"
"v_mfma_f32_16x16x32_bf16 v[68:71], a[88:91], v[160:163], v[68:71]\n"
"v_mfma_f32_16x16x32_bf16 v[28:31], a[40:43], v[160:163], v[28:31]\n"
"ds_read_b64_tr_b16 v[174:175], v249 offset:6144\n"
"v_mfma_f32_16x16x32_bf16 v[28:31], a[32:35], v[200:203], v[28:31]\n"
"s_waitcnt lgkmcnt(4)\n"
"buffer_load_dwordx4 v227, s[28:31], s74 offen offset:3072 lds\n"
"v_mfma_f32_16x16x32_bf16 v[32:35], a[32:35], v[164:167], v[32:35]\n"
"s_waitcnt lgkmcnt(2)\n"
"ds_read_b64_tr_b16 v[200:201], v249 offset:11264\n"
"v_mfma_f32_16x16x32_bf16 v[32:35], a[40:43], v[168:171], v[32:35]\n"
"v_mfma_f32_16x16x32_bf16 v[72:75], a[88:91], v[168:171], v[72:75]\n"
"ds_read_b64_tr_b16 v[202:203], v249 offset:16384\n"
"v_mfma_f32_16x16x32_bf16 v[72:75], a[80:83], v[164:167], v[72:75]\n"
"buffer_store_dwordx4 a[136:139], v253, s[56:59], s76 offen offset:1024 sc1\n"
"v_mfma_f32_16x16x32_bf16 v[112:115], a[128:131], v[164:167], v[112:115]\n"
"ds_read_b64_tr_b16 v[160:161], v228 offset:40960\n"
"v_mfma_f32_16x16x32_bf16 v[112:115], a[136:139], v[168:171], v[112:115]\n"
"v_mfma_f32_16x16x32_bf16 v[152:155], a[184:187], v[168:171], v[152:155]\n"
"s_add_u32 m0, s79, 147456\n"
"ds_read_b64_tr_b16 v[162:163], v228 offset:46080\n"
"v_mfma_f32_16x16x32_bf16 v[152:155], a[176:179], v[164:167], v[152:155]\n"
"s_waitcnt lgkmcnt(4)\n"
"buffer_load_dwordx4 v234, s[28:31], s74 offen offset:0 lds\n"
"v_mfma_f32_16x16x32_bf16 v[156:159], a[176:179], v[172:175], v[156:159]\n"
"s_waitcnt lgkmcnt(2)\n"
"ds_read_b64_tr_b16 v[164:165], v228 offset:51200\n"
"v_mfma_f32_16x16x32_bf16 v[156:159], a[184:187], v[200:203], v[156:159]\n"
"v_mfma_f32_16x16x32_bf16 v[116:119], a[136:139], v[200:203], v[116:119]\n"
"ds_read_b64_tr_b16 v[166:167], v228 offset:56320\n"
"v_mfma_f32_16x16x32_bf16 v[116:119], a[128:131], v[172:175], v[116:119]\n"
"buffer_store_dwordx4 a[184:187], v253, s[56:59], s76 offen offset:3072 sc1\n"
"v_mfma_f32_16x16x32_bf16 v[76:79], a[80:83], v[172:175], v[76:79]\n"
"ds_read_b64_tr_b16 v[168:169], v229 offset:40960\n"
"v_mfma_f32_16x16x32_bf16 v[76:79], a[88:91], v[200:203], v[76:79]\n"
"s_waitcnt vmcnt(19)\n"
"v_mfma_f32_16x16x32_bf16 v[36:39], a[40:43], v[200:203], v[36:39]\n"
"ds_read_b64_tr_b16 v[170:171], v229 offset:46080\n"
"v_mfma_f32_16x16x32_bf16 v[36:39], a[32:35], v[172:175], v[36:39]\n"
".Lat_ol:\n"
"s_waitcnt lgkmcnt(4)\n"
"v_mfma_f32_16x16x32_bf16 v[0:3], a[192:195], v[160:163], v[0:3]\n"
"s_waitcnt lgkmcnt(2)\n"
"ds_read_b64_tr_b16 v[172:173], v229 offset:51200\n"
"v_mfma_f32_16x16x32_bf16 v[0:3], a[196:199], v[164:167], v[0:3]\n"
"v_mfma_f32_16x16x32_bf16 v[40:43], a[204:207], v[164:167], v[40:43]\n"
"ds_read_b64_tr_b16 v[174:175], v229 offset:56320\n"
"v_mfma_f32_16x16x32_bf16 v[40:43], a[200:203], v[160:163], v[40:43]\n"
"buffer_load_dwordx4 a[4:7], v251, s[56:59], s48 offen offset:0 sc1\n"
"v_mfma_f32_16x16x32_bf16 v[80:83], a[208:211], v[160:163], v[80:83]\n"
"ds_read_b64_tr_b16 v[200:201], v228 offset:41216\n"
"v_mfma_f32_16x16x32_bf16 v[80:83], a[212:215], v[164:167], v[80:83]\n"
"v_mfma_f32_16x16x32_bf16 v[120:123], a[220:223], v[164:167], v[120:123]\n"
"s_mov_b32 m0, s17\n"
"ds_read_b64_tr_b16 v[202:203], v228 offset:46336\n"
"v_mfma_f32_16x16x32_bf16 v[120:123], a[216:219], v[160:163], v[120:123]\n"
"s_waitcnt lgkmcnt(4)\n"
"buffer_load_dwordx4 v224, s[28:31], s16 offen offset:0 lds\n"
"v_mfma_f32_16x16x32_bf16 v[124:127], a[216:219], v[168:171], v[124:127]\n"
"s_waitcnt lgkmcnt(2)\n"
"ds_read_b64_tr_b16 v[160:161], v228 offset:51456\n"
"v_mfma_f32_16x16x32_bf16 v[124:127], a[220:223], v[172:175], v[124:127]\n"
"v_mfma_f32_16x16x32_bf16 v[84:87], a[212:215], v[172:175], v[84:87]\n"
"ds_read_b64_tr_b16 v[162:163], v228 offset:56576\n"
"v_mfma_f32_16x16x32_bf16 v[84:87], a[208:211], v[168:171], v[84:87]\n"
"buffer_load_dwordx4 a[12:15], v251, s[56:59], s48 offen offset:1024 sc1\n"
"v_mfma_f32_16x16x32_bf16 v[44:47], a[200:203], v[168:171], v[44:47]\n"
"ds_read_b64_tr_b16 v[164:165], v229 offset:41216\n"
"v_mfma_f32_16x16x32_bf16 v[44:47], a[204:207], v[172:175], v[44:47]\n"
"v_mfma_f32_16x16x32_bf16 v[4:7], a[196:199], v[172:175], v[4:7]\n"
"ds_read_b64_tr_b16 v[166:167], v229 offset:46336\n"
"v_mfma_f32_16x16x32_bf16 v[4:7], a[192:195], v[168:171], v[4:7]\n"
"s_waitcnt lgkmcnt(4)\n"
"buffer_load_dwordx4 v225, s[28:31], s16 offen offset:1024 lds\n"
"v_mfma_f32_16x16x32_bf16 v[8:11], a[192:195], v[200:203], v[8:11]\n"
"s_waitcnt lgkmcnt(2)\n"
"ds_read_b64_tr_b16 v[168:169], v229 offset:51456\n"
"v_mfma_f32_16x16x32_bf16 v[8:11], a[196:199], v[160:163], v[8:11]\n"
"v_mfma_f32_16x16x32_bf16 v[48:51], a[204:207], v[160:163], v[48:51]\n"
"ds_read_b64_tr_b16 v[170:171], v229 offset:56576\n"
"v_mfma_f32_16x16x32_bf16 v[48:51], a[200:203], v[200:203], v[48:51]\n"
"buffer_load_dwordx4 a[52:55], v251, s[56:59], s48 offen offset:2048 sc1\n"
"v_mfma_f32_16x16x32_bf16 v[88:91], a[208:211], v[200:203], v[88:91]\n"
"ds_read_b64_tr_b16 v[172:173], v228 offset:41472\n"
"v_mfma_f32_16x16x32_bf16 v[88:91], a[212:215], v[160:163], v[88:91]\n"
"v_mfma_f32_16x16x32_bf16 v[128:131], a[220:223], v[160:163], v[128:131]\n"
"ds_read_b64_tr_b16 v[174:175], v228 offset:46592\n"
"v_mfma_f32_16x16x32_bf16 v[128:131], a[216:219], v[200:203], v[128:131]\n"
"s_waitcnt lgkmcnt(4)\n"
"buffer_load_dwordx4 v226, s[28:31], s16 offen offset:2048 lds\n"
"v_mfma_f32_16x16x32_bf16 v[132:135], a[216:219], v[164:167], v[132:135]\n"
"s_waitcnt lgkmcnt(2)\n"
"ds_read_b64_tr_b16 v[200:201], v228 offset:51712\n"
"v_mfma_f32_16x16x32_bf16 v[132:135], a[220:223], v[168:171], v[132:135]\n"
"v_mfma_f32_16x16x32_bf16 v[92:95], a[212:215], v[168:171], v[92:95]\n"
"ds_read_b64_tr_b16 v[202:203], v228 offset:56832\n"
"v_mfma_f32_16x16x32_bf16 v[92:95], a[208:211], v[164:167], v[92:95]\n"
"buffer_load_dwordx4 a[60:63], v251, s[56:59], s48 offen offset:3072 sc1\n"
"v_mfma_f32_16x16x32_bf16 v[52:55], a[200:203], v[164:167], v[52:55]\n"
"ds_read_b64_tr_b16 v[160:161], v229 offset:41472\n"
"v_mfma_f32_16x16x32_bf16 v[52:55], a[204:207], v[168:171], v[52:55]\n"
"v_mfma_f32_16x16x32_bf16 v[12:15], a[196:199], v[168:171], v[12:15]\n"
"ds_read_b64_tr_b16 v[162:163], v229 offset:46592\n"
"v_mfma_f32_16x16x32_bf16 v[12:15], a[192:195], v[164:167], v[12:15]\n"
"s_waitcnt lgkmcnt(4)\n"
"v_mfma_f32_16x16x32_bf16 v[16:19], a[192:195], v[172:175], v[16:19]\n"
"s_waitcnt lgkmcnt(2)\n"
"ds_read_b64_tr_b16 v[164:165], v229 offset:51712\n"
"v_mfma_f32_16x16x32_bf16 v[16:19], a[196:199], v[200:203], v[16:19]\n"
"s_waitcnt vmcnt(21)\n"
"s_barrier\n"
"v_mfma_f32_16x16x32_bf16 v[56:59], a[204:207], v[200:203], v[56:59]\n"
"ds_read_b64_tr_b16 v[166:167], v229 offset:56832\n"
"v_mfma_f32_16x16x32_bf16 v[56:59], a[200:203], v[172:175], v[56:59]\n"
"buffer_load_dwordx4 a[100:103], v253, s[56:59], s48 offen offset:0 sc1\n"
"v_mfma_f32_16x16x32_bf16 v[96:99], a[208:211], v[172:175], v[96:99]\n"
"ds_read_b64_tr_b16 v[168:169], v228 offset:41728\n"
"v_mfma_f32_16x16x32_bf16 v[96:99], a[212:215], v[200:203], v[96:99]\n"
"v_mfma_f32_16x16x32_bf16 v[136:139], a[220:223], v[200:203], v[136:139]\n"
"s_mov_b32 m0, s79\n"
"ds_read_b64_tr_b16 v[170:171], v228 offset:46848\n"
"v_mfma_f32_16x16x32_bf16 v[136:139], a[216:219], v[172:175], v[136:139]\n"
"s_waitcnt lgkmcnt(4)\n"
"buffer_load_dwordx4 v224, s[28:31], s82 offen offset:0 lds\n"
"v_mfma_f32_16x16x32_bf16 v[140:143], a[216:219], v[160:163], v[140:143]\n"
"s_waitcnt lgkmcnt(2)\n"
"ds_read_b64_tr_b16 v[172:173], v228 offset:51968\n"
"v_mfma_f32_16x16x32_bf16 v[140:143], a[220:223], v[164:167], v[140:143]\n"
"v_mfma_f32_16x16x32_bf16 v[100:103], a[212:215], v[164:167], v[100:103]\n"
"ds_read_b64_tr_b16 v[174:175], v228 offset:57088\n"
"v_mfma_f32_16x16x32_bf16 v[100:103], a[208:211], v[160:163], v[100:103]\n"
"buffer_load_dwordx4 a[108:111], v253, s[56:59], s48 offen offset:1024 sc1\n"
"v_mfma_f32_16x16x32_bf16 v[60:63], a[200:203], v[160:163], v[60:63]\n"
"ds_read_b64_tr_b16 v[200:201], v229 offset:41728\n"
"v_mfma_f32_16x16x32_bf16 v[60:63], a[204:207], v[164:167], v[60:63]\n"
"v_mfma_f32_16x16x32_bf16 v[20:23], a[196:199], v[164:167], v[20:23]\n"
"ds_read_b64_tr_b16 v[202:203], v229 offset:46848\n"
"v_mfma_f32_16x16x32_bf16 v[20:23], a[192:195], v[160:163], v[20:23]\n"
"s_waitcnt lgkmcnt(4)\n"
"buffer_load_dwordx4 v225, s[28:31], s82 offen offset:1024 lds\n"
"v_mfma_f32_16x16x32_bf16 v[24:27], a[192:195], v[168:171], v[24:27]\n"
"s_waitcnt lgkmcnt(2)\n"
"ds_read_b64_tr_b16 v[160:161], v229 offset:51968\n"
"v_mfma_f32_16x16x32_bf16 v[24:27], a[196:199], v[172:175], v[24:27]\n"
"v_mfma_f32_16x16x32_bf16 v[64:67], a[204:207], v[172:175], v[64:67]\n"
"ds_read_b64_tr_b16 v[162:163], v229 offset:57088\n"
"v_mfma_f32_16x16x32_bf16 v[64:67], a[200:203], v[168:171], v[64:67]\n"
"buffer_load_dwordx4 a[148:151], v253, s[56:59], s48 offen offset:2048 sc1\n"
"v_mfma_f32_16x16x32_bf16 v[104:107], a[208:211], v[168:171], v[104:107]\n"
"ds_read_b64_tr_b16 v[164:165], v228 offset:41984\n"
"v_mfma_f32_16x16x32_bf16 v[104:107], a[212:215], v[172:175], v[104:107]\n"
"v_mfma_f32_16x16x32_bf16 v[144:147], a[220:223], v[172:175], v[144:147]\n"
"ds_read_b64_tr_b16 v[166:167], v228 offset:47104\n"
"v_mfma_f32_16x16x32_bf16 v[144:147], a[216:219], v[168:171], v[144:147]\n"
"s_waitcnt lgkmcnt(4)\n"
"buffer_load_dwordx4 v226, s[28:31], s82 offen offset:2048 lds\n"
"v_mfma_f32_16x16x32_bf16 v[148:151], a[216:219], v[200:203], v[148:151]\n"
"s_waitcnt lgkmcnt(2)\n"
"ds_read_b64_tr_b16 v[168:169], v228 offset:52224\n"
"v_mfma_f32_16x16x32_bf16 v[148:151], a[220:223], v[160:163], v[148:151]\n"
"v_mfma_f32_16x16x32_bf16 v[108:111], a[212:215], v[160:163], v[108:111]\n"
"ds_read_b64_tr_b16 v[170:171], v228 offset:57344\n"
"v_mfma_f32_16x16x32_bf16 v[108:111], a[208:211], v[200:203], v[108:111]\n"
"buffer_load_dwordx4 a[156:159], v253, s[56:59], s48 offen offset:3072 sc1\n"
"v_mfma_f32_16x16x32_bf16 v[68:71], a[200:203], v[200:203], v[68:71]\n"
"s_mov_b32 s48, s52\n"
"ds_read_b64_tr_b16 v[172:173], v229 offset:41984\n"
"v_mfma_f32_16x16x32_bf16 v[68:71], a[204:207], v[160:163], v[68:71]\n"
"v_mfma_f32_16x16x32_bf16 v[28:31], a[196:199], v[160:163], v[28:31]\n"
"ds_read_b64_tr_b16 v[174:175], v229 offset:47104\n"
"v_mfma_f32_16x16x32_bf16 v[28:31], a[192:195], v[200:203], v[28:31]\n"
"s_waitcnt lgkmcnt(4)\n"
"buffer_load_dwordx4 v227, s[28:31], s82 offen offset:3072 lds\n"
"v_mfma_f32_16x16x32_bf16 v[32:35], a[192:195], v[164:167], v[32:35]\n"
"s_waitcnt lgkmcnt(2)\n"
"ds_read_b64_tr_b16 v[200:201], v229 offset:52224\n"
"v_mfma_f32_16x16x32_bf16 v[32:35], a[196:199], v[168:171], v[32:35]\n"
"v_mfma_f32_16x16x32_bf16 v[72:75], a[204:207], v[168:171], v[72:75]\n"
"ds_read_b64_tr_b16 v[202:203], v229 offset:57344\n"
"v_mfma_f32_16x16x32_bf16 v[72:75], a[200:203], v[164:167], v[72:75]\n"
"v_mfma_f32_16x16x32_bf16 v[112:115], a[208:211], v[164:167], v[112:115]\n"
"ds_read_b64_tr_b16 v[160:161], v230 offset:0\n"
"v_mfma_f32_16x16x32_bf16 v[112:115], a[212:215], v[168:171], v[112:115]\n"
"v_mfma_f32_16x16x32_bf16 v[152:155], a[220:223], v[168:171], v[152:155]\n"
"s_add_u32 m0, s79, 4096\n"
"ds_read_b64_tr_b16 v[162:163], v230 offset:5120\n"
"v_mfma_f32_16x16x32_bf16 v[152:155], a[216:219], v[164:167], v[152:155]\n"
"s_waitcnt lgkmcnt(4)\n"
"buffer_load_dwordx4 v234, s[28:31], s82 offen offset:0 lds\n"
"v_mfma_f32_16x16x32_bf16 v[156:159], a[216:219], v[172:175], v[156:159]\n"
"s_waitcnt lgkmcnt(2)\n"
"s_mov_b32 s82, s86\n"
"ds_read_b64_tr_b16 v[164:165], v230 offset:10240\n"
"v_mfma_f32_16x16x32_bf16 v[156:159], a[220:223], v[200:203], v[156:159]\n"
"v_mfma_f32_16x16x32_bf16 v[116:119], a[212:215], v[200:203], v[116:119]\n"
"ds_read_b64_tr_b16 v[166:167], v230 offset:15360\n"
"v_mfma_f32_16x16x32_bf16 v[116:119], a[208:211], v[172:175], v[116:119]\n"
"v_mfma_f32_16x16x32_bf16 v[76:79], a[200:203], v[172:175], v[76:79]\n"
"ds_read_b64_tr_b16 v[168:169], v231 offset:0\n"
"v_mfma_f32_16x16x32_bf16 v[76:79], a[204:207], v[200:203], v[76:79]\n"
"s_waitcnt vmcnt(19)\n"
"v_mfma_f32_16x16x32_bf16 v[36:39], a[196:199], v[200:203], v[36:39]\n"
"ds_read_b64_tr_b16 v[170:171], v231 offset:5120\n"
"v_mfma_f32_16x16x32_bf16 v[36:39], a[192:195], v[172:175], v[36:39]\n"
"s_waitcnt lgkmcnt(4)\n"
"v_mfma_f32_16x16x32_bf16 v[0:3], a[224:227], v[160:163], v[0:3]\n"
"s_waitcnt lgkmcnt(2)\n"
"ds_read_b64_tr_b16 v[172:173], v231 offset:10240\n"
"v_mfma_f32_16x16x32_bf16 v[0:3], a[228:231], v[164:167], v[0:3]\n"
"v_mfma_f32_16x16x32_bf16 v[40:43], a[236:239], v[164:167], v[40:43]\n"
"ds_read_b64_tr_b16 v[174:175], v231 offset:15360\n"
"v_mfma_f32_16x16x32_bf16 v[40:43], a[232:235], v[160:163], v[40:43]\n"
"buffer_load_dwordx4 a[20:23], v251, s[56:59], s49 offen offset:0 sc1\n"
"v_mfma_f32_16x16x32_bf16 v[80:83], a[240:243], v[160:163], v[80:83]\n"
"ds_read_b64_tr_b16 v[200:201], v230 offset:256\n"
"v_mfma_f32_16x16x32_bf16 v[80:83], a[244:247], v[164:167], v[80:83]\n"
"v_mfma_f32_16x16x32_bf16 v[120:123], a[252:255], v[164:167], v[120:123]\n"
"s_mov_b32 m0, s17\n"
"ds_read_b64_tr_b16 v[202:203], v230 offset:5376\n"
"v_mfma_f32_16x16x32_bf16 v[120:123], a[248:251], v[160:163], v[120:123]\n"
"s_waitcnt lgkmcnt(4)\n"
"buffer_load_dwordx4 v227, s[28:31], s16 offen offset:3072 lds\n"
"v_mfma_f32_16x16x32_bf16 v[124:127], a[248:251], v[168:171], v[124:127]\n"
"s_waitcnt lgkmcnt(2)\n"
"ds_read_b64_tr_b16 v[160:161], v230 offset:10496\n"
"v_mfma_f32_16x16x32_bf16 v[124:127], a[252:255], v[172:175], v[124:127]\n"
"v_mfma_f32_16x16x32_bf16 v[84:87], a[244:247], v[172:175], v[84:87]\n"
"ds_read_b64_tr_b16 v[162:163], v230 offset:15616\n"
"v_mfma_f32_16x16x32_bf16 v[84:87], a[240:243], v[168:171], v[84:87]\n"
"buffer_load_dwordx4 a[28:31], v251, s[56:59], s49 offen offset:1024 sc1\n"
"v_mfma_f32_16x16x32_bf16 v[44:47], a[232:235], v[168:171], v[44:47]\n"
"ds_read_b64_tr_b16 v[164:165], v231 offset:256\n"
"v_mfma_f32_16x16x32_bf16 v[44:47], a[236:239], v[172:175], v[44:47]\n"
"v_mfma_f32_16x16x32_bf16 v[4:7], a[228:231], v[172:175], v[4:7]\n"
"s_add_u32 m0, s17, 4096\n"
"ds_read_b64_tr_b16 v[166:167], v231 offset:5376\n"
"v_mfma_f32_16x16x32_bf16 v[4:7], a[224:227], v[168:171], v[4:7]\n"
"s_waitcnt lgkmcnt(4)\n"
"buffer_load_dwordx4 v234, s[28:31], s16 offen offset:0 lds\n"
"v_mfma_f32_16x16x32_bf16 v[8:11], a[224:227], v[200:203], v[8:11]\n"
"s_waitcnt lgkmcnt(2)\n"
"s_add_u32 s16, s16, 81920\n"
"ds_read_b64_tr_b16 v[168:169], v231 offset:10496\n"
"v_mfma_f32_16x16x32_bf16 v[8:11], a[228:231], v[160:163], v[8:11]\n"
"s_add_u32 s17, s17, 20480\n"
"v_mfma_f32_16x16x32_bf16 v[48:51], a[236:239], v[160:163], v[48:51]\n"
"ds_read_b64_tr_b16 v[170:171], v231 offset:15616\n"
"v_mfma_f32_16x16x32_bf16 v[48:51], a[232:235], v[200:203], v[48:51]\n"
"buffer_load_dwordx4 a[68:71], v251, s[56:59], s49 offen offset:2048 sc1\n"
"v_mfma_f32_16x16x32_bf16 v[88:91], a[240:243], v[200:203], v[88:91]\n"
"ds_read_b64_tr_b16 v[172:173], v230 offset:512\n"
"v_mfma_f32_16x16x32_bf16 v[88:91], a[244:247], v[160:163], v[88:91]\n"
"v_mfma_f32_16x16x32_bf16 v[128:131], a[252:255], v[160:163], v[128:131]\n"
"ds_read_b64_tr_b16 v[174:175], v230 offset:5632\n"
"v_mfma_f32_16x16x32_bf16 v[128:131], a[248:251], v[200:203], v[128:131]\n"
"s_waitcnt lgkmcnt(4)\n"
"v_mfma_f32_16x16x32_bf16 v[132:135], a[248:251], v[164:167], v[132:135]\n"
"s_waitcnt lgkmcnt(2)\n"
"ds_read_b64_tr_b16 v[200:201], v230 offset:10752\n"
"v_mfma_f32_16x16x32_bf16 v[132:135], a[252:255], v[168:171], v[132:135]\n"
"v_mfma_f32_16x16x32_bf16 v[92:95], a[244:247], v[168:171], v[92:95]\n"
"ds_read_b64_tr_b16 v[202:203], v230 offset:15872\n"
"v_mfma_f32_16x16x32_bf16 v[92:95], a[240:243], v[164:167], v[92:95]\n"
"buffer_load_dwordx4 a[76:79], v251, s[56:59], s49 offen offset:3072 sc1\n"
"v_mfma_f32_16x16x32_bf16 v[52:55], a[232:235], v[164:167], v[52:55]\n"
"ds_read_b64_tr_b16 v[160:161], v231 offset:512\n"
"v_mfma_f32_16x16x32_bf16 v[52:55], a[236:239], v[168:171], v[52:55]\n"
"v_mfma_f32_16x16x32_bf16 v[12:15], a[228:231], v[168:171], v[12:15]\n"
"ds_read_b64_tr_b16 v[162:163], v231 offset:5632\n"
"v_mfma_f32_16x16x32_bf16 v[12:15], a[224:227], v[164:167], v[12:15]\n"
"s_waitcnt lgkmcnt(4)\n"
"v_mfma_f32_16x16x32_bf16 v[16:19], a[224:227], v[172:175], v[16:19]\n"
"s_waitcnt lgkmcnt(2)\n"
"ds_read_b64_tr_b16 v[164:165], v231 offset:10752\n"
"v_mfma_f32_16x16x32_bf16 v[16:19], a[228:231], v[200:203], v[16:19]\n"
"s_waitcnt vmcnt(22)\n"
"s_barrier\n"
"v_mfma_f32_16x16x32_bf16 v[56:59], a[236:239], v[200:203], v[56:59]\n"
"ds_read_b64_tr_b16 v[166:167], v231 offset:15872\n"
"v_mfma_f32_16x16x32_bf16 v[56:59], a[232:235], v[172:175], v[56:59]\n"
"buffer_load_dwordx4 a[116:119], v253, s[56:59], s49 offen offset:0 sc1\n"
"v_mfma_f32_16x16x32_bf16 v[96:99], a[240:243], v[172:175], v[96:99]\n"
"ds_read_b64_tr_b16 v[168:169], v230 offset:768\n"
"v_mfma_f32_16x16x32_bf16 v[96:99], a[244:247], v[200:203], v[96:99]\n"
"v_mfma_f32_16x16x32_bf16 v[136:139], a[252:255], v[200:203], v[136:139]\n"
"s_add_u32 m0, s79, 102400\n"
"ds_read_b64_tr_b16 v[170:171], v230 offset:5888\n"
"v_mfma_f32_16x16x32_bf16 v[136:139], a[248:251], v[172:175], v[136:139]\n"
"s_waitcnt lgkmcnt(4)\n"
"buffer_load_dwordx4 v224, s[28:31], s83 offen offset:0 lds\n"
"v_mfma_f32_16x16x32_bf16 v[140:143], a[248:251], v[160:163], v[140:143]\n"
"s_waitcnt lgkmcnt(2)\n"
"ds_read_b64_tr_b16 v[172:173], v230 offset:11008\n"
"v_mfma_f32_16x16x32_bf16 v[140:143], a[252:255], v[164:167], v[140:143]\n"
"v_mfma_f32_16x16x32_bf16 v[100:103], a[244:247], v[164:167], v[100:103]\n"
"ds_read_b64_tr_b16 v[174:175], v230 offset:16128\n"
"v_mfma_f32_16x16x32_bf16 v[100:103], a[240:243], v[160:163], v[100:103]\n"
"buffer_load_dwordx4 a[124:127], v253, s[56:59], s49 offen offset:1024 sc1\n"
"v_mfma_f32_16x16x32_bf16 v[60:63], a[232:235], v[160:163], v[60:63]\n"
"ds_read_b64_tr_b16 v[200:201], v231 offset:768\n"
"v_mfma_f32_16x16x32_bf16 v[60:63], a[236:239], v[164:167], v[60:63]\n"
"v_mfma_f32_16x16x32_bf16 v[20:23], a[228:231], v[164:167], v[20:23]\n"
"ds_read_b64_tr_b16 v[202:203], v231 offset:5888\n"
"v_mfma_f32_16x16x32_bf16 v[20:23], a[224:227], v[160:163], v[20:23]\n"
"s_waitcnt lgkmcnt(4)\n"
"buffer_load_dwordx4 v225, s[28:31], s83 offen offset:1024 lds\n"
"v_mfma_f32_16x16x32_bf16 v[24:27], a[224:227], v[168:171], v[24:27]\n"
"s_waitcnt lgkmcnt(2)\n"
"ds_read_b64_tr_b16 v[160:161], v231 offset:11008\n"
"v_mfma_f32_16x16x32_bf16 v[24:27], a[228:231], v[172:175], v[24:27]\n"
"v_mfma_f32_16x16x32_bf16 v[64:67], a[236:239], v[172:175], v[64:67]\n"
"ds_read_b64_tr_b16 v[162:163], v231 offset:16128\n"
"v_mfma_f32_16x16x32_bf16 v[64:67], a[232:235], v[168:171], v[64:67]\n"
"buffer_load_dwordx4 a[164:167], v253, s[56:59], s49 offen offset:2048 sc1\n"
"v_mfma_f32_16x16x32_bf16 v[104:107], a[240:243], v[168:171], v[104:107]\n"
"ds_read_b64_tr_b16 v[164:165], v230 offset:1024\n"
"v_mfma_f32_16x16x32_bf16 v[104:107], a[244:247], v[172:175], v[104:107]\n"
"v_mfma_f32_16x16x32_bf16 v[144:147], a[252:255], v[172:175], v[144:147]\n"
"ds_read_b64_tr_b16 v[166:167], v230 offset:6144\n"
"v_mfma_f32_16x16x32_bf16 v[144:147], a[248:251], v[168:171], v[144:147]\n"
"s_waitcnt lgkmcnt(4)\n"
"buffer_load_dwordx4 v226, s[28:31], s83 offen offset:2048 lds\n"
"v_mfma_f32_16x16x32_bf16 v[148:151], a[248:251], v[200:203], v[148:151]\n"
"s_waitcnt lgkmcnt(2)\n"
"ds_read_b64_tr_b16 v[168:169], v230 offset:11264\n"
"v_mfma_f32_16x16x32_bf16 v[148:151], a[252:255], v[160:163], v[148:151]\n"
"v_mfma_f32_16x16x32_bf16 v[108:111], a[244:247], v[160:163], v[108:111]\n"
"ds_read_b64_tr_b16 v[170:171], v230 offset:16384\n"
"v_mfma_f32_16x16x32_bf16 v[108:111], a[240:243], v[200:203], v[108:111]\n"
"buffer_load_dwordx4 a[172:175], v253, s[56:59], s49 offen offset:3072 sc1\n"
"v_mfma_f32_16x16x32_bf16 v[68:71], a[232:235], v[200:203], v[68:71]\n"
"s_mov_b32 s49, s53\n"
"ds_read_b64_tr_b16 v[172:173], v231 offset:1024\n"
"v_mfma_f32_16x16x32_bf16 v[68:71], a[236:239], v[160:163], v[68:71]\n"
"v_mfma_f32_16x16x32_bf16 v[28:31], a[228:231], v[160:163], v[28:31]\n"
"ds_read_b64_tr_b16 v[174:175], v231 offset:6144\n"
"v_mfma_f32_16x16x32_bf16 v[28:31], a[224:227], v[200:203], v[28:31]\n"
"s_waitcnt lgkmcnt(4)\n"
"buffer_load_dwordx4 v227, s[28:31], s83 offen offset:3072 lds\n"
"v_mfma_f32_16x16x32_bf16 v[32:35], a[224:227], v[164:167], v[32:35]\n"
"s_waitcnt lgkmcnt(2)\n"
"ds_read_b64_tr_b16 v[200:201], v231 offset:11264\n"
"v_mfma_f32_16x16x32_bf16 v[32:35], a[228:231], v[168:171], v[32:35]\n"
"v_mfma_f32_16x16x32_bf16 v[72:75], a[236:239], v[168:171], v[72:75]\n"
"ds_read_b64_tr_b16 v[202:203], v231 offset:16384\n"
"v_mfma_f32_16x16x32_bf16 v[72:75], a[232:235], v[164:167], v[72:75]\n"
"global_load_dwordx4 v[192:195], v210, s[14:15] sc1\n"
"v_mfma_f32_16x16x32_bf16 v[112:115], a[240:243], v[164:167], v[112:115]\n"
"ds_read_b64_tr_b16 v[160:161], v230 offset:20480\n"
"v_mfma_f32_16x16x32_bf16 v[112:115], a[244:247], v[168:171], v[112:115]\n"
"v_mfma_f32_16x16x32_bf16 v[152:155], a[252:255], v[168:171], v[152:155]\n"
"s_add_u32 m0, s79, 106496\n"
"ds_read_b64_tr_b16 v[162:163], v230 offset:25600\n"
"v_mfma_f32_16x16x32_bf16 v[152:155], a[248:251], v[164:167], v[152:155]\n"
"s_waitcnt lgkmcnt(4)\n"
"buffer_load_dwordx4 v234, s[28:31], s83 offen offset:0 lds\n"
"v_mfma_f32_16x16x32_bf16 v[156:159], a[248:251], v[172:175], v[156:159]\n"
"s_waitcnt lgkmcnt(2)\n"
"s_mov_b32 s83, s87\n"
"ds_read_b64_tr_b16 v[164:165], v230 offset:30720\n"
"v_mfma_f32_16x16x32_bf16 v[156:159], a[252:255], v[200:203], v[156:159]\n"
"v_mfma_f32_16x16x32_bf16 v[116:119], a[244:247], v[200:203], v[116:119]\n"
"ds_read_b64_tr_b16 v[166:167], v230 offset:35840\n"
"v_mfma_f32_16x16x32_bf16 v[116:119], a[240:243], v[172:175], v[116:119]\n"
"v_mfma_f32_16x16x32_bf16 v[76:79], a[232:235], v[172:175], v[76:79]\n"
"ds_read_b64_tr_b16 v[168:169], v231 offset:20480\n"
"v_mfma_f32_16x16x32_bf16 v[76:79], a[236:239], v[200:203], v[76:79]\n"
"s_waitcnt vmcnt(18)\n"
"v_mfma_f32_16x16x32_bf16 v[36:39], a[228:231], v[200:203], v[36:39]\n"
"ds_read_b64_tr_b16 v[170:171], v231 offset:25600\n"
"v_mfma_f32_16x16x32_bf16 v[36:39], a[224:227], v[172:175], v[36:39]\n"
"s_waitcnt lgkmcnt(4)\n"
"v_mfma_f32_16x16x32_bf16 v[0:3], a[4:7], v[160:163], v[0:3]\n"
"s_waitcnt lgkmcnt(2)\n"
"ds_read_b64_tr_b16 v[172:173], v231 offset:30720\n"
"v_mfma_f32_16x16x32_bf16 v[0:3], a[12:15], v[164:167], v[0:3]\n"
"v_mfma_f32_16x16x32_bf16 v[40:43], a[60:63], v[164:167], v[40:43]\n"
"ds_read_b64_tr_b16 v[174:175], v231 offset:35840\n"
"v_mfma_f32_16x16x32_bf16 v[40:43], a[52:55], v[160:163], v[40:43]\n"
"buffer_load_dwordx4 a[192:195], v251, s[56:59], s50 offen offset:0 sc1\n"
"v_mfma_f32_16x16x32_bf16 v[80:83], a[100:103], v[160:163], v[80:83]\n"
"ds_read_b64_tr_b16 v[200:201], v230 offset:20736\n"
"v_mfma_f32_16x16x32_bf16 v[80:83], a[108:111], v[164:167], v[80:83]\n"
"v_mfma_f32_16x16x32_bf16 v[120:123], a[156:159], v[164:167], v[120:123]\n"
"s_mov_b32 m0, s17\n"
"ds_read_b64_tr_b16 v[202:203], v230 offset:25856\n"
"v_mfma_f32_16x16x32_bf16 v[120:123], a[148:151], v[160:163], v[120:123]\n"
"s_waitcnt lgkmcnt(4)\n"
"buffer_load_dwordx4 v224, s[28:31], s16 offen offset:0 lds\n"
"v_mfma_f32_16x16x32_bf16 v[124:127], a[148:151], v[168:171], v[124:127]\n"
"s_waitcnt lgkmcnt(2)\n"
"ds_read_b64_tr_b16 v[160:161], v230 offset:30976\n"
"v_mfma_f32_16x16x32_bf16 v[124:127], a[156:159], v[172:175], v[124:127]\n"
"v_mfma_f32_16x16x32_bf16 v[84:87], a[108:111], v[172:175], v[84:87]\n"
"ds_read_b64_tr_b16 v[162:163], v230 offset:36096\n"
"v_mfma_f32_16x16x32_bf16 v[84:87], a[100:103], v[168:171], v[84:87]\n"
"buffer_load_dwordx4 a[196:199], v251, s[56:59], s50 offen offset:1024 sc1\n"
"v_mfma_f32_16x16x32_bf16 v[44:47], a[52:55], v[168:171], v[44:47]\n"
"ds_read_b64_tr_b16 v[164:165], v231 offset:20736\n"
"v_mfma_f32_16x16x32_bf16 v[44:47], a[60:63], v[172:175], v[44:47]\n"
"v_mfma_f32_16x16x32_bf16 v[4:7], a[12:15], v[172:175], v[4:7]\n"
"ds_read_b64_tr_b16 v[166:167], v231 offset:25856\n"
"v_mfma_f32_16x16x32_bf16 v[4:7], a[4:7], v[168:171], v[4:7]\n"
"s_waitcnt lgkmcnt(4)\n"
"buffer_load_dwordx4 v225, s[28:31], s16 offen offset:1024 lds\n"
"v_mfma_f32_16x16x32_bf16 v[8:11], a[4:7], v[200:203], v[8:11]\n"
"s_waitcnt lgkmcnt(2)\n"
"ds_read_b64_tr_b16 v[168:169], v231 offset:30976\n"
"v_mfma_f32_16x16x32_bf16 v[8:11], a[12:15], v[160:163], v[8:11]\n"
"v_mfma_f32_16x16x32_bf16 v[48:51], a[60:63], v[160:163], v[48:51]\n"
"ds_read_b64_tr_b16 v[170:171], v231 offset:36096\n"
"v_mfma_f32_16x16x32_bf16 v[48:51], a[52:55], v[200:203], v[48:51]\n"
"buffer_load_dwordx4 a[200:203], v251, s[56:59], s50 offen offset:2048 sc1\n"
"v_mfma_f32_16x16x32_bf16 v[88:91], a[100:103], v[200:203], v[88:91]\n"
"ds_read_b64_tr_b16 v[172:173], v230 offset:20992\n"
"v_mfma_f32_16x16x32_bf16 v[88:91], a[108:111], v[160:163], v[88:91]\n"
"v_mfma_f32_16x16x32_bf16 v[128:131], a[156:159], v[160:163], v[128:131]\n"
"ds_read_b64_tr_b16 v[174:175], v230 offset:26112\n"
"v_mfma_f32_16x16x32_bf16 v[128:131], a[148:151], v[200:203], v[128:131]\n"
"s_waitcnt lgkmcnt(4)\n"
"buffer_load_dwordx4 v226, s[28:31], s16 offen offset:2048 lds\n"
"v_mfma_f32_16x16x32_bf16 v[132:135], a[148:151], v[164:167], v[132:135]\n"
"s_waitcnt lgkmcnt(2)\n"
"ds_read_b64_tr_b16 v[200:201], v230 offset:31232\n"
"v_mfma_f32_16x16x32_bf16 v[132:135], a[156:159], v[168:171], v[132:135]\n"
"v_mfma_f32_16x16x32_bf16 v[92:95], a[108:111], v[168:171], v[92:95]\n"
"ds_read_b64_tr_b16 v[202:203], v230 offset:36352\n"
"v_mfma_f32_16x16x32_bf16 v[92:95], a[100:103], v[164:167], v[92:95]\n"
"buffer_load_dwordx4 a[204:207], v251, s[56:59], s50 offen offset:3072 sc1\n"
"v_mfma_f32_16x16x32_bf16 v[52:55], a[52:55], v[164:167], v[52:55]\n"
"ds_read_b64_tr_b16 v[160:161], v231 offset:20992\n"
"v_mfma_f32_16x16x32_bf16 v[52:55], a[60:63], v[168:171], v[52:55]\n"
"v_mfma_f32_16x16x32_bf16 v[12:15], a[12:15], v[168:171], v[12:15]\n"
"ds_read_b64_tr_b16 v[162:163], v231 offset:26112\n"
"v_mfma_f32_16x16x32_bf16 v[12:15], a[4:7], v[164:167], v[12:15]\n"
"s_waitcnt lgkmcnt(4)\n"
"v_mfma_f32_16x16x32_bf16 v[16:19], a[4:7], v[172:175], v[16:19]\n"
"s_waitcnt lgkmcnt(2)\n"
"ds_read_b64_tr_b16 v[164:165], v231 offset:31232\n"
"v_mfma_f32_16x16x32_bf16 v[16:19], a[12:15], v[200:203], v[16:19]\n"
"s_waitcnt vmcnt(23)\n"
"s_barrier\n"
"v_mfma_f32_16x16x32_bf16 v[56:59], a[60:63], v[200:203], v[56:59]\n"
"ds_read_b64_tr_b16 v[166:167], v231 offset:36352\n"
"v_mfma_f32_16x16x32_bf16 v[56:59], a[52:55], v[172:175], v[56:59]\n"
"buffer_load_dwordx4 a[208:211], v253, s[56:59], s50 offen offset:0 sc1\n"
"v_mfma_f32_16x16x32_bf16 v[96:99], a[100:103], v[172:175], v[96:99]\n"
"ds_read_b64_tr_b16 v[168:169], v230 offset:21248\n"
"v_mfma_f32_16x16x32_bf16 v[96:99], a[108:111], v[200:203], v[96:99]\n"
"v_mfma_f32_16x16x32_bf16 v[136:139], a[156:159], v[200:203], v[136:139]\n"
"s_add_u32 m0, s79, 122880\n"
"ds_read_b64_tr_b16 v[170:171], v230 offset:26368\n"
"v_mfma_f32_16x16x32_bf16 v[136:139], a[148:151], v[172:175], v[136:139]\n"
"s_waitcnt lgkmcnt(4)\n"
"buffer_load_dwordx4 v224, s[28:31], s84 offen offset:0 lds\n"
"v_mfma_f32_16x16x32_bf16 v[140:143], a[148:151], v[160:163], v[140:143]\n"
"s_waitcnt lgkmcnt(2)\n"
"ds_read_b64_tr_b16 v[172:173], v230 offset:31488\n"
"v_mfma_f32_16x16x32_bf16 v[140:143], a[156:159], v[164:167], v[140:143]\n"
"v_mfma_f32_16x16x32_bf16 v[100:103], a[108:111], v[164:167], v[100:103]\n"
"ds_read_b64_tr_b16 v[174:175], v230 offset:36608\n"
"v_mfma_f32_16x16x32_bf16 v[100:103], a[100:103], v[160:163], v[100:103]\n"
"buffer_load_dwordx4 a[212:215], v253, s[56:59], s50 offen offset:1024 sc1\n"
"v_mfma_f32_16x16x32_bf16 v[60:63], a[52:55], v[160:163], v[60:63]\n"
"ds_read_b64_tr_b16 v[200:201], v231 offset:21248\n"
"v_mfma_f32_16x16x32_bf16 v[60:63], a[60:63], v[164:167], v[60:63]\n"
"v_mfma_f32_16x16x32_bf16 v[20:23], a[12:15], v[164:167], v[20:23]\n"
"ds_read_b64_tr_b16 v[202:203], v231 offset:26368\n"
"v_mfma_f32_16x16x32_bf16 v[20:23], a[4:7], v[160:163], v[20:23]\n"
"s_waitcnt lgkmcnt(4)\n"
"buffer_load_dwordx4 v225, s[28:31], s84 offen offset:1024 lds\n"
"v_mfma_f32_16x16x32_bf16 v[24:27], a[4:7], v[168:171], v[24:27]\n"
"s_waitcnt lgkmcnt(2)\n"
"ds_read_b64_tr_b16 v[160:161], v231 offset:31488\n"
"v_mfma_f32_16x16x32_bf16 v[24:27], a[12:15], v[172:175], v[24:27]\n"
"v_mfma_f32_16x16x32_bf16 v[64:67], a[60:63], v[172:175], v[64:67]\n"
"ds_read_b64_tr_b16 v[162:163], v231 offset:36608\n"
"v_mfma_f32_16x16x32_bf16 v[64:67], a[52:55], v[168:171], v[64:67]\n"
"buffer_load_dwordx4 a[216:219], v253, s[56:59], s50 offen offset:2048 sc1\n"
"v_mfma_f32_16x16x32_bf16 v[104:107], a[100:103], v[168:171], v[104:107]\n"
"ds_read_b64_tr_b16 v[164:165], v230 offset:21504\n"
"v_mfma_f32_16x16x32_bf16 v[104:107], a[108:111], v[172:175], v[104:107]\n"
"v_mfma_f32_16x16x32_bf16 v[144:147], a[156:159], v[172:175], v[144:147]\n"
"ds_read_b64_tr_b16 v[166:167], v230 offset:26624\n"
"v_mfma_f32_16x16x32_bf16 v[144:147], a[148:151], v[168:171], v[144:147]\n"
"s_waitcnt lgkmcnt(4)\n"
"buffer_load_dwordx4 v226, s[28:31], s84 offen offset:2048 lds\n"
"v_mfma_f32_16x16x32_bf16 v[148:151], a[148:151], v[200:203], v[148:151]\n"
"s_waitcnt lgkmcnt(2)\n"
"ds_read_b64_tr_b16 v[168:169], v230 offset:31744\n"
"v_mfma_f32_16x16x32_bf16 v[148:151], a[156:159], v[160:163], v[148:151]\n"
"v_mfma_f32_16x16x32_bf16 v[108:111], a[108:111], v[160:163], v[108:111]\n"
"ds_read_b64_tr_b16 v[170:171], v230 offset:36864\n"
"v_mfma_f32_16x16x32_bf16 v[108:111], a[100:103], v[200:203], v[108:111]\n"
"buffer_load_dwordx4 a[220:223], v253, s[56:59], s50 offen offset:3072 sc1\n"
"v_mfma_f32_16x16x32_bf16 v[68:71], a[52:55], v[200:203], v[68:71]\n"
"s_mov_b32 s50, s54\n"
"ds_read_b64_tr_b16 v[172:173], v231 offset:21504\n"
"v_mfma_f32_16x16x32_bf16 v[68:71], a[60:63], v[160:163], v[68:71]\n"
"v_mfma_f32_16x16x32_bf16 v[28:31], a[12:15], v[160:163], v[28:31]\n"
"ds_read_b64_tr_b16 v[174:175], v231 offset:26624\n"
"v_mfma_f32_16x16x32_bf16 v[28:31], a[4:7], v[200:203], v[28:31]\n"
"s_waitcnt lgkmcnt(4)\n"
"buffer_load_dwordx4 v227, s[28:31], s84 offen offset:3072 lds\n"
"v_mfma_f32_16x16x32_bf16 v[32:35], a[4:7], v[164:167], v[32:35]\n"
"s_waitcnt lgkmcnt(2)\n"
"ds_read_b64_tr_b16 v[200:201], v231 offset:31744\n"
"v_mfma_f32_16x16x32_bf16 v[32:35], a[12:15], v[168:171], v[32:35]\n"
"v_mfma_f32_16x16x32_bf16 v[72:75], a[60:63], v[168:171], v[72:75]\n"
"ds_read_b64_tr_b16 v[202:203], v231 offset:36864\n"
"v_mfma_f32_16x16x32_bf16 v[72:75], a[52:55], v[164:167], v[72:75]\n"
"v_mfma_f32_16x16x32_bf16 v[112:115], a[100:103], v[164:167], v[112:115]\n"
"ds_read_b64_tr_b16 v[160:161], v235 offset:0\n"
"v_mfma_f32_16x16x32_bf16 v[112:115], a[108:111], v[168:171], v[112:115]\n"
"v_mfma_f32_16x16x32_bf16 v[152:155], a[156:159], v[168:171], v[152:155]\n"
"s_add_u32 m0, s79, 126976\n"
"ds_read_b64_tr_b16 v[162:163], v235 offset:5120\n"
"v_mfma_f32_16x16x32_bf16 v[152:155], a[148:151], v[164:167], v[152:155]\n"
"s_waitcnt lgkmcnt(4)\n"
"buffer_load_dwordx4 v234, s[28:31], s84 offen offset:0 lds\n"
"v_mfma_f32_16x16x32_bf16 v[156:159], a[148:151], v[172:175], v[156:159]\n"
"s_waitcnt lgkmcnt(2)\n"
"s_mov_b32 s84, s88\n"
"ds_read_b64_tr_b16 v[164:165], v235 offset:10240\n"
"v_mfma_f32_16x16x32_bf16 v[156:159], a[156:159], v[200:203], v[156:159]\n"
"v_mfma_f32_16x16x32_bf16 v[116:119], a[108:111], v[200:203], v[116:119]\n"
"ds_read_b64_tr_b16 v[166:167], v235 offset:15360\n"
"v_mfma_f32_16x16x32_bf16 v[116:119], a[100:103], v[172:175], v[116:119]\n"
"v_mfma_f32_16x16x32_bf16 v[76:79], a[52:55], v[172:175], v[76:79]\n"
"ds_read_b64_tr_b16 v[168:169], v249 offset:0\n"
"v_mfma_f32_16x16x32_bf16 v[76:79], a[60:63], v[200:203], v[76:79]\n"
"s_waitcnt vmcnt(19)\n"
"v_mfma_f32_16x16x32_bf16 v[36:39], a[12:15], v[200:203], v[36:39]\n"
"ds_read_b64_tr_b16 v[170:171], v249 offset:5120\n"
"v_mfma_f32_16x16x32_bf16 v[36:39], a[4:7], v[172:175], v[36:39]\n"
"s_waitcnt lgkmcnt(4)\n"
"v_mfma_f32_16x16x32_bf16 v[0:3], a[20:23], v[160:163], v[0:3]\n"
"s_waitcnt lgkmcnt(2)\n"
"ds_read_b64_tr_b16 v[172:173], v249 offset:10240\n"
"v_mfma_f32_16x16x32_bf16 v[0:3], a[28:31], v[164:167], v[0:3]\n"
"v_mfma_f32_16x16x32_bf16 v[40:43], a[76:79], v[164:167], v[40:43]\n"
"ds_read_b64_tr_b16 v[174:175], v249 offset:15360\n"
"v_mfma_f32_16x16x32_bf16 v[40:43], a[68:71], v[160:163], v[40:43]\n"
"buffer_load_dwordx4 a[224:227], v251, s[56:59], s51 offen offset:0 sc1\n"
"v_mfma_f32_16x16x32_bf16 v[80:83], a[116:119], v[160:163], v[80:83]\n"
"s_sub_u32 s80, s80, 1\n"
"ds_read_b64_tr_b16 v[200:201], v235 offset:256\n"
"v_mfma_f32_16x16x32_bf16 v[80:83], a[124:127], v[164:167], v[80:83]\n"
"v_mfma_f32_16x16x32_bf16 v[120:123], a[172:175], v[164:167], v[120:123]\n"
"s_mov_b32 m0, s17\n"
"ds_read_b64_tr_b16 v[202:203], v235 offset:5376\n"
"v_mfma_f32_16x16x32_bf16 v[120:123], a[164:167], v[160:163], v[120:123]\n"
"s_waitcnt lgkmcnt(4)\n"
"buffer_load_dwordx4 v227, s[28:31], s16 offen offset:3072 lds\n"
"v_mfma_f32_16x16x32_bf16 v[124:127], a[164:167], v[168:171], v[124:127]\n"
"s_waitcnt lgkmcnt(2)\n"
"ds_read_b64_tr_b16 v[160:161], v235 offset:10496\n"
"v_mfma_f32_16x16x32_bf16 v[124:127], a[172:175], v[172:175], v[124:127]\n"
"v_mfma_f32_16x16x32_bf16 v[84:87], a[124:127], v[172:175], v[84:87]\n"
"ds_read_b64_tr_b16 v[162:163], v235 offset:15616\n"
"v_mfma_f32_16x16x32_bf16 v[84:87], a[116:119], v[168:171], v[84:87]\n"
"buffer_load_dwordx4 a[228:231], v251, s[56:59], s51 offen offset:1024 sc1\n"
"v_mfma_f32_16x16x32_bf16 v[44:47], a[68:71], v[168:171], v[44:47]\n"
"ds_read_b64_tr_b16 v[164:165], v249 offset:256\n"
"v_mfma_f32_16x16x32_bf16 v[44:47], a[76:79], v[172:175], v[44:47]\n"
"s_waitcnt vmcnt(20)\n"
"v_xor_b32 v195, s13, v195\n"
"v_mfma_f32_16x16x32_bf16 v[4:7], a[28:31], v[172:175], v[4:7]\n"
"s_add_u32 m0, s17, 4096\n"
"ds_read_b64_tr_b16 v[166:167], v249 offset:5376\n"
"v_mfma_f32_16x16x32_bf16 v[4:7], a[20:23], v[168:171], v[4:7]\n"
"s_waitcnt lgkmcnt(4)\n"
"buffer_load_dwordx4 v234, s[28:31], s16 offen offset:0 lds\n"
"v_mfma_f32_16x16x32_bf16 v[8:11], a[20:23], v[200:203], v[8:11]\n"
"s_waitcnt lgkmcnt(2)\n"
"s_add_u32 s16, s88, 81920\n"
"ds_read_b64_tr_b16 v[168:169], v249 offset:10496\n"
"v_xor_b32 v192, s13, v192\n"
"v_mfma_f32_16x16x32_bf16 v[8:11], a[28:31], v[160:163], v[8:11]\n"
"s_add_u32 s17, s17, 20480\n"
"v_mfma_f32_16x16x32_bf16 v[48:51], a[76:79], v[160:163], v[48:51]\n"
"ds_read_b64_tr_b16 v[170:171], v249 offset:15616\n"
"v_xor_b32 v193, s13, v193\n"
"v_mfma_f32_16x16x32_bf16 v[48:51], a[68:71], v[200:203], v[48:51]\n"
"buffer_load_dwordx4 a[232:235], v251, s[56:59], s51 offen offset:2048 sc1\n"
"v_mfma_f32_16x16x32_bf16 v[88:91], a[116:119], v[200:203], v[88:91]\n"
"ds_read_b64_tr_b16 v[172:173], v235 offset:512\n"
"v_xor_b32 v194, s13, v194\n"
"v_mfma_f32_16x16x32_bf16 v[88:91], a[124:127], v[160:163], v[88:91]\n"
"v_mfma_f32_16x16x32_bf16 v[128:131], a[172:175], v[160:163], v[128:131]\n"
"ds_read_b64_tr_b16 v[174:175], v235 offset:5632\n"
"v_or_b32 v195, v195, v192\n"
"v_mfma_f32_16x16x32_bf16 v[128:131], a[164:167], v[200:203], v[128:131]\n"
"s_waitcnt lgkmcnt(4)\n"
"v_mfma_f32_16x16x32_bf16 v[132:135], a[164:167], v[164:167], v[132:135]\n"
"s_waitcnt lgkmcnt(2)\n"
"ds_read_b64_tr_b16 v[200:201], v235 offset:10752\n"
"v_or3_b32 v195, v195, v193, v194\n"
"v_mfma_f32_16x16x32_bf16 v[132:135], a[172:175], v[168:171], v[132:135]\n"
"v_mfma_f32_16x16x32_bf16 v[92:95], a[124:127], v[168:171], v[92:95]\n"
"ds_read_b64_tr_b16 v[202:203], v235 offset:15872\n"
"v_cmp_ne_u32 vcc, 0, v195\n"
"v_mfma_f32_16x16x32_bf16 v[92:95], a[116:119], v[164:167], v[92:95]\n"
"buffer_load_dwordx4 a[236:239], v251, s[56:59], s51 offen offset:3072 sc1\n"
"v_mfma_f32_16x16x32_bf16 v[52:55], a[68:71], v[164:167], v[52:55]\n"
"ds_read_b64_tr_b16 v[160:161], v249 offset:512\n"
"v_mfma_f32_16x16x32_bf16 v[52:55], a[76:79], v[168:171], v[52:55]\n"
"s_cbranch_vccnz .Lat_slowa\n"
".Lat_oka:\n"
"v_mfma_f32_16x16x32_bf16 v[12:15], a[28:31], v[168:171], v[12:15]\n"
"ds_read_b64_tr_b16 v[162:163], v249 offset:5632\n"
"v_add_u32 v210, 16, v210\n"
"v_mfma_f32_16x16x32_bf16 v[12:15], a[20:23], v[164:167], v[12:15]\n"
"s_waitcnt lgkmcnt(4)\n"
"v_mfma_f32_16x16x32_bf16 v[16:19], a[20:23], v[172:175], v[16:19]\n"
"s_waitcnt lgkmcnt(2)\n"
"ds_read_b64_tr_b16 v[164:165], v249 offset:10752\n"
"v_mfma_f32_16x16x32_bf16 v[16:19], a[28:31], v[200:203], v[16:19]\n"
"s_waitcnt vmcnt(22)\n"
"s_barrier\n"
"v_mfma_f32_16x16x32_bf16 v[56:59], a[76:79], v[200:203], v[56:59]\n"
"ds_read_b64_tr_b16 v[166:167], v249 offset:15872\n"
"v_mfma_f32_16x16x32_bf16 v[56:59], a[68:71], v[172:175], v[56:59]\n"
"buffer_load_dwordx4 a[240:243], v253, s[56:59], s51 offen offset:0 sc1\n"
"v_mfma_f32_16x16x32_bf16 v[96:99], a[116:119], v[172:175], v[96:99]\n"
"ds_read_b64_tr_b16 v[168:169], v235 offset:768\n"
"v_mfma_f32_16x16x32_bf16 v[96:99], a[124:127], v[200:203], v[96:99]\n"
"v_mfma_f32_16x16x32_bf16 v[136:139], a[172:175], v[200:203], v[136:139]\n"
"s_add_u32 m0, s79, 143360\n"
"ds_read_b64_tr_b16 v[170:171], v235 offset:5888\n"
"v_mfma_f32_16x16x32_bf16 v[136:139], a[164:167], v[172:175], v[136:139]\n"
"s_waitcnt lgkmcnt(4)\n"
"buffer_load_dwordx4 v224, s[28:31], s85 offen offset:0 lds\n"
"v_mfma_f32_16x16x32_bf16 v[140:143], a[164:167], v[160:163], v[140:143]\n"
"s_waitcnt lgkmcnt(2)\n"
"ds_read_b64_tr_b16 v[172:173], v235 offset:11008\n"
"v_mfma_f32_16x16x32_bf16 v[140:143], a[172:175], v[164:167], v[140:143]\n"
"v_mfma_f32_16x16x32_bf16 v[100:103], a[124:127], v[164:167], v[100:103]\n"
"ds_read_b64_tr_b16 v[174:175], v235 offset:16128\n"
"v_mfma_f32_16x16x32_bf16 v[100:103], a[116:119], v[160:163], v[100:103]\n"
"buffer_load_dwordx4 a[244:247], v253, s[56:59], s51 offen offset:1024 sc1\n"
"v_mfma_f32_16x16x32_bf16 v[60:63], a[68:71], v[160:163], v[60:63]\n"
"ds_read_b64_tr_b16 v[200:201], v249 offset:768\n"
"v_mfma_f32_16x16x32_bf16 v[60:63], a[76:79], v[164:167], v[60:63]\n"
"v_mfma_f32_16x16x32_bf16 v[20:23], a[28:31], v[164:167], v[20:23]\n"
"ds_read_b64_tr_b16 v[202:203], v249 offset:5888\n"
"v_mfma_f32_16x16x32_bf16 v[20:23], a[20:23], v[160:163], v[20:23]\n"
"s_waitcnt lgkmcnt(4)\n"
"buffer_load_dwordx4 v225, s[28:31], s85 offen offset:1024 lds\n"
"v_mfma_f32_16x16x32_bf16 v[24:27], a[20:23], v[168:171], v[24:27]\n"
"s_waitcnt lgkmcnt(2)\n"
"ds_read_b64_tr_b16 v[160:161], v249 offset:11008\n"
"v_mfma_f32_16x16x32_bf16 v[24:27], a[28:31], v[172:175], v[24:27]\n"
"v_mfma_f32_16x16x32_bf16 v[64:67], a[76:79], v[172:175], v[64:67]\n"
"ds_read_b64_tr_b16 v[162:163], v249 offset:16128\n"
"v_mfma_f32_16x16x32_bf16 v[64:67], a[68:71], v[168:171], v[64:67]\n"
"buffer_load_dwordx4 a[248:251], v253, s[56:59], s51 offen offset:2048 sc1\n"
"v_mfma_f32_16x16x32_bf16 v[104:107], a[116:119], v[168:171], v[104:107]\n"
"ds_read_b64_tr_b16 v[164:165], v235 offset:1024\n"
"v_mfma_f32_16x16x32_bf16 v[104:107], a[124:127], v[172:175], v[104:107]\n"
"v_mfma_f32_16x16x32_bf16 v[144:147], a[172:175], v[172:175], v[144:147]\n"
"ds_read_b64_tr_b16 v[166:167], v235 offset:6144\n"
"v_mfma_f32_16x16x32_bf16 v[144:147], a[164:167], v[168:171], v[144:147]\n"
"s_waitcnt lgkmcnt(4)\n"
"buffer_load_dwordx4 v226, s[28:31], s85 offen offset:2048 lds\n"
"v_mfma_f32_16x16x32_bf16 v[148:151], a[164:167], v[200:203], v[148:151]\n"
"s_waitcnt lgkmcnt(2)\n"
"ds_read_b64_tr_b16 v[168:169], v235 offset:11264\n"
"v_mfma_f32_16x16x32_bf16 v[148:151], a[172:175], v[160:163], v[148:151]\n"
"v_mfma_f32_16x16x32_bf16 v[108:111], a[124:127], v[160:163], v[108:111]\n"
"ds_read_b64_tr_b16 v[170:171], v235 offset:16384\n"
"v_mfma_f32_16x16x32_bf16 v[108:111], a[116:119], v[200:203], v[108:111]\n"
"buffer_load_dwordx4 a[252:255], v253, s[56:59], s51 offen offset:3072 sc1\n"
"v_mfma_f32_16x16x32_bf16 v[68:71], a[68:71], v[200:203], v[68:71]\n"
"s_mov_b32 s51, s55\n"
"ds_read_b64_tr_b16 v[172:173], v249 offset:1024\n"
"v_mfma_f32_16x16x32_bf16 v[68:71], a[76:79], v[160:163], v[68:71]\n"
"v_mfma_f32_16x16x32_bf16 v[28:31], a[28:31], v[160:163], v[28:31]\n"
"ds_read_b64_tr_b16 v[174:175], v249 offset:6144\n"
"v_mfma_f32_16x16x32_bf16 v[28:31], a[20:23], v[200:203], v[28:31]\n"
"s_waitcnt lgkmcnt(4)\n"
"buffer_load_dwordx4 v227, s[28:31], s85 offen offset:3072 lds\n"
"v_mfma_f32_16x16x32_bf16 v[32:35], a[20:23], v[164:167], v[32:35]\n"
"s_waitcnt lgkmcnt(2)\n"
"ds_read_b64_tr_b16 v[200:201], v249 offset:11264\n"
"v_mfma_f32_16x16x32_bf16 v[32:35], a[28:31], v[168:171], v[32:35]\n"
"v_mfma_f32_16x16x32_bf16 v[72:75], a[76:79], v[168:171], v[72:75]\n"
"ds_read_b64_tr_b16 v[202:203], v249 offset:16384\n"
"v_mfma_f32_16x16x32_bf16 v[72:75], a[68:71], v[164:167], v[72:75]\n"
"global_store_dword v208, v209, s[14:15] sc1\n"
"v_mfma_f32_16x16x32_bf16 v[112:115], a[116:119], v[164:167], v[112:115]\n"
"ds_read_b64_tr_b16 v[160:161], v228 offset:40960\n"
"v_mfma_f32_16x16x32_bf16 v[112:115], a[124:127], v[168:171], v[112:115]\n"
"v_mfma_f32_16x16x32_bf16 v[152:155], a[172:175], v[168:171], v[152:155]\n"
"s_add_u32 m0, s79, 147456\n"
"ds_read_b64_tr_b16 v[162:163], v228 offset:46080\n"
"v_mfma_f32_16x16x32_bf16 v[152:155], a[164:167], v[164:167], v[152:155]\n"
"s_waitcnt lgkmcnt(4)\n"
"buffer_load_dwordx4 v234, s[28:31], s85 offen offset:0 lds\n"
"v_mfma_f32_16x16x32_bf16 v[156:159], a[164:167], v[172:175], v[156:159]\n"
"s_waitcnt lgkmcnt(2)\n"
"s_mov_b32 s85, s89\n"
"ds_read_b64_tr_b16 v[164:165], v228 offset:51200\n"
"v_mfma_f32_16x16x32_bf16 v[156:159], a[172:175], v[200:203], v[156:159]\n"
"v_mfma_f32_16x16x32_bf16 v[116:119], a[124:127], v[200:203], v[116:119]\n"
"ds_read_b64_tr_b16 v[166:167], v228 offset:56320\n"
"v_mfma_f32_16x16x32_bf16 v[116:119], a[116:119], v[172:175], v[116:119]\n"
"v_mfma_f32_16x16x32_bf16 v[76:79], a[68:71], v[172:175], v[76:79]\n"
"ds_read_b64_tr_b16 v[168:169], v229 offset:40960\n"
"v_mfma_f32_16x16x32_bf16 v[76:79], a[76:79], v[200:203], v[76:79]\n"
"s_waitcnt vmcnt(18)\n"
"v_mfma_f32_16x16x32_bf16 v[36:39], a[28:31], v[200:203], v[36:39]\n"
"s_cmp_lg_u32 s80, 0\n"
"ds_read_b64_tr_b16 v[170:171], v229 offset:46080\n"
"v_mfma_f32_16x16x32_bf16 v[36:39], a[20:23], v[172:175], v[36:39]\n"
"s_cbranch_scc1 .Lat_ol\n"
".Lat_ot:\n"
"s_waitcnt lgkmcnt(4)\n"
"v_mfma_f32_16x16x32_bf16 v[0:3], a[192:195], v[160:163], v[0:3]\n"
"s_waitcnt lgkmcnt(2)\n"
"s_add_u32 s72, s53, 524288\n"
"ds_read_b64_tr_b16 v[172:173], v229 offset:51200\n"
"v_mfma_f32_16x16x32_bf16 v[0:3], a[196:199], v[164:167], v[0:3]\n"
"buffer_load_dwordx4 a[4:7], v251, s[56:59], s72 offen offset:0 sc1\n"
"v_mfma_f32_16x16x32_bf16 v[40:43], a[204:207], v[164:167], v[40:43]\n"
"ds_read_b64_tr_b16 v[174:175], v229 offset:56320\n"
"v_mfma_f32_16x16x32_bf16 v[40:43], a[200:203], v[160:163], v[40:43]\n"
"v_mfma_f32_16x16x32_bf16 v[80:83], a[208:211], v[160:163], v[80:83]\n"
"ds_read_b64_tr_b16 v[200:201], v228 offset:41984\n"
"v_mfma_f32_16x16x32_bf16 v[80:83], a[212:215], v[164:167], v[80:83]\n"
"buffer_load_dwordx4 a[12:15], v251, s[56:59], s72 offen offset:1024 sc1\n"
"v_mfma_f32_16x16x32_bf16 v[120:123], a[220:223], v[164:167], v[120:123]\n"
"ds_read_b64_tr_b16 v[202:203], v228 offset:47104\n"
"v_mfma_f32_16x16x32_bf16 v[120:123], a[216:219], v[160:163], v[120:123]\n"
"s_waitcnt lgkmcnt(4)\n"
"v_mfma_f32_16x16x32_bf16 v[124:127], a[216:219], v[168:171], v[124:127]\n"
"s_waitcnt lgkmcnt(2)\n"
"ds_read_b64_tr_b16 v[160:161], v228 offset:52224\n"
"v_mfma_f32_16x16x32_bf16 v[124:127], a[220:223], v[172:175], v[124:127]\n"
"buffer_load_dwordx4 a[52:55], v251, s[56:59], s72 offen offset:2048 sc1\n"
"v_mfma_f32_16x16x32_bf16 v[84:87], a[212:215], v[172:175], v[84:87]\n"
"ds_read_b64_tr_b16 v[162:163], v228 offset:57344\n"
"v_mfma_f32_16x16x32_bf16 v[84:87], a[208:211], v[168:171], v[84:87]\n"
"v_mfma_f32_16x16x32_bf16 v[44:47], a[200:203], v[168:171], v[44:47]\n"
"ds_read_b64_tr_b16 v[164:165], v229 offset:41984\n"
"v_mfma_f32_16x16x32_bf16 v[44:47], a[204:207], v[172:175], v[44:47]\n"
"buffer_load_dwordx4 a[60:63], v251, s[56:59], s72 offen offset:3072 sc1\n"
"v_mfma_f32_16x16x32_bf16 v[4:7], a[196:199], v[172:175], v[4:7]\n"
"ds_read_b64_tr_b16 v[166:167], v229 offset:47104\n"
"v_mfma_f32_16x16x32_bf16 v[4:7], a[192:195], v[168:171], v[4:7]\n"
"s_waitcnt lgkmcnt(4)\n"
"v_mfma_f32_16x16x32_bf16 v[32:35], a[192:195], v[200:203], v[32:35]\n"
"s_waitcnt lgkmcnt(2)\n"
"ds_read_b64_tr_b16 v[168:169], v229 offset:52224\n"
"v_mfma_f32_16x16x32_bf16 v[32:35], a[196:199], v[160:163], v[32:35]\n"
"v_mfma_f32_16x16x32_bf16 v[72:75], a[204:207], v[160:163], v[72:75]\n"
"ds_read_b64_tr_b16 v[170:171], v229 offset:57344\n"
"v_mfma_f32_16x16x32_bf16 v[72:75], a[200:203], v[200:203], v[72:75]\n"
"buffer_load_dwordx4 a[100:103], v253, s[56:59], s72 offen offset:0 sc1\n"
"v_mfma_f32_16x16x32_bf16 v[112:115], a[208:211], v[200:203], v[112:115]\n"
"ds_read_b64_tr_b16 v[172:173], v228 offset:41728\n"
"v_mfma_f32_16x16x32_bf16 v[112:115], a[212:215], v[160:163], v[112:115]\n"
"v_mfma_f32_16x16x32_bf16 v[152:155], a[220:223], v[160:163], v[152:155]\n"
"ds_read_b64_tr_b16 v[174:175], v228 offset:46848\n"
"v_mfma_f32_16x16x32_bf16 v[152:155], a[216:219], v[200:203], v[152:155]\n"
"s_waitcnt lgkmcnt(4)\n"
"v_mfma_f32_16x16x32_bf16 v[156:159], a[216:219], v[164:167], v[156:159]\n"
"s_waitcnt lgkmcnt(2)\n"
"ds_read_b64_tr_b16 v[200:201], v228 offset:51968\n"
"v_mfma_f32_16x16x32_bf16 v[156:159], a[220:223], v[168:171], v[156:159]\n"
"buffer_load_dwordx4 a[108:111], v253, s[56:59], s72 offen offset:1024 sc1\n"
"v_mfma_f32_16x16x32_bf16 v[116:119], a[212:215], v[168:171], v[116:119]\n"
"ds_read_b64_tr_b16 v[202:203], v228 offset:57088\n"
"v_mfma_f32_16x16x32_bf16 v[116:119], a[208:211], v[164:167], v[116:119]\n"
"v_mfma_f32_16x16x32_bf16 v[76:79], a[200:203], v[164:167], v[76:79]\n"
"ds_read_b64_tr_b16 v[160:161], v229 offset:41728\n"
"v_mfma_f32_16x16x32_bf16 v[76:79], a[204:207], v[168:171], v[76:79]\n"
"buffer_load_dwordx4 a[148:151], v253, s[56:59], s72 offen offset:2048 sc1\n"
"v_mfma_f32_16x16x32_bf16 v[36:39], a[196:199], v[168:171], v[36:39]\n"
"ds_read_b64_tr_b16 v[162:163], v229 offset:46848\n"
"v_mfma_f32_16x16x32_bf16 v[36:39], a[192:195], v[164:167], v[36:39]\n"
"s_waitcnt lgkmcnt(4)\n"
"v_mfma_f32_16x16x32_bf16 v[24:27], a[192:195], v[172:175], v[24:27]\n"
"s_waitcnt lgkmcnt(2)\n"
"ds_read_b64_tr_b16 v[164:165], v229 offset:51968\n"
"v_mfma_f32_16x16x32_bf16 v[24:27], a[196:199], v[200:203], v[24:27]\n"
"buffer_load_dwordx4 a[156:159], v253, s[56:59], s72 offen offset:3072 sc1\n"
"v_mfma_f32_16x16x32_bf16 v[64:67], a[204:207], v[200:203], v[64:67]\n"
"ds_read_b64_tr_b16 v[166:167], v229 offset:57088\n"
"v_mfma_f32_16x16x32_bf16 v[64:67], a[200:203], v[172:175], v[64:67]\n"
"v_mfma_f32_16x16x32_bf16 v[104:107], a[208:211], v[172:175], v[104:107]\n"
"s_add_u32 s72, s53, 1048576\n"
"ds_read_b64_tr_b16 v[168:169], v228 offset:41472\n"
"v_mfma_f32_16x16x32_bf16 v[104:107], a[212:215], v[200:203], v[104:107]\n"
"buffer_load_dwordx4 a[20:23], v251, s[56:59], s72 offen offset:0 sc1\n"
"v_mfma_f32_16x16x32_bf16 v[144:147], a[220:223], v[200:203], v[144:147]\n"
"ds_read_b64_tr_b16 v[170:171], v228 offset:46592\n"
"v_mfma_f32_16x16x32_bf16 v[144:147], a[216:219], v[172:175], v[144:147]\n"
"s_waitcnt lgkmcnt(4)\n"
"v_mfma_f32_16x16x32_bf16 v[148:151], a[216:219], v[160:163], v[148:151]\n"
"s_waitcnt lgkmcnt(2)\n"
"ds_read_b64_tr_b16 v[172:173], v228 offset:51712\n"
"v_mfma_f32_16x16x32_bf16 v[148:151], a[220:223], v[164:167], v[148:151]\n"
"v_mfma_f32_16x16x32_bf16 v[108:111], a[212:215], v[164:167], v[108:111]\n"
"ds_read_b64_tr_b16 v[174:175], v228 offset:56832\n"
"v_mfma_f32_16x16x32_bf16 v[108:111], a[208:211], v[160:163], v[108:111]\n"
"buffer_load_dwordx4 a[28:31], v251, s[56:59], s72 offen offset:1024 sc1\n"
"v_mfma_f32_16x16x32_bf16 v[68:71], a[200:203], v[160:163], v[68:71]\n"
"ds_read_b64_tr_b16 v[200:201], v229 offset:41472\n"
"v_mfma_f32_16x16x32_bf16 v[68:71], a[204:207], v[164:167], v[68:71]\n"
"v_mfma_f32_16x16x32_bf16 v[28:31], a[196:199], v[164:167], v[28:31]\n"
"ds_read_b64_tr_b16 v[202:203], v229 offset:46592\n"
"v_mfma_f32_16x16x32_bf16 v[28:31], a[192:195], v[160:163], v[28:31]\n"
"s_waitcnt lgkmcnt(4)\n"
"v_mfma_f32_16x16x32_bf16 v[16:19], a[192:195], v[168:171], v[16:19]\n"
"s_waitcnt lgkmcnt(2)\n"
"ds_read_b64_tr_b16 v[160:161], v229 offset:51712\n"
"v_mfma_f32_16x16x32_bf16 v[16:19], a[196:199], v[172:175], v[16:19]\n"
"buffer_load_dwordx4 a[68:71], v251, s[56:59], s72 offen offset:2048 sc1\n"
"v_mfma_f32_16x16x32_bf16 v[56:59], a[204:207], v[172:175], v[56:59]\n"
"ds_read_b64_tr_b16 v[162:163], v229 offset:56832\n"
"v_mfma_f32_16x16x32_bf16 v[56:59], a[200:203], v[168:171], v[56:59]\n"
"v_mfma_f32_16x16x32_bf16 v[96:99], a[208:211], v[168:171], v[96:99]\n"
"ds_read_b64_tr_b16 v[164:165], v228 offset:41216\n"
"v_mfma_f32_16x16x32_bf16 v[96:99], a[212:215], v[172:175], v[96:99]\n"
"buffer_load_dwordx4 a[76:79], v251, s[56:59], s72 offen offset:3072 sc1\n"
"v_mfma_f32_16x16x32_bf16 v[136:139], a[220:223], v[172:175], v[136:139]\n"
"ds_read_b64_tr_b16 v[166:167], v228 offset:46336\n"
"v_mfma_f32_16x16x32_bf16 v[136:139], a[216:219], v[168:171], v[136:139]\n"
"s_waitcnt lgkmcnt(4)\n"
"v_mfma_f32_16x16x32_bf16 v[140:143], a[216:219], v[200:203], v[140:143]\n"
"s_waitcnt lgkmcnt(2)\n"
"ds_read_b64_tr_b16 v[168:169], v228 offset:51456\n"
"v_mfma_f32_16x16x32_bf16 v[140:143], a[220:223], v[160:163], v[140:143]\n"
"buffer_load_dwordx4 a[116:119], v253, s[56:59], s72 offen offset:0 sc1\n"
"v_mfma_f32_16x16x32_bf16 v[100:103], a[212:215], v[160:163], v[100:103]\n"
"ds_read_b64_tr_b16 v[170:171], v228 offset:56576\n"
"v_mfma_f32_16x16x32_bf16 v[100:103], a[208:211], v[200:203], v[100:103]\n"
"v_mfma_f32_16x16x32_bf16 v[60:63], a[200:203], v[200:203], v[60:63]\n"
"ds_read_b64_tr_b16 v[172:173], v229 offset:41216\n"
"v_mfma_f32_16x16x32_bf16 v[60:63], a[204:207], v[160:163], v[60:63]\n"
"buffer_load_dwordx4 a[124:127], v253, s[56:59], s72 offen offset:1024 sc1\n"
"v_mfma_f32_16x16x32_bf16 v[20:23], a[196:199], v[160:163], v[20:23]\n"
"ds_read_b64_tr_b16 v[174:175], v229 offset:46336\n"
"v_mfma_f32_16x16x32_bf16 v[20:23], a[192:195], v[200:203], v[20:23]\n"
"s_waitcnt lgkmcnt(4)\n"
"v_mfma_f32_16x16x32_bf16 v[8:11], a[192:195], v[164:167], v[8:11]\n"
"s_waitcnt lgkmcnt(2)\n"
"ds_read_b64_tr_b16 v[200:201], v229 offset:51456\n"
"v_mfma_f32_16x16x32_bf16 v[8:11], a[196:199], v[168:171], v[8:11]\n"
"v_mfma_f32_16x16x32_bf16 v[48:51], a[204:207], v[168:171], v[48:51]\n"
"ds_read_b64_tr_b16 v[202:203], v229 offset:56576\n"
"v_mfma_f32_16x16x32_bf16 v[48:51], a[200:203], v[164:167], v[48:51]\n"
"s_waitcnt vmcnt(14)\n"
"s_barrier\n"
"v_mfma_f32_16x16x32_bf16 v[88:91], a[208:211], v[164:167], v[88:91]\n"
"ds_read_b64_tr_b16 v[160:161], v230 offset:1024\n"
"v_mfma_f32_16x16x32_bf16 v[88:91], a[212:215], v[168:171], v[88:91]\n"
"buffer_load_dwordx4 a[164:167], v253, s[56:59], s72 offen offset:2048 sc1\n"
"v_mfma_f32_16x16x32_bf16 v[128:131], a[220:223], v[168:171], v[128:131]\n"
"ds_read_b64_tr_b16 v[162:163], v230 offset:6144\n"
"v_readlane_b32 s12, v255, 12\n"
"v_mfma_f32_16x16x32_bf16 v[128:131], a[216:219], v[164:167], v[128:131]\n"
"s_waitcnt lgkmcnt(4)\n"
"v_mfma_f32_16x16x32_bf16 v[132:135], a[216:219], v[172:175], v[132:135]\n"
"s_waitcnt lgkmcnt(2)\n"
"ds_read_b64_tr_b16 v[164:165], v230 offset:11264\n"
"v_readlane_b32 s36, v255, 13\n"
"v_mfma_f32_16x16x32_bf16 v[132:135], a[220:223], v[200:203], v[132:135]\n"
"buffer_load_dwordx4 a[172:175], v253, s[56:59], s72 offen offset:3072 sc1\n"
"v_mfma_f32_16x16x32_bf16 v[92:95], a[212:215], v[200:203], v[92:95]\n"
"ds_read_b64_tr_b16 v[166:167], v230 offset:16384\n"
"v_readlane_b32 s34, v255, 15\n"
"v_mfma_f32_16x16x32_bf16 v[92:95], a[208:211], v[172:175], v[92:95]\n"
"s_add_u32 s74, s86, 163840\n"
"v_mfma_f32_16x16x32_bf16 v[52:55], a[200:203], v[172:175], v[52:55]\n"
"s_mov_b32 m0, s79\n"
"ds_read_b64_tr_b16 v[168:169], v231 offset:1024\n"
"v_readlane_b32 s35, v255, 24\n"
"v_mfma_f32_16x16x32_bf16 v[52:55], a[204:207], v[200:203], v[52:55]\n"
"buffer_load_dwordx4 v224, s[28:31], s74 offen offset:0 lds\n"
"v_mfma_f32_16x16x32_bf16 v[12:15], a[196:199], v[200:203], v[12:15]\n"
"ds_read_b64_tr_b16 v[170:171], v231 offset:6144\n"
"v_bfe_u32 v237, v251, 4, 4\n"
"v_mfma_f32_16x16x32_bf16 v[12:15], a[192:195], v[172:175], v[12:15]\n"
"s_waitcnt lgkmcnt(4)\n"
"v_mfma_f32_16x16x32_bf16 v[32:35], a[224:227], v[160:163], v[32:35]\n"
"s_waitcnt lgkmcnt(2)\n"
"ds_read_b64_tr_b16 v[172:173], v231 offset:11264\n"
"v_lshrrev_b32 v238, 8, v251\n"
"v_mfma_f32_16x16x32_bf16 v[32:35], a[228:231], v[164:167], v[32:35]\n"
"buffer_load_dwordx4 v225, s[28:31], s74 offen offset:1024 lds\n"
"v_mfma_f32_16x16x32_bf16 v[72:75], a[236:239], v[164:167], v[72:75]\n"
"ds_read_b64_tr_b16 v[174:175], v231 offset:16384\n"
"v_mov_b32 v196, s12\n"
"v_mfma_f32_16x16x32_bf16 v[72:75], a[232:235], v[160:163], v[72:75]\n"
"v_mfma_f32_16x16x32_bf16 v[112:115], a[240:243], v[160:163], v[112:115]\n"
"ds_read_b64_tr_b16 v[200:201], v230 offset:768\n"
"v_mov_b32 v197, s36\n"
"v_mfma_f32_16x16x32_bf16 v[112:115], a[244:247], v[164:167], v[112:115]\n"
"buffer_load_dwordx4 v226, s[28:31], s74 offen offset:2048 lds\n"
"v_mfma_f32_16x16x32_bf16 v[152:155], a[252:255], v[164:167], v[152:155]\n"
"ds_read_b64_tr_b16 v[202:203], v230 offset:5888\n"
"v_mov_b32 v198, s34\n"
"v_mfma_f32_16x16x32_bf16 v[152:155], a[248:251], v[160:163], v[152:155]\n"
"s_waitcnt lgkmcnt(4)\n"
"v_mfma_f32_16x16x32_bf16 v[156:159], a[248:251], v[168:171], v[156:159]\n"
"s_waitcnt lgkmcnt(2)\n"
"ds_read_b64_tr_b16 v[160:161], v230 offset:11008\n"
"v_mov_b32 v214, s35\n"
"v_mfma_f32_16x16x32_bf16 v[156:159], a[252:255], v[172:175], v[156:159]\n"
"v_mfma_f32_16x16x32_bf16 v[116:119], a[244:247], v[172:175], v[116:119]\n"
"ds_read_b64_tr_b16 v[162:163], v230 offset:16128\n"
"v_readlane_b32 s12, v255, 7\n"
"v_mfma_f32_16x16x32_bf16 v[116:119], a[240:243], v[168:171], v[116:119]\n"
"buffer_load_dwordx4 v227, s[28:31], s74 offen offset:3072 lds\n"
"v_mfma_f32_16x16x32_bf16 v[76:79], a[232:235], v[168:171], v[76:79]\n"
"ds_read_b64_tr_b16 v[164:165], v231 offset:768\n"
"v_lshrrev_b32 v216, 2, v237\n"
"v_mfma_f32_16x16x32_bf16 v[76:79], a[236:239], v[172:175], v[76:79]\n"
"v_mfma_f32_16x16x32_bf16 v[36:39], a[228:231], v[172:175], v[36:39]\n"
"ds_read_b64_tr_b16 v[166:167], v231 offset:5888\n"
"v_cmp_eq_u32 vcc, v238, v216\n"
"v_mfma_f32_16x16x32_bf16 v[36:39], a[224:227], v[168:171], v[36:39]\n"
"s_waitcnt lgkmcnt(4)\n"
"v_mfma_f32_16x16x32_bf16 v[24:27], a[224:227], v[200:203], v[24:27]\n"
"s_waitcnt lgkmcnt(2)\n"
"s_add_u32 m0, s79, 4096\n"
"ds_read_b64_tr_b16 v[168:169], v231 offset:11008\n"
"v_and_b32 v217, 1, v237\n"
"v_mfma_f32_16x16x32_bf16 v[24:27], a[228:231], v[160:163], v[24:27]\n"
"buffer_load_dwordx4 v234, s[28:31], s74 offen offset:0 lds\n"
"v_mfma_f32_16x16x32_bf16 v[64:67], a[236:239], v[160:163], v[64:67]\n"
"ds_read_b64_tr_b16 v[170:171], v231 offset:16128\n"
"v_lshlrev_b32 v217, 4, v217\n"
"v_mfma_f32_16x16x32_bf16 v[64:67], a[232:235], v[200:203], v[64:67]\n"
"v_mfma_f32_16x16x32_bf16 v[104:107], a[240:243], v[200:203], v[104:107]\n"
"s_add_u32 s72, s54, 524288\n"
"ds_read_b64_tr_b16 v[172:173], v230 offset:512\n"
"v_mov_b32 v216, 0x3f80\n"
"v_mfma_f32_16x16x32_bf16 v[104:107], a[244:247], v[160:163], v[104:107]\n"
"buffer_load_dwordx4 a[36:39], v251, s[56:59], s72 offen offset:0 sc1\n"
"v_mfma_f32_16x16x32_bf16 v[144:147], a[252:255], v[160:163], v[144:147]\n"
"ds_read_b64_tr_b16 v[174:175], v230 offset:5632\n"
"v_lshlrev_b32 v216, v217, v216\n"
"v_mfma_f32_16x16x32_bf16 v[144:147], a[248:251], v[200:203], v[144:147]\n"
"s_waitcnt lgkmcnt(4)\n"
"v_mfma_f32_16x16x32_bf16 v[148:151], a[248:251], v[164:167], v[148:151]\n"
"s_waitcnt lgkmcnt(2)\n"
"ds_read_b64_tr_b16 v[200:201], v230 offset:10752\n"
"v_cndmask_b32 v216, 0, v216, vcc\n"
"v_mfma_f32_16x16x32_bf16 v[148:151], a[252:255], v[168:171], v[148:151]\n"
"buffer_load_dwordx4 a[44:47], v251, s[56:59], s72 offen offset:1024 sc1\n"
"v_mfma_f32_16x16x32_bf16 v[108:111], a[244:247], v[168:171], v[108:111]\n"
"ds_read_b64_tr_b16 v[202:203], v230 offset:15872\n"
"v_and_b32 v217, 2, v237\n"
"v_mfma_f32_16x16x32_bf16 v[108:111], a[240:243], v[164:167], v[108:111]\n"
"v_mfma_f32_16x16x32_bf16 v[68:71], a[232:235], v[164:167], v[68:71]\n"
"ds_read_b64_tr_b16 v[160:161], v231 offset:512\n"
"v_cmp_eq_u32 vcc, 0, v217\n"
"v_mfma_f32_16x16x32_bf16 v[68:71], a[236:239], v[168:171], v[68:71]\n"
"buffer_load_dwordx4 a[84:87], v251, s[56:59], s72 offen offset:2048 sc1\n"
"v_mfma_f32_16x16x32_bf16 v[28:31], a[228:231], v[168:171], v[28:31]\n"
"ds_read_b64_tr_b16 v[162:163], v231 offset:5632\n"
"v_mov_b32 v212, s12\n"
"v_mfma_f32_16x16x32_bf16 v[28:31], a[224:227], v[164:167], v[28:31]\n"
"s_waitcnt lgkmcnt(4)\n"
"v_mfma_f32_16x16x32_bf16 v[16:19], a[224:227], v[172:175], v[16:19]\n"
"s_waitcnt lgkmcnt(2)\n"
"ds_read_b64_tr_b16 v[164:165], v231 offset:10752\n"
"v_cndmask_b32 v218, 0, v216, vcc\n"
"v_mfma_f32_16x16x32_bf16 v[16:19], a[228:231], v[200:203], v[16:19]\n"
"v_mfma_f32_16x16x32_bf16 v[56:59], a[236:239], v[200:203], v[56:59]\n"
"ds_read_b64_tr_b16 v[166:167], v231 offset:15872\n"
"v_cndmask_b32 v219, v216, 0, vcc\n"
"v_mfma_f32_16x16x32_bf16 v[56:59], a[232:235], v[172:175], v[56:59]\n"
"buffer_load_dwordx4 a[92:95], v251, s[56:59], s72 offen offset:3072 sc1\n"
"v_mfma_f32_16x16x32_bf16 v[96:99], a[240:243], v[172:175], v[96:99]\n"
"ds_read_b64_tr_b16 v[168:169], v230 offset:21504\n"
"v_mov_b64 v[216:217], 0\n"
"v_mfma_f32_16x16x32_bf16 v[96:99], a[244:247], v[200:203], v[96:99]\n"
"v_mfma_f32_16x16x32_bf16 v[136:139], a[252:255], v[200:203], v[136:139]\n"
"ds_read_b64_tr_b16 v[170:171], v230 offset:26624\n"
"v_mov_b64 v[220:221], 0\n"
"v_mfma_f32_16x16x32_bf16 v[136:139], a[248:251], v[172:175], v[136:139]\n"
"s_waitcnt lgkmcnt(4)\n"
"v_mfma_f32_16x16x32_bf16 v[140:143], a[248:251], v[160:163], v[140:143]\n"
"s_waitcnt lgkmcnt(2)\n"
"ds_read_b64_tr_b16 v[172:173], v230 offset:31744\n"
"v_mul_u32_u24 v237, 2560, v237\n"
"v_mfma_f32_16x16x32_bf16 v[140:143], a[252:255], v[164:167], v[140:143]\n"
"buffer_load_dwordx4 a[132:135], v253, s[56:59], s72 offen offset:0 sc1\n"
"v_mfma_f32_16x16x32_bf16 v[100:103], a[244:247], v[164:167], v[100:103]\n"
"ds_read_b64_tr_b16 v[174:175], v230 offset:36864\n"
"v_and_b32 v236, 1, v238\n"
"v_mfma_f32_16x16x32_bf16 v[100:103], a[240:243], v[160:163], v[100:103]\n"
"s_waitcnt vmcnt(18)\n"
"v_mfma_f32_16x16x32_bf16 v[60:63], a[232:235], v[160:163], v[60:63]\n"
"ds_read_b64_tr_b16 v[200:201], v231 offset:21504\n"
"v_lshl_add_u32 v237, v236, 5, v237\n"
"v_mfma_f32_16x16x32_bf16 v[60:63], a[236:239], v[164:167], v[60:63]\n"
"buffer_load_dwordx4 a[140:143], v253, s[56:59], s72 offen offset:1024 sc1\n"
"v_mfma_f32_16x16x32_bf16 v[20:23], a[228:231], v[164:167], v[20:23]\n"
"ds_read_b64_tr_b16 v[202:203], v231 offset:26624\n"
"v_lshrrev_b32 v238, 1, v238\n"
"v_mfma_f32_16x16x32_bf16 v[20:23], a[224:227], v[160:163], v[20:23]\n"
"s_waitcnt lgkmcnt(4)\n"
"v_mfma_f32_16x16x32_bf16 v[32:35], a[4:7], v[168:171], v[32:35]\n"
"s_waitcnt lgkmcnt(2)\n"
"ds_read_b64_tr_b16 v[160:161], v231 offset:31744\n"
"v_lshl_add_u32 v236, v238, 4, v237\n"
"v_mfma_f32_16x16x32_bf16 v[32:35], a[12:15], v[172:175], v[32:35]\n"
"buffer_load_dwordx4 a[180:183], v253, s[56:59], s72 offen offset:2048 sc1\n"
"v_mfma_f32_16x16x32_bf16 v[72:75], a[60:63], v[172:175], v[72:75]\n"
"ds_read_b64_tr_b16 v[162:163], v231 offset:36864\n"
"v_mfma_f32_16x16x32_bf16 v[72:75], a[52:55], v[168:171], v[72:75]\n"
"v_mfma_f32_16x16x32_bf16 v[112:115], a[100:103], v[168:171], v[112:115]\n"
"ds_read_b64_tr_b16 v[164:165], v230 offset:21248\n"
"v_mfma_f32_16x16x32_bf16 v[112:115], a[108:111], v[172:175], v[112:115]\n"
"buffer_load_dwordx4 a[188:191], v253, s[56:59], s72 offen offset:3072 sc1\n"
"v_mfma_f32_16x16x32_bf16 v[152:155], a[156:159], v[172:175], v[152:155]\n"
"s_add_u32 s2, s73, s63\n"
"ds_read_b64_tr_b16 v[166:167], v230 offset:26368\n"
"v_mfma_f32_16x16x32_bf16 v[152:155], a[148:151], v[168:171], v[152:155]\n"
"s_waitcnt lgkmcnt(4)\n"
"v_mfma_f32_16x16x32_bf16 v[156:159], a[148:151], v[200:203], v[156:159]\n"
"s_waitcnt lgkmcnt(2)\n"
"ds_read_b64_tr_b16 v[168:169], v230 offset:31488\n"
"v_mfma_f32_16x16x32_bf16 v[156:159], a[156:159], v[160:163], v[156:159]\n"
"s_add_u32 s3, s73, s64\n"
"v_mfma_f32_16x16x32_bf16 v[116:119], a[108:111], v[160:163], v[116:119]\n"
"s_add_u32 s72, s54, 1048576\n"
"ds_read_b64_tr_b16 v[170:171], v230 offset:36608\n"
"v_mfma_f32_16x16x32_bf16 v[116:119], a[100:103], v[200:203], v[116:119]\n"
"buffer_load_dwordx4 a[0:3], v251, s[56:59], s72 offen offset:0 sc1\n"
"v_mfma_f32_16x16x32_bf16 v[76:79], a[52:55], v[200:203], v[76:79]\n"
"s_add_u32 s4, s73, s65\n"
"ds_read_b64_tr_b16 v[172:173], v231 offset:21248\n"
"v_mfma_f32_16x16x32_bf16 v[76:79], a[60:63], v[160:163], v[76:79]\n"
"s_add_u32 s67, s67, s75\n"
"v_mfma_f32_16x16x32_bf16 v[36:39], a[12:15], v[160:163], v[36:39]\n"
"s_add_u32 s68, s68, s75\n"
"ds_read_b64_tr_b16 v[174:175], v231 offset:26368\n"
"v_mfma_f32_16x16x32_bf16 v[36:39], a[4:7], v[200:203], v[36:39]\n"
"s_waitcnt lgkmcnt(4)\n"
"v_mfma_f32_16x16x32_bf16 v[24:27], a[4:7], v[164:167], v[24:27]\n"
"s_waitcnt lgkmcnt(2)\n"
"ds_read_b64_tr_b16 v[200:201], v231 offset:31488\n"
"v_mfma_f32_16x16x32_bf16 v[24:27], a[12:15], v[168:171], v[24:27]\n"
"buffer_load_dwordx4 a[8:11], v251, s[56:59], s72 offen offset:1024 sc1\n"
"v_mfma_f32_16x16x32_bf16 v[64:67], a[60:63], v[168:171], v[64:67]\n"
"s_add_u32 s69, s69, s75\n"
"ds_read_b64_tr_b16 v[202:203], v231 offset:36608\n"
"v_mfma_f32_16x16x32_bf16 v[64:67], a[52:55], v[164:167], v[64:67]\n"
"s_waitcnt vmcnt(10)\n"
"s_barrier\n"
"v_mfma_f32_16x16x32_bf16 v[104:107], a[100:103], v[164:167], v[104:107]\n"
"s_mul_i32 s66, s61, 4294926400\n"
"ds_read_b64_tr_b16 v[160:161], v235 offset:1024\n"
"v_mfma_f32_16x16x32_bf16 v[104:107], a[108:111], v[168:171], v[104:107]\n"
"buffer_load_dwordx4 a[48:51], v251, s[56:59], s72 offen offset:2048 sc1\n"
"v_mfma_f32_16x16x32_bf16 v[144:147], a[156:159], v[168:171], v[144:147]\n"
"s_add_u32 s66, s66, s60\n"
"ds_read_b64_tr_b16 v[162:163], v235 offset:6144\n"
"v_mfma_f32_16x16x32_bf16 v[144:147], a[148:151], v[164:167], v[144:147]\n"
"s_waitcnt lgkmcnt(4)\n"
"v_mfma_f32_16x16x32_bf16 v[148:151], a[148:151], v[172:175], v[148:151]\n"
"s_waitcnt lgkmcnt(2)\n"
"ds_read_b64_tr_b16 v[164:165], v235 offset:11264\n"
"v_mfma_f32_16x16x32_bf16 v[148:151], a[156:159], v[200:203], v[148:151]\n"
"buffer_load_dwordx4 a[56:59], v251, s[56:59], s72 offen offset:3072 sc1\n"
"v_mfma_f32_16x16x32_bf16 v[108:111], a[108:111], v[200:203], v[108:111]\n"
"s_lshl3_add_u32 s8, s62, s66\n"
"ds_read_b64_tr_b16 v[166:167], v235 offset:16384\n"
"v_mfma_f32_16x16x32_bf16 v[108:111], a[100:103], v[172:175], v[108:111]\n"
"v_mfma_f32_16x16x32_bf16 v[68:71], a[52:55], v[172:175], v[68:71]\n"
"s_lshl3_add_u32 s9, s63, s66\n"
"ds_read_b64_tr_b16 v[168:169], v249 offset:1024\n"
"v_mfma_f32_16x16x32_bf16 v[68:71], a[60:63], v[200:203], v[68:71]\n"
"buffer_load_dwordx4 a[96:99], v253, s[56:59], s72 offen offset:0 sc1\n"
"v_mfma_f32_16x16x32_bf16 v[28:31], a[12:15], v[200:203], v[28:31]\n"
"s_lshl3_add_u32 s10, s64, s66\n"
"ds_read_b64_tr_b16 v[170:171], v249 offset:6144\n"
"v_mfma_f32_16x16x32_bf16 v[28:31], a[4:7], v[172:175], v[28:31]\n"
"s_waitcnt lgkmcnt(4)\n"
"v_mfma_f32_16x16x32_bf16 v[32:35], a[20:23], v[160:163], v[32:35]\n"
"s_waitcnt lgkmcnt(2)\n"
"ds_read_b64_tr_b16 v[172:173], v249 offset:11264\n"
"v_mfma_f32_16x16x32_bf16 v[32:35], a[28:31], v[164:167], v[32:35]\n"
"s_lshl3_add_u32 s11, s65, s66\n"
"v_mfma_f32_16x16x32_bf16 v[72:75], a[76:79], v[164:167], v[72:75]\n"
"ds_read_b64_tr_b16 v[174:175], v249 offset:16384\n"
"v_mfma_f32_16x16x32_bf16 v[72:75], a[68:71], v[160:163], v[72:75]\n"
"buffer_load_dwordx4 a[104:107], v253, s[56:59], s72 offen offset:1024 sc1\n"
"v_mfma_f32_16x16x32_bf16 v[112:115], a[116:119], v[160:163], v[112:115]\n"
"ds_read_b64_tr_b16 v[200:201], v235 offset:768\n"
"v_mfma_f32_16x16x32_bf16 v[112:115], a[124:127], v[164:167], v[112:115]\n"
"v_mfma_f32_16x16x32_bf16 v[152:155], a[172:175], v[164:167], v[152:155]\n"
"ds_read_b64_tr_b16 v[202:203], v235 offset:5888\n"
"v_mfma_f32_16x16x32_bf16 v[152:155], a[164:167], v[160:163], v[152:155]\n"
"s_waitcnt lgkmcnt(4)\n"
"v_mfma_f32_16x16x32_bf16 v[156:159], a[164:167], v[168:171], v[156:159]\n"
"s_waitcnt lgkmcnt(2)\n"
"ds_read_b64_tr_b16 v[160:161], v235 offset:11008\n"
"v_mfma_f32_16x16x32_bf16 v[156:159], a[172:175], v[172:175], v[156:159]\n"
"buffer_load_dwordx4 a[144:147], v253, s[56:59], s72 offen offset:2048 sc1\n"
"v_mfma_f32_16x16x32_bf16 v[116:119], a[124:127], v[172:175], v[116:119]\n"
"ds_read_b64_tr_b16 v[162:163], v235 offset:16128\n"
"v_mfma_f32_16x16x32_bf16 v[116:119], a[116:119], v[168:171], v[116:119]\n"
"v_mfma_f32_16x16x32_bf16 v[76:79], a[68:71], v[168:171], v[76:79]\n"
"ds_read_b64_tr_b16 v[164:165], v249 offset:768\n"
"v_mfma_f32_16x16x32_bf16 v[76:79], a[76:79], v[172:175], v[76:79]\n"
"buffer_load_dwordx4 a[152:155], v253, s[56:59], s72 offen offset:3072 sc1\n"
"v_mfma_f32_16x16x32_bf16 v[36:39], a[28:31], v[172:175], v[36:39]\n"
"ds_read_b64_tr_b16 v[166:167], v249 offset:5888\n"
"v_mfma_f32_16x16x32_bf16 v[36:39], a[20:23], v[168:171], v[36:39]\n"
"s_waitcnt lgkmcnt(4)\n"
"v_mfma_f32_16x16x32_bf16 v[24:27], a[20:23], v[200:203], v[24:27]\n"
"s_waitcnt lgkmcnt(2)\n"
"s_add_u32 s72, s55, 524288\n"
"ds_read_b64_tr_b16 v[168:169], v249 offset:11008\n"
"v_mfma_f32_16x16x32_bf16 v[24:27], a[28:31], v[160:163], v[24:27]\n"
"buffer_load_dwordx4 a[16:19], v251, s[56:59], s72 offen offset:0 sc1\n"
"v_mfma_f32_16x16x32_bf16 v[64:67], a[76:79], v[160:163], v[64:67]\n"
"ds_read_b64_tr_b16 v[170:171], v249 offset:16128\n"
"v_mfma_f32_16x16x32_bf16 v[64:67], a[68:71], v[200:203], v[64:67]\n"
"v_mfma_f32_16x16x32_bf16 v[104:107], a[116:119], v[200:203], v[104:107]\n"
"ds_read_b64_tr_b16 v[172:173], v230 offset:256\n"
"v_mfma_f32_16x16x32_bf16 v[104:107], a[124:127], v[160:163], v[104:107]\n"
"buffer_load_dwordx4 a[24:27], v251, s[56:59], s72 offen offset:1024 sc1\n"
"v_mfma_f32_16x16x32_bf16 v[144:147], a[172:175], v[160:163], v[144:147]\n"
"ds_read_b64_tr_b16 v[174:175], v230 offset:5376\n"
"v_mfma_f32_16x16x32_bf16 v[144:147], a[164:167], v[200:203], v[144:147]\n"
"s_waitcnt lgkmcnt(4)\n"
"v_mfma_f32_16x16x32_bf16 v[148:151], a[164:167], v[164:167], v[148:151]\n"
"s_waitcnt lgkmcnt(2)\n"
"ds_read_b64_tr_b16 v[200:201], v230 offset:10496\n"
"v_mfma_f32_16x16x32_bf16 v[148:151], a[172:175], v[168:171], v[148:151]\n"
"v_mfma_f32_16x16x32_bf16 v[108:111], a[124:127], v[168:171], v[108:111]\n"
"ds_read_b64_tr_b16 v[202:203], v230 offset:15616\n"
"v_mfma_f32_16x16x32_bf16 v[108:111], a[116:119], v[164:167], v[108:111]\n"
"buffer_load_dwordx4 a[64:67], v251, s[56:59], s72 offen offset:2048 sc1\n"
"v_mfma_f32_16x16x32_bf16 v[68:71], a[68:71], v[164:167], v[68:71]\n"
"ds_read_b64_tr_b16 v[160:161], v231 offset:256\n"
"v_mfma_f32_16x16x32_bf16 v[68:71], a[76:79], v[168:171], v[68:71]\n"
"v_mfma_f32_16x16x32_bf16 v[28:31], a[28:31], v[168:171], v[28:31]\n"
"ds_read_b64_tr_b16 v[162:163], v231 offset:5376\n"
"v_mfma_f32_16x16x32_bf16 v[28:31], a[20:23], v[164:167], v[28:31]\n"
"s_waitcnt lgkmcnt(4)\n"
"v_mfma_f32_16x16x32_bf16 v[8:11], a[224:227], v[172:175], v[8:11]\n"
"s_waitcnt lgkmcnt(2)\n"
"ds_read_b64_tr_b16 v[164:165], v231 offset:10496\n"
"v_mfma_f32_16x16x32_bf16 v[8:11], a[228:231], v[200:203], v[8:11]\n"
"buffer_load_dwordx4 a[72:75], v251, s[56:59], s72 offen offset:3072 sc1\n"
"v_mfma_f32_16x16x32_bf16 v[48:51], a[236:239], v[200:203], v[48:51]\n"
"ds_read_b64_tr_b16 v[166:167], v231 offset:15616\n"
"v_mfma_f32_16x16x32_bf16 v[48:51], a[232:235], v[172:175], v[48:51]\n"
"v_mfma_f32_16x16x32_bf16 v[88:91], a[240:243], v[172:175], v[88:91]\n"
"ds_read_b64_tr_b16 v[168:169], v235 offset:21504\n"
"v_mfma_f32_16x16x32_bf16 v[88:91], a[244:247], v[200:203], v[88:91]\n"
"buffer_load_dwordx4 a[112:115], v253, s[56:59], s72 offen offset:0 sc1\n"
"v_mfma_f32_16x16x32_bf16 v[128:131], a[252:255], v[200:203], v[128:131]\n"
"ds_read_b64_tr_b16 v[170:171], v235 offset:26624\n"
"v_mfma_f32_16x16x32_bf16 v[128:131], a[248:251], v[172:175], v[128:131]\n"
"s_waitcnt lgkmcnt(4)\n"
"v_mfma_f32_16x16x32_bf16 v[132:135], a[248:251], v[160:163], v[132:135]\n"
"s_waitcnt lgkmcnt(2)\n"
"ds_read_b64_tr_b16 v[172:173], v235 offset:31744\n"
"v_mfma_f32_16x16x32_bf16 v[132:135], a[252:255], v[164:167], v[132:135]\n"
"buffer_load_dwordx4 a[120:123], v253, s[56:59], s72 offen offset:1024 sc1\n"
"v_mfma_f32_16x16x32_bf16 v[92:95], a[244:247], v[164:167], v[92:95]\n"
"ds_read_b64_tr_b16 v[174:175], v235 offset:36864\n"
"v_mfma_f32_16x16x32_bf16 v[92:95], a[240:243], v[160:163], v[92:95]\n"
"s_waitcnt vmcnt(14)\n"
"v_mfma_f32_16x16x32_bf16 v[52:55], a[232:235], v[160:163], v[52:55]\n"
"ds_read_b64_tr_b16 v[200:201], v249 offset:21504\n"
"v_mfma_f32_16x16x32_bf16 v[52:55], a[236:239], v[164:167], v[52:55]\n"
"buffer_load_dwordx4 a[160:163], v253, s[56:59], s72 offen offset:2048 sc1\n"
"v_mfma_f32_16x16x32_bf16 v[12:15], a[228:231], v[164:167], v[12:15]\n"
"ds_read_b64_tr_b16 v[202:203], v249 offset:26624\n"
"v_mfma_f32_16x16x32_bf16 v[12:15], a[224:227], v[160:163], v[12:15]\n"
"s_waitcnt lgkmcnt(4)\n"
"v_mfma_f32_16x16x32_bf16 v[32:35], a[36:39], v[168:171], v[32:35]\n"
"s_waitcnt lgkmcnt(2)\n"
"ds_read_b64_tr_b16 v[160:161], v249 offset:31744\n"
"v_mfma_f32_16x16x32_bf16 v[32:35], a[44:47], v[172:175], v[32:35]\n"
"v_mfma_f32_16x16x32_bf16 v[72:75], a[92:95], v[172:175], v[72:75]\n"
"ds_read_b64_tr_b16 v[162:163], v249 offset:36864\n"
"v_mfma_f32_16x16x32_bf16 v[72:75], a[84:87], v[168:171], v[72:75]\n"
"buffer_load_dwordx4 a[168:171], v253, s[56:59], s72 offen offset:3072 sc1\n"
"v_mfma_f32_16x16x32_bf16 v[112:115], a[132:135], v[168:171], v[112:115]\n"
"ds_read_b64_tr_b16 v[164:165], v235 offset:21248\n"
"v_mfma_f32_16x16x32_bf16 v[112:115], a[140:143], v[172:175], v[112:115]\n"
"v_mfma_f32_16x16x32_bf16 v[152:155], a[188:191], v[172:175], v[152:155]\n"
"ds_read_b64_tr_b16 v[166:167], v235 offset:26368\n"
"v_mfma_f32_16x16x32_bf16 v[152:155], a[180:183], v[168:171], v[152:155]\n"
"s_waitcnt lgkmcnt(4)\n"
"v_mfma_f32_16x16x32_bf16 v[156:159], a[180:183], v[200:203], v[156:159]\n"
"s_waitcnt lgkmcnt(2)\n"
"s_add_u32 s72, s55, 1048576\n"
"ds_read_b64_tr_b16 v[168:169], v235 offset:31488\n"
"v_mfma_f32_16x16x32_bf16 v[156:159], a[188:191], v[160:163], v[156:159]\n"
"buffer_load_dwordx4 a[32:35], v251, s[56:59], s72 offen offset:0 sc1\n"
"v_mfma_f32_16x16x32_bf16 v[116:119], a[140:143], v[160:163], v[116:119]\n"
"ds_read_b64_tr_b16 v[170:171], v235 offset:36608\n"
"v_mfma_f32_16x16x32_bf16 v[116:119], a[132:135], v[200:203], v[116:119]\n"
"v_mfma_f32_16x16x32_bf16 v[76:79], a[84:87], v[200:203], v[76:79]\n"
"ds_read_b64_tr_b16 v[172:173], v249 offset:21248\n"
"v_mfma_f32_16x16x32_bf16 v[76:79], a[92:95], v[160:163], v[76:79]\n"
"buffer_load_dwordx4 a[40:43], v251, s[56:59], s72 offen offset:1024 sc1\n"
"v_mfma_f32_16x16x32_bf16 v[36:39], a[44:47], v[160:163], v[36:39]\n"
"ds_read_b64_tr_b16 v[174:175], v249 offset:26368\n"
"v_mfma_f32_16x16x32_bf16 v[36:39], a[36:39], v[200:203], v[36:39]\n"
"s_waitcnt lgkmcnt(4)\n"
"v_mfma_f32_16x16x32_bf16 v[24:27], a[36:39], v[164:167], v[24:27]\n"
"s_waitcnt lgkmcnt(2)\n"
"ds_read_b64_tr_b16 v[200:201], v249 offset:31488\n"
"v_mfma_f32_16x16x32_bf16 v[24:27], a[44:47], v[168:171], v[24:27]\n"
"buffer_load_dwordx4 a[80:83], v251, s[56:59], s72 offen offset:2048 sc1\n"
"v_mfma_f32_16x16x32_bf16 v[64:67], a[92:95], v[168:171], v[64:67]\n"
"ds_read_b64_tr_b16 v[202:203], v249 offset:36608\n"
"v_mfma_f32_16x16x32_bf16 v[64:67], a[84:87], v[164:167], v[64:67]\n"
"v_mfma_f32_16x16x32_bf16 v[104:107], a[132:135], v[164:167], v[104:107]\n"
"ds_read_b64_tr_b16 v[160:161], v235 offset:41984\n"
"v_mfma_f32_16x16x32_bf16 v[104:107], a[140:143], v[168:171], v[104:107]\n"
"buffer_load_dwordx4 a[88:91], v251, s[56:59], s72 offen offset:3072 sc1\n"
"v_mfma_f32_16x16x32_bf16 v[144:147], a[188:191], v[168:171], v[144:147]\n"
"ds_read_b64_tr_b16 v[162:163], v235 offset:47104\n"
"v_mfma_f32_16x16x32_bf16 v[144:147], a[180:183], v[164:167], v[144:147]\n"
"s_waitcnt lgkmcnt(4)\n"
"v_mfma_f32_16x16x32_bf16 v[148:151], a[180:183], v[172:175], v[148:151]\n"
"s_waitcnt lgkmcnt(2)\n"
"ds_read_b64_tr_b16 v[164:165], v235 offset:52224\n"
"v_mfma_f32_16x16x32_bf16 v[148:151], a[188:191], v[200:203], v[148:151]\n"
"v_mfma_f32_16x16x32_bf16 v[108:111], a[140:143], v[200:203], v[108:111]\n"
"ds_read_b64_tr_b16 v[166:167], v235 offset:57344\n"
"v_mfma_f32_16x16x32_bf16 v[108:111], a[132:135], v[172:175], v[108:111]\n"
"s_waitcnt vmcnt(12)\n"
"v_mfma_f32_16x16x32_bf16 v[68:71], a[84:87], v[172:175], v[68:71]\n"
"ds_read_b64_tr_b16 v[168:169], v249 offset:41984\n"
"v_mfma_f32_16x16x32_bf16 v[68:71], a[92:95], v[200:203], v[68:71]\n"
"buffer_load_dwordx4 a[128:131], v253, s[56:59], s72 offen offset:0 sc1\n"
"v_mfma_f32_16x16x32_bf16 v[28:31], a[44:47], v[200:203], v[28:31]\n"
"ds_read_b64_tr_b16 v[170:171], v249 offset:47104\n"
"v_mfma_f32_16x16x32_bf16 v[28:31], a[36:39], v[172:175], v[28:31]\n"
"s_waitcnt lgkmcnt(4)\n"
"v_mfma_f32_16x16x32_bf16 v[32:35], a[0:3], v[160:163], v[32:35]\n"
"s_waitcnt lgkmcnt(2)\n"
"ds_read_b64_tr_b16 v[172:173], v249 offset:52224\n"
"v_mfma_f32_16x16x32_bf16 v[32:35], a[8:11], v[164:167], v[32:35]\n"
"buffer_load_dwordx4 a[136:139], v253, s[56:59], s72 offen offset:1024 sc1\n"
"v_mfma_f32_16x16x32_bf16 v[72:75], a[56:59], v[164:167], v[72:75]\n"
"ds_read_b64_tr_b16 v[174:175], v249 offset:57344\n"
"v_mfma_f32_16x16x32_bf16 v[72:75], a[48:51], v[160:163], v[72:75]\n"
"v_mfma_f32_16x16x32_bf16 v[112:115], a[96:99], v[160:163], v[112:115]\n"
"ds_read_b64_tr_b16 v[200:201], v235 offset:41728\n"
"v_mfma_f32_16x16x32_bf16 v[112:115], a[104:107], v[164:167], v[112:115]\n"
"buffer_load_dwordx4 a[176:179], v253, s[56:59], s72 offen offset:2048 sc1\n"
"v_mfma_f32_16x16x32_bf16 v[152:155], a[152:155], v[164:167], v[152:155]\n"
"ds_read_b64_tr_b16 v[202:203], v235 offset:46848\n"
"v_mfma_f32_16x16x32_bf16 v[152:155], a[144:147], v[160:163], v[152:155]\n"
"s_waitcnt lgkmcnt(4)\n"
"v_mfma_f32_16x16x32_bf16 v[156:159], a[144:147], v[168:171], v[156:159]\n"
"s_waitcnt lgkmcnt(2)\n"
"ds_read_b64_tr_b16 v[160:161], v235 offset:51968\n"
"v_mfma_f32_16x16x32_bf16 v[156:159], a[152:155], v[172:175], v[156:159]\n"
"buffer_load_dwordx4 a[184:187], v253, s[56:59], s72 offen offset:3072 sc1\n"
"v_mfma_f32_16x16x32_bf16 v[116:119], a[104:107], v[172:175], v[116:119]\n"
"ds_read_b64_tr_b16 v[162:163], v235 offset:57088\n"
"v_mfma_f32_16x16x32_bf16 v[116:119], a[96:99], v[168:171], v[116:119]\n"
"v_mfma_f32_16x16x32_bf16 v[76:79], a[48:51], v[168:171], v[76:79]\n"
"ds_read_b64_tr_b16 v[164:165], v249 offset:41728\n"
"v_mfma_f32_16x16x32_bf16 v[76:79], a[56:59], v[172:175], v[76:79]\n"
"v_mfma_f32_16x16x32_bf16 v[36:39], a[8:11], v[172:175], v[36:39]\n"
"ds_read_b64_tr_b16 v[166:167], v249 offset:46848\n"
"v_mfma_f32_16x16x32_bf16 v[36:39], a[0:3], v[168:171], v[36:39]\n"
"s_waitcnt lgkmcnt(4)\n"
"v_mfma_f32_16x16x32_bf16 v[24:27], a[0:3], v[200:203], v[24:27]\n"
"s_waitcnt lgkmcnt(2)\n"
"ds_read_b64_tr_b16 v[168:169], v249 offset:51968\n"
"v_mfma_f32_16x16x32_bf16 v[24:27], a[8:11], v[160:163], v[24:27]\n"
"v_mfma_f32_16x16x32_bf16 v[64:67], a[56:59], v[160:163], v[64:67]\n"
"ds_read_b64_tr_b16 v[170:171], v249 offset:57088\n"
"v_mfma_f32_16x16x32_bf16 v[64:67], a[48:51], v[200:203], v[64:67]\n"
"v_mfma_f32_16x16x32_bf16 v[104:107], a[96:99], v[200:203], v[104:107]\n"
"ds_read_b64_tr_b16 v[172:173], v228 offset:1024\n"
"v_mfma_f32_16x16x32_bf16 v[104:107], a[104:107], v[160:163], v[104:107]\n"
"v_mfma_f32_16x16x32_bf16 v[144:147], a[152:155], v[160:163], v[144:147]\n"
"ds_read_b64_tr_b16 v[174:175], v228 offset:6144\n"
"v_mfma_f32_16x16x32_bf16 v[144:147], a[144:147], v[200:203], v[144:147]\n"
"s_waitcnt lgkmcnt(4)\n"
"v_mfma_f32_16x16x32_bf16 v[148:151], a[144:147], v[164:167], v[148:151]\n"
"s_waitcnt lgkmcnt(2)\n"
"ds_read_b64_tr_b16 v[200:201], v228 offset:11264\n"
"v_mfma_f32_16x16x32_bf16 v[148:151], a[152:155], v[168:171], v[148:151]\n"
"v_mfma_f32_16x16x32_bf16 v[108:111], a[104:107], v[168:171], v[108:111]\n"
"ds_read_b64_tr_b16 v[202:203], v228 offset:16384\n"
"v_mfma_f32_16x16x32_bf16 v[108:111], a[96:99], v[164:167], v[108:111]\n"
"s_waitcnt vmcnt(8)\n"
"v_mfma_f32_16x16x32_bf16 v[68:71], a[48:51], v[164:167], v[68:71]\n"
"ds_read_b64_tr_b16 v[160:161], v229 offset:1024\n"
"v_mfma_f32_16x16x32_bf16 v[68:71], a[56:59], v[168:171], v[68:71]\n"
"v_mfma_f32_16x16x32_bf16 v[28:31], a[8:11], v[168:171], v[28:31]\n"
"ds_read_b64_tr_b16 v[162:163], v229 offset:6144\n"
"v_mfma_f32_16x16x32_bf16 v[28:31], a[0:3], v[164:167], v[28:31]\n"
"s_waitcnt lgkmcnt(4)\n"
"v_mfma_f32_16x16x32_bf16 v[32:35], a[16:19], v[172:175], v[32:35]\n"
"s_waitcnt lgkmcnt(2)\n"
"ds_read_b64_tr_b16 v[164:165], v229 offset:11264\n"
"v_mfma_f32_16x16x32_bf16 v[32:35], a[24:27], v[200:203], v[32:35]\n"
"v_mfma_f32_16x16x32_bf16 v[72:75], a[72:75], v[200:203], v[72:75]\n"
"ds_read_b64_tr_b16 v[166:167], v229 offset:16384\n"
"v_mfma_f32_16x16x32_bf16 v[72:75], a[64:67], v[172:175], v[72:75]\n"
"v_mfma_f32_16x16x32_bf16 v[112:115], a[112:115], v[172:175], v[112:115]\n"
"ds_read_b64_tr_b16 v[168:169], v228 offset:768\n"
"v_mfma_f32_16x16x32_bf16 v[112:115], a[120:123], v[200:203], v[112:115]\n"
"v_mfma_f32_16x16x32_bf16 v[152:155], a[168:171], v[200:203], v[152:155]\n"
"ds_read_b64_tr_b16 v[170:171], v228 offset:5888\n"
"v_mfma_f32_16x16x32_bf16 v[152:155], a[160:163], v[172:175], v[152:155]\n"
"s_waitcnt lgkmcnt(4)\n"
"v_mfma_f32_16x16x32_bf16 v[156:159], a[160:163], v[160:163], v[156:159]\n"
"s_waitcnt lgkmcnt(2)\n"
"ds_read_b64_tr_b16 v[172:173], v228 offset:11008\n"
"v_mfma_f32_16x16x32_bf16 v[156:159], a[168:171], v[164:167], v[156:159]\n"
"v_mfma_f32_16x16x32_bf16 v[116:119], a[120:123], v[164:167], v[116:119]\n"
"ds_read_b64_tr_b16 v[174:175], v228 offset:16128\n"
"v_mfma_f32_16x16x32_bf16 v[116:119], a[112:115], v[160:163], v[116:119]\n"
"v_mfma_f32_16x16x32_bf16 v[76:79], a[64:67], v[160:163], v[76:79]\n"
"ds_read_b64_tr_b16 v[200:201], v229 offset:768\n"
"v_mfma_f32_16x16x32_bf16 v[76:79], a[72:75], v[164:167], v[76:79]\n"
"v_mfma_f32_16x16x32_bf16 v[36:39], a[24:27], v[164:167], v[36:39]\n"
"ds_read_b64_tr_b16 v[202:203], v229 offset:5888\n"
"v_mfma_f32_16x16x32_bf16 v[36:39], a[16:19], v[160:163], v[36:39]\n"
"s_waitcnt lgkmcnt(4)\n"
"v_mfma_f32_16x16x32_bf16 v[24:27], a[16:19], v[168:171], v[24:27]\n"
"s_waitcnt lgkmcnt(2)\n"
"ds_read_b64_tr_b16 v[160:161], v229 offset:11008\n"
"v_mfma_f32_16x16x32_bf16 v[24:27], a[24:27], v[172:175], v[24:27]\n"
"v_mfma_f32_16x16x32_bf16 v[64:67], a[72:75], v[172:175], v[64:67]\n"
"ds_read_b64_tr_b16 v[162:163], v229 offset:16128\n"
"v_mfma_f32_16x16x32_bf16 v[64:67], a[64:67], v[168:171], v[64:67]\n"
"v_mfma_f32_16x16x32_bf16 v[104:107], a[112:115], v[168:171], v[104:107]\n"
"ds_read_b64_tr_b16 v[164:165], v228 offset:21504\n"
"v_mfma_f32_16x16x32_bf16 v[104:107], a[120:123], v[172:175], v[104:107]\n"
"v_mfma_f32_16x16x32_bf16 v[144:147], a[168:171], v[172:175], v[144:147]\n"
"ds_read_b64_tr_b16 v[166:167], v228 offset:26624\n"
"v_mfma_f32_16x16x32_bf16 v[144:147], a[160:163], v[168:171], v[144:147]\n"
"s_waitcnt lgkmcnt(4)\n"
"v_mfma_f32_16x16x32_bf16 v[148:151], a[160:163], v[200:203], v[148:151]\n"
"s_waitcnt lgkmcnt(2)\n"
"ds_read_b64_tr_b16 v[168:169], v228 offset:31744\n"
"v_mfma_f32_16x16x32_bf16 v[148:151], a[168:171], v[160:163], v[148:151]\n"
"v_mfma_f32_16x16x32_bf16 v[108:111], a[120:123], v[160:163], v[108:111]\n"
"ds_read_b64_tr_b16 v[170:171], v228 offset:36864\n"
"v_mfma_f32_16x16x32_bf16 v[108:111], a[112:115], v[200:203], v[108:111]\n"
"s_waitcnt vmcnt(0)\n"
"v_mfma_f32_16x16x32_bf16 v[68:71], a[64:67], v[200:203], v[68:71]\n"
"ds_read_b64_tr_b16 v[172:173], v229 offset:21504\n"
"v_mfma_f32_16x16x32_bf16 v[68:71], a[72:75], v[160:163], v[68:71]\n"
"v_mfma_f32_16x16x32_bf16 v[28:31], a[24:27], v[160:163], v[28:31]\n"
"ds_read_b64_tr_b16 v[174:175], v229 offset:26624\n"
"v_mfma_f32_16x16x32_bf16 v[28:31], a[16:19], v[200:203], v[28:31]\n"
"s_waitcnt lgkmcnt(4)\n"
"v_mfma_f32_16x16x32_bf16 v[32:35], a[32:35], v[164:167], v[32:35]\n"
"s_waitcnt lgkmcnt(2)\n"
"ds_read_b64_tr_b16 v[200:201], v229 offset:31744\n"
"v_mfma_f32_16x16x32_bf16 v[32:35], a[40:43], v[168:171], v[32:35]\n"
"v_mfma_f32_16x16x32_bf16 v[72:75], a[88:91], v[168:171], v[72:75]\n"
"ds_read_b64_tr_b16 v[202:203], v229 offset:36864\n"
"v_mfma_f32_16x16x32_bf16 v[72:75], a[80:83], v[164:167], v[72:75]\n"
"v_mfma_f32_16x16x32_bf16 v[112:115], a[128:131], v[164:167], v[112:115]\n"
"ds_read_b64_tr_b16 v[160:161], v228 offset:21248\n"
"v_mfma_f32_16x16x32_bf16 v[112:115], a[136:139], v[168:171], v[112:115]\n"
"v_mfma_f32_16x16x32_bf16 v[152:155], a[184:187], v[168:171], v[152:155]\n"
"ds_read_b64_tr_b16 v[162:163], v228 offset:26368\n"
"v_mfma_f32_16x16x32_bf16 v[152:155], a[176:179], v[164:167], v[152:155]\n"
"s_waitcnt lgkmcnt(4)\n"
"v_mfma_f32_16x16x32_bf16 v[156:159], a[176:179], v[172:175], v[156:159]\n"
"s_waitcnt lgkmcnt(2)\n"
"ds_read_b64_tr_b16 v[164:165], v228 offset:31488\n"
"v_mfma_f32_16x16x32_bf16 v[156:159], a[184:187], v[200:203], v[156:159]\n"
"v_mfma_f32_16x16x32_bf16 v[116:119], a[136:139], v[200:203], v[116:119]\n"
"ds_read_b64_tr_b16 v[166:167], v228 offset:36608\n"
"v_mfma_f32_16x16x32_bf16 v[116:119], a[128:131], v[172:175], v[116:119]\n"
"v_mfma_f32_16x16x32_bf16 v[76:79], a[80:83], v[172:175], v[76:79]\n"
"ds_read_b64_tr_b16 v[168:169], v229 offset:21248\n"
"v_mfma_f32_16x16x32_bf16 v[76:79], a[88:91], v[200:203], v[76:79]\n"
"v_mfma_f32_16x16x32_bf16 v[36:39], a[40:43], v[200:203], v[36:39]\n"
"ds_read_b64_tr_b16 v[170:171], v229 offset:26368\n"
"v_cvt_pk_bf16_f32 v152, v152, v153\n"
"v_mfma_f32_16x16x32_bf16 v[36:39], a[32:35], v[172:175], v[36:39]\n"
"s_waitcnt lgkmcnt(4)\n"
"v_cvt_pk_bf16_f32 v153, v154, v155\n"
"v_cvt_pk_bf16_f32 v154, v156, v157\n"
"v_mfma_f32_16x16x32_bf16 v[24:27], a[32:35], v[160:163], v[24:27]\n"
"s_waitcnt lgkmcnt(2)\n"
"ds_read_b64_tr_b16 v[172:173], v229 offset:31488\n"
"v_cvt_pk_bf16_f32 v155, v158, v159\n"
"v_mfma_f32_16x16x32_bf16 v[24:27], a[40:43], v[164:167], v[24:27]\n"
"v_mfma_f32_16x16x32_bf16 v[64:67], a[88:91], v[164:167], v[64:67]\n"
"ds_read_b64_tr_b16 v[174:175], v229 offset:36608\n"
"v_mfma_f32_16x16x32_bf16 v[64:67], a[80:83], v[160:163], v[64:67]\n"
"buffer_store_dwordx4 v[152:155], v253, s[40:43], s4 offen offset:0 sc1\n"
"v_mfma_f32_16x16x32_bf16 v[104:107], a[128:131], v[160:163], v[104:107]\n"
"ds_read_b64_tr_b16 v[200:201], v230 offset:20992\n"
"v_mfma_f32_16x16x32_bf16 v[104:107], a[136:139], v[164:167], v[104:107]\n"
"v_mfma_f32_16x16x32_bf16 v[144:147], a[184:187], v[164:167], v[144:147]\n"
"ds_read_b64_tr_b16 v[202:203], v230 offset:26112\n"
"v_mfma_f32_16x16x32_bf16 v[144:147], a[176:179], v[160:163], v[144:147]\n"
"s_waitcnt lgkmcnt(4)\n"
"v_mfma_f32_16x16x32_bf16 v[148:151], a[176:179], v[168:171], v[148:151]\n"
"s_waitcnt lgkmcnt(2)\n"
"ds_read_b64_tr_b16 v[160:161], v230 offset:31232\n"
"v_cvt_pk_bf16_f32 v24, v24, v25\n"
"v_mfma_f32_16x16x32_bf16 v[148:151], a[184:187], v[172:175], v[148:151]\n"
"v_cvt_pk_bf16_f32 v25, v26, v27\n"
"v_cvt_pk_bf16_f32 v26, v64, v65\n"
"v_mfma_f32_16x16x32_bf16 v[108:111], a[136:139], v[172:175], v[108:111]\n"
"ds_read_b64_tr_b16 v[162:163], v230 offset:36352\n"
"v_cvt_pk_bf16_f32 v27, v66, v67\n"
"v_mfma_f32_16x16x32_bf16 v[108:111], a[128:131], v[168:171], v[108:111]\n"
"v_mfma_f32_16x16x32_bf16 v[68:71], a[80:83], v[168:171], v[68:71]\n"
"ds_read_b64_tr_b16 v[164:165], v235 offset:512\n"
"v_cvt_pk_bf16_f32 v104, v104, v105\n"
"v_mfma_f32_16x16x32_bf16 v[68:71], a[88:91], v[172:175], v[68:71]\n"
"v_cvt_pk_bf16_f32 v105, v106, v107\n"
"v_cvt_pk_bf16_f32 v106, v144, v145\n"
"v_mfma_f32_16x16x32_bf16 v[28:31], a[40:43], v[172:175], v[28:31]\n"
"ds_read_b64_tr_b16 v[166:167], v235 offset:5632\n"
"v_cvt_pk_bf16_f32 v107, v146, v147\n"
"v_mfma_f32_16x16x32_bf16 v[28:31], a[32:35], v[168:171], v[28:31]\n"
"s_waitcnt lgkmcnt(4)\n"
"v_mfma_f32_16x16x32_bf16 v[16:19], a[4:7], v[200:203], v[16:19]\n"
"s_waitcnt lgkmcnt(2)\n"
"ds_read_b64_tr_b16 v[168:169], v235 offset:10752\n"
"v_cvt_pk_bf16_f32 v108, v108, v109\n"
"v_mfma_f32_16x16x32_bf16 v[16:19], a[12:15], v[160:163], v[16:19]\n"
"v_cvt_pk_bf16_f32 v109, v110, v111\n"
"v_cvt_pk_bf16_f32 v110, v148, v149\n"
"v_mfma_f32_16x16x32_bf16 v[56:59], a[60:63], v[160:163], v[56:59]\n"
"ds_read_b64_tr_b16 v[170:171], v235 offset:15872\n"
"v_cvt_pk_bf16_f32 v111, v150, v151\n"
"v_mfma_f32_16x16x32_bf16 v[56:59], a[52:55], v[200:203], v[56:59]\n"
"buffer_store_dwordx4 v[24:27], v251, s[40:43], s4 offen offset:0 sc1\n"
"v_mfma_f32_16x16x32_bf16 v[96:99], a[100:103], v[200:203], v[96:99]\n"
"ds_read_b64_tr_b16 v[172:173], v235 offset:20992\n"
"v_cvt_pk_bf16_f32 v28, v28, v29\n"
"v_mfma_f32_16x16x32_bf16 v[96:99], a[108:111], v[160:163], v[96:99]\n"
"v_cvt_pk_bf16_f32 v29, v30, v31\n"
"v_cvt_pk_bf16_f32 v30, v68, v69\n"
"v_mfma_f32_16x16x32_bf16 v[136:139], a[156:159], v[160:163], v[136:139]\n"
"ds_read_b64_tr_b16 v[174:175], v235 offset:26112\n"
"v_cvt_pk_bf16_f32 v31, v70, v71\n"
"v_mfma_f32_16x16x32_bf16 v[136:139], a[148:151], v[200:203], v[136:139]\n"
"s_waitcnt lgkmcnt(4)\n"
"v_cvt_pk_bf16_f32 v112, v112, v113\n"
"v_cvt_pk_bf16_f32 v113, v114, v115\n"
"v_mfma_f32_16x16x32_bf16 v[136:139], a[164:167], v[164:167], v[136:139]\n"
"s_waitcnt lgkmcnt(2)\n"
"ds_read_b64_tr_b16 v[200:201], v235 offset:31232\n"
"v_cvt_pk_bf16_f32 v114, v116, v117\n"
"v_mfma_f32_16x16x32_bf16 v[136:139], a[172:175], v[168:171], v[136:139]\n"
"v_cvt_pk_bf16_f32 v115, v118, v119\n"
"v_mfma_f32_16x16x32_bf16 v[96:99], a[124:127], v[168:171], v[96:99]\n"
"ds_read_b64_tr_b16 v[202:203], v235 offset:36352\n"
"v_mfma_f32_16x16x32_bf16 v[96:99], a[116:119], v[164:167], v[96:99]\n"
"v_mfma_f32_16x16x32_bf16 v[56:59], a[68:71], v[164:167], v[56:59]\n"
"ds_read_b64_tr_b16 v[160:161], v235 offset:41472\n"
"v_mfma_f32_16x16x32_bf16 v[56:59], a[76:79], v[168:171], v[56:59]\n"
"v_mfma_f32_16x16x32_bf16 v[16:19], a[28:31], v[168:171], v[16:19]\n"
"ds_read_b64_tr_b16 v[162:163], v235 offset:46592\n"
"v_mfma_f32_16x16x32_bf16 v[16:19], a[20:23], v[164:167], v[16:19]\n"
"s_waitcnt lgkmcnt(4)\n"
"v_mfma_f32_16x16x32_bf16 v[16:19], a[36:39], v[172:175], v[16:19]\n"
"s_waitcnt lgkmcnt(2)\n"
"ds_read_b64_tr_b16 v[164:165], v235 offset:51712\n"
"v_mfma_f32_16x16x32_bf16 v[16:19], a[44:47], v[200:203], v[16:19]\n"
"v_mfma_f32_16x16x32_bf16 v[56:59], a[92:95], v[200:203], v[56:59]\n"
"ds_read_b64_tr_b16 v[166:167], v235 offset:56832\n"
"v_mfma_f32_16x16x32_bf16 v[56:59], a[84:87], v[172:175], v[56:59]\n"
"buffer_store_dwordx4 v[104:107], v251, s[40:43], s4 offen offset:1024 sc1\n"
"v_mfma_f32_16x16x32_bf16 v[96:99], a[132:135], v[172:175], v[96:99]\n"
"ds_read_b64_tr_b16 v[168:169], v228 offset:512\n"
"v_mfma_f32_16x16x32_bf16 v[96:99], a[140:143], v[200:203], v[96:99]\n"
"v_mfma_f32_16x16x32_bf16 v[136:139], a[188:191], v[200:203], v[136:139]\n"
"ds_read_b64_tr_b16 v[170:171], v228 offset:5632\n"
"v_mfma_f32_16x16x32_bf16 v[136:139], a[180:183], v[172:175], v[136:139]\n"
"s_waitcnt lgkmcnt(4)\n"
"v_mfma_f32_16x16x32_bf16 v[136:139], a[144:147], v[160:163], v[136:139]\n"
"s_waitcnt lgkmcnt(2)\n"
"ds_read_b64_tr_b16 v[172:173], v228 offset:10752\n"
"v_mfma_f32_16x16x32_bf16 v[136:139], a[152:155], v[164:167], v[136:139]\n"
"v_mfma_f32_16x16x32_bf16 v[96:99], a[104:107], v[164:167], v[96:99]\n"
"ds_read_b64_tr_b16 v[174:175], v228 offset:15872\n"
"v_mfma_f32_16x16x32_bf16 v[96:99], a[96:99], v[160:163], v[96:99]\n"
"v_mfma_f32_16x16x32_bf16 v[56:59], a[48:51], v[160:163], v[56:59]\n"
"ds_read_b64_tr_b16 v[200:201], v228 offset:20992\n"
"v_mfma_f32_16x16x32_bf16 v[56:59], a[56:59], v[164:167], v[56:59]\n"
"v_mfma_f32_16x16x32_bf16 v[16:19], a[8:11], v[164:167], v[16:19]\n"
"ds_read_b64_tr_b16 v[202:203], v228 offset:26112\n"
"v_mfma_f32_16x16x32_bf16 v[16:19], a[0:3], v[160:163], v[16:19]\n"
"s_waitcnt lgkmcnt(4)\n"
"v_mfma_f32_16x16x32_bf16 v[16:19], a[16:19], v[168:171], v[16:19]\n"
"s_waitcnt lgkmcnt(2)\n"
"ds_read_b64_tr_b16 v[160:161], v228 offset:31232\n"
"v_mfma_f32_16x16x32_bf16 v[16:19], a[24:27], v[172:175], v[16:19]\n"
"v_mfma_f32_16x16x32_bf16 v[56:59], a[72:75], v[172:175], v[56:59]\n"
"ds_read_b64_tr_b16 v[162:163], v228 offset:36352\n"
"v_mfma_f32_16x16x32_bf16 v[56:59], a[64:67], v[168:171], v[56:59]\n"
"buffer_store_dwordx4 v[108:111], v251, s[40:43], s4 offen offset:3072 sc1\n"
"v_mfma_f32_16x16x32_bf16 v[96:99], a[112:115], v[168:171], v[96:99]\n"
"ds_read_b64_tr_b16 v[164:165], v231 offset:20992\n"
"v_mfma_f32_16x16x32_bf16 v[96:99], a[120:123], v[172:175], v[96:99]\n"
"v_mfma_f32_16x16x32_bf16 v[136:139], a[168:171], v[172:175], v[136:139]\n"
"ds_read_b64_tr_b16 v[166:167], v231 offset:26112\n"
"v_mfma_f32_16x16x32_bf16 v[136:139], a[160:163], v[168:171], v[136:139]\n"
"s_waitcnt lgkmcnt(4)\n"
"v_mfma_f32_16x16x32_bf16 v[136:139], a[176:179], v[200:203], v[136:139]\n"
"s_waitcnt lgkmcnt(2)\n"
"ds_read_b64_tr_b16 v[168:169], v231 offset:31232\n"
"v_mfma_f32_16x16x32_bf16 v[136:139], a[184:187], v[160:163], v[136:139]\n"
"v_mfma_f32_16x16x32_bf16 v[96:99], a[136:139], v[160:163], v[96:99]\n"
"ds_read_b64_tr_b16 v[170:171], v231 offset:36352\n"
"v_mfma_f32_16x16x32_bf16 v[96:99], a[128:131], v[200:203], v[96:99]\n"
"v_mfma_f32_16x16x32_bf16 v[56:59], a[80:83], v[200:203], v[56:59]\n"
"ds_read_b64_tr_b16 v[172:173], v249 offset:512\n"
"v_mfma_f32_16x16x32_bf16 v[56:59], a[88:91], v[160:163], v[56:59]\n"
"v_mfma_f32_16x16x32_bf16 v[16:19], a[40:43], v[160:163], v[16:19]\n"
"ds_read_b64_tr_b16 v[174:175], v249 offset:5632\n"
"v_mfma_f32_16x16x32_bf16 v[16:19], a[32:35], v[200:203], v[16:19]\n"
"s_waitcnt lgkmcnt(4)\n"
"v_mfma_f32_16x16x32_bf16 v[20:23], a[4:7], v[164:167], v[20:23]\n"
"s_waitcnt lgkmcnt(2)\n"
"ds_read_b64_tr_b16 v[200:201], v249 offset:10752\n"
"v_cvt_pk_bf16_f32 v96, v96, v97\n"
"v_mfma_f32_16x16x32_bf16 v[20:23], a[12:15], v[168:171], v[20:23]\n"
"v_cvt_pk_bf16_f32 v97, v98, v99\n"
"v_cvt_pk_bf16_f32 v98, v136, v137\n"
"v_mfma_f32_16x16x32_bf16 v[60:63], a[60:63], v[168:171], v[60:63]\n"
"ds_read_b64_tr_b16 v[202:203], v249 offset:15872\n"
"v_cvt_pk_bf16_f32 v99, v138, v139\n"
"v_mfma_f32_16x16x32_bf16 v[60:63], a[52:55], v[164:167], v[60:63]\n"
"buffer_store_dwordx4 v[28:31], v251, s[40:43], s4 offen offset:2048 sc1\n"
"v_mfma_f32_16x16x32_bf16 v[100:103], a[100:103], v[164:167], v[100:103]\n"
"ds_read_b64_tr_b16 v[160:161], v249 offset:20992\n"
"v_cvt_pk_bf16_f32 v16, v16, v17\n"
"v_mfma_f32_16x16x32_bf16 v[100:103], a[108:111], v[168:171], v[100:103]\n"
"v_cvt_pk_bf16_f32 v17, v18, v19\n"
"v_cvt_pk_bf16_f32 v18, v56, v57\n"
"v_mfma_f32_16x16x32_bf16 v[140:143], a[156:159], v[168:171], v[140:143]\n"
"ds_read_b64_tr_b16 v[162:163], v249 offset:26112\n"
"v_cvt_pk_bf16_f32 v19, v58, v59\n"
"v_mfma_f32_16x16x32_bf16 v[140:143], a[148:151], v[164:167], v[140:143]\n"
"s_waitcnt lgkmcnt(4)\n"
"v_mfma_f32_16x16x32_bf16 v[140:143], a[164:167], v[172:175], v[140:143]\n"
"s_waitcnt lgkmcnt(2)\n"
"ds_read_b64_tr_b16 v[164:165], v249 offset:31232\n"
"v_mfma_f32_16x16x32_bf16 v[140:143], a[172:175], v[200:203], v[140:143]\n"
"v_mfma_f32_16x16x32_bf16 v[100:103], a[124:127], v[200:203], v[100:103]\n"
"ds_read_b64_tr_b16 v[166:167], v249 offset:36352\n"
"v_mfma_f32_16x16x32_bf16 v[100:103], a[116:119], v[172:175], v[100:103]\n"
"v_mfma_f32_16x16x32_bf16 v[60:63], a[68:71], v[172:175], v[60:63]\n"
"ds_read_b64_tr_b16 v[168:169], v249 offset:41472\n"
"v_mfma_f32_16x16x32_bf16 v[60:63], a[76:79], v[200:203], v[60:63]\n"
"v_mfma_f32_16x16x32_bf16 v[20:23], a[28:31], v[200:203], v[20:23]\n"
"ds_read_b64_tr_b16 v[170:171], v249 offset:46592\n"
"v_mfma_f32_16x16x32_bf16 v[20:23], a[20:23], v[172:175], v[20:23]\n"
"s_waitcnt lgkmcnt(4)\n"
"v_mfma_f32_16x16x32_bf16 v[20:23], a[36:39], v[160:163], v[20:23]\n"
"s_waitcnt lgkmcnt(2)\n"
"ds_read_b64_tr_b16 v[172:173], v249 offset:51712\n"
"v_mfma_f32_16x16x32_bf16 v[20:23], a[44:47], v[164:167], v[20:23]\n"
"v_mfma_f32_16x16x32_bf16 v[60:63], a[92:95], v[164:167], v[60:63]\n"
"ds_read_b64_tr_b16 v[174:175], v249 offset:56832\n"
"v_mfma_f32_16x16x32_bf16 v[60:63], a[84:87], v[160:163], v[60:63]\n"
"buffer_store_dwordx4 v[112:115], v253, s[40:43], s3 offen offset:0 sc1\n"
"v_mfma_f32_16x16x32_bf16 v[100:103], a[132:135], v[160:163], v[100:103]\n"
"ds_read_b64_tr_b16 v[200:201], v229 offset:512\n"
"v_mfma_f32_16x16x32_bf16 v[100:103], a[140:143], v[164:167], v[100:103]\n"
"v_mfma_f32_16x16x32_bf16 v[140:143], a[188:191], v[164:167], v[140:143]\n"
"ds_read_b64_tr_b16 v[202:203], v229 offset:5632\n"
"v_mfma_f32_16x16x32_bf16 v[140:143], a[180:183], v[160:163], v[140:143]\n"
"s_waitcnt lgkmcnt(4)\n"
"v_mfma_f32_16x16x32_bf16 v[140:143], a[144:147], v[168:171], v[140:143]\n"
"s_waitcnt lgkmcnt(2)\n"
"ds_read_b64_tr_b16 v[160:161], v229 offset:10752\n"
"v_mfma_f32_16x16x32_bf16 v[140:143], a[152:155], v[172:175], v[140:143]\n"
"v_mfma_f32_16x16x32_bf16 v[100:103], a[104:107], v[172:175], v[100:103]\n"
"ds_read_b64_tr_b16 v[162:163], v229 offset:15872\n"
"v_mfma_f32_16x16x32_bf16 v[100:103], a[96:99], v[168:171], v[100:103]\n"
"v_mfma_f32_16x16x32_bf16 v[60:63], a[48:51], v[168:171], v[60:63]\n"
"ds_read_b64_tr_b16 v[164:165], v229 offset:20992\n"
"v_mfma_f32_16x16x32_bf16 v[60:63], a[56:59], v[172:175], v[60:63]\n"
"v_mfma_f32_16x16x32_bf16 v[20:23], a[8:11], v[172:175], v[20:23]\n"
"ds_read_b64_tr_b16 v[166:167], v229 offset:26112\n"
"v_mfma_f32_16x16x32_bf16 v[20:23], a[0:3], v[168:171], v[20:23]\n"
"s_waitcnt lgkmcnt(4)\n"
"v_mfma_f32_16x16x32_bf16 v[20:23], a[16:19], v[200:203], v[20:23]\n"
"s_waitcnt lgkmcnt(2)\n"
"ds_read_b64_tr_b16 v[168:169], v229 offset:31232\n"
"v_mfma_f32_16x16x32_bf16 v[20:23], a[24:27], v[160:163], v[20:23]\n"
"v_mfma_f32_16x16x32_bf16 v[60:63], a[72:75], v[160:163], v[60:63]\n"
"ds_read_b64_tr_b16 v[170:171], v229 offset:36352\n"
"v_mfma_f32_16x16x32_bf16 v[60:63], a[64:67], v[200:203], v[60:63]\n"
"buffer_store_dwordx4 v[96:99], v251, s[40:43], s3 offen offset:1024 sc1\n"
"v_mfma_f32_16x16x32_bf16 v[100:103], a[112:115], v[200:203], v[100:103]\n"
"ds_read_b64_tr_b16 v[172:173], v230 offset:20736\n"
"v_mfma_f32_16x16x32_bf16 v[100:103], a[120:123], v[160:163], v[100:103]\n"
"v_mfma_f32_16x16x32_bf16 v[140:143], a[168:171], v[160:163], v[140:143]\n"
"ds_read_b64_tr_b16 v[174:175], v230 offset:25856\n"
"v_mfma_f32_16x16x32_bf16 v[140:143], a[160:163], v[200:203], v[140:143]\n"
"s_waitcnt lgkmcnt(4)\n"
"v_mfma_f32_16x16x32_bf16 v[140:143], a[176:179], v[164:167], v[140:143]\n"
"s_waitcnt lgkmcnt(2)\n"
"ds_read_b64_tr_b16 v[200:201], v230 offset:30976\n"
"v_mfma_f32_16x16x32_bf16 v[140:143], a[184:187], v[168:171], v[140:143]\n"
"v_mfma_f32_16x16x32_bf16 v[100:103], a[136:139], v[168:171], v[100:103]\n"
"ds_read_b64_tr_b16 v[202:203], v230 offset:36096\n"
"v_mfma_f32_16x16x32_bf16 v[100:103], a[128:131], v[164:167], v[100:103]\n"
"v_mfma_f32_16x16x32_bf16 v[60:63], a[80:83], v[164:167], v[60:63]\n"
"ds_read_b64_tr_b16 v[160:161], v235 offset:256\n"
"v_mfma_f32_16x16x32_bf16 v[60:63], a[88:91], v[168:171], v[60:63]\n"
"v_mfma_f32_16x16x32_bf16 v[20:23], a[40:43], v[168:171], v[20:23]\n"
"ds_read_b64_tr_b16 v[162:163], v235 offset:5376\n"
"v_mfma_f32_16x16x32_bf16 v[20:23], a[32:35], v[164:167], v[20:23]\n"
"s_waitcnt lgkmcnt(4)\n"
"v_mfma_f32_16x16x32_bf16 v[8:11], a[4:7], v[172:175], v[8:11]\n"
"s_waitcnt lgkmcnt(2)\n"
"ds_read_b64_tr_b16 v[164:165], v235 offset:10496\n"
"v_cvt_pk_bf16_f32 v100, v100, v101\n"
"v_mfma_f32_16x16x32_bf16 v[8:11], a[12:15], v[200:203], v[8:11]\n"
"v_cvt_pk_bf16_f32 v101, v102, v103\n"
"v_cvt_pk_bf16_f32 v102, v140, v141\n"
"v_mfma_f32_16x16x32_bf16 v[48:51], a[60:63], v[200:203], v[48:51]\n"
"ds_read_b64_tr_b16 v[166:167], v235 offset:15616\n"
"v_cvt_pk_bf16_f32 v103, v142, v143\n"
"v_mfma_f32_16x16x32_bf16 v[48:51], a[52:55], v[172:175], v[48:51]\n"
"buffer_store_dwordx4 v[16:19], v251, s[40:43], s3 offen offset:0 sc1\n"
"v_mfma_f32_16x16x32_bf16 v[88:91], a[100:103], v[172:175], v[88:91]\n"
"ds_read_b64_tr_b16 v[168:169], v235 offset:20736\n"
"v_cvt_pk_bf16_f32 v20, v20, v21\n"
"v_mfma_f32_16x16x32_bf16 v[88:91], a[108:111], v[200:203], v[88:91]\n"
"v_cvt_pk_bf16_f32 v21, v22, v23\n"
"v_cvt_pk_bf16_f32 v22, v60, v61\n"
"v_mfma_f32_16x16x32_bf16 v[128:131], a[156:159], v[200:203], v[128:131]\n"
"ds_read_b64_tr_b16 v[170:171], v235 offset:25856\n"
"v_cvt_pk_bf16_f32 v23, v62, v63\n"
"v_mfma_f32_16x16x32_bf16 v[128:131], a[148:151], v[172:175], v[128:131]\n"
"s_waitcnt lgkmcnt(4)\n"
"v_cvt_pk_bf16_f32 v72, v72, v73\n"
"v_cvt_pk_bf16_f32 v73, v74, v75\n"
"v_mfma_f32_16x16x32_bf16 v[128:131], a[164:167], v[160:163], v[128:131]\n"
"s_waitcnt lgkmcnt(2)\n"
"ds_read_b64_tr_b16 v[172:173], v235 offset:30976\n"
"v_cvt_pk_bf16_f32 v74, v76, v77\n"
"v_mfma_f32_16x16x32_bf16 v[128:131], a[172:175], v[164:167], v[128:131]\n"
"v_cvt_pk_bf16_f32 v75, v78, v79\n"
"v_mfma_f32_16x16x32_bf16 v[88:91], a[124:127], v[164:167], v[88:91]\n"
"ds_read_b64_tr_b16 v[174:175], v235 offset:36096\n"
"v_mfma_f32_16x16x32_bf16 v[88:91], a[116:119], v[160:163], v[88:91]\n"
"s_waitcnt vmcnt(3)\n"
"global_store_dword v196, v209, s[14:15] sc1\n"
"v_mfma_f32_16x16x32_bf16 v[48:51], a[68:71], v[160:163], v[48:51]\n"
"ds_read_b64_tr_b16 v[200:201], v235 offset:41216\n"
"v_mfma_f32_16x16x32_bf16 v[48:51], a[76:79], v[164:167], v[48:51]\n"
"v_mfma_f32_16x16x32_bf16 v[8:11], a[28:31], v[164:167], v[8:11]\n"
"ds_read_b64_tr_b16 v[202:203], v235 offset:46336\n"
"v_mfma_f32_16x16x32_bf16 v[8:11], a[20:23], v[160:163], v[8:11]\n"
"s_waitcnt lgkmcnt(4)\n"
"v_mfma_f32_16x16x32_bf16 v[8:11], a[36:39], v[168:171], v[8:11]\n"
"s_waitcnt lgkmcnt(2)\n"
"ds_read_b64_tr_b16 v[160:161], v235 offset:51456\n"
"v_mfma_f32_16x16x32_bf16 v[8:11], a[44:47], v[172:175], v[8:11]\n"
"v_mfma_f32_16x16x32_bf16 v[48:51], a[92:95], v[172:175], v[48:51]\n"
"ds_read_b64_tr_b16 v[162:163], v235 offset:56576\n"
"v_mfma_f32_16x16x32_bf16 v[48:51], a[84:87], v[168:171], v[48:51]\n"
"buffer_store_dwordx4 v[100:103], v251, s[40:43], s3 offen offset:3072 sc1\n"
"v_mfma_f32_16x16x32_bf16 v[88:91], a[132:135], v[168:171], v[88:91]\n"
"ds_read_b64_tr_b16 v[164:165], v228 offset:256\n"
"v_mfma_f32_16x16x32_bf16 v[88:91], a[140:143], v[172:175], v[88:91]\n"
"v_mfma_f32_16x16x32_bf16 v[128:131], a[188:191], v[172:175], v[128:131]\n"
"ds_read_b64_tr_b16 v[166:167], v228 offset:5376\n"
"v_mfma_f32_16x16x32_bf16 v[128:131], a[180:183], v[168:171], v[128:131]\n"
"s_waitcnt lgkmcnt(4)\n"
"v_mfma_f32_16x16x32_bf16 v[128:131], a[144:147], v[200:203], v[128:131]\n"
"s_waitcnt lgkmcnt(2)\n"
"ds_read_b64_tr_b16 v[168:169], v228 offset:10496\n"
"v_mfma_f32_16x16x32_bf16 v[128:131], a[152:155], v[160:163], v[128:131]\n"
"v_mfma_f32_16x16x32_bf16 v[88:91], a[104:107], v[160:163], v[88:91]\n"
"ds_read_b64_tr_b16 v[170:171], v228 offset:15616\n"
"v_mfma_f32_16x16x32_bf16 v[88:91], a[96:99], v[200:203], v[88:91]\n"
"v_mfma_f32_16x16x32_bf16 v[48:51], a[48:51], v[200:203], v[48:51]\n"
"ds_read_b64_tr_b16 v[172:173], v228 offset:20736\n"
"v_mfma_f32_16x16x32_bf16 v[48:51], a[56:59], v[160:163], v[48:51]\n"
"v_mfma_f32_16x16x32_bf16 v[8:11], a[8:11], v[160:163], v[8:11]\n"
"ds_read_b64_tr_b16 v[174:175], v228 offset:25856\n"
"v_mfma_f32_16x16x32_bf16 v[8:11], a[0:3], v[200:203], v[8:11]\n"
"s_waitcnt lgkmcnt(4)\n"
"v_mfma_f32_16x16x32_bf16 v[8:11], a[16:19], v[164:167], v[8:11]\n"
"s_waitcnt lgkmcnt(2)\n"
"ds_read_b64_tr_b16 v[200:201], v228 offset:30976\n"
"v_mfma_f32_16x16x32_bf16 v[8:11], a[24:27], v[168:171], v[8:11]\n"
"v_mfma_f32_16x16x32_bf16 v[48:51], a[72:75], v[168:171], v[48:51]\n"
"ds_read_b64_tr_b16 v[202:203], v228 offset:36096\n"
"v_mfma_f32_16x16x32_bf16 v[48:51], a[64:67], v[164:167], v[48:51]\n"
"buffer_store_dwordx4 v[20:23], v251, s[40:43], s3 offen offset:2048 sc1\n"
"v_mfma_f32_16x16x32_bf16 v[88:91], a[112:115], v[164:167], v[88:91]\n"
"ds_read_b64_tr_b16 v[160:161], v231 offset:20736\n"
"v_mfma_f32_16x16x32_bf16 v[88:91], a[120:123], v[168:171], v[88:91]\n"
"v_mfma_f32_16x16x32_bf16 v[128:131], a[168:171], v[168:171], v[128:131]\n"
"ds_read_b64_tr_b16 v[162:163], v231 offset:25856\n"
"v_mfma_f32_16x16x32_bf16 v[128:131], a[160:163], v[164:167], v[128:131]\n"
"s_waitcnt lgkmcnt(4)\n"
"v_mfma_f32_16x16x32_bf16 v[128:131], a[176:179], v[172:175], v[128:131]\n"
"s_waitcnt lgkmcnt(2)\n"
"ds_read_b64_tr_b16 v[164:165], v231 offset:30976\n"
"v_mfma_f32_16x16x32_bf16 v[128:131], a[184:187], v[200:203], v[128:131]\n"
"v_mfma_f32_16x16x32_bf16 v[88:91], a[136:139], v[200:203], v[88:91]\n"
"ds_read_b64_tr_b16 v[166:167], v231 offset:36096\n"
"v_mfma_f32_16x16x32_bf16 v[88:91], a[128:131], v[172:175], v[88:91]\n"
"v_mfma_f32_16x16x32_bf16 v[48:51], a[80:83], v[172:175], v[48:51]\n"
"ds_read_b64_tr_b16 v[168:169], v249 offset:256\n"
"v_mfma_f32_16x16x32_bf16 v[48:51], a[88:91], v[200:203], v[48:51]\n"
"v_mfma_f32_16x16x32_bf16 v[8:11], a[40:43], v[200:203], v[8:11]\n"
"ds_read_b64_tr_b16 v[170:171], v249 offset:5376\n"
"v_mfma_f32_16x16x32_bf16 v[8:11], a[32:35], v[172:175], v[8:11]\n"
"s_waitcnt lgkmcnt(4)\n"
"v_mfma_f32_16x16x32_bf16 v[12:15], a[4:7], v[160:163], v[12:15]\n"
"s_waitcnt lgkmcnt(2)\n"
"ds_read_b64_tr_b16 v[172:173], v249 offset:10496\n"
"v_cvt_pk_bf16_f32 v88, v88, v89\n"
"v_mfma_f32_16x16x32_bf16 v[12:15], a[12:15], v[164:167], v[12:15]\n"
"v_cvt_pk_bf16_f32 v89, v90, v91\n"
"v_cvt_pk_bf16_f32 v90, v128, v129\n"
"v_mfma_f32_16x16x32_bf16 v[52:55], a[60:63], v[164:167], v[52:55]\n"
"ds_read_b64_tr_b16 v[174:175], v249 offset:15616\n"
"v_cvt_pk_bf16_f32 v91, v130, v131\n"
"v_mfma_f32_16x16x32_bf16 v[52:55], a[52:55], v[160:163], v[52:55]\n"
"buffer_store_dwordx4 v[72:75], v253, s[40:43], s2 offen offset:0 sc1\n"
"v_mfma_f32_16x16x32_bf16 v[92:95], a[100:103], v[160:163], v[92:95]\n"
"ds_read_b64_tr_b16 v[200:201], v249 offset:20736\n"
"v_cvt_pk_bf16_f32 v8, v8, v9\n"
"v_mfma_f32_16x16x32_bf16 v[92:95], a[108:111], v[164:167], v[92:95]\n"
"v_cvt_pk_bf16_f32 v9, v10, v11\n"
"v_cvt_pk_bf16_f32 v10, v48, v49\n"
"v_mfma_f32_16x16x32_bf16 v[132:135], a[156:159], v[164:167], v[132:135]\n"
"ds_read_b64_tr_b16 v[202:203], v249 offset:25856\n"
"v_cvt_pk_bf16_f32 v11, v50, v51\n"
"v_mfma_f32_16x16x32_bf16 v[132:135], a[148:151], v[160:163], v[132:135]\n"
"s_waitcnt lgkmcnt(4)\n"
"v_mfma_f32_16x16x32_bf16 v[132:135], a[164:167], v[168:171], v[132:135]\n"
"s_waitcnt lgkmcnt(2)\n"
"ds_read_b64_tr_b16 v[160:161], v249 offset:30976\n"
"v_mfma_f32_16x16x32_bf16 v[132:135], a[172:175], v[172:175], v[132:135]\n"
"v_mfma_f32_16x16x32_bf16 v[92:95], a[124:127], v[172:175], v[92:95]\n"
"ds_read_b64_tr_b16 v[162:163], v249 offset:36096\n"
"v_mfma_f32_16x16x32_bf16 v[92:95], a[116:119], v[168:171], v[92:95]\n"
"v_mfma_f32_16x16x32_bf16 v[52:55], a[68:71], v[168:171], v[52:55]\n"
"ds_read_b64_tr_b16 v[164:165], v249 offset:41216\n"
"v_mfma_f32_16x16x32_bf16 v[52:55], a[76:79], v[172:175], v[52:55]\n"
"v_mfma_f32_16x16x32_bf16 v[12:15], a[28:31], v[172:175], v[12:15]\n"
"ds_read_b64_tr_b16 v[166:167], v249 offset:46336\n"
"v_mfma_f32_16x16x32_bf16 v[12:15], a[20:23], v[168:171], v[12:15]\n"
"s_waitcnt lgkmcnt(4)\n"
"v_mfma_f32_16x16x32_bf16 v[12:15], a[36:39], v[200:203], v[12:15]\n"
"s_waitcnt lgkmcnt(2)\n"
"ds_read_b64_tr_b16 v[168:169], v249 offset:51456\n"
"v_mfma_f32_16x16x32_bf16 v[12:15], a[44:47], v[160:163], v[12:15]\n"
"v_mfma_f32_16x16x32_bf16 v[52:55], a[92:95], v[160:163], v[52:55]\n"
"ds_read_b64_tr_b16 v[170:171], v249 offset:56576\n"
"v_mfma_f32_16x16x32_bf16 v[52:55], a[84:87], v[200:203], v[52:55]\n"
"buffer_store_dwordx4 v[88:91], v251, s[40:43], s2 offen offset:1024 sc1\n"
"v_mfma_f32_16x16x32_bf16 v[92:95], a[132:135], v[200:203], v[92:95]\n"
"ds_read_b64_tr_b16 v[172:173], v229 offset:256\n"
"v_mfma_f32_16x16x32_bf16 v[92:95], a[140:143], v[160:163], v[92:95]\n"
"v_mfma_f32_16x16x32_bf16 v[132:135], a[188:191], v[160:163], v[132:135]\n"
"ds_read_b64_tr_b16 v[174:175], v229 offset:5376\n"
"v_mfma_f32_16x16x32_bf16 v[132:135], a[180:183], v[200:203], v[132:135]\n"
"s_waitcnt lgkmcnt(4)\n"
"v_mfma_f32_16x16x32_bf16 v[132:135], a[144:147], v[164:167], v[132:135]\n"
"s_waitcnt lgkmcnt(2)\n"
"ds_read_b64_tr_b16 v[200:201], v229 offset:10496\n"
"v_mfma_f32_16x16x32_bf16 v[132:135], a[152:155], v[168:171], v[132:135]\n"
"v_mfma_f32_16x16x32_bf16 v[92:95], a[104:107], v[168:171], v[92:95]\n"
"ds_read_b64_tr_b16 v[202:203], v229 offset:15616\n"
"v_mfma_f32_16x16x32_bf16 v[92:95], a[96:99], v[164:167], v[92:95]\n"
"v_mfma_f32_16x16x32_bf16 v[52:55], a[48:51], v[164:167], v[52:55]\n"
"ds_read_b64_tr_b16 v[160:161], v229 offset:20736\n"
"v_mfma_f32_16x16x32_bf16 v[52:55], a[56:59], v[168:171], v[52:55]\n"
"v_mfma_f32_16x16x32_bf16 v[12:15], a[8:11], v[168:171], v[12:15]\n"
"ds_read_b64_tr_b16 v[162:163], v229 offset:25856\n"
"v_mfma_f32_16x16x32_bf16 v[12:15], a[0:3], v[164:167], v[12:15]\n"
"s_waitcnt lgkmcnt(4)\n"
"v_mfma_f32_16x16x32_bf16 v[12:15], a[16:19], v[172:175], v[12:15]\n"
"s_waitcnt lgkmcnt(2)\n"
"ds_read_b64_tr_b16 v[164:165], v229 offset:30976\n"
"v_mfma_f32_16x16x32_bf16 v[12:15], a[24:27], v[200:203], v[12:15]\n"
"s_waitcnt vmcnt(2)\n"
"global_store_dword v197, v209, s[14:15] sc1\n"
"v_mfma_f32_16x16x32_bf16 v[52:55], a[72:75], v[200:203], v[52:55]\n"
"ds_read_b64_tr_b16 v[166:167], v229 offset:36096\n"
"v_mfma_f32_16x16x32_bf16 v[52:55], a[64:67], v[172:175], v[52:55]\n"
"buffer_store_dwordx4 v[8:11], v251, s[40:43], s2 offen offset:0 sc1\n"
"v_mfma_f32_16x16x32_bf16 v[92:95], a[112:115], v[172:175], v[92:95]\n"
"ds_read_b64_tr_b16 v[168:169], v230 offset:0\n"
"v_mfma_f32_16x16x32_bf16 v[92:95], a[120:123], v[200:203], v[92:95]\n"
"v_mfma_f32_16x16x32_bf16 v[132:135], a[168:171], v[200:203], v[132:135]\n"
"ds_read_b64_tr_b16 v[170:171], v230 offset:5120\n"
"v_mfma_f32_16x16x32_bf16 v[132:135], a[160:163], v[172:175], v[132:135]\n"
"s_waitcnt lgkmcnt(4)\n"
"v_mfma_f32_16x16x32_bf16 v[132:135], a[176:179], v[160:163], v[132:135]\n"
"s_waitcnt lgkmcnt(2)\n"
"ds_read_b64_tr_b16 v[172:173], v230 offset:10240\n"
"v_mfma_f32_16x16x32_bf16 v[132:135], a[184:187], v[164:167], v[132:135]\n"
"global_load_dwordx4 v[192:195], v214, s[14:15] sc1\n"
"v_mfma_f32_16x16x32_bf16 v[92:95], a[136:139], v[164:167], v[92:95]\n"
"ds_read_b64_tr_b16 v[174:175], v230 offset:15360\n"
"v_mfma_f32_16x16x32_bf16 v[92:95], a[128:131], v[160:163], v[92:95]\n"
"v_mfma_f32_16x16x32_bf16 v[52:55], a[80:83], v[160:163], v[52:55]\n"
"ds_read_b64_tr_b16 v[200:201], v231 offset:0\n"
"v_mfma_f32_16x16x32_bf16 v[52:55], a[88:91], v[164:167], v[52:55]\n"
"v_mfma_f32_16x16x32_bf16 v[12:15], a[40:43], v[164:167], v[12:15]\n"
"ds_read_b64_tr_b16 v[202:203], v231 offset:5120\n"
"v_mfma_f32_16x16x32_bf16 v[12:15], a[32:35], v[160:163], v[12:15]\n"
"s_waitcnt lgkmcnt(4)\n"
"v_mfma_f32_16x16x32_bf16 v[0:3], a[224:227], v[168:171], v[0:3]\n"
"s_waitcnt lgkmcnt(2)\n"
"ds_read_b64_tr_b16 v[160:161], v231 offset:10240\n"
"v_cvt_pk_bf16_f32 v92, v92, v93\n"
"v_mfma_f32_16x16x32_bf16 v[0:3], a[228:231], v[172:175], v[0:3]\n"
"v_cvt_pk_bf16_f32 v93, v94, v95\n"
"v_cvt_pk_bf16_f32 v94, v132, v133\n"
"v_mfma_f32_16x16x32_bf16 v[40:43], a[236:239], v[172:175], v[40:43]\n"
"ds_read_b64_tr_b16 v[162:163], v231 offset:15360\n"
"v_cvt_pk_bf16_f32 v95, v134, v135\n"
"v_mfma_f32_16x16x32_bf16 v[40:43], a[232:235], v[168:171], v[40:43]\n"
"buffer_store_dwordx4 v[92:95], v251, s[40:43], s2 offen offset:3072 sc1\n"
"v_mfma_f32_16x16x32_bf16 v[80:83], a[240:243], v[168:171], v[80:83]\n"
"ds_read_b64_tr_b16 v[164:165], v230 offset:20480\n"
"v_cvt_pk_bf16_f32 v12, v12, v13\n"
"v_mfma_f32_16x16x32_bf16 v[80:83], a[244:247], v[172:175], v[80:83]\n"
"v_cvt_pk_bf16_f32 v13, v14, v15\n"
"v_cvt_pk_bf16_f32 v14, v52, v53\n"
"v_mfma_f32_16x16x32_bf16 v[120:123], a[252:255], v[172:175], v[120:123]\n"
"ds_read_b64_tr_b16 v[166:167], v230 offset:25600\n"
"v_cvt_pk_bf16_f32 v15, v54, v55\n"
"v_mfma_f32_16x16x32_bf16 v[120:123], a[248:251], v[168:171], v[120:123]\n"
"s_waitcnt lgkmcnt(4)\n"
"v_cvt_pk_bf16_f32 v32, v32, v33\n"
"v_cvt_pk_bf16_f32 v33, v34, v35\n"
"v_mfma_f32_16x16x32_bf16 v[124:127], a[248:251], v[200:203], v[124:127]\n"
"s_waitcnt lgkmcnt(2)\n"
"ds_read_b64_tr_b16 v[168:169], v230 offset:30720\n"
"v_cvt_pk_bf16_f32 v34, v36, v37\n"
"v_mfma_f32_16x16x32_bf16 v[124:127], a[252:255], v[160:163], v[124:127]\n"
"v_cvt_pk_bf16_f32 v35, v38, v39\n"
"v_mfma_f32_16x16x32_bf16 v[84:87], a[244:247], v[160:163], v[84:87]\n"
"ds_read_b64_tr_b16 v[170:171], v230 offset:35840\n"
"v_mfma_f32_16x16x32_bf16 v[84:87], a[240:243], v[200:203], v[84:87]\n"
"v_mfma_f32_16x16x32_bf16 v[44:47], a[232:235], v[200:203], v[44:47]\n"
"ds_read_b64_tr_b16 v[172:173], v231 offset:20480\n"
"v_mfma_f32_16x16x32_bf16 v[44:47], a[236:239], v[160:163], v[44:47]\n"
"v_mfma_f32_16x16x32_bf16 v[4:7], a[228:231], v[160:163], v[4:7]\n"
"ds_read_b64_tr_b16 v[174:175], v231 offset:25600\n"
"v_mfma_f32_16x16x32_bf16 v[4:7], a[224:227], v[200:203], v[4:7]\n"
"s_waitcnt lgkmcnt(4)\n"
"v_mfma_f32_16x16x32_bf16 v[0:3], a[4:7], v[164:167], v[0:3]\n"
"s_waitcnt lgkmcnt(2)\n"
"ds_read_b64_tr_b16 v[200:201], v231 offset:30720\n"
"v_mfma_f32_16x16x32_bf16 v[0:3], a[12:15], v[168:171], v[0:3]\n"
"v_mfma_f32_16x16x32_bf16 v[40:43], a[60:63], v[168:171], v[40:43]\n"
"ds_read_b64_tr_b16 v[202:203], v231 offset:35840\n"
"v_mfma_f32_16x16x32_bf16 v[40:43], a[52:55], v[164:167], v[40:43]\n"
"buffer_store_dwordx4 v[12:15], v251, s[40:43], s2 offen offset:2048 sc1\n"
"v_mfma_f32_16x16x32_bf16 v[80:83], a[100:103], v[164:167], v[80:83]\n"
"ds_read_b64_tr_b16 v[160:161], v235 offset:0\n"
"v_mfma_f32_16x16x32_bf16 v[80:83], a[108:111], v[168:171], v[80:83]\n"
"v_mfma_f32_16x16x32_bf16 v[120:123], a[156:159], v[168:171], v[120:123]\n"
"ds_read_b64_tr_b16 v[162:163], v235 offset:5120\n"
"v_mfma_f32_16x16x32_bf16 v[120:123], a[148:151], v[164:167], v[120:123]\n"
"s_waitcnt lgkmcnt(4)\n"
"v_mfma_f32_16x16x32_bf16 v[124:127], a[148:151], v[172:175], v[124:127]\n"
"s_waitcnt lgkmcnt(2)\n"
"ds_read_b64_tr_b16 v[164:165], v235 offset:10240\n"
"v_mfma_f32_16x16x32_bf16 v[124:127], a[156:159], v[200:203], v[124:127]\n"
"v_mfma_f32_16x16x32_bf16 v[84:87], a[108:111], v[200:203], v[84:87]\n"
"ds_read_b64_tr_b16 v[166:167], v235 offset:15360\n"
"v_mfma_f32_16x16x32_bf16 v[84:87], a[100:103], v[172:175], v[84:87]\n"
"s_waitcnt vmcnt(2)\n"
"v_mfma_f32_16x16x32_bf16 v[44:47], a[52:55], v[172:175], v[44:47]\n"
"ds_read_b64_tr_b16 v[168:169], v249 offset:0\n"
"v_readfirstlane_b32 s5, v193\n"
"v_mfma_f32_16x16x32_bf16 v[44:47], a[60:63], v[200:203], v[44:47]\n"
"v_mfma_f32_16x16x32_bf16 v[4:7], a[12:15], v[200:203], v[4:7]\n"
"ds_read_b64_tr_b16 v[170:171], v249 offset:5120\n"
"v_readfirstlane_b32 s6, v194\n"
"v_mfma_f32_16x16x32_bf16 v[4:7], a[4:7], v[172:175], v[4:7]\n"
"s_waitcnt lgkmcnt(4)\n"
"v_mfma_f32_16x16x32_bf16 v[0:3], a[20:23], v[160:163], v[0:3]\n"
"s_waitcnt lgkmcnt(2)\n"
"ds_read_b64_tr_b16 v[172:173], v249 offset:10240\n"
"v_readfirstlane_b32 s7, v195\n"
"v_mfma_f32_16x16x32_bf16 v[0:3], a[28:31], v[164:167], v[0:3]\n"
"v_mfma_f32_16x16x32_bf16 v[40:43], a[76:79], v[164:167], v[40:43]\n"
"s_cmp_eq_u32 s5, s13\n"
"s_cselect_b32 s5, s67, 0xf0000000\n"
"ds_read_b64_tr_b16 v[174:175], v249 offset:15360\n"
"v_mfma_f32_16x16x32_bf16 v[40:43], a[68:71], v[160:163], v[40:43]\n"
"s_cmp_eq_u32 s6, s13\n"
"s_cselect_b32 s6, s68, 0xf0000000\n"
"v_mfma_f32_16x16x32_bf16 v[80:83], a[116:119], v[160:163], v[80:83]\n"
"s_cmp_eq_u32 s7, s13\n"
"s_cselect_b32 s7, s69, 0xf0000000\n"
"ds_read_b64_tr_b16 v[200:201], v235 offset:20480\n"
"v_mfma_f32_16x16x32_bf16 v[80:83], a[124:127], v[164:167], v[80:83]\n"
"s_barrier\n"
"v_mfma_f32_16x16x32_bf16 v[120:123], a[172:175], v[164:167], v[120:123]\n"
"ds_read_b64_tr_b16 v[202:203], v235 offset:25600\n"
"v_mfma_f32_16x16x32_bf16 v[120:123], a[164:167], v[160:163], v[120:123]\n"
"s_waitcnt lgkmcnt(4)\n"
"v_mfma_f32_16x16x32_bf16 v[124:127], a[164:167], v[168:171], v[124:127]\n"
"s_waitcnt lgkmcnt(2)\n"
"ds_read_b64_tr_b16 v[160:161], v235 offset:30720\n"
"v_mfma_f32_16x16x32_bf16 v[124:127], a[172:175], v[172:175], v[124:127]\n"
"buffer_load_dwordx4 v[156:159], v253, s[40:43], s5 offen offset:0 sc1\n"
"v_mfma_f32_16x16x32_bf16 v[84:87], a[124:127], v[172:175], v[84:87]\n"
"ds_read_b64_tr_b16 v[162:163], v235 offset:35840\n"
"v_mfma_f32_16x16x32_bf16 v[84:87], a[116:119], v[168:171], v[84:87]\n"
"v_mfma_f32_16x16x32_bf16 v[44:47], a[68:71], v[168:171], v[44:47]\n"
"ds_read_b64_tr_b16 v[164:165], v249 offset:20480\n"
"v_mfma_f32_16x16x32_bf16 v[44:47], a[76:79], v[172:175], v[44:47]\n"
"buffer_load_dwordx4 v[64:67], v251, s[40:43], s5 offen offset:0 sc1\n"
"v_mfma_f32_16x16x32_bf16 v[4:7], a[28:31], v[172:175], v[4:7]\n"
"ds_read_b64_tr_b16 v[166:167], v249 offset:25600\n"
"v_mfma_f32_16x16x32_bf16 v[4:7], a[20:23], v[168:171], v[4:7]\n"
"s_waitcnt lgkmcnt(4)\n"
"v_mfma_f32_16x16x32_bf16 v[0:3], a[36:39], v[200:203], v[0:3]\n"
"s_waitcnt lgkmcnt(2)\n"
"ds_read_b64_tr_b16 v[168:169], v249 offset:30720\n"
"v_mfma_f32_16x16x32_bf16 v[0:3], a[44:47], v[160:163], v[0:3]\n"
"buffer_load_dwordx4 v[144:147], v251, s[40:43], s5 offen offset:1024 sc1\n"
"v_mfma_f32_16x16x32_bf16 v[40:43], a[92:95], v[160:163], v[40:43]\n"
"ds_read_b64_tr_b16 v[170:171], v249 offset:35840\n"
"v_mfma_f32_16x16x32_bf16 v[40:43], a[84:87], v[200:203], v[40:43]\n"
"v_mfma_f32_16x16x32_bf16 v[80:83], a[132:135], v[200:203], v[80:83]\n"
"ds_read_b64_tr_b16 v[172:173], v235 offset:40960\n"
"v_mfma_f32_16x16x32_bf16 v[80:83], a[140:143], v[160:163], v[80:83]\n"
"buffer_load_dwordx4 v[68:71], v251, s[40:43], s5 offen offset:2048 sc1\n"
"v_mfma_f32_16x16x32_bf16 v[120:123], a[188:191], v[160:163], v[120:123]\n"
"ds_read_b64_tr_b16 v[174:175], v235 offset:46080\n"
"v_mfma_f32_16x16x32_bf16 v[120:123], a[180:183], v[200:203], v[120:123]\n"
"s_waitcnt lgkmcnt(4)\n"
"v_mfma_f32_16x16x32_bf16 v[124:127], a[180:183], v[164:167], v[124:127]\n"
"s_waitcnt lgkmcnt(2)\n"
"ds_read_b64_tr_b16 v[200:201], v235 offset:51200\n"
"v_mfma_f32_16x16x32_bf16 v[124:127], a[188:191], v[168:171], v[124:127]\n"
"buffer_load_dwordx4 v[148:151], v251, s[40:43], s5 offen offset:3072 sc1\n"
"v_mfma_f32_16x16x32_bf16 v[84:87], a[140:143], v[168:171], v[84:87]\n"
"ds_read_b64_tr_b16 v[202:203], v235 offset:56320\n"
"v_mfma_f32_16x16x32_bf16 v[84:87], a[132:135], v[164:167], v[84:87]\n"
"v_mfma_f32_16x16x32_bf16 v[44:47], a[84:87], v[164:167], v[44:47]\n"
"ds_read_b64_tr_b16 v[160:161], v249 offset:40960\n"
"v_mfma_f32_16x16x32_bf16 v[44:47], a[92:95], v[168:171], v[44:47]\n"
"buffer_load_dwordx4 v[116:119], v253, s[40:43], s6 offen offset:0 sc1\n"
"v_mfma_f32_16x16x32_bf16 v[4:7], a[44:47], v[168:171], v[4:7]\n"
"ds_read_b64_tr_b16 v[162:163], v249 offset:46080\n"
"v_mfma_f32_16x16x32_bf16 v[4:7], a[36:39], v[164:167], v[4:7]\n"
"s_waitcnt lgkmcnt(4)\n"
"v_mfma_f32_16x16x32_bf16 v[0:3], a[0:3], v[172:175], v[0:3]\n"
"s_waitcnt lgkmcnt(2)\n"
"ds_read_b64_tr_b16 v[164:165], v249 offset:51200\n"
"v_mfma_f32_16x16x32_bf16 v[0:3], a[8:11], v[200:203], v[0:3]\n"
"s_waitcnt vmcnt(6)\n"
"global_store_dword v198, v209, s[14:15] sc1\n"
"v_mfma_f32_16x16x32_bf16 v[40:43], a[56:59], v[200:203], v[40:43]\n"
"ds_read_b64_tr_b16 v[166:167], v249 offset:56320\n"
"v_mfma_f32_16x16x32_bf16 v[40:43], a[48:51], v[172:175], v[40:43]\n"
"buffer_load_dwordx4 v[56:59], v251, s[40:43], s6 offen offset:0 sc1\n"
"v_mfma_f32_16x16x32_bf16 v[80:83], a[96:99], v[172:175], v[80:83]\n"
"ds_read_b64_tr_b16 v[168:169], v228 offset:0\n"
"v_mfma_f32_16x16x32_bf16 v[80:83], a[104:107], v[200:203], v[80:83]\n"
"v_mfma_f32_16x16x32_bf16 v[120:123], a[152:155], v[200:203], v[120:123]\n"
"ds_read_b64_tr_b16 v[170:171], v228 offset:5120\n"
"v_mfma_f32_16x16x32_bf16 v[120:123], a[144:147], v[172:175], v[120:123]\n"
"s_waitcnt lgkmcnt(4)\n"
"v_mfma_f32_16x16x32_bf16 v[124:127], a[144:147], v[160:163], v[124:127]\n"
"s_waitcnt lgkmcnt(2)\n"
"ds_read_b64_tr_b16 v[172:173], v228 offset:10240\n"
"v_mfma_f32_16x16x32_bf16 v[124:127], a[152:155], v[164:167], v[124:127]\n"
"buffer_load_dwordx4 v[136:139], v251, s[40:43], s6 offen offset:1024 sc1\n"
"v_mfma_f32_16x16x32_bf16 v[84:87], a[104:107], v[164:167], v[84:87]\n"
"ds_read_b64_tr_b16 v[174:175], v228 offset:15360\n"
"v_mfma_f32_16x16x32_bf16 v[84:87], a[96:99], v[160:163], v[84:87]\n"
"v_mfma_f32_16x16x32_bf16 v[44:47], a[48:51], v[160:163], v[44:47]\n"
"ds_read_b64_tr_b16 v[200:201], v229 offset:0\n"
"v_mfma_f32_16x16x32_bf16 v[44:47], a[56:59], v[164:167], v[44:47]\n"
"buffer_load_dwordx4 v[60:63], v251, s[40:43], s6 offen offset:2048 sc1\n"
"v_mfma_f32_16x16x32_bf16 v[4:7], a[8:11], v[164:167], v[4:7]\n"
"ds_read_b64_tr_b16 v[202:203], v229 offset:5120\n"
"v_mfma_f32_16x16x32_bf16 v[4:7], a[0:3], v[160:163], v[4:7]\n"
"s_waitcnt lgkmcnt(4)\n"
"v_mfma_f32_16x16x32_bf16 v[0:3], a[16:19], v[168:171], v[0:3]\n"
"s_waitcnt lgkmcnt(2)\n"
"ds_read_b64_tr_b16 v[160:161], v229 offset:10240\n"
"v_mfma_f32_16x16x32_bf16 v[0:3], a[24:27], v[172:175], v[0:3]\n"
"buffer_load_dwordx4 v[140:143], v251, s[40:43], s6 offen offset:3072 sc1\n"
"v_mfma_f32_16x16x32_bf16 v[40:43], a[72:75], v[172:175], v[40:43]\n"
"ds_read_b64_tr_b16 v[162:163], v229 offset:15360\n"
"v_mfma_f32_16x16x32_bf16 v[40:43], a[64:67], v[168:171], v[40:43]\n"
"v_mfma_f32_16x16x32_bf16 v[80:83], a[112:115], v[168:171], v[80:83]\n"
"ds_read_b64_tr_b16 v[164:165], v228 offset:20480\n"
"v_mfma_f32_16x16x32_bf16 v[80:83], a[120:123], v[172:175], v[80:83]\n"
"buffer_load_dwordx4 v[76:79], v253, s[40:43], s7 offen offset:0 sc1\n"
"v_mfma_f32_16x16x32_bf16 v[120:123], a[168:171], v[172:175], v[120:123]\n"
"ds_read_b64_tr_b16 v[166:167], v228 offset:25600\n"
"v_mfma_f32_16x16x32_bf16 v[120:123], a[160:163], v[168:171], v[120:123]\n"
"s_waitcnt lgkmcnt(4)\n"
"v_mfma_f32_16x16x32_bf16 v[124:127], a[160:163], v[200:203], v[124:127]\n"
"s_waitcnt lgkmcnt(2)\n"
"ds_read_b64_tr_b16 v[168:169], v228 offset:30720\n"
"v_mfma_f32_16x16x32_bf16 v[124:127], a[168:171], v[160:163], v[124:127]\n"
"buffer_load_dwordx4 v[48:51], v251, s[40:43], s7 offen offset:0 sc1\n"
"v_mfma_f32_16x16x32_bf16 v[84:87], a[120:123], v[160:163], v[84:87]\n"
"ds_read_b64_tr_b16 v[170:171], v228 offset:35840\n"
"v_mfma_f32_16x16x32_bf16 v[84:87], a[112:115], v[200:203], v[84:87]\n"
"v_mfma_f32_16x16x32_bf16 v[44:47], a[64:67], v[200:203], v[44:47]\n"
"ds_read_b64_tr_b16 v[172:173], v229 offset:20480\n"
"v_mfma_f32_16x16x32_bf16 v[44:47], a[72:75], v[160:163], v[44:47]\n"
"buffer_load_dwordx4 v[128:131], v251, s[40:43], s7 offen offset:1024 sc1\n"
"v_mfma_f32_16x16x32_bf16 v[4:7], a[24:27], v[160:163], v[4:7]\n"
"ds_read_b64_tr_b16 v[174:175], v229 offset:25600\n"
"v_mfma_f32_16x16x32_bf16 v[4:7], a[16:19], v[200:203], v[4:7]\n"
"s_waitcnt lgkmcnt(4)\n"
"v_mfma_f32_16x16x32_bf16 v[0:3], a[32:35], v[164:167], v[0:3]\n"
"s_waitcnt lgkmcnt(2)\n"
"ds_read_b64_tr_b16 v[200:201], v229 offset:30720\n"
"v_mfma_f32_16x16x32_bf16 v[0:3], a[40:43], v[168:171], v[0:3]\n"
"global_load_dwordx4 v[192:195], v214, s[14:15] sc1\n"
"v_mfma_f32_16x16x32_bf16 v[40:43], a[88:91], v[168:171], v[40:43]\n"
"ds_read_b64_tr_b16 v[202:203], v229 offset:35840\n"
"v_mfma_f32_16x16x32_bf16 v[40:43], a[80:83], v[164:167], v[40:43]\n"
"buffer_load_dwordx4 v[52:55], v251, s[40:43], s7 offen offset:2048 sc1\n"
"v_mfma_f32_16x16x32_bf16 v[80:83], a[128:131], v[164:167], v[80:83]\n"
"v_mfma_f32_16x16x32_bf16 v[80:83], a[136:139], v[168:171], v[80:83]\n"
"v_mfma_f32_16x16x32_bf16 v[120:123], a[184:187], v[168:171], v[120:123]\n"
"v_mfma_f32_16x16x32_bf16 v[120:123], a[176:179], v[164:167], v[120:123]\n"
"s_waitcnt lgkmcnt(2)\n"
"v_cvt_pk_bf16_f32 v0, v0, v1\n"
"v_cvt_pk_bf16_f32 v1, v2, v3\n"
"v_mfma_f32_16x16x32_bf16 v[124:127], a[176:179], v[172:175], v[124:127]\n"
"s_waitcnt lgkmcnt(0)\n"
"v_cvt_pk_bf16_f32 v2, v40, v41\n"
"v_cvt_pk_bf16_f32 v3, v42, v43\n"
"v_mfma_f32_16x16x32_bf16 v[124:127], a[184:187], v[200:203], v[124:127]\n"
"buffer_load_dwordx4 v[132:135], v251, s[40:43], s7 offen offset:3072 sc1\n"
"v_mfma_f32_16x16x32_bf16 v[84:87], a[136:139], v[200:203], v[84:87]\n"
"v_mfma_f32_16x16x32_bf16 v[84:87], a[128:131], v[172:175], v[84:87]\n"
"v_cvt_pk_bf16_f32 v80, v80, v81\n"
"v_cvt_pk_bf16_f32 v81, v82, v83\n"
"v_mfma_f32_16x16x32_bf16 v[44:47], a[80:83], v[172:175], v[44:47]\n"
"v_cvt_pk_bf16_f32 v82, v120, v121\n"
"v_cvt_pk_bf16_f32 v83, v122, v123\n"
"v_mfma_f32_16x16x32_bf16 v[44:47], a[88:91], v[200:203], v[44:47]\n"
"v_mfma_f32_16x16x32_bf16 v[4:7], a[40:43], v[200:203], v[4:7]\n"
"v_mfma_f32_16x16x32_bf16 v[4:7], a[32:35], v[172:175], v[4:7]\n"
"v_cvt_pk_bf16_f32 v84, v84, v85\n"
"v_cvt_pk_bf16_f32 v85, v86, v87\n"
"v_mfma_f32_16x16x32_bf16 v[240:243], v[32:35], v[218:221], 0\n"
"v_cvt_pk_bf16_f32 v86, v124, v125\n"
"v_cvt_pk_bf16_f32 v87, v126, v127\n"
"v_mfma_f32_16x16x32_bf16 v[244:247], v[32:35], v[216:219], 0\n"
"v_mfma_f32_16x16x32_bf16 v[160:163], v[0:3], v[218:221], 0\n"
"v_mfma_f32_16x16x32_bf16 v[168:171], v[0:3], v[216:219], 0\n"
"v_cvt_pk_bf16_f32 v4, v4, v5\n"
"v_cvt_pk_bf16_f32 v5, v6, v7\n"
"v_mfma_f32_16x16x32_bf16 v[176:179], v[80:83], v[218:221], 0\n"
"v_cvt_pk_bf16_f32 v6, v44, v45\n"
"v_cvt_pk_bf16_f32 v7, v46, v47\n"
"v_mfma_f32_16x16x32_bf16 v[184:187], v[80:83], v[216:219], 0\n"
"v_mfma_f32_16x16x32_bf16 v[180:183], v[84:87], v[218:221], 0\n"
"v_mfma_f32_16x16x32_bf16 v[188:191], v[84:87], v[216:219], 0\n"
"v_mfma_f32_16x16x32_bf16 v[164:167], v[4:7], v[218:221], 0\n"
"v_mfma_f32_16x16x32_bf16 v[172:175], v[4:7], v[216:219], 0\n"
"s_waitcnt vmcnt(0)\n"
"s_cmp_lg_u32 s5, 0xf0000000\n"
"s_cbranch_scc1 .Lat_k1\n"
"s_mov_b32 s36, 0\n"
".Lat_sp1:\n"
"v_readfirstlane_b32 s12, v193\n"
"s_add_u32 s36, s36, 1\n"
"s_cmp_eq_u32 s12, s13\n"
"s_cbranch_scc1 .Lat_k1\n"
"global_load_dwordx4 v[192:195], v214, s[14:15] sc1\n"
"s_cmp_lt_u32 s36, 1048576\n"
"s_waitcnt vmcnt(0)\n"
"s_cbranch_scc1 .Lat_sp1\n"
".Lat_k1:\n"
"s_barrier\n"
"s_cmp_lg_u32 s5, 0xf0000000\n"
"s_cbranch_scc1 .Lat_l1\n"
"buffer_load_dwordx4 v[156:159], v253, s[40:43], s67 offen offset:0 sc1\n"
"buffer_load_dwordx4 v[64:67], v251, s[40:43], s67 offen offset:0 sc1\n"
"buffer_load_dwordx4 v[144:147], v251, s[40:43], s67 offen offset:1024 sc1\n"
"buffer_load_dwordx4 v[68:71], v251, s[40:43], s67 offen offset:2048 sc1\n"
"buffer_load_dwordx4 v[148:151], v251, s[40:43], s67 offen offset:3072 sc1\n"
".Lat_l1:\n"
"s_cmp_lg_u32 s6, 0xf0000000\n"
"s_cbranch_scc1 .Lat_k2\n"
"s_mov_b32 s36, 0\n"
".Lat_sp2:\n"
"v_readfirstlane_b32 s12, v194\n"
"s_add_u32 s36, s36, 1\n"
"s_cmp_eq_u32 s12, s13\n"
"s_cbranch_scc1 .Lat_k2\n"
"global_load_dwordx4 v[192:195], v214, s[14:15] sc1\n"
"s_cmp_lt_u32 s36, 1048576\n"
"s_waitcnt vmcnt(0)\n"
"s_cbranch_scc1 .Lat_sp2\n"
".Lat_k2:\n"
"s_barrier\n"
"s_cmp_lg_u32 s6, 0xf0000000\n"
"s_cbranch_scc1 .Lat_l2\n"
"buffer_load_dwordx4 v[116:119], v253, s[40:43], s68 offen offset:0 sc1\n"
"buffer_load_dwordx4 v[56:59], v251, s[40:43], s68 offen offset:0 sc1\n"
"buffer_load_dwordx4 v[136:139], v251, s[40:43], s68 offen offset:1024 sc1\n"
"buffer_load_dwordx4 v[60:63], v251, s[40:43], s68 offen offset:2048 sc1\n"
"buffer_load_dwordx4 v[140:143], v251, s[40:43], s68 offen offset:3072 sc1\n"
".Lat_l2:\n"
"s_max_u32 s12, s5, s6\n"
"s_max_u32 s12, s12, s7\n"
"s_cmp_lg_u32 s12, 0xf0000000\n"
"s_cbranch_scc1 .Lat_m\n"
"global_load_dwordx4 v[192:195], v214, s[14:15] sc1\n"
"s_waitcnt vmcnt(1)\n"
".Lat_m:\n"
"v_mfma_f32_16x16x32_bf16 v[240:243], v[156:159], v[218:221], v[240:243]\n"
"v_mfma_f32_16x16x32_bf16 v[244:247], v[156:159], v[216:219], v[244:247]\n"
"v_mfma_f32_16x16x32_bf16 v[184:187], v[64:67], v[218:221], v[184:187]\n"
"v_mfma_f32_16x16x32_bf16 v[176:179], v[64:67], v[216:219], v[176:179]\n"
"v_mfma_f32_16x16x32_bf16 v[168:171], v[144:147], v[218:221], v[168:171]\n"
"v_mfma_f32_16x16x32_bf16 v[160:163], v[144:147], v[216:219], v[160:163]\n"
"v_mfma_f32_16x16x32_bf16 v[188:191], v[68:71], v[218:221], v[188:191]\n"
"v_mfma_f32_16x16x32_bf16 v[180:183], v[68:71], v[216:219], v[180:183]\n"
"v_mfma_f32_16x16x32_bf16 v[172:175], v[148:151], v[218:221], v[172:175]\n"
"v_mfma_f32_16x16x32_bf16 v[164:167], v[148:151], v[216:219], v[164:167]\n"
"v_mfma_f32_16x16x32_bf16 v[240:243], v[116:119], v[218:221], v[240:243]\n"
"v_mfma_f32_16x16x32_bf16 v[244:247], v[116:119], v[216:219], v[244:247]\n"
"v_mfma_f32_16x16x32_bf16 v[176:179], v[56:59], v[218:221], v[176:179]\n"
"v_mfma_f32_16x16x32_bf16 v[184:187], v[56:59], v[216:219], v[184:187]\n"
"v_mfma_f32_16x16x32_bf16 v[160:163], v[136:139], v[218:221], v[160:163]\n"
"v_mfma_f32_16x16x32_bf16 v[168:171], v[136:139], v[216:219], v[168:171]\n"
"v_mfma_f32_16x16x32_bf16 v[180:183], v[60:63], v[218:221], v[180:183]\n"
"v_mfma_f32_16x16x32_bf16 v[188:191], v[60:63], v[216:219], v[188:191]\n"
"v_mfma_f32_16x16x32_bf16 v[164:167], v[140:143], v[218:221], v[164:167]\n"
"v_mfma_f32_16x16x32_bf16 v[172:175], v[140:143], v[216:219], v[172:175]\n"
"s_cmp_lg_u32 s7, 0xf0000000\n"
"s_cbranch_scc1 .Lat_k3\n"
"s_mov_b32 s36, 0\n"
"s_waitcnt vmcnt(0)\n"
".Lat_sp3:\n"
"v_readfirstlane_b32 s12, v195\n"
"s_add_u32 s36, s36, 1\n"
"s_cmp_eq_u32 s12, s13\n"
"s_cbranch_scc1 .Lat_k3\n"
"global_load_dwordx4 v[192:195], v214, s[14:15] sc1\n"
"s_cmp_lt_u32 s36, 1048576\n"
"s_waitcnt vmcnt(0)\n"
"s_cbranch_scc1 .Lat_sp3\n"
".Lat_k3:\n"
"s_barrier\n"
"s_cmp_lg_u32 s7, 0xf0000000\n"
"s_cbranch_scc1 .Lat_l3\n"
"buffer_load_dwordx4 v[76:79], v253, s[40:43], s69 offen offset:0 sc1\n"
"buffer_load_dwordx4 v[48:51], v251, s[40:43], s69 offen offset:0 sc1\n"
"buffer_load_dwordx4 v[128:131], v251, s[40:43], s69 offen offset:1024 sc1\n"
"buffer_load_dwordx4 v[52:55], v251, s[40:43], s69 offen offset:2048 sc1\n"
"buffer_load_dwordx4 v[132:135], v251, s[40:43], s69 offen offset:3072 sc1\n"
"s_waitcnt vmcnt(0)\n"
".Lat_l3:\n"
"v_mfma_f32_16x16x32_bf16 v[240:243], v[76:79], v[218:221], v[240:243]\n"
"global_store_dword v212, v209, s[14:15] sc1\n"
"v_mfma_f32_16x16x32_bf16 v[244:247], v[76:79], v[216:219], v[244:247]\n"
"v_mfma_f32_16x16x32_bf16 v[168:171], v[48:51], v[218:221], v[168:171]\n"
"v_mfma_f32_16x16x32_bf16 v[160:163], v[48:51], v[216:219], v[160:163]\n"
"v_mfma_f32_16x16x32_bf16 v[172:175], v[52:55], v[218:221], v[172:175]\n"
"v_mfma_f32_16x16x32_bf16 v[164:167], v[52:55], v[216:219], v[164:167]\n"
"s_nop 3\n"
"v_cvt_pk_bf16_f32 v240, v240, v241\n"
"v_cvt_pk_bf16_f32 v241, v242, v243\n"
"v_mfma_f32_16x16x32_bf16 v[184:187], v[128:131], v[218:221], v[184:187]\n"
"v_cvt_pk_bf16_f32 v242, v244, v245\n"
"v_cvt_pk_bf16_f32 v243, v246, v247\n"
"v_mfma_f32_16x16x32_bf16 v[176:179], v[128:131], v[216:219], v[176:179]\n"
"v_permlane16_swap_b32 v240, v242\n"
"v_permlane16_swap_b32 v241, v243\n"
"v_mfma_f32_16x16x32_bf16 v[188:191], v[132:135], v[218:221], v[188:191]\n"
"buffer_store_dwordx4 v[240:243], v236, s[44:47], s60 offen offset:256 sc1\n"
"v_mfma_f32_16x16x32_bf16 v[180:183], v[132:135], v[216:219], v[180:183]\n"
"v_cvt_pk_bf16_f32 v168, v168, v169\n"
"v_cvt_pk_bf16_f32 v169, v170, v171\n"
"v_cvt_pk_bf16_f32 v170, v172, v173\n"
"v_cvt_pk_bf16_f32 v171, v174, v175\n"
"v_cvt_pk_bf16_f32 v160, v160, v161\n"
"v_cvt_pk_bf16_f32 v161, v162, v163\n"
"v_cvt_pk_bf16_f32 v162, v164, v165\n"
"v_cvt_pk_bf16_f32 v163, v166, v167\n"
"v_permlane16_swap_b32 v168, v170\n"
"v_permlane16_swap_b32 v169, v171\n"
"v_permlane16_swap_b32 v160, v162\n"
"v_permlane16_swap_b32 v161, v163\n"
"buffer_store_dwordx4 v[168:171], v236, s[44:47], s9 offen offset:0 sc1\n"
"buffer_store_dwordx4 v[160:163], v236, s[44:47], s8 offen offset:0 sc1\n"
"v_cvt_pk_bf16_f32 v184, v184, v185\n"
"v_cvt_pk_bf16_f32 v185, v186, v187\n"
"v_cvt_pk_bf16_f32 v186, v188, v189\n"
"v_cvt_pk_bf16_f32 v187, v190, v191\n"
"v_cvt_pk_bf16_f32 v176, v176, v177\n"
"v_cvt_pk_bf16_f32 v177, v178, v179\n"
"v_cvt_pk_bf16_f32 v178, v180, v181\n"
"v_cvt_pk_bf16_f32 v179, v182, v183\n"
"v_permlane16_swap_b32 v184, v186\n"
"v_permlane16_swap_b32 v185, v187\n"
"v_permlane16_swap_b32 v176, v178\n"
"v_permlane16_swap_b32 v177, v179\n"
"buffer_store_dwordx4 v[184:187], v236, s[44:47], s11 offen offset:0 sc1\n"
"buffer_store_dwordx4 v[176:179], v236, s[44:47], s10 offen offset:0 sc1\n"
".Lat_fin:\n"
"s_branch .Lat_end\n"
".Lat_slowa:\n"
"s_mov_b32 s36, 0\n"
".Lat_pollsa:\n"
"global_load_dwordx4 v[192:195], v210, s[14:15] sc1\n"
"s_waitcnt vmcnt(0)\n"
"v_cmp_ne_u32 vcc, s13, v192\n"
"s_nop 1\n"
"s_mov_b32 s12, vcc_lo\n"
"v_cmp_ne_u32 vcc, s13, v193\n"
"s_nop 1\n"
"s_or_b32 s12, s12, vcc_lo\n"
"v_cmp_ne_u32 vcc, s13, v194\n"
"s_nop 1\n"
"s_or_b32 s12, s12, vcc_lo\n"
"v_cmp_ne_u32 vcc, s13, v195\n"
"s_nop 1\n"
"s_or_b32 s12, s12, vcc_lo\n"
"s_cmp_eq_u32 s12, 0\n"
"s_cbranch_scc1 .Lat_pdonesa\n"
"s_add_u32 s36, s36, 1\n"
"s_sleep 1\n"
"s_cmp_lt_u32 s36, 1048576\n"
"s_cbranch_scc1 .Lat_pollsa\n"
".Lat_pdonesa:\n"
"s_branch .Lat_oka\n"
".Lat_pace5:\n"
"s_sleep 1\n"
"s_sub_u32 s97, s97, 4\n"
"s_cmp_gt_i32 s97, 0\n"
"s_cbranch_scc1 .Lat_pace5\n"
"s_branch .Lat_pbk5\n"
".Lat_pace7:\n"
"s_sleep 1\n"
"s_sub_u32 s97, s97, 4\n"
"s_cmp_gt_i32 s97, 0\n"
"s_cbranch_scc1 .Lat_pace7\n"
"s_branch .Lat_pbk7\n"
".Lat_tr0:\n"
"s_call_b64 s[86:87], .Lat_rare4\n"
"s_branch .Lat_bk0\n"
".Lat_tr1:\n"
"s_call_b64 s[86:87], .Lat_rare1\n"
"s_branch .Lat_bk1\n"
".Lat_tr2:\n"
"s_call_b64 s[86:87], .Lat_rare3\n"
"s_branch .Lat_bk2\n"
".Lat_tr3:\n"
"s_call_b64 s[86:87], .Lat_rare0\n"
"s_branch .Lat_bk3\n"
".Lat_tr4:\n"
"s_call_b64 s[86:87], .Lat_rare1\n"
"s_branch .Lat_bk4\n"
".Lat_tr5:\n"
"s_call_b64 s[86:87], .Lat_rare2\n"
"s_branch .Lat_bk5\n"
".Lat_tr6:\n"
"s_call_b64 s[86:87], .Lat_rare3\n"
"s_branch .Lat_bk6\n"
".Lat_tr7:\n"
"s_call_b64 s[86:87], .Lat_rare3\n"
"s_branch .Lat_bk7\n"
".Lat_rare0:\n"
"s_nop 15\n"
"v_log_f32 v116, v204\n"
"v_log_f32 v117, v205\n"
"v_log_f32 v118, v206\n"
"v_log_f32 v119, v207\n"
"v_max3_f32 v112, v0, v1, v2\n"
"v_max3_f32 v112, v112, v3, v4\n"
"v_max3_f32 v112, v112, v5, v6\n"
"v_max3_f32 v112, v112, v7, v116\n"
"v_max3_f32 v113, v8, v9, v10\n"
"v_max3_f32 v113, v113, v11, v12\n"
"v_max3_f32 v113, v113, v13, v14\n"
"v_max3_f32 v113, v113, v15, v117\n"
"v_max3_f32 v114, v16, v17, v18\n"
"v_max3_f32 v114, v114, v19, v20\n"
"v_max3_f32 v114, v114, v21, v22\n"
"v_max3_f32 v114, v114, v23, v118\n"
"v_max3_f32 v115, v24, v25, v26\n"
"v_max3_f32 v115, v115, v27, v28\n"
"v_max3_f32 v115, v115, v29, v30\n"
"v_max3_f32 v115, v115, v31, v119\n"
"v_mov_b32 v116, v112\n"
"v_mov_b32 v117, v113\n"
"v_mov_b32 v118, v114\n"
"v_mov_b32 v119, v115\n"
"s_nop 1\n"
"v_permlane16_swap_b32 v112, v116\n"
"v_permlane16_swap_b32 v113, v117\n"
"v_permlane16_swap_b32 v114, v118\n"
"v_permlane16_swap_b32 v115, v119\n"
"s_nop 0\n"
"v_max_f32 v112, v112, v116\n"
"v_max_f32 v113, v113, v117\n"
"v_max_f32 v114, v114, v118\n"
"v_max_f32 v115, v115, v119\n"
"v_mov_b32 v116, v112\n"
"v_mov_b32 v117, v113\n"
"v_mov_b32 v118, v114\n"
"v_mov_b32 v119, v115\n"
"s_nop 1\n"
"v_permlane32_swap_b32 v112, v116\n"
"v_permlane32_swap_b32 v113, v117\n"
"v_permlane32_swap_b32 v114, v118\n"
"v_permlane32_swap_b32 v115, v119\n"
"s_nop 0\n"
"v_max_f32 v112, v112, v116\n"
"v_max_f32 v113, v113, v117\n"
"v_max_f32 v114, v114, v118\n"
"v_max_f32 v115, v115, v119\n"
"v_max_f32 v112, 0, v112\n"
"v_max_f32 v113, 0, v113\n"
"v_max_f32 v114, 0, v114\n"
"v_max_f32 v115, 0, v115\n"
"v_sub_f32 v116, 0, v112\n"
"v_sub_f32 v117, 0, v113\n"
"v_sub_f32 v118, 0, v114\n"
"v_sub_f32 v119, 0, v115\n"
"v_exp_f32 v120, v116\n"
"v_exp_f32 v121, v117\n"
"v_exp_f32 v122, v118\n"
"v_exp_f32 v123, v119\n"
"s_nop 0\n"
"v_mul_f32 v204, v204, v120\n"
"v_lshlrev_b32 v192, 16, v80\n"
"v_and_b32 v193, 0xffff0000, v80\n"
"v_mul_f32 v192, v192, v120\n"
"v_mul_f32 v193, v193, v120\n"
"v_cvt_pk_bf16_f32 v80, v192, v193\n"
"v_lshlrev_b32 v192, 16, v81\n"
"v_and_b32 v193, 0xffff0000, v81\n"
"v_mul_f32 v192, v192, v120\n"
"v_mul_f32 v193, v193, v120\n"
"v_cvt_pk_bf16_f32 v81, v192, v193\n"
"v_lshlrev_b32 v192, 16, v82\n"
"v_and_b32 v193, 0xffff0000, v82\n"
"v_mul_f32 v192, v192, v120\n"
"v_mul_f32 v193, v193, v120\n"
"v_cvt_pk_bf16_f32 v82, v192, v193\n"
"v_lshlrev_b32 v192, 16, v83\n"
"v_and_b32 v193, 0xffff0000, v83\n"
"v_mul_f32 v192, v192, v120\n"
"v_mul_f32 v193, v193, v120\n"
"v_cvt_pk_bf16_f32 v83, v192, v193\n"
"v_mul_f32 v205, v205, v121\n"
"v_lshlrev_b32 v192, 16, v84\n"
"v_and_b32 v193, 0xffff0000, v84\n"
"v_mul_f32 v192, v192, v121\n"
"v_mul_f32 v193, v193, v121\n"
"v_cvt_pk_bf16_f32 v84, v192, v193\n"
"v_lshlrev_b32 v192, 16, v85\n"
"v_and_b32 v193, 0xffff0000, v85\n"
"v_mul_f32 v192, v192, v121\n"
"v_mul_f32 v193, v193, v121\n"
"v_cvt_pk_bf16_f32 v85, v192, v193\n"
"v_lshlrev_b32 v192, 16, v86\n"
"v_and_b32 v193, 0xffff0000, v86\n"
"v_mul_f32 v192, v192, v121\n"
"v_mul_f32 v193, v193, v121\n"
"v_cvt_pk_bf16_f32 v86, v192, v193\n"
"v_lshlrev_b32 v192, 16, v87\n"
"v_and_b32 v193, 0xffff0000, v87\n"
"v_mul_f32 v192, v192, v121\n"
"v_mul_f32 v193, v193, v121\n"
"v_cvt_pk_bf16_f32 v87, v192, v193\n"
"v_mul_f32 v206, v206, v122\n"
"v_lshlrev_b32 v192, 16, v88\n"
"v_and_b32 v193, 0xffff0000, v88\n"
"v_mul_f32 v192, v192, v122\n"
"v_mul_f32 v193, v193, v122\n"
"v_cvt_pk_bf16_f32 v88, v192, v193\n"
"v_lshlrev_b32 v192, 16, v89\n"
"v_and_b32 v193, 0xffff0000, v89\n"
"v_mul_f32 v192, v192, v122\n"
"v_mul_f32 v193, v193, v122\n"
"v_cvt_pk_bf16_f32 v89, v192, v193\n"
"v_lshlrev_b32 v192, 16, v90\n"
"v_and_b32 v193, 0xffff0000, v90\n"
"v_mul_f32 v192, v192, v122\n"
"v_mul_f32 v193, v193, v122\n"
"v_cvt_pk_bf16_f32 v90, v192, v193\n"
"v_lshlrev_b32 v192, 16, v91\n"
"v_and_b32 v193, 0xffff0000, v91\n"
"v_mul_f32 v192, v192, v122\n"
"v_mul_f32 v193, v193, v122\n"
"v_cvt_pk_bf16_f32 v91, v192, v193\n"
"v_mul_f32 v207, v207, v123\n"
"v_lshlrev_b32 v192, 16, v92\n"
"v_and_b32 v193, 0xffff0000, v92\n"
"v_mul_f32 v192, v192, v123\n"
"v_mul_f32 v193, v193, v123\n"
"v_cvt_pk_bf16_f32 v92, v192, v193\n"
"v_lshlrev_b32 v192, 16, v93\n"
"v_and_b32 v193, 0xffff0000, v93\n"
"v_mul_f32 v192, v192, v123\n"
"v_mul_f32 v193, v193, v123\n"
"v_cvt_pk_bf16_f32 v93, v192, v193\n"
"v_lshlrev_b32 v192, 16, v94\n"
"v_and_b32 v193, 0xffff0000, v94\n"
"v_mul_f32 v192, v192, v123\n"
"v_mul_f32 v193, v193, v123\n"
"v_cvt_pk_bf16_f32 v94, v192, v193\n"
"v_lshlrev_b32 v192, 16, v95\n"
"v_and_b32 v193, 0xffff0000, v95\n"
"v_mul_f32 v192, v192, v123\n"
"v_mul_f32 v193, v193, v123\n"
"v_cvt_pk_bf16_f32 v95, v192, v193\n"
"v_lshrrev_b32 v116, 4, v251\n"
"ds_bpermute_b32 v120, v116, v120 offset:0\n"
"ds_bpermute_b32 v121, v116, v121 offset:0\n"
"ds_bpermute_b32 v122, v116, v122 offset:0\n"
"ds_bpermute_b32 v123, v116, v123 offset:0\n"
"v_accvgpr_read_b32 v192, a0\n"
"v_accvgpr_read_b32 v193, a1\n"
"v_accvgpr_read_b32 v194, a2\n"
"v_accvgpr_read_b32 v195, a3\n"
"v_accvgpr_read_b32 v196, a4\n"
"v_accvgpr_read_b32 v197, a5\n"
"v_accvgpr_read_b32 v198, a6\n"
"v_accvgpr_read_b32 v199, a7\n"
"s_waitcnt lgkmcnt(3)\n"
"v_mul_f32_dpp v192, v120, v192 row_newbcast:0 row_mask:0xf bank_mask:0xf\n"
"v_mul_f32_dpp v193, v120, v193 row_newbcast:4 row_mask:0xf bank_mask:0xf\n"
"v_mul_f32_dpp v194, v120, v194 row_newbcast:8 row_mask:0xf bank_mask:0xf\n"
"v_mul_f32_dpp v195, v120, v195 row_newbcast:12 row_mask:0xf bank_mask:0xf\n"
"v_mul_f32_dpp v196, v120, v196 row_newbcast:0 row_mask:0xf bank_mask:0xf\n"
"v_mul_f32_dpp v197, v120, v197 row_newbcast:4 row_mask:0xf bank_mask:0xf\n"
"v_mul_f32_dpp v198, v120, v198 row_newbcast:8 row_mask:0xf bank_mask:0xf\n"
"v_mul_f32_dpp v199, v120, v199 row_newbcast:12 row_mask:0xf bank_mask:0xf\n"
"v_accvgpr_write_b32 a0, v192\n"
"v_accvgpr_write_b32 a1, v193\n"
"v_accvgpr_write_b32 a2, v194\n"
"v_accvgpr_write_b32 a3, v195\n"
"v_accvgpr_write_b32 a4, v196\n"
"v_accvgpr_write_b32 a5, v197\n"
"v_accvgpr_write_b32 a6, v198\n"
"v_accvgpr_write_b32 a7, v199\n"
"v_accvgpr_read_b32 v192, a8\n"
"v_accvgpr_read_b32 v193, a9\n"
"v_accvgpr_read_b32 v194, a10\n"
"v_accvgpr_read_b32 v195, a11\n"
"v_accvgpr_read_b32 v196, a12\n"
"v_accvgpr_read_b32 v197, a13\n"
"v_accvgpr_read_b32 v198, a14\n"
"v_accvgpr_read_b32 v199, a15\n"
"v_mul_f32_dpp v192, v120, v192 row_newbcast:0 row_mask:0xf bank_mask:0xf\n"
"v_mul_f32_dpp v193, v120, v193 row_newbcast:4 row_mask:0xf bank_mask:0xf\n"
"v_mul_f32_dpp v194, v120, v194 row_newbcast:8 row_mask:0xf bank_mask:0xf\n"
"v_mul_f32_dpp v195, v120, v195 row_newbcast:12 row_mask:0xf bank_mask:0xf\n"
"v_mul_f32_dpp v196, v120, v196 row_newbcast:0 row_mask:0xf bank_mask:0xf\n"
"v_mul_f32_dpp v197, v120, v197 row_newbcast:4 row_mask:0xf bank_mask:0xf\n"
"v_mul_f32_dpp v198, v120, v198 row_newbcast:8 row_mask:0xf bank_mask:0xf\n"
"v_mul_f32_dpp v199, v120, v199 row_newbcast:12 row_mask:0xf bank_mask:0xf\n"
"v_accvgpr_write_b32 a8, v192\n"
"v_accvgpr_write_b32 a9, v193\n"
"v_accvgpr_write_b32 a10, v194\n"
"v_accvgpr_write_b32 a11, v195\n"
"v_accvgpr_write_b32 a12, v196\n"
"v_accvgpr_write_b32 a13, v197\n"
"v_accvgpr_write_b32 a14, v198\n"
"v_accvgpr_write_b32 a15, v199\n"
"v_accvgpr_read_b32 v192, a16\n"
"v_accvgpr_read_b32 v193, a17\n"
"v_accvgpr_read_b32 v194, a18\n"
"v_accvgpr_read_b32 v195, a19\n"
"v_accvgpr_read_b32 v196, a20\n"
"v_accvgpr_read_b32 v197, a21\n"
"v_accvgpr_read_b32 v198, a22\n"
"v_accvgpr_read_b32 v199, a23\n"
"v_mul_f32_dpp v192, v120, v192 row_newbcast:0 row_mask:0xf bank_mask:0xf\n"
"v_mul_f32_dpp v193, v120, v193 row_newbcast:4 row_mask:0xf bank_mask:0xf\n"
"v_mul_f32_dpp v194, v120, v194 row_newbcast:8 row_mask:0xf bank_mask:0xf\n"
"v_mul_f32_dpp v195, v120, v195 row_newbcast:12 row_mask:0xf bank_mask:0xf\n"
"v_mul_f32_dpp v196, v120, v196 row_newbcast:0 row_mask:0xf bank_mask:0xf\n"
"v_mul_f32_dpp v197, v120, v197 row_newbcast:4 row_mask:0xf bank_mask:0xf\n"
"v_mul_f32_dpp v198, v120, v198 row_newbcast:8 row_mask:0xf bank_mask:0xf\n"
"v_mul_f32_dpp v199, v120, v199 row_newbcast:12 row_mask:0xf bank_mask:0xf\n"
"v_accvgpr_write_b32 a16, v192\n"
"v_accvgpr_write_b32 a17, v193\n"
"v_accvgpr_write_b32 a18, v194\n"
"v_accvgpr_write_b32 a19, v195\n"
"v_accvgpr_write_b32 a20, v196\n"
"v_accvgpr_write_b32 a21, v197\n"
"v_accvgpr_write_b32 a22, v198\n"
"v_accvgpr_write_b32 a23, v199\n"
"v_accvgpr_read_b32 v192, a24\n"
"v_accvgpr_read_b32 v193, a25\n"
"v_accvgpr_read_b32 v194, a26\n"
"v_accvgpr_read_b32 v195, a27\n"
"v_accvgpr_read_b32 v196, a28\n"
"v_accvgpr_read_b32 v197, a29\n"
"v_accvgpr_read_b32 v198, a30\n"
"v_accvgpr_read_b32 v199, a31\n"
"v_mul_f32_dpp v192, v120, v192 row_newbcast:0 row_mask:0xf bank_mask:0xf\n"
"v_mul_f32_dpp v193, v120, v193 row_newbcast:4 row_mask:0xf bank_mask:0xf\n"
"v_mul_f32_dpp v194, v120, v194 row_newbcast:8 row_mask:0xf bank_mask:0xf\n"
"v_mul_f32_dpp v195, v120, v195 row_newbcast:12 row_mask:0xf bank_mask:0xf\n"
"v_mul_f32_dpp v196, v120, v196 row_newbcast:0 row_mask:0xf bank_mask:0xf\n"
"v_mul_f32_dpp v197, v120, v197 row_newbcast:4 row_mask:0xf bank_mask:0xf\n"
"v_mul_f32_dpp v198, v120, v198 row_newbcast:8 row_mask:0xf bank_mask:0xf\n"
"v_mul_f32_dpp v199, v120, v199 row_newbcast:12 row_mask:0xf bank_mask:0xf\n"
"v_accvgpr_write_b32 a24, v192\n"
"v_accvgpr_write_b32 a25, v193\n"
"v_accvgpr_write_b32 a26, v194\n"
"v_accvgpr_write_b32 a27, v195\n"
"v_accvgpr_write_b32 a28, v196\n"
"v_accvgpr_write_b32 a29, v197\n"
"v_accvgpr_write_b32 a30, v198\n"
"v_accvgpr_write_b32 a31, v199\n"
"v_accvgpr_read_b32 v192, a32\n"
"v_accvgpr_read_b32 v193, a33\n"
"v_accvgpr_read_b32 v194, a34\n"
"v_accvgpr_read_b32 v195, a35\n"
"v_accvgpr_read_b32 v196, a36\n"
"v_accvgpr_read_b32 v197, a37\n"
"v_accvgpr_read_b32 v198, a38\n"
"v_accvgpr_read_b32 v199, a39\n"
"v_mul_f32_dpp v192, v120, v192 row_newbcast:0 row_mask:0xf bank_mask:0xf\n"
"v_mul_f32_dpp v193, v120, v193 row_newbcast:4 row_mask:0xf bank_mask:0xf\n"
"v_mul_f32_dpp v194, v120, v194 row_newbcast:8 row_mask:0xf bank_mask:0xf\n"
"v_mul_f32_dpp v195, v120, v195 row_newbcast:12 row_mask:0xf bank_mask:0xf\n"
"v_mul_f32_dpp v196, v120, v196 row_newbcast:0 row_mask:0xf bank_mask:0xf\n"
"v_mul_f32_dpp v197, v120, v197 row_newbcast:4 row_mask:0xf bank_mask:0xf\n"
"v_mul_f32_dpp v198, v120, v198 row_newbcast:8 row_mask:0xf bank_mask:0xf\n"
"v_mul_f32_dpp v199, v120, v199 row_newbcast:12 row_mask:0xf bank_mask:0xf\n"
"v_accvgpr_write_b32 a32, v192\n"
"v_accvgpr_write_b32 a33, v193\n"
"v_accvgpr_write_b32 a34, v194\n"
"v_accvgpr_write_b32 a35, v195\n"
"v_accvgpr_write_b32 a36, v196\n"
"v_accvgpr_write_b32 a37, v197\n"
"v_accvgpr_write_b32 a38, v198\n"
"v_accvgpr_write_b32 a39, v199\n"
"v_accvgpr_read_b32 v192, a40\n"
"v_accvgpr_read_b32 v193, a41\n"
"v_accvgpr_read_b32 v194, a42\n"
"v_accvgpr_read_b32 v195, a43\n"
"v_accvgpr_read_b32 v196, a44\n"
"v_accvgpr_read_b32 v197, a45\n"
"v_accvgpr_read_b32 v198, a46\n"
"v_accvgpr_read_b32 v199, a47\n"
"v_mul_f32_dpp v192, v120, v192 row_newbcast:0 row_mask:0xf bank_mask:0xf\n"
"v_mul_f32_dpp v193, v120, v193 row_newbcast:4 row_mask:0xf bank_mask:0xf\n"
"v_mul_f32_dpp v194, v120, v194 row_newbcast:8 row_mask:0xf bank_mask:0xf\n"
"v_mul_f32_dpp v195, v120, v195 row_newbcast:12 row_mask:0xf bank_mask:0xf\n"
"v_mul_f32_dpp v196, v120, v196 row_newbcast:0 row_mask:0xf bank_mask:0xf\n"
"v_mul_f32_dpp v197, v120, v197 row_newbcast:4 row_mask:0xf bank_mask:0xf\n"
"v_mul_f32_dpp v198, v120, v198 row_newbcast:8 row_mask:0xf bank_mask:0xf\n"
"v_mul_f32_dpp v199, v120, v199 row_newbcast:12 row_mask:0xf bank_mask:0xf\n"
"v_accvgpr_write_b32 a40, v192\n"
"v_accvgpr_write_b32 a41, v193\n"
"v_accvgpr_write_b32 a42, v194\n"
"v_accvgpr_write_b32 a43, v195\n"
"v_accvgpr_write_b32 a44, v196\n"
"v_accvgpr_write_b32 a45, v197\n"
"v_accvgpr_write_b32 a46, v198\n"
"v_accvgpr_write_b32 a47, v199\n"
"v_accvgpr_read_b32 v192, a48\n"
"v_accvgpr_read_b32 v193, a49\n"
"v_accvgpr_read_b32 v194, a50\n"
"v_accvgpr_read_b32 v195, a51\n"
"v_accvgpr_read_b32 v196, a52\n"
"v_accvgpr_read_b32 v197, a53\n"
"v_accvgpr_read_b32 v198, a54\n"
"v_accvgpr_read_b32 v199, a55\n"
"s_waitcnt lgkmcnt(2)\n"
"v_mul_f32_dpp v192, v121, v192 row_newbcast:0 row_mask:0xf bank_mask:0xf\n"
"v_mul_f32_dpp v193, v121, v193 row_newbcast:4 row_mask:0xf bank_mask:0xf\n"
"v_mul_f32_dpp v194, v121, v194 row_newbcast:8 row_mask:0xf bank_mask:0xf\n"
"v_mul_f32_dpp v195, v121, v195 row_newbcast:12 row_mask:0xf bank_mask:0xf\n"
"v_mul_f32_dpp v196, v121, v196 row_newbcast:0 row_mask:0xf bank_mask:0xf\n"
"v_mul_f32_dpp v197, v121, v197 row_newbcast:4 row_mask:0xf bank_mask:0xf\n"
"v_mul_f32_dpp v198, v121, v198 row_newbcast:8 row_mask:0xf bank_mask:0xf\n"
"v_mul_f32_dpp v199, v121, v199 row_newbcast:12 row_mask:0xf bank_mask:0xf\n"
"v_accvgpr_write_b32 a48, v192\n"
"v_accvgpr_write_b32 a49, v193\n"
"v_accvgpr_write_b32 a50, v194\n"
"v_accvgpr_write_b32 a51, v195\n"
"v_accvgpr_write_b32 a52, v196\n"
"v_accvgpr_write_b32 a53, v197\n"
"v_accvgpr_write_b32 a54, v198\n"
"v_accvgpr_write_b32 a55, v199\n"
"v_accvgpr_read_b32 v192, a56\n"
"v_accvgpr_read_b32 v193, a57\n"
"v_accvgpr_read_b32 v194, a58\n"
"v_accvgpr_read_b32 v195, a59\n"
"v_accvgpr_read_b32 v196, a60\n"
"v_accvgpr_read_b32 v197, a61\n"
"v_accvgpr_read_b32 v198, a62\n"
"v_accvgpr_read_b32 v199, a63\n"
"v_mul_f32_dpp v192, v121, v192 row_newbcast:0 row_mask:0xf bank_mask:0xf\n"
"v_mul_f32_dpp v193, v121, v193 row_newbcast:4 row_mask:0xf bank_mask:0xf\n"
"v_mul_f32_dpp v194, v121, v194 row_newbcast:8 row_mask:0xf bank_mask:0xf\n"
"v_mul_f32_dpp v195, v121, v195 row_newbcast:12 row_mask:0xf bank_mask:0xf\n"
"v_mul_f32_dpp v196, v121, v196 row_newbcast:0 row_mask:0xf bank_mask:0xf\n"
"v_mul_f32_dpp v197, v121, v197 row_newbcast:4 row_mask:0xf bank_mask:0xf\n"
"v_mul_f32_dpp v198, v121, v198 row_newbcast:8 row_mask:0xf bank_mask:0xf\n"
"v_mul_f32_dpp v199, v121, v199 row_newbcast:12 row_mask:0xf bank_mask:0xf\n"
"v_accvgpr_write_b32 a56, v192\n"
"v_accvgpr_write_b32 a57, v193\n"
"v_accvgpr_write_b32 a58, v194\n"
"v_accvgpr_write_b32 a59, v195\n"
"v_accvgpr_write_b32 a60, v196\n"
"v_accvgpr_write_b32 a61, v197\n"
"v_accvgpr_write_b32 a62, v198\n"
"v_accvgpr_write_b32 a63, v199\n"
"v_accvgpr_read_b32 v192, a64\n"
"v_accvgpr_read_b32 v193, a65\n"
"v_accvgpr_read_b32 v194, a66\n"
"v_accvgpr_read_b32 v195, a67\n"
"v_accvgpr_read_b32 v196, a68\n"
"v_accvgpr_read_b32 v197, a69\n"
"v_accvgpr_read_b32 v198, a70\n"
"v_accvgpr_read_b32 v199, a71\n"
"v_mul_f32_dpp v192, v121, v192 row_newbcast:0 row_mask:0xf bank_mask:0xf\n"
"v_mul_f32_dpp v193, v121, v193 row_newbcast:4 row_mask:0xf bank_mask:0xf\n"
"v_mul_f32_dpp v194, v121, v194 row_newbcast:8 row_mask:0xf bank_mask:0xf\n"
"v_mul_f32_dpp v195, v121, v195 row_newbcast:12 row_mask:0xf bank_mask:0xf\n"
"v_mul_f32_dpp v196, v121, v196 row_newbcast:0 row_mask:0xf bank_mask:0xf\n"
"v_mul_f32_dpp v197, v121, v197 row_newbcast:4 row_mask:0xf bank_mask:0xf\n"
"v_mul_f32_dpp v198, v121, v198 row_newbcast:8 row_mask:0xf bank_mask:0xf\n"
"v_mul_f32_dpp v199, v121, v199 row_newbcast:12 row_mask:0xf bank_mask:0xf\n"
"v_accvgpr_write_b32 a64, v192\n"
"v_accvgpr_write_b32 a65, v193\n"
"v_accvgpr_write_b32 a66, v194\n"
"v_accvgpr_write_b32 a67, v195\n"
"v_accvgpr_write_b32 a68, v196\n"
"v_accvgpr_write_b32 a69, v197\n"
"v_accvgpr_write_b32 a70, v198\n"
"v_accvgpr_write_b32 a71, v199\n"
"v_accvgpr_read_b32 v192, a72\n"
"v_accvgpr_read_b32 v193, a73\n"
"v_accvgpr_read_b32 v194, a74\n"
"v_accvgpr_read_b32 v195, a75\n"
"v_accvgpr_read_b32 v196, a76\n"
"v_accvgpr_read_b32 v197, a77\n"
"v_accvgpr_read_b32 v198, a78\n"
"v_accvgpr_read_b32 v199, a79\n"
"v_mul_f32_dpp v192, v121, v192 row_newbcast:0 row_mask:0xf bank_mask:0xf\n"
"v_mul_f32_dpp v193, v121, v193 row_newbcast:4 row_mask:0xf bank_mask:0xf\n"
"v_mul_f32_dpp v194, v121, v194 row_newbcast:8 row_mask:0xf bank_mask:0xf\n"
"v_mul_f32_dpp v195, v121, v195 row_newbcast:12 row_mask:0xf bank_mask:0xf\n"
"v_mul_f32_dpp v196, v121, v196 row_newbcast:0 row_mask:0xf bank_mask:0xf\n"
"v_mul_f32_dpp v197, v121, v197 row_newbcast:4 row_mask:0xf bank_mask:0xf\n"
"v_mul_f32_dpp v198, v121, v198 row_newbcast:8 row_mask:0xf bank_mask:0xf\n"
"v_mul_f32_dpp v199, v121, v199 row_newbcast:12 row_mask:0xf bank_mask:0xf\n"
"v_accvgpr_write_b32 a72, v192\n"
"v_accvgpr_write_b32 a73, v193\n"
"v_accvgpr_write_b32 a74, v194\n"
"v_accvgpr_write_b32 a75, v195\n"
"v_accvgpr_write_b32 a76, v196\n"
"v_accvgpr_write_b32 a77, v197\n"
"v_accvgpr_write_b32 a78, v198\n"
"v_accvgpr_write_b32 a79, v199\n"
"v_accvgpr_read_b32 v192, a80\n"
"v_accvgpr_read_b32 v193, a81\n"
"v_accvgpr_read_b32 v194, a82\n"
"v_accvgpr_read_b32 v195, a83\n"
"v_accvgpr_read_b32 v196, a84\n"
"v_accvgpr_read_b32 v197, a85\n"
"v_accvgpr_read_b32 v198, a86\n"
"v_accvgpr_read_b32 v199, a87\n"
"v_mul_f32_dpp v192, v121, v192 row_newbcast:0 row_mask:0xf bank_mask:0xf\n"
"v_mul_f32_dpp v193, v121, v193 row_newbcast:4 row_mask:0xf bank_mask:0xf\n"
"v_mul_f32_dpp v194, v121, v194 row_newbcast:8 row_mask:0xf bank_mask:0xf\n"
"v_mul_f32_dpp v195, v121, v195 row_newbcast:12 row_mask:0xf bank_mask:0xf\n"
"v_mul_f32_dpp v196, v121, v196 row_newbcast:0 row_mask:0xf bank_mask:0xf\n"
"v_mul_f32_dpp v197, v121, v197 row_newbcast:4 row_mask:0xf bank_mask:0xf\n"
"v_mul_f32_dpp v198, v121, v198 row_newbcast:8 row_mask:0xf bank_mask:0xf\n"
"v_mul_f32_dpp v199, v121, v199 row_newbcast:12 row_mask:0xf bank_mask:0xf\n"
"v_accvgpr_write_b32 a80, v192\n"
"v_accvgpr_write_b32 a81, v193\n"
"v_accvgpr_write_b32 a82, v194\n"
"v_accvgpr_write_b32 a83, v195\n"
"v_accvgpr_write_b32 a84, v196\n"
"v_accvgpr_write_b32 a85, v197\n"
"v_accvgpr_write_b32 a86, v198\n"
"v_accvgpr_write_b32 a87, v199\n"
"v_accvgpr_read_b32 v192, a88\n"
"v_accvgpr_read_b32 v193, a89\n"
"v_accvgpr_read_b32 v194, a90\n"
"v_accvgpr_read_b32 v195, a91\n"
"v_accvgpr_read_b32 v196, a92\n"
"v_accvgpr_read_b32 v197, a93\n"
"v_accvgpr_read_b32 v198, a94\n"
"v_accvgpr_read_b32 v199, a95\n"
"v_mul_f32_dpp v192, v121, v192 row_newbcast:0 row_mask:0xf bank_mask:0xf\n"
"v_mul_f32_dpp v193, v121, v193 row_newbcast:4 row_mask:0xf bank_mask:0xf\n"
"v_mul_f32_dpp v194, v121, v194 row_newbcast:8 row_mask:0xf bank_mask:0xf\n"
"v_mul_f32_dpp v195, v121, v195 row_newbcast:12 row_mask:0xf bank_mask:0xf\n"
"v_mul_f32_dpp v196, v121, v196 row_newbcast:0 row_mask:0xf bank_mask:0xf\n"
"v_mul_f32_dpp v197, v121, v197 row_newbcast:4 row_mask:0xf bank_mask:0xf\n"
"v_mul_f32_dpp v198, v121, v198 row_newbcast:8 row_mask:0xf bank_mask:0xf\n"
"v_mul_f32_dpp v199, v121, v199 row_newbcast:12 row_mask:0xf bank_mask:0xf\n"
"v_accvgpr_write_b32 a88, v192\n"
"v_accvgpr_write_b32 a89, v193\n"
"v_accvgpr_write_b32 a90, v194\n"
"v_accvgpr_write_b32 a91, v195\n"
"v_accvgpr_write_b32 a92, v196\n"
"v_accvgpr_write_b32 a93, v197\n"
"v_accvgpr_write_b32 a94, v198\n"
"v_accvgpr_write_b32 a95, v199\n"
"v_accvgpr_read_b32 v192, a96\n"
"v_accvgpr_read_b32 v193, a97\n"
"v_accvgpr_read_b32 v194, a98\n"
"v_accvgpr_read_b32 v195, a99\n"
"v_accvgpr_read_b32 v196, a100\n"
"v_accvgpr_read_b32 v197, a101\n"
"v_accvgpr_read_b32 v198, a102\n"
"v_accvgpr_read_b32 v199, a103\n"
"s_waitcnt lgkmcnt(1)\n"
"v_mul_f32_dpp v192, v122, v192 row_newbcast:0 row_mask:0xf bank_mask:0xf\n"
"v_mul_f32_dpp v193, v122, v193 row_newbcast:4 row_mask:0xf bank_mask:0xf\n"
"v_mul_f32_dpp v194, v122, v194 row_newbcast:8 row_mask:0xf bank_mask:0xf\n"
"v_mul_f32_dpp v195, v122, v195 row_newbcast:12 row_mask:0xf bank_mask:0xf\n"
"v_mul_f32_dpp v196, v122, v196 row_newbcast:0 row_mask:0xf bank_mask:0xf\n"
"v_mul_f32_dpp v197, v122, v197 row_newbcast:4 row_mask:0xf bank_mask:0xf\n"
"v_mul_f32_dpp v198, v122, v198 row_newbcast:8 row_mask:0xf bank_mask:0xf\n"
"v_mul_f32_dpp v199, v122, v199 row_newbcast:12 row_mask:0xf bank_mask:0xf\n"
"v_accvgpr_write_b32 a96, v192\n"
"v_accvgpr_write_b32 a97, v193\n"
"v_accvgpr_write_b32 a98, v194\n"
"v_accvgpr_write_b32 a99, v195\n"
"v_accvgpr_write_b32 a100, v196\n"
"v_accvgpr_write_b32 a101, v197\n"
"v_accvgpr_write_b32 a102, v198\n"
"v_accvgpr_write_b32 a103, v199\n"
"v_accvgpr_read_b32 v192, a104\n"
"v_accvgpr_read_b32 v193, a105\n"
"v_accvgpr_read_b32 v194, a106\n"
"v_accvgpr_read_b32 v195, a107\n"
"v_accvgpr_read_b32 v196, a108\n"
"v_accvgpr_read_b32 v197, a109\n"
"v_accvgpr_read_b32 v198, a110\n"
"v_accvgpr_read_b32 v199, a111\n"
"v_mul_f32_dpp v192, v122, v192 row_newbcast:0 row_mask:0xf bank_mask:0xf\n"
"v_mul_f32_dpp v193, v122, v193 row_newbcast:4 row_mask:0xf bank_mask:0xf\n"
"v_mul_f32_dpp v194, v122, v194 row_newbcast:8 row_mask:0xf bank_mask:0xf\n"
"v_mul_f32_dpp v195, v122, v195 row_newbcast:12 row_mask:0xf bank_mask:0xf\n"
"v_mul_f32_dpp v196, v122, v196 row_newbcast:0 row_mask:0xf bank_mask:0xf\n"
"v_mul_f32_dpp v197, v122, v197 row_newbcast:4 row_mask:0xf bank_mask:0xf\n"
"v_mul_f32_dpp v198, v122, v198 row_newbcast:8 row_mask:0xf bank_mask:0xf\n"
"v_mul_f32_dpp v199, v122, v199 row_newbcast:12 row_mask:0xf bank_mask:0xf\n"
"v_accvgpr_write_b32 a104, v192\n"
"v_accvgpr_write_b32 a105, v193\n"
"v_accvgpr_write_b32 a106, v194\n"
"v_accvgpr_write_b32 a107, v195\n"
"v_accvgpr_write_b32 a108, v196\n"
"v_accvgpr_write_b32 a109, v197\n"
"v_accvgpr_write_b32 a110, v198\n"
"v_accvgpr_write_b32 a111, v199\n"
"v_accvgpr_read_b32 v192, a112\n"
"v_accvgpr_read_b32 v193, a113\n"
"v_accvgpr_read_b32 v194, a114\n"
"v_accvgpr_read_b32 v195, a115\n"
"v_accvgpr_read_b32 v196, a116\n"
"v_accvgpr_read_b32 v197, a117\n"
"v_accvgpr_read_b32 v198, a118\n"
"v_accvgpr_read_b32 v199, a119\n"
"v_mul_f32_dpp v192, v122, v192 row_newbcast:0 row_mask:0xf bank_mask:0xf\n"
"v_mul_f32_dpp v193, v122, v193 row_newbcast:4 row_mask:0xf bank_mask:0xf\n"
"v_mul_f32_dpp v194, v122, v194 row_newbcast:8 row_mask:0xf bank_mask:0xf\n"
"v_mul_f32_dpp v195, v122, v195 row_newbcast:12 row_mask:0xf bank_mask:0xf\n"
"v_mul_f32_dpp v196, v122, v196 row_newbcast:0 row_mask:0xf bank_mask:0xf\n"
"v_mul_f32_dpp v197, v122, v197 row_newbcast:4 row_mask:0xf bank_mask:0xf\n"
"v_mul_f32_dpp v198, v122, v198 row_newbcast:8 row_mask:0xf bank_mask:0xf\n"
"v_mul_f32_dpp v199, v122, v199 row_newbcast:12 row_mask:0xf bank_mask:0xf\n"
"v_accvgpr_write_b32 a112, v192\n"
"v_accvgpr_write_b32 a113, v193\n"
"v_accvgpr_write_b32 a114, v194\n"
"v_accvgpr_write_b32 a115, v195\n"
"v_accvgpr_write_b32 a116, v196\n"
"v_accvgpr_write_b32 a117, v197\n"
"v_accvgpr_write_b32 a118, v198\n"
"v_accvgpr_write_b32 a119, v199\n"
"v_accvgpr_read_b32 v192, a120\n"
"v_accvgpr_read_b32 v193, a121\n"
"v_accvgpr_read_b32 v194, a122\n"
"v_accvgpr_read_b32 v195, a123\n"
"v_accvgpr_read_b32 v196, a124\n"
"v_accvgpr_read_b32 v197, a125\n"
"v_accvgpr_read_b32 v198, a126\n"
"v_accvgpr_read_b32 v199, a127\n"
"v_mul_f32_dpp v192, v122, v192 row_newbcast:0 row_mask:0xf bank_mask:0xf\n"
"v_mul_f32_dpp v193, v122, v193 row_newbcast:4 row_mask:0xf bank_mask:0xf\n"
"v_mul_f32_dpp v194, v122, v194 row_newbcast:8 row_mask:0xf bank_mask:0xf\n"
"v_mul_f32_dpp v195, v122, v195 row_newbcast:12 row_mask:0xf bank_mask:0xf\n"
"v_mul_f32_dpp v196, v122, v196 row_newbcast:0 row_mask:0xf bank_mask:0xf\n"
"v_mul_f32_dpp v197, v122, v197 row_newbcast:4 row_mask:0xf bank_mask:0xf\n"
"v_mul_f32_dpp v198, v122, v198 row_newbcast:8 row_mask:0xf bank_mask:0xf\n"
"v_mul_f32_dpp v199, v122, v199 row_newbcast:12 row_mask:0xf bank_mask:0xf\n"
"v_accvgpr_write_b32 a120, v192\n"
"v_accvgpr_write_b32 a121, v193\n"
"v_accvgpr_write_b32 a122, v194\n"
"v_accvgpr_write_b32 a123, v195\n"
"v_accvgpr_write_b32 a124, v196\n"
"v_accvgpr_write_b32 a125, v197\n"
"v_accvgpr_write_b32 a126, v198\n"
"v_accvgpr_write_b32 a127, v199\n"
"v_accvgpr_read_b32 v192, a128\n"
"v_accvgpr_read_b32 v193, a129\n"
"v_accvgpr_read_b32 v194, a130\n"
"v_accvgpr_read_b32 v195, a131\n"
"v_accvgpr_read_b32 v196, a132\n"
"v_accvgpr_read_b32 v197, a133\n"
"v_accvgpr_read_b32 v198, a134\n"
"v_accvgpr_read_b32 v199, a135\n"
"v_mul_f32_dpp v192, v122, v192 row_newbcast:0 row_mask:0xf bank_mask:0xf\n"
"v_mul_f32_dpp v193, v122, v193 row_newbcast:4 row_mask:0xf bank_mask:0xf\n"
"v_mul_f32_dpp v194, v122, v194 row_newbcast:8 row_mask:0xf bank_mask:0xf\n"
"v_mul_f32_dpp v195, v122, v195 row_newbcast:12 row_mask:0xf bank_mask:0xf\n"
"v_mul_f32_dpp v196, v122, v196 row_newbcast:0 row_mask:0xf bank_mask:0xf\n"
"v_mul_f32_dpp v197, v122, v197 row_newbcast:4 row_mask:0xf bank_mask:0xf\n"
"v_mul_f32_dpp v198, v122, v198 row_newbcast:8 row_mask:0xf bank_mask:0xf\n"
"v_mul_f32_dpp v199, v122, v199 row_newbcast:12 row_mask:0xf bank_mask:0xf\n"
"v_accvgpr_write_b32 a128, v192\n"
"v_accvgpr_write_b32 a129, v193\n"
"v_accvgpr_write_b32 a130, v194\n"
"v_accvgpr_write_b32 a131, v195\n"
"v_accvgpr_write_b32 a132, v196\n"
"v_accvgpr_write_b32 a133, v197\n"
"v_accvgpr_write_b32 a134, v198\n"
"v_accvgpr_write_b32 a135, v199\n"
"v_accvgpr_read_b32 v192, a136\n"
"v_accvgpr_read_b32 v193, a137\n"
"v_accvgpr_read_b32 v194, a138\n"
"v_accvgpr_read_b32 v195, a139\n"
"v_accvgpr_read_b32 v196, a140\n"
"v_accvgpr_read_b32 v197, a141\n"
"v_accvgpr_read_b32 v198, a142\n"
"v_accvgpr_read_b32 v199, a143\n"
"v_mul_f32_dpp v192, v122, v192 row_newbcast:0 row_mask:0xf bank_mask:0xf\n"
"v_mul_f32_dpp v193, v122, v193 row_newbcast:4 row_mask:0xf bank_mask:0xf\n"
"v_mul_f32_dpp v194, v122, v194 row_newbcast:8 row_mask:0xf bank_mask:0xf\n"
"v_mul_f32_dpp v195, v122, v195 row_newbcast:12 row_mask:0xf bank_mask:0xf\n"
"v_mul_f32_dpp v196, v122, v196 row_newbcast:0 row_mask:0xf bank_mask:0xf\n"
"v_mul_f32_dpp v197, v122, v197 row_newbcast:4 row_mask:0xf bank_mask:0xf\n"
"v_mul_f32_dpp v198, v122, v198 row_newbcast:8 row_mask:0xf bank_mask:0xf\n"
"v_mul_f32_dpp v199, v122, v199 row_newbcast:12 row_mask:0xf bank_mask:0xf\n"
"v_accvgpr_write_b32 a136, v192\n"
"v_accvgpr_write_b32 a137, v193\n"
"v_accvgpr_write_b32 a138, v194\n"
"v_accvgpr_write_b32 a139, v195\n"
"v_accvgpr_write_b32 a140, v196\n"
"v_accvgpr_write_b32 a141, v197\n"
"v_accvgpr_write_b32 a142, v198\n"
"v_accvgpr_write_b32 a143, v199\n"
"v_accvgpr_read_b32 v192, a144\n"
"v_accvgpr_read_b32 v193, a145\n"
"v_accvgpr_read_b32 v194, a146\n"
"v_accvgpr_read_b32 v195, a147\n"
"v_accvgpr_read_b32 v196, a148\n"
"v_accvgpr_read_b32 v197, a149\n"
"v_accvgpr_read_b32 v198, a150\n"
"v_accvgpr_read_b32 v199, a151\n"
"s_waitcnt lgkmcnt(0)\n"
"v_mul_f32_dpp v192, v123, v192 row_newbcast:0 row_mask:0xf bank_mask:0xf\n"
"v_mul_f32_dpp v193, v123, v193 row_newbcast:4 row_mask:0xf bank_mask:0xf\n"
"v_mul_f32_dpp v194, v123, v194 row_newbcast:8 row_mask:0xf bank_mask:0xf\n"
"v_mul_f32_dpp v195, v123, v195 row_newbcast:12 row_mask:0xf bank_mask:0xf\n"
"v_mul_f32_dpp v196, v123, v196 row_newbcast:0 row_mask:0xf bank_mask:0xf\n"
"v_mul_f32_dpp v197, v123, v197 row_newbcast:4 row_mask:0xf bank_mask:0xf\n"
"v_mul_f32_dpp v198, v123, v198 row_newbcast:8 row_mask:0xf bank_mask:0xf\n"
"v_mul_f32_dpp v199, v123, v199 row_newbcast:12 row_mask:0xf bank_mask:0xf\n"
"v_accvgpr_write_b32 a144, v192\n"
"v_accvgpr_write_b32 a145, v193\n"
"v_accvgpr_write_b32 a146, v194\n"
"v_accvgpr_write_b32 a147, v195\n"
"v_accvgpr_write_b32 a148, v196\n"
"v_accvgpr_write_b32 a149, v197\n"
"v_accvgpr_write_b32 a150, v198\n"
"v_accvgpr_write_b32 a151, v199\n"
"v_accvgpr_read_b32 v192, a152\n"
"v_accvgpr_read_b32 v193, a153\n"
"v_accvgpr_read_b32 v194, a154\n"
"v_accvgpr_read_b32 v195, a155\n"
"v_accvgpr_read_b32 v196, a156\n"
"v_accvgpr_read_b32 v197, a157\n"
"v_accvgpr_read_b32 v198, a158\n"
"v_accvgpr_read_b32 v199, a159\n"
"v_mul_f32_dpp v192, v123, v192 row_newbcast:0 row_mask:0xf bank_mask:0xf\n"
"v_mul_f32_dpp v193, v123, v193 row_newbcast:4 row_mask:0xf bank_mask:0xf\n"
"v_mul_f32_dpp v194, v123, v194 row_newbcast:8 row_mask:0xf bank_mask:0xf\n"
"v_mul_f32_dpp v195, v123, v195 row_newbcast:12 row_mask:0xf bank_mask:0xf\n"
"v_mul_f32_dpp v196, v123, v196 row_newbcast:0 row_mask:0xf bank_mask:0xf\n"
"v_mul_f32_dpp v197, v123, v197 row_newbcast:4 row_mask:0xf bank_mask:0xf\n"
"v_mul_f32_dpp v198, v123, v198 row_newbcast:8 row_mask:0xf bank_mask:0xf\n"
"v_mul_f32_dpp v199, v123, v199 row_newbcast:12 row_mask:0xf bank_mask:0xf\n"
"v_accvgpr_write_b32 a152, v192\n"
"v_accvgpr_write_b32 a153, v193\n"
"v_accvgpr_write_b32 a154, v194\n"
"v_accvgpr_write_b32 a155, v195\n"
"v_accvgpr_write_b32 a156, v196\n"
"v_accvgpr_write_b32 a157, v197\n"
"v_accvgpr_write_b32 a158, v198\n"
"v_accvgpr_write_b32 a159, v199\n"
"v_accvgpr_read_b32 v192, a160\n"
"v_accvgpr_read_b32 v193, a161\n"
"v_accvgpr_read_b32 v194, a162\n"
"v_accvgpr_read_b32 v195, a163\n"
"v_accvgpr_read_b32 v196, a164\n"
"v_accvgpr_read_b32 v197, a165\n"
"v_accvgpr_read_b32 v198, a166\n"
"v_accvgpr_read_b32 v199, a167\n"
"v_mul_f32_dpp v192, v123, v192 row_newbcast:0 row_mask:0xf bank_mask:0xf\n"
"v_mul_f32_dpp v193, v123, v193 row_newbcast:4 row_mask:0xf bank_mask:0xf\n"
"v_mul_f32_dpp v194, v123, v194 row_newbcast:8 row_mask:0xf bank_mask:0xf\n"
"v_mul_f32_dpp v195, v123, v195 row_newbcast:12 row_mask:0xf bank_mask:0xf\n"
"v_mul_f32_dpp v196, v123, v196 row_newbcast:0 row_mask:0xf bank_mask:0xf\n"
"v_mul_f32_dpp v197, v123, v197 row_newbcast:4 row_mask:0xf bank_mask:0xf\n"
"v_mul_f32_dpp v198, v123, v198 row_newbcast:8 row_mask:0xf bank_mask:0xf\n"
"v_mul_f32_dpp v199, v123, v199 row_newbcast:12 row_mask:0xf bank_mask:0xf\n"
"v_accvgpr_write_b32 a160, v192\n"
"v_accvgpr_write_b32 a161, v193\n"
"v_accvgpr_write_b32 a162, v194\n"
"v_accvgpr_write_b32 a163, v195\n"
"v_accvgpr_write_b32 a164, v196\n"
"v_accvgpr_write_b32 a165, v197\n"
"v_accvgpr_write_b32 a166, v198\n"
"v_accvgpr_write_b32 a167, v199\n"
"v_accvgpr_read_b32 v192, a168\n"
"v_accvgpr_read_b32 v193, a169\n"
"v_accvgpr_read_b32 v194, a170\n"
"v_accvgpr_read_b32 v195, a171\n"
"v_accvgpr_read_b32 v196, a172\n"
"v_accvgpr_read_b32 v197, a173\n"
"v_accvgpr_read_b32 v198, a174\n"
"v_accvgpr_read_b32 v199, a175\n"
"v_mul_f32_dpp v192, v123, v192 row_newbcast:0 row_mask:0xf bank_mask:0xf\n"
"v_mul_f32_dpp v193, v123, v193 row_newbcast:4 row_mask:0xf bank_mask:0xf\n"
"v_mul_f32_dpp v194, v123, v194 row_newbcast:8 row_mask:0xf bank_mask:0xf\n"
"v_mul_f32_dpp v195, v123, v195 row_newbcast:12 row_mask:0xf bank_mask:0xf\n"
"v_mul_f32_dpp v196, v123, v196 row_newbcast:0 row_mask:0xf bank_mask:0xf\n"
"v_mul_f32_dpp v197, v123, v197 row_newbcast:4 row_mask:0xf bank_mask:0xf\n"
"v_mul_f32_dpp v198, v123, v198 row_newbcast:8 row_mask:0xf bank_mask:0xf\n"
"v_mul_f32_dpp v199, v123, v199 row_newbcast:12 row_mask:0xf bank_mask:0xf\n"
"v_accvgpr_write_b32 a168, v192\n"
"v_accvgpr_write_b32 a169, v193\n"
"v_accvgpr_write_b32 a170, v194\n"
"v_accvgpr_write_b32 a171, v195\n"
"v_accvgpr_write_b32 a172, v196\n"
"v_accvgpr_write_b32 a173, v197\n"
"v_accvgpr_write_b32 a174, v198\n"
"v_accvgpr_write_b32 a175, v199\n"
"v_accvgpr_read_b32 v192, a176\n"
"v_accvgpr_read_b32 v193, a177\n"
"v_accvgpr_read_b32 v194, a178\n"
"v_accvgpr_read_b32 v195, a179\n"
"v_accvgpr_read_b32 v196, a180\n"
"v_accvgpr_read_b32 v197, a181\n"
"v_accvgpr_read_b32 v198, a182\n"
"v_accvgpr_read_b32 v199, a183\n"
"v_mul_f32_dpp v192, v123, v192 row_newbcast:0 row_mask:0xf bank_mask:0xf\n"
"v_mul_f32_dpp v193, v123, v193 row_newbcast:4 row_mask:0xf bank_mask:0xf\n"
"v_mul_f32_dpp v194, v123, v194 row_newbcast:8 row_mask:0xf bank_mask:0xf\n"
"v_mul_f32_dpp v195, v123, v195 row_newbcast:12 row_mask:0xf bank_mask:0xf\n"
"v_mul_f32_dpp v196, v123, v196 row_newbcast:0 row_mask:0xf bank_mask:0xf\n"
"v_mul_f32_dpp v197, v123, v197 row_newbcast:4 row_mask:0xf bank_mask:0xf\n"
"v_mul_f32_dpp v198, v123, v198 row_newbcast:8 row_mask:0xf bank_mask:0xf\n"
"v_mul_f32_dpp v199, v123, v199 row_newbcast:12 row_mask:0xf bank_mask:0xf\n"
"v_accvgpr_write_b32 a176, v192\n"
"v_accvgpr_write_b32 a177, v193\n"
"v_accvgpr_write_b32 a178, v194\n"
"v_accvgpr_write_b32 a179, v195\n"
"v_accvgpr_write_b32 a180, v196\n"
"v_accvgpr_write_b32 a181, v197\n"
"v_accvgpr_write_b32 a182, v198\n"
"v_accvgpr_write_b32 a183, v199\n"
"v_accvgpr_read_b32 v192, a184\n"
"v_accvgpr_read_b32 v193, a185\n"
"v_accvgpr_read_b32 v194, a186\n"
"v_accvgpr_read_b32 v195, a187\n"
"v_accvgpr_read_b32 v196, a188\n"
"v_accvgpr_read_b32 v197, a189\n"
"v_accvgpr_read_b32 v198, a190\n"
"v_accvgpr_read_b32 v199, a191\n"
"v_mul_f32_dpp v192, v123, v192 row_newbcast:0 row_mask:0xf bank_mask:0xf\n"
"v_mul_f32_dpp v193, v123, v193 row_newbcast:4 row_mask:0xf bank_mask:0xf\n"
"v_mul_f32_dpp v194, v123, v194 row_newbcast:8 row_mask:0xf bank_mask:0xf\n"
"v_mul_f32_dpp v195, v123, v195 row_newbcast:12 row_mask:0xf bank_mask:0xf\n"
"v_mul_f32_dpp v196, v123, v196 row_newbcast:0 row_mask:0xf bank_mask:0xf\n"
"v_mul_f32_dpp v197, v123, v197 row_newbcast:4 row_mask:0xf bank_mask:0xf\n"
"v_mul_f32_dpp v198, v123, v198 row_newbcast:8 row_mask:0xf bank_mask:0xf\n"
"v_mul_f32_dpp v199, v123, v199 row_newbcast:12 row_mask:0xf bank_mask:0xf\n"
"v_accvgpr_write_b32 a184, v192\n"
"v_accvgpr_write_b32 a185, v193\n"
"v_accvgpr_write_b32 a186, v194\n"
"v_accvgpr_write_b32 a187, v195\n"
"v_accvgpr_write_b32 a188, v196\n"
"v_accvgpr_write_b32 a189, v197\n"
"v_accvgpr_write_b32 a190, v198\n"
"v_accvgpr_write_b32 a191, v199\n"
"v_sub_f32 v0, v0, v112\n"
"v_sub_f32 v1, v1, v112\n"
"v_sub_f32 v2, v2, v112\n"
"v_sub_f32 v3, v3, v112\n"
"v_sub_f32 v4, v4, v112\n"
"v_sub_f32 v5, v5, v112\n"
"v_sub_f32 v6, v6, v112\n"
"v_sub_f32 v7, v7, v112\n"
"v_sub_f32 v32, v32, v112\n"
"v_sub_f32 v33, v33, v112\n"
"v_sub_f32 v34, v34, v112\n"
"v_sub_f32 v35, v35, v112\n"
"v_sub_f32 v36, v36, v112\n"
"v_sub_f32 v37, v37, v112\n"
"v_sub_f32 v38, v38, v112\n"
"v_sub_f32 v39, v39, v112\n"
"v_sub_f32 v64, v64, v112\n"
"v_sub_f32 v65, v65, v112\n"
"v_sub_f32 v66, v66, v112\n"
"v_sub_f32 v67, v67, v112\n"
"v_sub_f32 v8, v8, v113\n"
"v_sub_f32 v9, v9, v113\n"
"v_sub_f32 v10, v10, v113\n"
"v_sub_f32 v11, v11, v113\n"
"v_sub_f32 v12, v12, v113\n"
"v_sub_f32 v13, v13, v113\n"
"v_sub_f32 v14, v14, v113\n"
"v_sub_f32 v15, v15, v113\n"
"v_sub_f32 v40, v40, v113\n"
"v_sub_f32 v41, v41, v113\n"
"v_sub_f32 v42, v42, v113\n"
"v_sub_f32 v43, v43, v113\n"
"v_sub_f32 v44, v44, v113\n"
"v_sub_f32 v45, v45, v113\n"
"v_sub_f32 v46, v46, v113\n"
"v_sub_f32 v47, v47, v113\n"
"v_sub_f32 v68, v68, v113\n"
"v_sub_f32 v69, v69, v113\n"
"v_sub_f32 v70, v70, v113\n"
"v_sub_f32 v71, v71, v113\n"
"v_sub_f32 v16, v16, v114\n"
"v_sub_f32 v17, v17, v114\n"
"v_sub_f32 v18, v18, v114\n"
"v_sub_f32 v19, v19, v114\n"
"v_sub_f32 v20, v20, v114\n"
"v_sub_f32 v21, v21, v114\n"
"v_sub_f32 v22, v22, v114\n"
"v_sub_f32 v23, v23, v114\n"
"v_sub_f32 v48, v48, v114\n"
"v_sub_f32 v49, v49, v114\n"
"v_sub_f32 v50, v50, v114\n"
"v_sub_f32 v51, v51, v114\n"
"v_sub_f32 v52, v52, v114\n"
"v_sub_f32 v53, v53, v114\n"
"v_sub_f32 v54, v54, v114\n"
"v_sub_f32 v55, v55, v114\n"
"v_sub_f32 v72, v72, v114\n"
"v_sub_f32 v73, v73, v114\n"
"v_sub_f32 v74, v74, v114\n"
"v_sub_f32 v75, v75, v114\n"
"v_sub_f32 v24, v24, v115\n"
"v_sub_f32 v25, v25, v115\n"
"v_sub_f32 v26, v26, v115\n"
"v_sub_f32 v27, v27, v115\n"
"v_sub_f32 v28, v28, v115\n"
"v_sub_f32 v29, v29, v115\n"
"v_sub_f32 v30, v30, v115\n"
"v_sub_f32 v31, v31, v115\n"
"v_sub_f32 v56, v56, v115\n"
"v_sub_f32 v57, v57, v115\n"
"v_sub_f32 v58, v58, v115\n"
"v_sub_f32 v59, v59, v115\n"
"v_sub_f32 v60, v60, v115\n"
"v_sub_f32 v61, v61, v115\n"
"v_sub_f32 v62, v62, v115\n"
"v_sub_f32 v63, v63, v115\n"
"v_sub_f32 v76, v76, v115\n"
"v_sub_f32 v77, v77, v115\n"
"v_sub_f32 v78, v78, v115\n"
"v_sub_f32 v79, v79, v115\n"
"v_exp_f32 v192, v0\n"
"s_nop 0\n"
"v_add_f32 v200, v204, v192\n"
"v_exp_f32 v193, v1\n"
"s_nop 0\n"
"v_add_f32 v200, v200, v193\n"
"v_exp_f32 v194, v2\n"
"v_cvt_pk_bf16_f32 v96, v192, v193\n"
"v_exp_f32 v195, v3\n"
"v_add_f32 v200, v200, v194\n"
"v_exp_f32 v196, v4\n"
"v_add_f32 v200, v200, v195\n"
"v_exp_f32 v197, v5\n"
"v_cvt_pk_bf16_f32 v97, v194, v195\n"
"v_exp_f32 v198, v6\n"
"v_add_f32 v200, v200, v196\n"
"v_add_f32 v200, v200, v197\n"
"v_cvt_pk_bf16_f32 v98, v196, v197\n"
"v_add_f32 v200, v200, v198\n"
"v_exp_f32 v199, v7\n"
"s_nop 0\n"
"v_add_f32 v200, v200, v199\n"
"v_exp_f32 v192, v8\n"
"v_cvt_pk_bf16_f32 v99, v198, v199\n"
"v_exp_f32 v193, v9\n"
"v_add_f32 v201, v205, v192\n"
"v_add_f32 v201, v201, v193\n"
"v_exp_f32 v194, v10\n"
"v_exp_f32 v195, v11\n"
"v_cvt_pk_bf16_f32 v100, v192, v193\n"
"v_exp_f32 v196, v12\n"
"v_add_f32 v201, v201, v194\n"
"v_add_f32 v201, v201, v195\n"
"v_exp_f32 v197, v13\n"
"v_cvt_pk_bf16_f32 v101, v194, v195\n"
"v_exp_f32 v198, v14\n"
"v_exp_f32 v199, v15\n"
"v_add_f32 v201, v201, v196\n"
"v_add_f32 v201, v201, v197\n"
"v_cvt_pk_bf16_f32 v102, v196, v197\n"
"v_exp_f32 v192, v16\n"
"v_add_f32 v201, v201, v198\n"
"v_exp_f32 v193, v17\n"
"v_exp_f32 v194, v18\n"
"v_add_f32 v201, v201, v199\n"
"v_cvt_pk_bf16_f32 v103, v198, v199\n"
"v_add_f32 v202, v206, v192\n"
"v_exp_f32 v195, v19\n"
"v_exp_f32 v196, v20\n"
"v_add_f32 v202, v202, v193\n"
"v_exp_f32 v197, v21\n"
"v_cvt_pk_bf16_f32 v104, v192, v193\n"
"v_add_f32 v202, v202, v194\n"
"v_add_f32 v202, v202, v195\n"
"v_exp_f32 v198, v22\n"
"v_exp_f32 v199, v23\n"
"v_cvt_pk_bf16_f32 v105, v194, v195\n"
"v_exp_f32 v192, v24\n"
"v_add_f32 v202, v202, v196\n"
"v_add_f32 v202, v202, v197\n"
"v_exp_f32 v193, v25\n"
"v_cvt_pk_bf16_f32 v106, v196, v197\n"
"v_exp_f32 v194, v26\n"
"v_exp_f32 v195, v27\n"
"v_add_f32 v202, v202, v198\n"
"v_add_f32 v202, v202, v199\n"
"v_cvt_pk_bf16_f32 v107, v198, v199\n"
"v_exp_f32 v196, v28\n"
"v_add_f32 v203, v207, v192\n"
"v_exp_f32 v197, v29\n"
"v_exp_f32 v198, v30\n"
"v_add_f32 v203, v203, v193\n"
"v_cvt_pk_bf16_f32 v108, v192, v193\n"
"v_exp_f32 v199, v31\n"
"v_add_f32 v203, v203, v194\n"
"v_add_f32 v203, v203, v195\n"
"v_cvt_pk_bf16_f32 v109, v194, v195\n"
"v_add_f32 v203, v203, v196\n"
"v_add_f32 v203, v203, v197\n"
"v_cvt_pk_bf16_f32 v110, v196, v197\n"
"v_add_f32 v203, v203, v198\n"
"v_add_f32 v203, v203, v199\n"
"v_cvt_pk_bf16_f32 v111, v198, v199\n"
"s_nop 2\n"
"s_setpc_b64 s[86:87]\n"
".Lat_rare1:\n"
"s_nop 15\n"
"v_log_f32 v116, v200\n"
"v_log_f32 v117, v201\n"
"v_log_f32 v118, v202\n"
"v_log_f32 v119, v203\n"
"v_max3_f32 v112, v32, v33, v34\n"
"v_max3_f32 v112, v112, v35, v36\n"
"v_max3_f32 v112, v112, v37, v38\n"
"v_max3_f32 v112, v112, v39, v116\n"
"v_max3_f32 v113, v40, v41, v42\n"
"v_max3_f32 v113, v113, v43, v44\n"
"v_max3_f32 v113, v113, v45, v46\n"
"v_max3_f32 v113, v113, v47, v117\n"
"v_max3_f32 v114, v48, v49, v50\n"
"v_max3_f32 v114, v114, v51, v52\n"
"v_max3_f32 v114, v114, v53, v54\n"
"v_max3_f32 v114, v114, v55, v118\n"
"v_max3_f32 v115, v56, v57, v58\n"
"v_max3_f32 v115, v115, v59, v60\n"
"v_max3_f32 v115, v115, v61, v62\n"
"v_max3_f32 v115, v115, v63, v119\n"
"v_mov_b32 v116, v112\n"
"v_mov_b32 v117, v113\n"
"v_mov_b32 v118, v114\n"
"v_mov_b32 v119, v115\n"
"s_nop 1\n"
"v_permlane16_swap_b32 v112, v116\n"
"v_permlane16_swap_b32 v113, v117\n"
"v_permlane16_swap_b32 v114, v118\n"
"v_permlane16_swap_b32 v115, v119\n"
"s_nop 0\n"
"v_max_f32 v112, v112, v116\n"
"v_max_f32 v113, v113, v117\n"
"v_max_f32 v114, v114, v118\n"
"v_max_f32 v115, v115, v119\n"
"v_mov_b32 v116, v112\n"
"v_mov_b32 v117, v113\n"
"v_mov_b32 v118, v114\n"
"v_mov_b32 v119, v115\n"
"s_nop 1\n"
"v_permlane32_swap_b32 v112, v116\n"
"v_permlane32_swap_b32 v113, v117\n"
"v_permlane32_swap_b32 v114, v118\n"
"v_permlane32_swap_b32 v115, v119\n"
"s_nop 0\n"
"v_max_f32 v112, v112, v116\n"
"v_max_f32 v113, v113, v117\n"
"v_max_f32 v114, v114, v118\n"
"v_max_f32 v115, v115, v119\n"
"v_max_f32 v112, 0, v112\n"
"v_max_f32 v113, 0, v113\n"
"v_max_f32 v114, 0, v114\n"
"v_max_f32 v115, 0, v115\n"
"v_sub_f32 v116, 0, v112\n"
"v_sub_f32 v117, 0, v113\n"
"v_sub_f32 v118, 0, v114\n"
"v_sub_f32 v119, 0, v115\n"
"v_exp_f32 v120, v116\n"
"v_exp_f32 v121, v117\n"
"v_exp_f32 v122, v118\n"
"v_exp_f32 v123, v119\n"
"s_nop 0\n"
"v_mul_f32 v200, v200, v120\n"
"v_lshlrev_b32 v192, 16, v96\n"
"v_and_b32 v193, 0xffff0000, v96\n"
"v_mul_f32 v192, v192, v120\n"
"v_mul_f32 v193, v193, v120\n"
"v_cvt_pk_bf16_f32 v96, v192, v193\n"
"v_lshlrev_b32 v192, 16, v97\n"
"v_and_b32 v193, 0xffff0000, v97\n"
"v_mul_f32 v192, v192, v120\n"
"v_mul_f32 v193, v193, v120\n"
"v_cvt_pk_bf16_f32 v97, v192, v193\n"
"v_lshlrev_b32 v192, 16, v98\n"
"v_and_b32 v193, 0xffff0000, v98\n"
"v_mul_f32 v192, v192, v120\n"
"v_mul_f32 v193, v193, v120\n"
"v_cvt_pk_bf16_f32 v98, v192, v193\n"
"v_lshlrev_b32 v192, 16, v99\n"
"v_and_b32 v193, 0xffff0000, v99\n"
"v_mul_f32 v192, v192, v120\n"
"v_mul_f32 v193, v193, v120\n"
"v_cvt_pk_bf16_f32 v99, v192, v193\n"
"v_lshlrev_b32 v192, 16, v80\n"
"v_and_b32 v193, 0xffff0000, v80\n"
"v_mul_f32 v192, v192, v120\n"
"v_mul_f32 v193, v193, v120\n"
"v_cvt_pk_bf16_f32 v80, v192, v193\n"
"v_lshlrev_b32 v192, 16, v81\n"
"v_and_b32 v193, 0xffff0000, v81\n"
"v_mul_f32 v192, v192, v120\n"
"v_mul_f32 v193, v193, v120\n"
"v_cvt_pk_bf16_f32 v81, v192, v193\n"
"v_lshlrev_b32 v192, 16, v82\n"
"v_and_b32 v193, 0xffff0000, v82\n"
"v_mul_f32 v192, v192, v120\n"
"v_mul_f32 v193, v193, v120\n"
"v_cvt_pk_bf16_f32 v82, v192, v193\n"
"v_lshlrev_b32 v192, 16, v83\n"
"v_and_b32 v193, 0xffff0000, v83\n"
"v_mul_f32 v192, v192, v120\n"
"v_mul_f32 v193, v193, v120\n"
"v_cvt_pk_bf16_f32 v83, v192, v193\n"
"v_mul_f32 v201, v201, v121\n"
"v_lshlrev_b32 v192, 16, v100\n"
"v_and_b32 v193, 0xffff0000, v100\n"
"v_mul_f32 v192, v192, v121\n"
"v_mul_f32 v193, v193, v121\n"
"v_cvt_pk_bf16_f32 v100, v192, v193\n"
"v_lshlrev_b32 v192, 16, v101\n"
"v_and_b32 v193, 0xffff0000, v101\n"
"v_mul_f32 v192, v192, v121\n"
"v_mul_f32 v193, v193, v121\n"
"v_cvt_pk_bf16_f32 v101, v192, v193\n"
"v_lshlrev_b32 v192, 16, v102\n"
"v_and_b32 v193, 0xffff0000, v102\n"
"v_mul_f32 v192, v192, v121\n"
"v_mul_f32 v193, v193, v121\n"
"v_cvt_pk_bf16_f32 v102, v192, v193\n"
"v_lshlrev_b32 v192, 16, v103\n"
"v_and_b32 v193, 0xffff0000, v103\n"
"v_mul_f32 v192, v192, v121\n"
"v_mul_f32 v193, v193, v121\n"
"v_cvt_pk_bf16_f32 v103, v192, v193\n"
"v_lshlrev_b32 v192, 16, v84\n"
"v_and_b32 v193, 0xffff0000, v84\n"
"v_mul_f32 v192, v192, v121\n"
"v_mul_f32 v193, v193, v121\n"
"v_cvt_pk_bf16_f32 v84, v192, v193\n"
"v_lshlrev_b32 v192, 16, v85\n"
"v_and_b32 v193, 0xffff0000, v85\n"
"v_mul_f32 v192, v192, v121\n"
"v_mul_f32 v193, v193, v121\n"
"v_cvt_pk_bf16_f32 v85, v192, v193\n"
"v_lshlrev_b32 v192, 16, v86\n"
"v_and_b32 v193, 0xffff0000, v86\n"
"v_mul_f32 v192, v192, v121\n"
"v_mul_f32 v193, v193, v121\n"
"v_cvt_pk_bf16_f32 v86, v192, v193\n"
"v_lshlrev_b32 v192, 16, v87\n"
"v_and_b32 v193, 0xffff0000, v87\n"
"v_mul_f32 v192, v192, v121\n"
"v_mul_f32 v193, v193, v121\n"
"v_cvt_pk_bf16_f32 v87, v192, v193\n"
"v_mul_f32 v202, v202, v122\n"
"v_lshlrev_b32 v192, 16, v104\n"
"v_and_b32 v193, 0xffff0000, v104\n"
"v_mul_f32 v192, v192, v122\n"
"v_mul_f32 v193, v193, v122\n"
"v_cvt_pk_bf16_f32 v104, v192, v193\n"
"v_lshlrev_b32 v192, 16, v105\n"
"v_and_b32 v193, 0xffff0000, v105\n"
"v_mul_f32 v192, v192, v122\n"
"v_mul_f32 v193, v193, v122\n"
"v_cvt_pk_bf16_f32 v105, v192, v193\n"
"v_lshlrev_b32 v192, 16, v106\n"
"v_and_b32 v193, 0xffff0000, v106\n"
"v_mul_f32 v192, v192, v122\n"
"v_mul_f32 v193, v193, v122\n"
"v_cvt_pk_bf16_f32 v106, v192, v193\n"
"v_lshlrev_b32 v192, 16, v107\n"
"v_and_b32 v193, 0xffff0000, v107\n"
"v_mul_f32 v192, v192, v122\n"
"v_mul_f32 v193, v193, v122\n"
"v_cvt_pk_bf16_f32 v107, v192, v193\n"
"v_lshlrev_b32 v192, 16, v88\n"
"v_and_b32 v193, 0xffff0000, v88\n"
"v_mul_f32 v192, v192, v122\n"
"v_mul_f32 v193, v193, v122\n"
"v_cvt_pk_bf16_f32 v88, v192, v193\n"
"v_lshlrev_b32 v192, 16, v89\n"
"v_and_b32 v193, 0xffff0000, v89\n"
"v_mul_f32 v192, v192, v122\n"
"v_mul_f32 v193, v193, v122\n"
"v_cvt_pk_bf16_f32 v89, v192, v193\n"
"v_lshlrev_b32 v192, 16, v90\n"
"v_and_b32 v193, 0xffff0000, v90\n"
"v_mul_f32 v192, v192, v122\n"
"v_mul_f32 v193, v193, v122\n"
"v_cvt_pk_bf16_f32 v90, v192, v193\n"
"v_lshlrev_b32 v192, 16, v91\n"
"v_and_b32 v193, 0xffff0000, v91\n"
"v_mul_f32 v192, v192, v122\n"
"v_mul_f32 v193, v193, v122\n"
"v_cvt_pk_bf16_f32 v91, v192, v193\n"
"v_mul_f32 v203, v203, v123\n"
"v_lshlrev_b32 v192, 16, v108\n"
"v_and_b32 v193, 0xffff0000, v108\n"
"v_mul_f32 v192, v192, v123\n"
"v_mul_f32 v193, v193, v123\n"
"v_cvt_pk_bf16_f32 v108, v192, v193\n"
"v_lshlrev_b32 v192, 16, v109\n"
"v_and_b32 v193, 0xffff0000, v109\n"
"v_mul_f32 v192, v192, v123\n"
"v_mul_f32 v193, v193, v123\n"
"v_cvt_pk_bf16_f32 v109, v192, v193\n"
"v_lshlrev_b32 v192, 16, v110\n"
"v_and_b32 v193, 0xffff0000, v110\n"
"v_mul_f32 v192, v192, v123\n"
"v_mul_f32 v193, v193, v123\n"
"v_cvt_pk_bf16_f32 v110, v192, v193\n"
"v_lshlrev_b32 v192, 16, v111\n"
"v_and_b32 v193, 0xffff0000, v111\n"
"v_mul_f32 v192, v192, v123\n"
"v_mul_f32 v193, v193, v123\n"
"v_cvt_pk_bf16_f32 v111, v192, v193\n"
"v_lshlrev_b32 v192, 16, v92\n"
"v_and_b32 v193, 0xffff0000, v92\n"
"v_mul_f32 v192, v192, v123\n"
"v_mul_f32 v193, v193, v123\n"
"v_cvt_pk_bf16_f32 v92, v192, v193\n"
"v_lshlrev_b32 v192, 16, v93\n"
"v_and_b32 v193, 0xffff0000, v93\n"
"v_mul_f32 v192, v192, v123\n"
"v_mul_f32 v193, v193, v123\n"
"v_cvt_pk_bf16_f32 v93, v192, v193\n"
"v_lshlrev_b32 v192, 16, v94\n"
"v_and_b32 v193, 0xffff0000, v94\n"
"v_mul_f32 v192, v192, v123\n"
"v_mul_f32 v193, v193, v123\n"
"v_cvt_pk_bf16_f32 v94, v192, v193\n"
"v_lshlrev_b32 v192, 16, v95\n"
"v_and_b32 v193, 0xffff0000, v95\n"
"v_mul_f32 v192, v192, v123\n"
"v_mul_f32 v193, v193, v123\n"
"v_cvt_pk_bf16_f32 v95, v192, v193\n"
"v_lshrrev_b32 v116, 4, v251\n"
"ds_bpermute_b32 v120, v116, v120 offset:0\n"
"ds_bpermute_b32 v121, v116, v121 offset:0\n"
"ds_bpermute_b32 v122, v116, v122 offset:0\n"
"ds_bpermute_b32 v123, v116, v123 offset:0\n"
"v_accvgpr_read_b32 v192, a0\n"
"v_accvgpr_read_b32 v193, a1\n"
"v_accvgpr_read_b32 v194, a2\n"
"v_accvgpr_read_b32 v195, a3\n"
"v_accvgpr_read_b32 v196, a4\n"
"v_accvgpr_read_b32 v197, a5\n"
"v_accvgpr_read_b32 v198, a6\n"
"v_accvgpr_read_b32 v199, a7\n"
"s_waitcnt lgkmcnt(3)\n"
"v_mul_f32_dpp v192, v120, v192 row_newbcast:0 row_mask:0xf bank_mask:0xf\n"
"v_mul_f32_dpp v193, v120, v193 row_newbcast:4 row_mask:0xf bank_mask:0xf\n"
"v_mul_f32_dpp v194, v120, v194 row_newbcast:8 row_mask:0xf bank_mask:0xf\n"
"v_mul_f32_dpp v195, v120, v195 row_newbcast:12 row_mask:0xf bank_mask:0xf\n"
"v_mul_f32_dpp v196, v120, v196 row_newbcast:0 row_mask:0xf bank_mask:0xf\n"
"v_mul_f32_dpp v197, v120, v197 row_newbcast:4 row_mask:0xf bank_mask:0xf\n"
"v_mul_f32_dpp v198, v120, v198 row_newbcast:8 row_mask:0xf bank_mask:0xf\n"
"v_mul_f32_dpp v199, v120, v199 row_newbcast:12 row_mask:0xf bank_mask:0xf\n"
"v_accvgpr_write_b32 a0, v192\n"
"v_accvgpr_write_b32 a1, v193\n"
"v_accvgpr_write_b32 a2, v194\n"
"v_accvgpr_write_b32 a3, v195\n"
"v_accvgpr_write_b32 a4, v196\n"
"v_accvgpr_write_b32 a5, v197\n"
"v_accvgpr_write_b32 a6, v198\n"
"v_accvgpr_write_b32 a7, v199\n"
"v_accvgpr_read_b32 v192, a8\n"
"v_accvgpr_read_b32 v193, a9\n"
"v_accvgpr_read_b32 v194, a10\n"
"v_accvgpr_read_b32 v195, a11\n"
"v_accvgpr_read_b32 v196, a12\n"
"v_accvgpr_read_b32 v197, a13\n"
"v_accvgpr_read_b32 v198, a14\n"
"v_accvgpr_read_b32 v199, a15\n"
"v_mul_f32_dpp v192, v120, v192 row_newbcast:0 row_mask:0xf bank_mask:0xf\n"
"v_mul_f32_dpp v193, v120, v193 row_newbcast:4 row_mask:0xf bank_mask:0xf\n"
"v_mul_f32_dpp v194, v120, v194 row_newbcast:8 row_mask:0xf bank_mask:0xf\n"
"v_mul_f32_dpp v195, v120, v195 row_newbcast:12 row_mask:0xf bank_mask:0xf\n"
"v_mul_f32_dpp v196, v120, v196 row_newbcast:0 row_mask:0xf bank_mask:0xf\n"
"v_mul_f32_dpp v197, v120, v197 row_newbcast:4 row_mask:0xf bank_mask:0xf\n"
"v_mul_f32_dpp v198, v120, v198 row_newbcast:8 row_mask:0xf bank_mask:0xf\n"
"v_mul_f32_dpp v199, v120, v199 row_newbcast:12 row_mask:0xf bank_mask:0xf\n"
"v_accvgpr_write_b32 a8, v192\n"
"v_accvgpr_write_b32 a9, v193\n"
"v_accvgpr_write_b32 a10, v194\n"
"v_accvgpr_write_b32 a11, v195\n"
"v_accvgpr_write_b32 a12, v196\n"
"v_accvgpr_write_b32 a13, v197\n"
"v_accvgpr_write_b32 a14, v198\n"
"v_accvgpr_write_b32 a15, v199\n"
"v_accvgpr_read_b32 v192, a16\n"
"v_accvgpr_read_b32 v193, a17\n"
"v_accvgpr_read_b32 v194, a18\n"
"v_accvgpr_read_b32 v195, a19\n"
"v_accvgpr_read_b32 v196, a20\n"
"v_accvgpr_read_b32 v197, a21\n"
"v_accvgpr_read_b32 v198, a22\n"
"v_accvgpr_read_b32 v199, a23\n"
"v_mul_f32_dpp v192, v120, v192 row_newbcast:0 row_mask:0xf bank_mask:0xf\n"
"v_mul_f32_dpp v193, v120, v193 row_newbcast:4 row_mask:0xf bank_mask:0xf\n"
"v_mul_f32_dpp v194, v120, v194 row_newbcast:8 row_mask:0xf bank_mask:0xf\n"
"v_mul_f32_dpp v195, v120, v195 row_newbcast:12 row_mask:0xf bank_mask:0xf\n"
"v_mul_f32_dpp v196, v120, v196 row_newbcast:0 row_mask:0xf bank_mask:0xf\n"
"v_mul_f32_dpp v197, v120, v197 row_newbcast:4 row_mask:0xf bank_mask:0xf\n"
"v_mul_f32_dpp v198, v120, v198 row_newbcast:8 row_mask:0xf bank_mask:0xf\n"
"v_mul_f32_dpp v199, v120, v199 row_newbcast:12 row_mask:0xf bank_mask:0xf\n"
"v_accvgpr_write_b32 a16, v192\n"
"v_accvgpr_write_b32 a17, v193\n"
"v_accvgpr_write_b32 a18, v194\n"
"v_accvgpr_write_b32 a19, v195\n"
"v_accvgpr_write_b32 a20, v196\n"
"v_accvgpr_write_b32 a21, v197\n"
"v_accvgpr_write_b32 a22, v198\n"
"v_accvgpr_write_b32 a23, v199\n"
"v_accvgpr_read_b32 v192, a24\n"
"v_accvgpr_read_b32 v193, a25\n"
"v_accvgpr_read_b32 v194, a26\n"
"v_accvgpr_read_b32 v195, a27\n"
"v_accvgpr_read_b32 v196, a28\n"
"v_accvgpr_read_b32 v197, a29\n"
"v_accvgpr_read_b32 v198, a30\n"
"v_accvgpr_read_b32 v199, a31\n"
"v_mul_f32_dpp v192, v120, v192 row_newbcast:0 row_mask:0xf bank_mask:0xf\n"
"v_mul_f32_dpp v193, v120, v193 row_newbcast:4 row_mask:0xf bank_mask:0xf\n"
"v_mul_f32_dpp v194, v120, v194 row_newbcast:8 row_mask:0xf bank_mask:0xf\n"
"v_mul_f32_dpp v195, v120, v195 row_newbcast:12 row_mask:0xf bank_mask:0xf\n"
"v_mul_f32_dpp v196, v120, v196 row_newbcast:0 row_mask:0xf bank_mask:0xf\n"
"v_mul_f32_dpp v197, v120, v197 row_newbcast:4 row_mask:0xf bank_mask:0xf\n"
"v_mul_f32_dpp v198, v120, v198 row_newbcast:8 row_mask:0xf bank_mask:0xf\n"
"v_mul_f32_dpp v199, v120, v199 row_newbcast:12 row_mask:0xf bank_mask:0xf\n"
"v_accvgpr_write_b32 a24, v192\n"
"v_accvgpr_write_b32 a25, v193\n"
"v_accvgpr_write_b32 a26, v194\n"
"v_accvgpr_write_b32 a27, v195\n"
"v_accvgpr_write_b32 a28, v196\n"
"v_accvgpr_write_b32 a29, v197\n"
"v_accvgpr_write_b32 a30, v198\n"
"v_accvgpr_write_b32 a31, v199\n"
"v_accvgpr_read_b32 v192, a32\n"
"v_accvgpr_read_b32 v193, a33\n"
"v_accvgpr_read_b32 v194, a34\n"
"v_accvgpr_read_b32 v195, a35\n"
"v_accvgpr_read_b32 v196, a36\n"
"v_accvgpr_read_b32 v197, a37\n"
"v_accvgpr_read_b32 v198, a38\n"
"v_accvgpr_read_b32 v199, a39\n"
"v_mul_f32_dpp v192, v120, v192 row_newbcast:0 row_mask:0xf bank_mask:0xf\n"
"v_mul_f32_dpp v193, v120, v193 row_newbcast:4 row_mask:0xf bank_mask:0xf\n"
"v_mul_f32_dpp v194, v120, v194 row_newbcast:8 row_mask:0xf bank_mask:0xf\n"
"v_mul_f32_dpp v195, v120, v195 row_newbcast:12 row_mask:0xf bank_mask:0xf\n"
"v_mul_f32_dpp v196, v120, v196 row_newbcast:0 row_mask:0xf bank_mask:0xf\n"
"v_mul_f32_dpp v197, v120, v197 row_newbcast:4 row_mask:0xf bank_mask:0xf\n"
"v_mul_f32_dpp v198, v120, v198 row_newbcast:8 row_mask:0xf bank_mask:0xf\n"
"v_mul_f32_dpp v199, v120, v199 row_newbcast:12 row_mask:0xf bank_mask:0xf\n"
"v_accvgpr_write_b32 a32, v192\n"
"v_accvgpr_write_b32 a33, v193\n"
"v_accvgpr_write_b32 a34, v194\n"
"v_accvgpr_write_b32 a35, v195\n"
"v_accvgpr_write_b32 a36, v196\n"
"v_accvgpr_write_b32 a37, v197\n"
"v_accvgpr_write_b32 a38, v198\n"
"v_accvgpr_write_b32 a39, v199\n"
"v_accvgpr_read_b32 v192, a40\n"
"v_accvgpr_read_b32 v193, a41\n"
"v_accvgpr_read_b32 v194, a42\n"
"v_accvgpr_read_b32 v195, a43\n"
"v_accvgpr_read_b32 v196, a44\n"
"v_accvgpr_read_b32 v197, a45\n"
"v_accvgpr_read_b32 v198, a46\n"
"v_accvgpr_read_b32 v199, a47\n"
"v_mul_f32_dpp v192, v120, v192 row_newbcast:0 row_mask:0xf bank_mask:0xf\n"
"v_mul_f32_dpp v193, v120, v193 row_newbcast:4 row_mask:0xf bank_mask:0xf\n"
"v_mul_f32_dpp v194, v120, v194 row_newbcast:8 row_mask:0xf bank_mask:0xf\n"
"v_mul_f32_dpp v195, v120, v195 row_newbcast:12 row_mask:0xf bank_mask:0xf\n"
"v_mul_f32_dpp v196, v120, v196 row_newbcast:0 row_mask:0xf bank_mask:0xf\n"
"v_mul_f32_dpp v197, v120, v197 row_newbcast:4 row_mask:0xf bank_mask:0xf\n"
"v_mul_f32_dpp v198, v120, v198 row_newbcast:8 row_mask:0xf bank_mask:0xf\n"
"v_mul_f32_dpp v199, v120, v199 row_newbcast:12 row_mask:0xf bank_mask:0xf\n"
"v_accvgpr_write_b32 a40, v192\n"
"v_accvgpr_write_b32 a41, v193\n"
"v_accvgpr_write_b32 a42, v194\n"
"v_accvgpr_write_b32 a43, v195\n"
"v_accvgpr_write_b32 a44, v196\n"
"v_accvgpr_write_b32 a45, v197\n"
"v_accvgpr_write_b32 a46, v198\n"
"v_accvgpr_write_b32 a47, v199\n"
"v_accvgpr_read_b32 v192, a48\n"
"v_accvgpr_read_b32 v193, a49\n"
"v_accvgpr_read_b32 v194, a50\n"
"v_accvgpr_read_b32 v195, a51\n"
"v_accvgpr_read_b32 v196, a52\n"
"v_accvgpr_read_b32 v197, a53\n"
"v_accvgpr_read_b32 v198, a54\n"
"v_accvgpr_read_b32 v199, a55\n"
"s_waitcnt lgkmcnt(2)\n"
"v_mul_f32_dpp v192, v121, v192 row_newbcast:0 row_mask:0xf bank_mask:0xf\n"
"v_mul_f32_dpp v193, v121, v193 row_newbcast:4 row_mask:0xf bank_mask:0xf\n"
"v_mul_f32_dpp v194, v121, v194 row_newbcast:8 row_mask:0xf bank_mask:0xf\n"
"v_mul_f32_dpp v195, v121, v195 row_newbcast:12 row_mask:0xf bank_mask:0xf\n"
"v_mul_f32_dpp v196, v121, v196 row_newbcast:0 row_mask:0xf bank_mask:0xf\n"
"v_mul_f32_dpp v197, v121, v197 row_newbcast:4 row_mask:0xf bank_mask:0xf\n"
"v_mul_f32_dpp v198, v121, v198 row_newbcast:8 row_mask:0xf bank_mask:0xf\n"
"v_mul_f32_dpp v199, v121, v199 row_newbcast:12 row_mask:0xf bank_mask:0xf\n"
"v_accvgpr_write_b32 a48, v192\n"
"v_accvgpr_write_b32 a49, v193\n"
"v_accvgpr_write_b32 a50, v194\n"
"v_accvgpr_write_b32 a51, v195\n"
"v_accvgpr_write_b32 a52, v196\n"
"v_accvgpr_write_b32 a53, v197\n"
"v_accvgpr_write_b32 a54, v198\n"
"v_accvgpr_write_b32 a55, v199\n"
"v_accvgpr_read_b32 v192, a56\n"
"v_accvgpr_read_b32 v193, a57\n"
"v_accvgpr_read_b32 v194, a58\n"
"v_accvgpr_read_b32 v195, a59\n"
"v_accvgpr_read_b32 v196, a60\n"
"v_accvgpr_read_b32 v197, a61\n"
"v_accvgpr_read_b32 v198, a62\n"
"v_accvgpr_read_b32 v199, a63\n"
"v_mul_f32_dpp v192, v121, v192 row_newbcast:0 row_mask:0xf bank_mask:0xf\n"
"v_mul_f32_dpp v193, v121, v193 row_newbcast:4 row_mask:0xf bank_mask:0xf\n"
"v_mul_f32_dpp v194, v121, v194 row_newbcast:8 row_mask:0xf bank_mask:0xf\n"
"v_mul_f32_dpp v195, v121, v195 row_newbcast:12 row_mask:0xf bank_mask:0xf\n"
"v_mul_f32_dpp v196, v121, v196 row_newbcast:0 row_mask:0xf bank_mask:0xf\n"
"v_mul_f32_dpp v197, v121, v197 row_newbcast:4 row_mask:0xf bank_mask:0xf\n"
"v_mul_f32_dpp v198, v121, v198 row_newbcast:8 row_mask:0xf bank_mask:0xf\n"
"v_mul_f32_dpp v199, v121, v199 row_newbcast:12 row_mask:0xf bank_mask:0xf\n"
"v_accvgpr_write_b32 a56, v192\n"
"v_accvgpr_write_b32 a57, v193\n"
"v_accvgpr_write_b32 a58, v194\n"
"v_accvgpr_write_b32 a59, v195\n"
"v_accvgpr_write_b32 a60, v196\n"
"v_accvgpr_write_b32 a61, v197\n"
"v_accvgpr_write_b32 a62, v198\n"
"v_accvgpr_write_b32 a63, v199\n"
"v_accvgpr_read_b32 v192, a64\n"
"v_accvgpr_read_b32 v193, a65\n"
"v_accvgpr_read_b32 v194, a66\n"
"v_accvgpr_read_b32 v195, a67\n"
"v_accvgpr_read_b32 v196, a68\n"
"v_accvgpr_read_b32 v197, a69\n"
"v_accvgpr_read_b32 v198, a70\n"
"v_accvgpr_read_b32 v199, a71\n"
"v_mul_f32_dpp v192, v121, v192 row_newbcast:0 row_mask:0xf bank_mask:0xf\n"
"v_mul_f32_dpp v193, v121, v193 row_newbcast:4 row_mask:0xf bank_mask:0xf\n"
"v_mul_f32_dpp v194, v121, v194 row_newbcast:8 row_mask:0xf bank_mask:0xf\n"
"v_mul_f32_dpp v195, v121, v195 row_newbcast:12 row_mask:0xf bank_mask:0xf\n"
"v_mul_f32_dpp v196, v121, v196 row_newbcast:0 row_mask:0xf bank_mask:0xf\n"
"v_mul_f32_dpp v197, v121, v197 row_newbcast:4 row_mask:0xf bank_mask:0xf\n"
"v_mul_f32_dpp v198, v121, v198 row_newbcast:8 row_mask:0xf bank_mask:0xf\n"
"v_mul_f32_dpp v199, v121, v199 row_newbcast:12 row_mask:0xf bank_mask:0xf\n"
"v_accvgpr_write_b32 a64, v192\n"
"v_accvgpr_write_b32 a65, v193\n"
"v_accvgpr_write_b32 a66, v194\n"
"v_accvgpr_write_b32 a67, v195\n"
"v_accvgpr_write_b32 a68, v196\n"
"v_accvgpr_write_b32 a69, v197\n"
"v_accvgpr_write_b32 a70, v198\n"
"v_accvgpr_write_b32 a71, v199\n"
"v_accvgpr_read_b32 v192, a72\n"
"v_accvgpr_read_b32 v193, a73\n"
"v_accvgpr_read_b32 v194, a74\n"
"v_accvgpr_read_b32 v195, a75\n"
"v_accvgpr_read_b32 v196, a76\n"
"v_accvgpr_read_b32 v197, a77\n"
"v_accvgpr_read_b32 v198, a78\n"
"v_accvgpr_read_b32 v199, a79\n"
"v_mul_f32_dpp v192, v121, v192 row_newbcast:0 row_mask:0xf bank_mask:0xf\n"
"v_mul_f32_dpp v193, v121, v193 row_newbcast:4 row_mask:0xf bank_mask:0xf\n"
"v_mul_f32_dpp v194, v121, v194 row_newbcast:8 row_mask:0xf bank_mask:0xf\n"
"v_mul_f32_dpp v195, v121, v195 row_newbcast:12 row_mask:0xf bank_mask:0xf\n"
"v_mul_f32_dpp v196, v121, v196 row_newbcast:0 row_mask:0xf bank_mask:0xf\n"
"v_mul_f32_dpp v197, v121, v197 row_newbcast:4 row_mask:0xf bank_mask:0xf\n"
"v_mul_f32_dpp v198, v121, v198 row_newbcast:8 row_mask:0xf bank_mask:0xf\n"
"v_mul_f32_dpp v199, v121, v199 row_newbcast:12 row_mask:0xf bank_mask:0xf\n"
"v_accvgpr_write_b32 a72, v192\n"
"v_accvgpr_write_b32 a73, v193\n"
"v_accvgpr_write_b32 a74, v194\n"
"v_accvgpr_write_b32 a75, v195\n"
"v_accvgpr_write_b32 a76, v196\n"
"v_accvgpr_write_b32 a77, v197\n"
"v_accvgpr_write_b32 a78, v198\n"
"v_accvgpr_write_b32 a79, v199\n"
"v_accvgpr_read_b32 v192, a80\n"
"v_accvgpr_read_b32 v193, a81\n"
"v_accvgpr_read_b32 v194, a82\n"
"v_accvgpr_read_b32 v195, a83\n"
"v_accvgpr_read_b32 v196, a84\n"
"v_accvgpr_read_b32 v197, a85\n"
"v_accvgpr_read_b32 v198, a86\n"
"v_accvgpr_read_b32 v199, a87\n"
"v_mul_f32_dpp v192, v121, v192 row_newbcast:0 row_mask:0xf bank_mask:0xf\n"
"v_mul_f32_dpp v193, v121, v193 row_newbcast:4 row_mask:0xf bank_mask:0xf\n"
"v_mul_f32_dpp v194, v121, v194 row_newbcast:8 row_mask:0xf bank_mask:0xf\n"
"v_mul_f32_dpp v195, v121, v195 row_newbcast:12 row_mask:0xf bank_mask:0xf\n"
"v_mul_f32_dpp v196, v121, v196 row_newbcast:0 row_mask:0xf bank_mask:0xf\n"
"v_mul_f32_dpp v197, v121, v197 row_newbcast:4 row_mask:0xf bank_mask:0xf\n"
"v_mul_f32_dpp v198, v121, v198 row_newbcast:8 row_mask:0xf bank_mask:0xf\n"
"v_mul_f32_dpp v199, v121, v199 row_newbcast:12 row_mask:0xf bank_mask:0xf\n"
"v_accvgpr_write_b32 a80, v192\n"
"v_accvgpr_write_b32 a81, v193\n"
"v_accvgpr_write_b32 a82, v194\n"
"v_accvgpr_write_b32 a83, v195\n"
"v_accvgpr_write_b32 a84, v196\n"
"v_accvgpr_write_b32 a85, v197\n"
"v_accvgpr_write_b32 a86, v198\n"
"v_accvgpr_write_b32 a87, v199\n"
"v_accvgpr_read_b32 v192, a88\n"
"v_accvgpr_read_b32 v193, a89\n"
"v_accvgpr_read_b32 v194, a90\n"
"v_accvgpr_read_b32 v195, a91\n"
"v_accvgpr_read_b32 v196, a92\n"
"v_accvgpr_read_b32 v197, a93\n"
"v_accvgpr_read_b32 v198, a94\n"
"v_accvgpr_read_b32 v199, a95\n"
"v_mul_f32_dpp v192, v121, v192 row_newbcast:0 row_mask:0xf bank_mask:0xf\n"
"v_mul_f32_dpp v193, v121, v193 row_newbcast:4 row_mask:0xf bank_mask:0xf\n"
"v_mul_f32_dpp v194, v121, v194 row_newbcast:8 row_mask:0xf bank_mask:0xf\n"
"v_mul_f32_dpp v195, v121, v195 row_newbcast:12 row_mask:0xf bank_mask:0xf\n"
"v_mul_f32_dpp v196, v121, v196 row_newbcast:0 row_mask:0xf bank_mask:0xf\n"
"v_mul_f32_dpp v197, v121, v197 row_newbcast:4 row_mask:0xf bank_mask:0xf\n"
"v_mul_f32_dpp v198, v121, v198 row_newbcast:8 row_mask:0xf bank_mask:0xf\n"
"v_mul_f32_dpp v199, v121, v199 row_newbcast:12 row_mask:0xf bank_mask:0xf\n"
"v_accvgpr_write_b32 a88, v192\n"
"v_accvgpr_write_b32 a89, v193\n"
"v_accvgpr_write_b32 a90, v194\n"
"v_accvgpr_write_b32 a91, v195\n"
"v_accvgpr_write_b32 a92, v196\n"
"v_accvgpr_write_b32 a93, v197\n"
"v_accvgpr_write_b32 a94, v198\n"
"v_accvgpr_write_b32 a95, v199\n"
"v_accvgpr_read_b32 v192, a96\n"
"v_accvgpr_read_b32 v193, a97\n"
"v_accvgpr_read_b32 v194, a98\n"
"v_accvgpr_read_b32 v195, a99\n"
"v_accvgpr_read_b32 v196, a100\n"
"v_accvgpr_read_b32 v197, a101\n"
"v_accvgpr_read_b32 v198, a102\n"
"v_accvgpr_read_b32 v199, a103\n"
"s_waitcnt lgkmcnt(1)\n"
"v_mul_f32_dpp v192, v122, v192 row_newbcast:0 row_mask:0xf bank_mask:0xf\n"
"v_mul_f32_dpp v193, v122, v193 row_newbcast:4 row_mask:0xf bank_mask:0xf\n"
"v_mul_f32_dpp v194, v122, v194 row_newbcast:8 row_mask:0xf bank_mask:0xf\n"
"v_mul_f32_dpp v195, v122, v195 row_newbcast:12 row_mask:0xf bank_mask:0xf\n"
"v_mul_f32_dpp v196, v122, v196 row_newbcast:0 row_mask:0xf bank_mask:0xf\n"
"v_mul_f32_dpp v197, v122, v197 row_newbcast:4 row_mask:0xf bank_mask:0xf\n"
"v_mul_f32_dpp v198, v122, v198 row_newbcast:8 row_mask:0xf bank_mask:0xf\n"
"v_mul_f32_dpp v199, v122, v199 row_newbcast:12 row_mask:0xf bank_mask:0xf\n"
"v_accvgpr_write_b32 a96, v192\n"
"v_accvgpr_write_b32 a97, v193\n"
"v_accvgpr_write_b32 a98, v194\n"
"v_accvgpr_write_b32 a99, v195\n"
"v_accvgpr_write_b32 a100, v196\n"
"v_accvgpr_write_b32 a101, v197\n"
"v_accvgpr_write_b32 a102, v198\n"
"v_accvgpr_write_b32 a103, v199\n"
"v_accvgpr_read_b32 v192, a104\n"
"v_accvgpr_read_b32 v193, a105\n"
"v_accvgpr_read_b32 v194, a106\n"
"v_accvgpr_read_b32 v195, a107\n"
"v_accvgpr_read_b32 v196, a108\n"
"v_accvgpr_read_b32 v197, a109\n"
"v_accvgpr_read_b32 v198, a110\n"
"v_accvgpr_read_b32 v199, a111\n"
"v_mul_f32_dpp v192, v122, v192 row_newbcast:0 row_mask:0xf bank_mask:0xf\n"
"v_mul_f32_dpp v193, v122, v193 row_newbcast:4 row_mask:0xf bank_mask:0xf\n"
"v_mul_f32_dpp v194, v122, v194 row_newbcast:8 row_mask:0xf bank_mask:0xf\n"
"v_mul_f32_dpp v195, v122, v195 row_newbcast:12 row_mask:0xf bank_mask:0xf\n"
"v_mul_f32_dpp v196, v122, v196 row_newbcast:0 row_mask:0xf bank_mask:0xf\n"
"v_mul_f32_dpp v197, v122, v197 row_newbcast:4 row_mask:0xf bank_mask:0xf\n"
"v_mul_f32_dpp v198, v122, v198 row_newbcast:8 row_mask:0xf bank_mask:0xf\n"
"v_mul_f32_dpp v199, v122, v199 row_newbcast:12 row_mask:0xf bank_mask:0xf\n"
"v_accvgpr_write_b32 a104, v192\n"
"v_accvgpr_write_b32 a105, v193\n"
"v_accvgpr_write_b32 a106, v194\n"
"v_accvgpr_write_b32 a107, v195\n"
"v_accvgpr_write_b32 a108, v196\n"
"v_accvgpr_write_b32 a109, v197\n"
"v_accvgpr_write_b32 a110, v198\n"
"v_accvgpr_write_b32 a111, v199\n"
"v_accvgpr_read_b32 v192, a112\n"
"v_accvgpr_read_b32 v193, a113\n"
"v_accvgpr_read_b32 v194, a114\n"
"v_accvgpr_read_b32 v195, a115\n"
"v_accvgpr_read_b32 v196, a116\n"
"v_accvgpr_read_b32 v197, a117\n"
"v_accvgpr_read_b32 v198, a118\n"
"v_accvgpr_read_b32 v199, a119\n"
"v_mul_f32_dpp v192, v122, v192 row_newbcast:0 row_mask:0xf bank_mask:0xf\n"
"v_mul_f32_dpp v193, v122, v193 row_newbcast:4 row_mask:0xf bank_mask:0xf\n"
"v_mul_f32_dpp v194, v122, v194 row_newbcast:8 row_mask:0xf bank_mask:0xf\n"
"v_mul_f32_dpp v195, v122, v195 row_newbcast:12 row_mask:0xf bank_mask:0xf\n"
"v_mul_f32_dpp v196, v122, v196 row_newbcast:0 row_mask:0xf bank_mask:0xf\n"
"v_mul_f32_dpp v197, v122, v197 row_newbcast:4 row_mask:0xf bank_mask:0xf\n"
"v_mul_f32_dpp v198, v122, v198 row_newbcast:8 row_mask:0xf bank_mask:0xf\n"
"v_mul_f32_dpp v199, v122, v199 row_newbcast:12 row_mask:0xf bank_mask:0xf\n"
"v_accvgpr_write_b32 a112, v192\n"
"v_accvgpr_write_b32 a113, v193\n"
"v_accvgpr_write_b32 a114, v194\n"
"v_accvgpr_write_b32 a115, v195\n"
"v_accvgpr_write_b32 a116, v196\n"
"v_accvgpr_write_b32 a117, v197\n"
"v_accvgpr_write_b32 a118, v198\n"
"v_accvgpr_write_b32 a119, v199\n"
"v_accvgpr_read_b32 v192, a120\n"
"v_accvgpr_read_b32 v193, a121\n"
"v_accvgpr_read_b32 v194, a122\n"
"v_accvgpr_read_b32 v195, a123\n"
"v_accvgpr_read_b32 v196, a124\n"
"v_accvgpr_read_b32 v197, a125\n"
"v_accvgpr_read_b32 v198, a126\n"
"v_accvgpr_read_b32 v199, a127\n"
"v_mul_f32_dpp v192, v122, v192 row_newbcast:0 row_mask:0xf bank_mask:0xf\n"
"v_mul_f32_dpp v193, v122, v193 row_newbcast:4 row_mask:0xf bank_mask:0xf\n"
"v_mul_f32_dpp v194, v122, v194 row_newbcast:8 row_mask:0xf bank_mask:0xf\n"
"v_mul_f32_dpp v195, v122, v195 row_newbcast:12 row_mask:0xf bank_mask:0xf\n"
"v_mul_f32_dpp v196, v122, v196 row_newbcast:0 row_mask:0xf bank_mask:0xf\n"
"v_mul_f32_dpp v197, v122, v197 row_newbcast:4 row_mask:0xf bank_mask:0xf\n"
"v_mul_f32_dpp v198, v122, v198 row_newbcast:8 row_mask:0xf bank_mask:0xf\n"
"v_mul_f32_dpp v199, v122, v199 row_newbcast:12 row_mask:0xf bank_mask:0xf\n"
"v_accvgpr_write_b32 a120, v192\n"
"v_accvgpr_write_b32 a121, v193\n"
"v_accvgpr_write_b32 a122, v194\n"
"v_accvgpr_write_b32 a123, v195\n"
"v_accvgpr_write_b32 a124, v196\n"
"v_accvgpr_write_b32 a125, v197\n"
"v_accvgpr_write_b32 a126, v198\n"
"v_accvgpr_write_b32 a127, v199\n"
"v_accvgpr_read_b32 v192, a128\n"
"v_accvgpr_read_b32 v193, a129\n"
"v_accvgpr_read_b32 v194, a130\n"
"v_accvgpr_read_b32 v195, a131\n"
"v_accvgpr_read_b32 v196, a132\n"
"v_accvgpr_read_b32 v197, a133\n"
"v_accvgpr_read_b32 v198, a134\n"
"v_accvgpr_read_b32 v199, a135\n"
"v_mul_f32_dpp v192, v122, v192 row_newbcast:0 row_mask:0xf bank_mask:0xf\n"
"v_mul_f32_dpp v193, v122, v193 row_newbcast:4 row_mask:0xf bank_mask:0xf\n"
"v_mul_f32_dpp v194, v122, v194 row_newbcast:8 row_mask:0xf bank_mask:0xf\n"
"v_mul_f32_dpp v195, v122, v195 row_newbcast:12 row_mask:0xf bank_mask:0xf\n"
"v_mul_f32_dpp v196, v122, v196 row_newbcast:0 row_mask:0xf bank_mask:0xf\n"
"v_mul_f32_dpp v197, v122, v197 row_newbcast:4 row_mask:0xf bank_mask:0xf\n"
"v_mul_f32_dpp v198, v122, v198 row_newbcast:8 row_mask:0xf bank_mask:0xf\n"
"v_mul_f32_dpp v199, v122, v199 row_newbcast:12 row_mask:0xf bank_mask:0xf\n"
"v_accvgpr_write_b32 a128, v192\n"
"v_accvgpr_write_b32 a129, v193\n"
"v_accvgpr_write_b32 a130, v194\n"
"v_accvgpr_write_b32 a131, v195\n"
"v_accvgpr_write_b32 a132, v196\n"
"v_accvgpr_write_b32 a133, v197\n"
"v_accvgpr_write_b32 a134, v198\n"
"v_accvgpr_write_b32 a135, v199\n"
"v_accvgpr_read_b32 v192, a136\n"
"v_accvgpr_read_b32 v193, a137\n"
"v_accvgpr_read_b32 v194, a138\n"
"v_accvgpr_read_b32 v195, a139\n"
"v_accvgpr_read_b32 v196, a140\n"
"v_accvgpr_read_b32 v197, a141\n"
"v_accvgpr_read_b32 v198, a142\n"
"v_accvgpr_read_b32 v199, a143\n"
"v_mul_f32_dpp v192, v122, v192 row_newbcast:0 row_mask:0xf bank_mask:0xf\n"
"v_mul_f32_dpp v193, v122, v193 row_newbcast:4 row_mask:0xf bank_mask:0xf\n"
"v_mul_f32_dpp v194, v122, v194 row_newbcast:8 row_mask:0xf bank_mask:0xf\n"
"v_mul_f32_dpp v195, v122, v195 row_newbcast:12 row_mask:0xf bank_mask:0xf\n"
"v_mul_f32_dpp v196, v122, v196 row_newbcast:0 row_mask:0xf bank_mask:0xf\n"
"v_mul_f32_dpp v197, v122, v197 row_newbcast:4 row_mask:0xf bank_mask:0xf\n"
"v_mul_f32_dpp v198, v122, v198 row_newbcast:8 row_mask:0xf bank_mask:0xf\n"
"v_mul_f32_dpp v199, v122, v199 row_newbcast:12 row_mask:0xf bank_mask:0xf\n"
"v_accvgpr_write_b32 a136, v192\n"
"v_accvgpr_write_b32 a137, v193\n"
"v_accvgpr_write_b32 a138, v194\n"
"v_accvgpr_write_b32 a139, v195\n"
"v_accvgpr_write_b32 a140, v196\n"
"v_accvgpr_write_b32 a141, v197\n"
"v_accvgpr_write_b32 a142, v198\n"
"v_accvgpr_write_b32 a143, v199\n"
"v_accvgpr_read_b32 v192, a144\n"
"v_accvgpr_read_b32 v193, a145\n"
"v_accvgpr_read_b32 v194, a146\n"
"v_accvgpr_read_b32 v195, a147\n"
"v_accvgpr_read_b32 v196, a148\n"
"v_accvgpr_read_b32 v197, a149\n"
"v_accvgpr_read_b32 v198, a150\n"
"v_accvgpr_read_b32 v199, a151\n"
"s_waitcnt lgkmcnt(0)\n"
"v_mul_f32_dpp v192, v123, v192 row_newbcast:0 row_mask:0xf bank_mask:0xf\n"
"v_mul_f32_dpp v193, v123, v193 row_newbcast:4 row_mask:0xf bank_mask:0xf\n"
"v_mul_f32_dpp v194, v123, v194 row_newbcast:8 row_mask:0xf bank_mask:0xf\n"
"v_mul_f32_dpp v195, v123, v195 row_newbcast:12 row_mask:0xf bank_mask:0xf\n"
"v_mul_f32_dpp v196, v123, v196 row_newbcast:0 row_mask:0xf bank_mask:0xf\n"
"v_mul_f32_dpp v197, v123, v197 row_newbcast:4 row_mask:0xf bank_mask:0xf\n"
"v_mul_f32_dpp v198, v123, v198 row_newbcast:8 row_mask:0xf bank_mask:0xf\n"
"v_mul_f32_dpp v199, v123, v199 row_newbcast:12 row_mask:0xf bank_mask:0xf\n"
"v_accvgpr_write_b32 a144, v192\n"
"v_accvgpr_write_b32 a145, v193\n"
"v_accvgpr_write_b32 a146, v194\n"
"v_accvgpr_write_b32 a147, v195\n"
"v_accvgpr_write_b32 a148, v196\n"
"v_accvgpr_write_b32 a149, v197\n"
"v_accvgpr_write_b32 a150, v198\n"
"v_accvgpr_write_b32 a151, v199\n"
"v_accvgpr_read_b32 v192, a152\n"
"v_accvgpr_read_b32 v193, a153\n"
"v_accvgpr_read_b32 v194, a154\n"
"v_accvgpr_read_b32 v195, a155\n"
"v_accvgpr_read_b32 v196, a156\n"
"v_accvgpr_read_b32 v197, a157\n"
"v_accvgpr_read_b32 v198, a158\n"
"v_accvgpr_read_b32 v199, a159\n"
"v_mul_f32_dpp v192, v123, v192 row_newbcast:0 row_mask:0xf bank_mask:0xf\n"
"v_mul_f32_dpp v193, v123, v193 row_newbcast:4 row_mask:0xf bank_mask:0xf\n"
"v_mul_f32_dpp v194, v123, v194 row_newbcast:8 row_mask:0xf bank_mask:0xf\n"
"v_mul_f32_dpp v195, v123, v195 row_newbcast:12 row_mask:0xf bank_mask:0xf\n"
"v_mul_f32_dpp v196, v123, v196 row_newbcast:0 row_mask:0xf bank_mask:0xf\n"
"v_mul_f32_dpp v197, v123, v197 row_newbcast:4 row_mask:0xf bank_mask:0xf\n"
"v_mul_f32_dpp v198, v123, v198 row_newbcast:8 row_mask:0xf bank_mask:0xf\n"
"v_mul_f32_dpp v199, v123, v199 row_newbcast:12 row_mask:0xf bank_mask:0xf\n"
"v_accvgpr_write_b32 a152, v192\n"
"v_accvgpr_write_b32 a153, v193\n"
"v_accvgpr_write_b32 a154, v194\n"
"v_accvgpr_write_b32 a155, v195\n"
"v_accvgpr_write_b32 a156, v196\n"
"v_accvgpr_write_b32 a157, v197\n"
"v_accvgpr_write_b32 a158, v198\n"
"v_accvgpr_write_b32 a159, v199\n"
"v_accvgpr_read_b32 v192, a160\n"
"v_accvgpr_read_b32 v193, a161\n"
"v_accvgpr_read_b32 v194, a162\n"
"v_accvgpr_read_b32 v195, a163\n"
"v_accvgpr_read_b32 v196, a164\n"
"v_accvgpr_read_b32 v197, a165\n"
"v_accvgpr_read_b32 v198, a166\n"
"v_accvgpr_read_b32 v199, a167\n"
"v_mul_f32_dpp v192, v123, v192 row_newbcast:0 row_mask:0xf bank_mask:0xf\n"
"v_mul_f32_dpp v193, v123, v193 row_newbcast:4 row_mask:0xf bank_mask:0xf\n"
"v_mul_f32_dpp v194, v123, v194 row_newbcast:8 row_mask:0xf bank_mask:0xf\n"
"v_mul_f32_dpp v195, v123, v195 row_newbcast:12 row_mask:0xf bank_mask:0xf\n"
"v_mul_f32_dpp v196, v123, v196 row_newbcast:0 row_mask:0xf bank_mask:0xf\n"
"v_mul_f32_dpp v197, v123, v197 row_newbcast:4 row_mask:0xf bank_mask:0xf\n"
"v_mul_f32_dpp v198, v123, v198 row_newbcast:8 row_mask:0xf bank_mask:0xf\n"
"v_mul_f32_dpp v199, v123, v199 row_newbcast:12 row_mask:0xf bank_mask:0xf\n"
"v_accvgpr_write_b32 a160, v192\n"
"v_accvgpr_write_b32 a161, v193\n"
"v_accvgpr_write_b32 a162, v194\n"
"v_accvgpr_write_b32 a163, v195\n"
"v_accvgpr_write_b32 a164, v196\n"
"v_accvgpr_write_b32 a165, v197\n"
"v_accvgpr_write_b32 a166, v198\n"
"v_accvgpr_write_b32 a167, v199\n"
"v_accvgpr_read_b32 v192, a168\n"
"v_accvgpr_read_b32 v193, a169\n"
"v_accvgpr_read_b32 v194, a170\n"
"v_accvgpr_read_b32 v195, a171\n"
"v_accvgpr_read_b32 v196, a172\n"
"v_accvgpr_read_b32 v197, a173\n"
"v_accvgpr_read_b32 v198, a174\n"
"v_accvgpr_read_b32 v199, a175\n"
"v_mul_f32_dpp v192, v123, v192 row_newbcast:0 row_mask:0xf bank_mask:0xf\n"
"v_mul_f32_dpp v193, v123, v193 row_newbcast:4 row_mask:0xf bank_mask:0xf\n"
"v_mul_f32_dpp v194, v123, v194 row_newbcast:8 row_mask:0xf bank_mask:0xf\n"
"v_mul_f32_dpp v195, v123, v195 row_newbcast:12 row_mask:0xf bank_mask:0xf\n"
"v_mul_f32_dpp v196, v123, v196 row_newbcast:0 row_mask:0xf bank_mask:0xf\n"
"v_mul_f32_dpp v197, v123, v197 row_newbcast:4 row_mask:0xf bank_mask:0xf\n"
"v_mul_f32_dpp v198, v123, v198 row_newbcast:8 row_mask:0xf bank_mask:0xf\n"
"v_mul_f32_dpp v199, v123, v199 row_newbcast:12 row_mask:0xf bank_mask:0xf\n"
"v_accvgpr_write_b32 a168, v192\n"
"v_accvgpr_write_b32 a169, v193\n"
"v_accvgpr_write_b32 a170, v194\n"
"v_accvgpr_write_b32 a171, v195\n"
"v_accvgpr_write_b32 a172, v196\n"
"v_accvgpr_write_b32 a173, v197\n"
"v_accvgpr_write_b32 a174, v198\n"
"v_accvgpr_write_b32 a175, v199\n"
"v_accvgpr_read_b32 v192, a176\n"
"v_accvgpr_read_b32 v193, a177\n"
"v_accvgpr_read_b32 v194, a178\n"
"v_accvgpr_read_b32 v195, a179\n"
"v_accvgpr_read_b32 v196, a180\n"
"v_accvgpr_read_b32 v197, a181\n"
"v_accvgpr_read_b32 v198, a182\n"
"v_accvgpr_read_b32 v199, a183\n"
"v_mul_f32_dpp v192, v123, v192 row_newbcast:0 row_mask:0xf bank_mask:0xf\n"
"v_mul_f32_dpp v193, v123, v193 row_newbcast:4 row_mask:0xf bank_mask:0xf\n"
"v_mul_f32_dpp v194, v123, v194 row_newbcast:8 row_mask:0xf bank_mask:0xf\n"
"v_mul_f32_dpp v195, v123, v195 row_newbcast:12 row_mask:0xf bank_mask:0xf\n"
"v_mul_f32_dpp v196, v123, v196 row_newbcast:0 row_mask:0xf bank_mask:0xf\n"
"v_mul_f32_dpp v197, v123, v197 row_newbcast:4 row_mask:0xf bank_mask:0xf\n"
"v_mul_f32_dpp v198, v123, v198 row_newbcast:8 row_mask:0xf bank_mask:0xf\n"
"v_mul_f32_dpp v199, v123, v199 row_newbcast:12 row_mask:0xf bank_mask:0xf\n"
"v_accvgpr_write_b32 a176, v192\n"
"v_accvgpr_write_b32 a177, v193\n"
"v_accvgpr_write_b32 a178, v194\n"
"v_accvgpr_write_b32 a179, v195\n"
"v_accvgpr_write_b32 a180, v196\n"
"v_accvgpr_write_b32 a181, v197\n"
"v_accvgpr_write_b32 a182, v198\n"
"v_accvgpr_write_b32 a183, v199\n"
"v_accvgpr_read_b32 v192, a184\n"
"v_accvgpr_read_b32 v193, a185\n"
"v_accvgpr_read_b32 v194, a186\n"
"v_accvgpr_read_b32 v195, a187\n"
"v_accvgpr_read_b32 v196, a188\n"
"v_accvgpr_read_b32 v197, a189\n"
"v_accvgpr_read_b32 v198, a190\n"
"v_accvgpr_read_b32 v199, a191\n"
"v_mul_f32_dpp v192, v123, v192 row_newbcast:0 row_mask:0xf bank_mask:0xf\n"
"v_mul_f32_dpp v193, v123, v193 row_newbcast:4 row_mask:0xf bank_mask:0xf\n"
"v_mul_f32_dpp v194, v123, v194 row_newbcast:8 row_mask:0xf bank_mask:0xf\n"
"v_mul_f32_dpp v195, v123, v195 row_newbcast:12 row_mask:0xf bank_mask:0xf\n"
"v_mul_f32_dpp v196, v123, v196 row_newbcast:0 row_mask:0xf bank_mask:0xf\n"
"v_mul_f32_dpp v197, v123, v197 row_newbcast:4 row_mask:0xf bank_mask:0xf\n"
"v_mul_f32_dpp v198, v123, v198 row_newbcast:8 row_mask:0xf bank_mask:0xf\n"
"v_mul_f32_dpp v199, v123, v199 row_newbcast:12 row_mask:0xf bank_mask:0xf\n"
"v_accvgpr_write_b32 a184, v192\n"
"v_accvgpr_write_b32 a185, v193\n"
"v_accvgpr_write_b32 a186, v194\n"
"v_accvgpr_write_b32 a187, v195\n"
"v_accvgpr_write_b32 a188, v196\n"
"v_accvgpr_write_b32 a189, v197\n"
"v_accvgpr_write_b32 a190, v198\n"
"v_accvgpr_write_b32 a191, v199\n"
"v_sub_f32 v32, v32, v112\n"
"v_sub_f32 v33, v33, v112\n"
"v_sub_f32 v34, v34, v112\n"
"v_sub_f32 v35, v35, v112\n"
"v_sub_f32 v36, v36, v112\n"
"v_sub_f32 v37, v37, v112\n"
"v_sub_f32 v38, v38, v112\n"
"v_sub_f32 v39, v39, v112\n"
"v_sub_f32 v0, v0, v112\n"
"v_sub_f32 v1, v1, v112\n"
"v_sub_f32 v2, v2, v112\n"
"v_sub_f32 v3, v3, v112\n"
"v_sub_f32 v4, v4, v112\n"
"v_sub_f32 v5, v5, v112\n"
"v_sub_f32 v6, v6, v112\n"
"v_sub_f32 v7, v7, v112\n"
"v_sub_f32 v64, v64, v112\n"
"v_sub_f32 v65, v65, v112\n"
"v_sub_f32 v66, v66, v112\n"
"v_sub_f32 v67, v67, v112\n"
"v_sub_f32 v40, v40, v113\n"
"v_sub_f32 v41, v41, v113\n"
"v_sub_f32 v42, v42, v113\n"
"v_sub_f32 v43, v43, v113\n"
"v_sub_f32 v44, v44, v113\n"
"v_sub_f32 v45, v45, v113\n"
"v_sub_f32 v46, v46, v113\n"
"v_sub_f32 v47, v47, v113\n"
"v_sub_f32 v8, v8, v113\n"
"v_sub_f32 v9, v9, v113\n"
"v_sub_f32 v10, v10, v113\n"
"v_sub_f32 v11, v11, v113\n"
"v_sub_f32 v12, v12, v113\n"
"v_sub_f32 v13, v13, v113\n"
"v_sub_f32 v14, v14, v113\n"
"v_sub_f32 v15, v15, v113\n"
"v_sub_f32 v68, v68, v113\n"
"v_sub_f32 v69, v69, v113\n"
"v_sub_f32 v70, v70, v113\n"
"v_sub_f32 v71, v71, v113\n"
"v_sub_f32 v48, v48, v114\n"
"v_sub_f32 v49, v49, v114\n"
"v_sub_f32 v50, v50, v114\n"
"v_sub_f32 v51, v51, v114\n"
"v_sub_f32 v52, v52, v114\n"
"v_sub_f32 v53, v53, v114\n"
"v_sub_f32 v54, v54, v114\n"
"v_sub_f32 v55, v55, v114\n"
"v_sub_f32 v16, v16, v114\n"
"v_sub_f32 v17, v17, v114\n"
"v_sub_f32 v18, v18, v114\n"
"v_sub_f32 v19, v19, v114\n"
"v_sub_f32 v20, v20, v114\n"
"v_sub_f32 v21, v21, v114\n"
"v_sub_f32 v22, v22, v114\n"
"v_sub_f32 v23, v23, v114\n"
"v_sub_f32 v72, v72, v114\n"
"v_sub_f32 v73, v73, v114\n"
"v_sub_f32 v74, v74, v114\n"
"v_sub_f32 v75, v75, v114\n"
"v_sub_f32 v56, v56, v115\n"
"v_sub_f32 v57, v57, v115\n"
"v_sub_f32 v58, v58, v115\n"
"v_sub_f32 v59, v59, v115\n"
"v_sub_f32 v60, v60, v115\n"
"v_sub_f32 v61, v61, v115\n"
"v_sub_f32 v62, v62, v115\n"
"v_sub_f32 v63, v63, v115\n"
"v_sub_f32 v24, v24, v115\n"
"v_sub_f32 v25, v25, v115\n"
"v_sub_f32 v26, v26, v115\n"
"v_sub_f32 v27, v27, v115\n"
"v_sub_f32 v28, v28, v115\n"
"v_sub_f32 v29, v29, v115\n"
"v_sub_f32 v30, v30, v115\n"
"v_sub_f32 v31, v31, v115\n"
"v_sub_f32 v76, v76, v115\n"
"v_sub_f32 v77, v77, v115\n"
"v_sub_f32 v78, v78, v115\n"
"v_sub_f32 v79, v79, v115\n"
"v_exp_f32 v192, v32\n"
"s_nop 0\n"
"v_add_f32 v204, v200, v192\n"
"v_exp_f32 v193, v33\n"
"s_nop 0\n"
"v_add_f32 v204, v204, v193\n"
"v_exp_f32 v194, v34\n"
"v_cvt_pk_bf16_f32 v112, v192, v193\n"
"v_exp_f32 v195, v35\n"
"v_add_f32 v204, v204, v194\n"
"v_exp_f32 v196, v36\n"
"v_add_f32 v204, v204, v195\n"
"v_exp_f32 v197, v37\n"
"v_cvt_pk_bf16_f32 v113, v194, v195\n"
"v_exp_f32 v198, v38\n"
"v_add_f32 v204, v204, v196\n"
"v_add_f32 v204, v204, v197\n"
"v_cvt_pk_bf16_f32 v114, v196, v197\n"
"v_add_f32 v204, v204, v198\n"
"v_exp_f32 v199, v39\n"
"s_nop 0\n"
"v_add_f32 v204, v204, v199\n"
"v_exp_f32 v192, v40\n"
"v_cvt_pk_bf16_f32 v115, v198, v199\n"
"v_exp_f32 v193, v41\n"
"v_add_f32 v205, v201, v192\n"
"v_add_f32 v205, v205, v193\n"
"v_exp_f32 v194, v42\n"
"v_exp_f32 v195, v43\n"
"v_cvt_pk_bf16_f32 v116, v192, v193\n"
"v_exp_f32 v196, v44\n"
"v_add_f32 v205, v205, v194\n"
"v_add_f32 v205, v205, v195\n"
"v_exp_f32 v197, v45\n"
"v_cvt_pk_bf16_f32 v117, v194, v195\n"
"v_exp_f32 v198, v46\n"
"v_exp_f32 v199, v47\n"
"v_add_f32 v205, v205, v196\n"
"v_add_f32 v205, v205, v197\n"
"v_cvt_pk_bf16_f32 v118, v196, v197\n"
"v_exp_f32 v192, v48\n"
"v_add_f32 v205, v205, v198\n"
"v_exp_f32 v193, v49\n"
"v_exp_f32 v194, v50\n"
"v_add_f32 v205, v205, v199\n"
"v_cvt_pk_bf16_f32 v119, v198, v199\n"
"v_add_f32 v206, v202, v192\n"
"v_exp_f32 v195, v51\n"
"v_exp_f32 v196, v52\n"
"v_add_f32 v206, v206, v193\n"
"v_exp_f32 v197, v53\n"
"v_cvt_pk_bf16_f32 v120, v192, v193\n"
"v_add_f32 v206, v206, v194\n"
"v_add_f32 v206, v206, v195\n"
"v_exp_f32 v198, v54\n"
"v_exp_f32 v199, v55\n"
"v_cvt_pk_bf16_f32 v121, v194, v195\n"
"v_exp_f32 v192, v56\n"
"v_add_f32 v206, v206, v196\n"
"v_add_f32 v206, v206, v197\n"
"v_exp_f32 v193, v57\n"
"v_cvt_pk_bf16_f32 v122, v196, v197\n"
"v_exp_f32 v194, v58\n"
"v_exp_f32 v195, v59\n"
"v_add_f32 v206, v206, v198\n"
"v_add_f32 v206, v206, v199\n"
"v_cvt_pk_bf16_f32 v123, v198, v199\n"
"v_exp_f32 v196, v60\n"
"v_add_f32 v207, v203, v192\n"
"v_exp_f32 v197, v61\n"
"v_exp_f32 v198, v62\n"
"v_add_f32 v207, v207, v193\n"
"v_cvt_pk_bf16_f32 v124, v192, v193\n"
"v_exp_f32 v199, v63\n"
"v_add_f32 v207, v207, v194\n"
"v_add_f32 v207, v207, v195\n"
"v_cvt_pk_bf16_f32 v125, v194, v195\n"
"v_add_f32 v207, v207, v196\n"
"v_add_f32 v207, v207, v197\n"
"v_cvt_pk_bf16_f32 v126, v196, v197\n"
"v_add_f32 v207, v207, v198\n"
"v_add_f32 v207, v207, v199\n"
"v_cvt_pk_bf16_f32 v127, v198, v199\n"
"s_nop 2\n"
"s_setpc_b64 s[86:87]\n"
".Lat_rare2:\n"
"s_nop 15\n"
"v_log_f32 v84, v204\n"
"v_log_f32 v85, v205\n"
"v_log_f32 v86, v206\n"
"v_log_f32 v87, v207\n"
"v_max3_f32 v80, v0, v1, v2\n"
"v_max3_f32 v80, v80, v3, v4\n"
"v_max3_f32 v80, v80, v5, v6\n"
"v_max3_f32 v80, v80, v7, v84\n"
"v_max3_f32 v81, v8, v9, v10\n"
"v_max3_f32 v81, v81, v11, v12\n"
"v_max3_f32 v81, v81, v13, v14\n"
"v_max3_f32 v81, v81, v15, v85\n"
"v_max3_f32 v82, v16, v17, v18\n"
"v_max3_f32 v82, v82, v19, v20\n"
"v_max3_f32 v82, v82, v21, v22\n"
"v_max3_f32 v82, v82, v23, v86\n"
"v_max3_f32 v83, v24, v25, v26\n"
"v_max3_f32 v83, v83, v27, v28\n"
"v_max3_f32 v83, v83, v29, v30\n"
"v_max3_f32 v83, v83, v31, v87\n"
"v_mov_b32 v84, v80\n"
"v_mov_b32 v85, v81\n"
"v_mov_b32 v86, v82\n"
"v_mov_b32 v87, v83\n"
"s_nop 1\n"
"v_permlane16_swap_b32 v80, v84\n"
"v_permlane16_swap_b32 v81, v85\n"
"v_permlane16_swap_b32 v82, v86\n"
"v_permlane16_swap_b32 v83, v87\n"
"s_nop 0\n"
"v_max_f32 v80, v80, v84\n"
"v_max_f32 v81, v81, v85\n"
"v_max_f32 v82, v82, v86\n"
"v_max_f32 v83, v83, v87\n"
"v_mov_b32 v84, v80\n"
"v_mov_b32 v85, v81\n"
"v_mov_b32 v86, v82\n"
"v_mov_b32 v87, v83\n"
"s_nop 1\n"
"v_permlane32_swap_b32 v80, v84\n"
"v_permlane32_swap_b32 v81, v85\n"
"v_permlane32_swap_b32 v82, v86\n"
"v_permlane32_swap_b32 v83, v87\n"
"s_nop 0\n"
"v_max_f32 v80, v80, v84\n"
"v_max_f32 v81, v81, v85\n"
"v_max_f32 v82, v82, v86\n"
"v_max_f32 v83, v83, v87\n"
"v_max_f32 v80, 0, v80\n"
"v_max_f32 v81, 0, v81\n"
"v_max_f32 v82, 0, v82\n"
"v_max_f32 v83, 0, v83\n"
"v_sub_f32 v84, 0, v80\n"
"v_sub_f32 v85, 0, v81\n"
"v_sub_f32 v86, 0, v82\n"
"v_sub_f32 v87, 0, v83\n"
"v_exp_f32 v88, v84\n"
"v_exp_f32 v89, v85\n"
"v_exp_f32 v90, v86\n"
"v_exp_f32 v91, v87\n"
"s_nop 0\n"
"v_mul_f32 v204, v204, v88\n"
"v_lshlrev_b32 v192, 16, v112\n"
"v_and_b32 v193, 0xffff0000, v112\n"
"v_mul_f32 v192, v192, v88\n"
"v_mul_f32 v193, v193, v88\n"
"v_cvt_pk_bf16_f32 v112, v192, v193\n"
"v_lshlrev_b32 v192, 16, v113\n"
"v_and_b32 v193, 0xffff0000, v113\n"
"v_mul_f32 v192, v192, v88\n"
"v_mul_f32 v193, v193, v88\n"
"v_cvt_pk_bf16_f32 v113, v192, v193\n"
"v_lshlrev_b32 v192, 16, v114\n"
"v_and_b32 v193, 0xffff0000, v114\n"
"v_mul_f32 v192, v192, v88\n"
"v_mul_f32 v193, v193, v88\n"
"v_cvt_pk_bf16_f32 v114, v192, v193\n"
"v_lshlrev_b32 v192, 16, v115\n"
"v_and_b32 v193, 0xffff0000, v115\n"
"v_mul_f32 v192, v192, v88\n"
"v_mul_f32 v193, v193, v88\n"
"v_cvt_pk_bf16_f32 v115, v192, v193\n"
"v_mul_f32 v205, v205, v89\n"
"v_lshlrev_b32 v192, 16, v116\n"
"v_and_b32 v193, 0xffff0000, v116\n"
"v_mul_f32 v192, v192, v89\n"
"v_mul_f32 v193, v193, v89\n"
"v_cvt_pk_bf16_f32 v116, v192, v193\n"
"v_lshlrev_b32 v192, 16, v117\n"
"v_and_b32 v193, 0xffff0000, v117\n"
"v_mul_f32 v192, v192, v89\n"
"v_mul_f32 v193, v193, v89\n"
"v_cvt_pk_bf16_f32 v117, v192, v193\n"
"v_lshlrev_b32 v192, 16, v118\n"
"v_and_b32 v193, 0xffff0000, v118\n"
"v_mul_f32 v192, v192, v89\n"
"v_mul_f32 v193, v193, v89\n"
"v_cvt_pk_bf16_f32 v118, v192, v193\n"
"v_lshlrev_b32 v192, 16, v119\n"
"v_and_b32 v193, 0xffff0000, v119\n"
"v_mul_f32 v192, v192, v89\n"
"v_mul_f32 v193, v193, v89\n"
"v_cvt_pk_bf16_f32 v119, v192, v193\n"
"v_mul_f32 v206, v206, v90\n"
"v_lshlrev_b32 v192, 16, v120\n"
"v_and_b32 v193, 0xffff0000, v120\n"
"v_mul_f32 v192, v192, v90\n"
"v_mul_f32 v193, v193, v90\n"
"v_cvt_pk_bf16_f32 v120, v192, v193\n"
"v_lshlrev_b32 v192, 16, v121\n"
"v_and_b32 v193, 0xffff0000, v121\n"
"v_mul_f32 v192, v192, v90\n"
"v_mul_f32 v193, v193, v90\n"
"v_cvt_pk_bf16_f32 v121, v192, v193\n"
"v_lshlrev_b32 v192, 16, v122\n"
"v_and_b32 v193, 0xffff0000, v122\n"
"v_mul_f32 v192, v192, v90\n"
"v_mul_f32 v193, v193, v90\n"
"v_cvt_pk_bf16_f32 v122, v192, v193\n"
"v_lshlrev_b32 v192, 16, v123\n"
"v_and_b32 v193, 0xffff0000, v123\n"
"v_mul_f32 v192, v192, v90\n"
"v_mul_f32 v193, v193, v90\n"
"v_cvt_pk_bf16_f32 v123, v192, v193\n"
"v_mul_f32 v207, v207, v91\n"
"v_lshlrev_b32 v192, 16, v124\n"
"v_and_b32 v193, 0xffff0000, v124\n"
"v_mul_f32 v192, v192, v91\n"
"v_mul_f32 v193, v193, v91\n"
"v_cvt_pk_bf16_f32 v124, v192, v193\n"
"v_lshlrev_b32 v192, 16, v125\n"
"v_and_b32 v193, 0xffff0000, v125\n"
"v_mul_f32 v192, v192, v91\n"
"v_mul_f32 v193, v193, v91\n"
"v_cvt_pk_bf16_f32 v125, v192, v193\n"
"v_lshlrev_b32 v192, 16, v126\n"
"v_and_b32 v193, 0xffff0000, v126\n"
"v_mul_f32 v192, v192, v91\n"
"v_mul_f32 v193, v193, v91\n"
"v_cvt_pk_bf16_f32 v126, v192, v193\n"
"v_lshlrev_b32 v192, 16, v127\n"
"v_and_b32 v193, 0xffff0000, v127\n"
"v_mul_f32 v192, v192, v91\n"
"v_mul_f32 v193, v193, v91\n"
"v_cvt_pk_bf16_f32 v127, v192, v193\n"
"v_lshrrev_b32 v84, 4, v251\n"
"ds_bpermute_b32 v88, v84, v88 offset:0\n"
"ds_bpermute_b32 v89, v84, v89 offset:0\n"
"ds_bpermute_b32 v90, v84, v90 offset:0\n"
"ds_bpermute_b32 v91, v84, v91 offset:0\n"
"v_accvgpr_read_b32 v192, a0\n"
"v_accvgpr_read_b32 v193, a1\n"
"v_accvgpr_read_b32 v194, a2\n"
"v_accvgpr_read_b32 v195, a3\n"
"v_accvgpr_read_b32 v196, a4\n"
"v_accvgpr_read_b32 v197, a5\n"
"v_accvgpr_read_b32 v198, a6\n"
"v_accvgpr_read_b32 v199, a7\n"
"s_waitcnt lgkmcnt(3)\n"
"v_mul_f32_dpp v192, v88, v192 row_newbcast:0 row_mask:0xf bank_mask:0xf\n"
"v_mul_f32_dpp v193, v88, v193 row_newbcast:4 row_mask:0xf bank_mask:0xf\n"
"v_mul_f32_dpp v194, v88, v194 row_newbcast:8 row_mask:0xf bank_mask:0xf\n"
"v_mul_f32_dpp v195, v88, v195 row_newbcast:12 row_mask:0xf bank_mask:0xf\n"
"v_mul_f32_dpp v196, v88, v196 row_newbcast:0 row_mask:0xf bank_mask:0xf\n"
"v_mul_f32_dpp v197, v88, v197 row_newbcast:4 row_mask:0xf bank_mask:0xf\n"
"v_mul_f32_dpp v198, v88, v198 row_newbcast:8 row_mask:0xf bank_mask:0xf\n"
"v_mul_f32_dpp v199, v88, v199 row_newbcast:12 row_mask:0xf bank_mask:0xf\n"
"v_accvgpr_write_b32 a0, v192\n"
"v_accvgpr_write_b32 a1, v193\n"
"v_accvgpr_write_b32 a2, v194\n"
"v_accvgpr_write_b32 a3, v195\n"
"v_accvgpr_write_b32 a4, v196\n"
"v_accvgpr_write_b32 a5, v197\n"
"v_accvgpr_write_b32 a6, v198\n"
"v_accvgpr_write_b32 a7, v199\n"
"v_accvgpr_read_b32 v192, a8\n"
"v_accvgpr_read_b32 v193, a9\n"
"v_accvgpr_read_b32 v194, a10\n"
"v_accvgpr_read_b32 v195, a11\n"
"v_accvgpr_read_b32 v196, a12\n"
"v_accvgpr_read_b32 v197, a13\n"
"v_accvgpr_read_b32 v198, a14\n"
"v_accvgpr_read_b32 v199, a15\n"
"v_mul_f32_dpp v192, v88, v192 row_newbcast:0 row_mask:0xf bank_mask:0xf\n"
"v_mul_f32_dpp v193, v88, v193 row_newbcast:4 row_mask:0xf bank_mask:0xf\n"
"v_mul_f32_dpp v194, v88, v194 row_newbcast:8 row_mask:0xf bank_mask:0xf\n"
"v_mul_f32_dpp v195, v88, v195 row_newbcast:12 row_mask:0xf bank_mask:0xf\n"
"v_mul_f32_dpp v196, v88, v196 row_newbcast:0 row_mask:0xf bank_mask:0xf\n"
"v_mul_f32_dpp v197, v88, v197 row_newbcast:4 row_mask:0xf bank_mask:0xf\n"
"v_mul_f32_dpp v198, v88, v198 row_newbcast:8 row_mask:0xf bank_mask:0xf\n"
"v_mul_f32_dpp v199, v88, v199 row_newbcast:12 row_mask:0xf bank_mask:0xf\n"
"v_accvgpr_write_b32 a8, v192\n"
"v_accvgpr_write_b32 a9, v193\n"
"v_accvgpr_write_b32 a10, v194\n"
"v_accvgpr_write_b32 a11, v195\n"
"v_accvgpr_write_b32 a12, v196\n"
"v_accvgpr_write_b32 a13, v197\n"
"v_accvgpr_write_b32 a14, v198\n"
"v_accvgpr_write_b32 a15, v199\n"
"v_accvgpr_read_b32 v192, a16\n"
"v_accvgpr_read_b32 v193, a17\n"
"v_accvgpr_read_b32 v194, a18\n"
"v_accvgpr_read_b32 v195, a19\n"
"v_accvgpr_read_b32 v196, a20\n"
"v_accvgpr_read_b32 v197, a21\n"
"v_accvgpr_read_b32 v198, a22\n"
"v_accvgpr_read_b32 v199, a23\n"
"v_mul_f32_dpp v192, v88, v192 row_newbcast:0 row_mask:0xf bank_mask:0xf\n"
"v_mul_f32_dpp v193, v88, v193 row_newbcast:4 row_mask:0xf bank_mask:0xf\n"
"v_mul_f32_dpp v194, v88, v194 row_newbcast:8 row_mask:0xf bank_mask:0xf\n"
"v_mul_f32_dpp v195, v88, v195 row_newbcast:12 row_mask:0xf bank_mask:0xf\n"
"v_mul_f32_dpp v196, v88, v196 row_newbcast:0 row_mask:0xf bank_mask:0xf\n"
"v_mul_f32_dpp v197, v88, v197 row_newbcast:4 row_mask:0xf bank_mask:0xf\n"
"v_mul_f32_dpp v198, v88, v198 row_newbcast:8 row_mask:0xf bank_mask:0xf\n"
"v_mul_f32_dpp v199, v88, v199 row_newbcast:12 row_mask:0xf bank_mask:0xf\n"
"v_accvgpr_write_b32 a16, v192\n"
"v_accvgpr_write_b32 a17, v193\n"
"v_accvgpr_write_b32 a18, v194\n"
"v_accvgpr_write_b32 a19, v195\n"
"v_accvgpr_write_b32 a20, v196\n"
"v_accvgpr_write_b32 a21, v197\n"
"v_accvgpr_write_b32 a22, v198\n"
"v_accvgpr_write_b32 a23, v199\n"
"v_accvgpr_read_b32 v192, a24\n"
"v_accvgpr_read_b32 v193, a25\n"
"v_accvgpr_read_b32 v194, a26\n"
"v_accvgpr_read_b32 v195, a27\n"
"v_accvgpr_read_b32 v196, a28\n"
"v_accvgpr_read_b32 v197, a29\n"
"v_accvgpr_read_b32 v198, a30\n"
"v_accvgpr_read_b32 v199, a31\n"
"v_mul_f32_dpp v192, v88, v192 row_newbcast:0 row_mask:0xf bank_mask:0xf\n"
"v_mul_f32_dpp v193, v88, v193 row_newbcast:4 row_mask:0xf bank_mask:0xf\n"
"v_mul_f32_dpp v194, v88, v194 row_newbcast:8 row_mask:0xf bank_mask:0xf\n"
"v_mul_f32_dpp v195, v88, v195 row_newbcast:12 row_mask:0xf bank_mask:0xf\n"
"v_mul_f32_dpp v196, v88, v196 row_newbcast:0 row_mask:0xf bank_mask:0xf\n"
"v_mul_f32_dpp v197, v88, v197 row_newbcast:4 row_mask:0xf bank_mask:0xf\n"
"v_mul_f32_dpp v198, v88, v198 row_newbcast:8 row_mask:0xf bank_mask:0xf\n"
"v_mul_f32_dpp v199, v88, v199 row_newbcast:12 row_mask:0xf bank_mask:0xf\n"
"v_accvgpr_write_b32 a24, v192\n"
"v_accvgpr_write_b32 a25, v193\n"
"v_accvgpr_write_b32 a26, v194\n"
"v_accvgpr_write_b32 a27, v195\n"
"v_accvgpr_write_b32 a28, v196\n"
"v_accvgpr_write_b32 a29, v197\n"
"v_accvgpr_write_b32 a30, v198\n"
"v_accvgpr_write_b32 a31, v199\n"
"v_accvgpr_read_b32 v192, a32\n"
"v_accvgpr_read_b32 v193, a33\n"
"v_accvgpr_read_b32 v194, a34\n"
"v_accvgpr_read_b32 v195, a35\n"
"v_accvgpr_read_b32 v196, a36\n"
"v_accvgpr_read_b32 v197, a37\n"
"v_accvgpr_read_b32 v198, a38\n"
"v_accvgpr_read_b32 v199, a39\n"
"v_mul_f32_dpp v192, v88, v192 row_newbcast:0 row_mask:0xf bank_mask:0xf\n"
"v_mul_f32_dpp v193, v88, v193 row_newbcast:4 row_mask:0xf bank_mask:0xf\n"
"v_mul_f32_dpp v194, v88, v194 row_newbcast:8 row_mask:0xf bank_mask:0xf\n"
"v_mul_f32_dpp v195, v88, v195 row_newbcast:12 row_mask:0xf bank_mask:0xf\n"
"v_mul_f32_dpp v196, v88, v196 row_newbcast:0 row_mask:0xf bank_mask:0xf\n"
"v_mul_f32_dpp v197, v88, v197 row_newbcast:4 row_mask:0xf bank_mask:0xf\n"
"v_mul_f32_dpp v198, v88, v198 row_newbcast:8 row_mask:0xf bank_mask:0xf\n"
"v_mul_f32_dpp v199, v88, v199 row_newbcast:12 row_mask:0xf bank_mask:0xf\n"
"v_accvgpr_write_b32 a32, v192\n"
"v_accvgpr_write_b32 a33, v193\n"
"v_accvgpr_write_b32 a34, v194\n"
"v_accvgpr_write_b32 a35, v195\n"
"v_accvgpr_write_b32 a36, v196\n"
"v_accvgpr_write_b32 a37, v197\n"
"v_accvgpr_write_b32 a38, v198\n"
"v_accvgpr_write_b32 a39, v199\n"
"v_accvgpr_read_b32 v192, a40\n"
"v_accvgpr_read_b32 v193, a41\n"
"v_accvgpr_read_b32 v194, a42\n"
"v_accvgpr_read_b32 v195, a43\n"
"v_accvgpr_read_b32 v196, a44\n"
"v_accvgpr_read_b32 v197, a45\n"
"v_accvgpr_read_b32 v198, a46\n"
"v_accvgpr_read_b32 v199, a47\n"
"v_mul_f32_dpp v192, v88, v192 row_newbcast:0 row_mask:0xf bank_mask:0xf\n"
"v_mul_f32_dpp v193, v88, v193 row_newbcast:4 row_mask:0xf bank_mask:0xf\n"
"v_mul_f32_dpp v194, v88, v194 row_newbcast:8 row_mask:0xf bank_mask:0xf\n"
"v_mul_f32_dpp v195, v88, v195 row_newbcast:12 row_mask:0xf bank_mask:0xf\n"
"v_mul_f32_dpp v196, v88, v196 row_newbcast:0 row_mask:0xf bank_mask:0xf\n"
"v_mul_f32_dpp v197, v88, v197 row_newbcast:4 row_mask:0xf bank_mask:0xf\n"
"v_mul_f32_dpp v198, v88, v198 row_newbcast:8 row_mask:0xf bank_mask:0xf\n"
"v_mul_f32_dpp v199, v88, v199 row_newbcast:12 row_mask:0xf bank_mask:0xf\n"
"v_accvgpr_write_b32 a40, v192\n"
"v_accvgpr_write_b32 a41, v193\n"
"v_accvgpr_write_b32 a42, v194\n"
"v_accvgpr_write_b32 a43, v195\n"
"v_accvgpr_write_b32 a44, v196\n"
"v_accvgpr_write_b32 a45, v197\n"
"v_accvgpr_write_b32 a46, v198\n"
"v_accvgpr_write_b32 a47, v199\n"
"v_accvgpr_read_b32 v192, a48\n"
"v_accvgpr_read_b32 v193, a49\n"
"v_accvgpr_read_b32 v194, a50\n"
"v_accvgpr_read_b32 v195, a51\n"
"v_accvgpr_read_b32 v196, a52\n"
"v_accvgpr_read_b32 v197, a53\n"
"v_accvgpr_read_b32 v198, a54\n"
"v_accvgpr_read_b32 v199, a55\n"
"s_waitcnt lgkmcnt(2)\n"
"v_mul_f32_dpp v192, v89, v192 row_newbcast:0 row_mask:0xf bank_mask:0xf\n"
"v_mul_f32_dpp v193, v89, v193 row_newbcast:4 row_mask:0xf bank_mask:0xf\n"
"v_mul_f32_dpp v194, v89, v194 row_newbcast:8 row_mask:0xf bank_mask:0xf\n"
"v_mul_f32_dpp v195, v89, v195 row_newbcast:12 row_mask:0xf bank_mask:0xf\n"
"v_mul_f32_dpp v196, v89, v196 row_newbcast:0 row_mask:0xf bank_mask:0xf\n"
"v_mul_f32_dpp v197, v89, v197 row_newbcast:4 row_mask:0xf bank_mask:0xf\n"
"v_mul_f32_dpp v198, v89, v198 row_newbcast:8 row_mask:0xf bank_mask:0xf\n"
"v_mul_f32_dpp v199, v89, v199 row_newbcast:12 row_mask:0xf bank_mask:0xf\n"
"v_accvgpr_write_b32 a48, v192\n"
"v_accvgpr_write_b32 a49, v193\n"
"v_accvgpr_write_b32 a50, v194\n"
"v_accvgpr_write_b32 a51, v195\n"
"v_accvgpr_write_b32 a52, v196\n"
"v_accvgpr_write_b32 a53, v197\n"
"v_accvgpr_write_b32 a54, v198\n"
"v_accvgpr_write_b32 a55, v199\n"
"v_accvgpr_read_b32 v192, a56\n"
"v_accvgpr_read_b32 v193, a57\n"
"v_accvgpr_read_b32 v194, a58\n"
"v_accvgpr_read_b32 v195, a59\n"
"v_accvgpr_read_b32 v196, a60\n"
"v_accvgpr_read_b32 v197, a61\n"
"v_accvgpr_read_b32 v198, a62\n"
"v_accvgpr_read_b32 v199, a63\n"
"v_mul_f32_dpp v192, v89, v192 row_newbcast:0 row_mask:0xf bank_mask:0xf\n"
"v_mul_f32_dpp v193, v89, v193 row_newbcast:4 row_mask:0xf bank_mask:0xf\n"
"v_mul_f32_dpp v194, v89, v194 row_newbcast:8 row_mask:0xf bank_mask:0xf\n"
"v_mul_f32_dpp v195, v89, v195 row_newbcast:12 row_mask:0xf bank_mask:0xf\n"
"v_mul_f32_dpp v196, v89, v196 row_newbcast:0 row_mask:0xf bank_mask:0xf\n"
"v_mul_f32_dpp v197, v89, v197 row_newbcast:4 row_mask:0xf bank_mask:0xf\n"
"v_mul_f32_dpp v198, v89, v198 row_newbcast:8 row_mask:0xf bank_mask:0xf\n"
"v_mul_f32_dpp v199, v89, v199 row_newbcast:12 row_mask:0xf bank_mask:0xf\n"
"v_accvgpr_write_b32 a56, v192\n"
"v_accvgpr_write_b32 a57, v193\n"
"v_accvgpr_write_b32 a58, v194\n"
"v_accvgpr_write_b32 a59, v195\n"
"v_accvgpr_write_b32 a60, v196\n"
"v_accvgpr_write_b32 a61, v197\n"
"v_accvgpr_write_b32 a62, v198\n"
"v_accvgpr_write_b32 a63, v199\n"
"v_accvgpr_read_b32 v192, a64\n"
"v_accvgpr_read_b32 v193, a65\n"
"v_accvgpr_read_b32 v194, a66\n"
"v_accvgpr_read_b32 v195, a67\n"
"v_accvgpr_read_b32 v196, a68\n"
"v_accvgpr_read_b32 v197, a69\n"
"v_accvgpr_read_b32 v198, a70\n"
"v_accvgpr_read_b32 v199, a71\n"
"v_mul_f32_dpp v192, v89, v192 row_newbcast:0 row_mask:0xf bank_mask:0xf\n"
"v_mul_f32_dpp v193, v89, v193 row_newbcast:4 row_mask:0xf bank_mask:0xf\n"
"v_mul_f32_dpp v194, v89, v194 row_newbcast:8 row_mask:0xf bank_mask:0xf\n"
"v_mul_f32_dpp v195, v89, v195 row_newbcast:12 row_mask:0xf bank_mask:0xf\n"
"v_mul_f32_dpp v196, v89, v196 row_newbcast:0 row_mask:0xf bank_mask:0xf\n"
"v_mul_f32_dpp v197, v89, v197 row_newbcast:4 row_mask:0xf bank_mask:0xf\n"
"v_mul_f32_dpp v198, v89, v198 row_newbcast:8 row_mask:0xf bank_mask:0xf\n"
"v_mul_f32_dpp v199, v89, v199 row_newbcast:12 row_mask:0xf bank_mask:0xf\n"
"v_accvgpr_write_b32 a64, v192\n"
"v_accvgpr_write_b32 a65, v193\n"
"v_accvgpr_write_b32 a66, v194\n"
"v_accvgpr_write_b32 a67, v195\n"
"v_accvgpr_write_b32 a68, v196\n"
"v_accvgpr_write_b32 a69, v197\n"
"v_accvgpr_write_b32 a70, v198\n"
"v_accvgpr_write_b32 a71, v199\n"
"v_accvgpr_read_b32 v192, a72\n"
"v_accvgpr_read_b32 v193, a73\n"
"v_accvgpr_read_b32 v194, a74\n"
"v_accvgpr_read_b32 v195, a75\n"
"v_accvgpr_read_b32 v196, a76\n"
"v_accvgpr_read_b32 v197, a77\n"
"v_accvgpr_read_b32 v198, a78\n"
"v_accvgpr_read_b32 v199, a79\n"
"v_mul_f32_dpp v192, v89, v192 row_newbcast:0 row_mask:0xf bank_mask:0xf\n"
"v_mul_f32_dpp v193, v89, v193 row_newbcast:4 row_mask:0xf bank_mask:0xf\n"
"v_mul_f32_dpp v194, v89, v194 row_newbcast:8 row_mask:0xf bank_mask:0xf\n"
"v_mul_f32_dpp v195, v89, v195 row_newbcast:12 row_mask:0xf bank_mask:0xf\n"
"v_mul_f32_dpp v196, v89, v196 row_newbcast:0 row_mask:0xf bank_mask:0xf\n"
"v_mul_f32_dpp v197, v89, v197 row_newbcast:4 row_mask:0xf bank_mask:0xf\n"
"v_mul_f32_dpp v198, v89, v198 row_newbcast:8 row_mask:0xf bank_mask:0xf\n"
"v_mul_f32_dpp v199, v89, v199 row_newbcast:12 row_mask:0xf bank_mask:0xf\n"
"v_accvgpr_write_b32 a72, v192\n"
"v_accvgpr_write_b32 a73, v193\n"
"v_accvgpr_write_b32 a74, v194\n"
"v_accvgpr_write_b32 a75, v195\n"
"v_accvgpr_write_b32 a76, v196\n"
"v_accvgpr_write_b32 a77, v197\n"
"v_accvgpr_write_b32 a78, v198\n"
"v_accvgpr_write_b32 a79, v199\n"
"v_accvgpr_read_b32 v192, a80\n"
"v_accvgpr_read_b32 v193, a81\n"
"v_accvgpr_read_b32 v194, a82\n"
"v_accvgpr_read_b32 v195, a83\n"
"v_accvgpr_read_b32 v196, a84\n"
"v_accvgpr_read_b32 v197, a85\n"
"v_accvgpr_read_b32 v198, a86\n"
"v_accvgpr_read_b32 v199, a87\n"
"v_mul_f32_dpp v192, v89, v192 row_newbcast:0 row_mask:0xf bank_mask:0xf\n"
"v_mul_f32_dpp v193, v89, v193 row_newbcast:4 row_mask:0xf bank_mask:0xf\n"
"v_mul_f32_dpp v194, v89, v194 row_newbcast:8 row_mask:0xf bank_mask:0xf\n"
"v_mul_f32_dpp v195, v89, v195 row_newbcast:12 row_mask:0xf bank_mask:0xf\n"
"v_mul_f32_dpp v196, v89, v196 row_newbcast:0 row_mask:0xf bank_mask:0xf\n"
"v_mul_f32_dpp v197, v89, v197 row_newbcast:4 row_mask:0xf bank_mask:0xf\n"
"v_mul_f32_dpp v198, v89, v198 row_newbcast:8 row_mask:0xf bank_mask:0xf\n"
"v_mul_f32_dpp v199, v89, v199 row_newbcast:12 row_mask:0xf bank_mask:0xf\n"
"v_accvgpr_write_b32 a80, v192\n"
"v_accvgpr_write_b32 a81, v193\n"
"v_accvgpr_write_b32 a82, v194\n"
"v_accvgpr_write_b32 a83, v195\n"
"v_accvgpr_write_b32 a84, v196\n"
"v_accvgpr_write_b32 a85, v197\n"
"v_accvgpr_write_b32 a86, v198\n"
"v_accvgpr_write_b32 a87, v199\n"
"v_accvgpr_read_b32 v192, a88\n"
"v_accvgpr_read_b32 v193, a89\n"
"v_accvgpr_read_b32 v194, a90\n"
"v_accvgpr_read_b32 v195, a91\n"
"v_accvgpr_read_b32 v196, a92\n"
"v_accvgpr_read_b32 v197, a93\n"
"v_accvgpr_read_b32 v198, a94\n"
"v_accvgpr_read_b32 v199, a95\n"
"v_mul_f32_dpp v192, v89, v192 row_newbcast:0 row_mask:0xf bank_mask:0xf\n"
"v_mul_f32_dpp v193, v89, v193 row_newbcast:4 row_mask:0xf bank_mask:0xf\n"
"v_mul_f32_dpp v194, v89, v194 row_newbcast:8 row_mask:0xf bank_mask:0xf\n"
"v_mul_f32_dpp v195, v89, v195 row_newbcast:12 row_mask:0xf bank_mask:0xf\n"
"v_mul_f32_dpp v196, v89, v196 row_newbcast:0 row_mask:0xf bank_mask:0xf\n"
"v_mul_f32_dpp v197, v89, v197 row_newbcast:4 row_mask:0xf bank_mask:0xf\n"
"v_mul_f32_dpp v198, v89, v198 row_newbcast:8 row_mask:0xf bank_mask:0xf\n"
"v_mul_f32_dpp v199, v89, v199 row_newbcast:12 row_mask:0xf bank_mask:0xf\n"
"v_accvgpr_write_b32 a88, v192\n"
"v_accvgpr_write_b32 a89, v193\n"
"v_accvgpr_write_b32 a90, v194\n"
"v_accvgpr_write_b32 a91, v195\n"
"v_accvgpr_write_b32 a92, v196\n"
"v_accvgpr_write_b32 a93, v197\n"
"v_accvgpr_write_b32 a94, v198\n"
"v_accvgpr_write_b32 a95, v199\n"
"v_accvgpr_read_b32 v192, a96\n"
"v_accvgpr_read_b32 v193, a97\n"
"v_accvgpr_read_b32 v194, a98\n"
"v_accvgpr_read_b32 v195, a99\n"
"v_accvgpr_read_b32 v196, a100\n"
"v_accvgpr_read_b32 v197, a101\n"
"v_accvgpr_read_b32 v198, a102\n"
"v_accvgpr_read_b32 v199, a103\n"
"s_waitcnt lgkmcnt(1)\n"
"v_mul_f32_dpp v192, v90, v192 row_newbcast:0 row_mask:0xf bank_mask:0xf\n"
"v_mul_f32_dpp v193, v90, v193 row_newbcast:4 row_mask:0xf bank_mask:0xf\n"
"v_mul_f32_dpp v194, v90, v194 row_newbcast:8 row_mask:0xf bank_mask:0xf\n"
"v_mul_f32_dpp v195, v90, v195 row_newbcast:12 row_mask:0xf bank_mask:0xf\n"
"v_mul_f32_dpp v196, v90, v196 row_newbcast:0 row_mask:0xf bank_mask:0xf\n"
"v_mul_f32_dpp v197, v90, v197 row_newbcast:4 row_mask:0xf bank_mask:0xf\n"
"v_mul_f32_dpp v198, v90, v198 row_newbcast:8 row_mask:0xf bank_mask:0xf\n"
"v_mul_f32_dpp v199, v90, v199 row_newbcast:12 row_mask:0xf bank_mask:0xf\n"
"v_accvgpr_write_b32 a96, v192\n"
"v_accvgpr_write_b32 a97, v193\n"
"v_accvgpr_write_b32 a98, v194\n"
"v_accvgpr_write_b32 a99, v195\n"
"v_accvgpr_write_b32 a100, v196\n"
"v_accvgpr_write_b32 a101, v197\n"
"v_accvgpr_write_b32 a102, v198\n"
"v_accvgpr_write_b32 a103, v199\n"
"v_accvgpr_read_b32 v192, a104\n"
"v_accvgpr_read_b32 v193, a105\n"
"v_accvgpr_read_b32 v194, a106\n"
"v_accvgpr_read_b32 v195, a107\n"
"v_accvgpr_read_b32 v196, a108\n"
"v_accvgpr_read_b32 v197, a109\n"
"v_accvgpr_read_b32 v198, a110\n"
"v_accvgpr_read_b32 v199, a111\n"
"v_mul_f32_dpp v192, v90, v192 row_newbcast:0 row_mask:0xf bank_mask:0xf\n"
"v_mul_f32_dpp v193, v90, v193 row_newbcast:4 row_mask:0xf bank_mask:0xf\n"
"v_mul_f32_dpp v194, v90, v194 row_newbcast:8 row_mask:0xf bank_mask:0xf\n"
"v_mul_f32_dpp v195, v90, v195 row_newbcast:12 row_mask:0xf bank_mask:0xf\n"
"v_mul_f32_dpp v196, v90, v196 row_newbcast:0 row_mask:0xf bank_mask:0xf\n"
"v_mul_f32_dpp v197, v90, v197 row_newbcast:4 row_mask:0xf bank_mask:0xf\n"
"v_mul_f32_dpp v198, v90, v198 row_newbcast:8 row_mask:0xf bank_mask:0xf\n"
"v_mul_f32_dpp v199, v90, v199 row_newbcast:12 row_mask:0xf bank_mask:0xf\n"
"v_accvgpr_write_b32 a104, v192\n"
"v_accvgpr_write_b32 a105, v193\n"
"v_accvgpr_write_b32 a106, v194\n"
"v_accvgpr_write_b32 a107, v195\n"
"v_accvgpr_write_b32 a108, v196\n"
"v_accvgpr_write_b32 a109, v197\n"
"v_accvgpr_write_b32 a110, v198\n"
"v_accvgpr_write_b32 a111, v199\n"
"v_accvgpr_read_b32 v192, a112\n"
"v_accvgpr_read_b32 v193, a113\n"
"v_accvgpr_read_b32 v194, a114\n"
"v_accvgpr_read_b32 v195, a115\n"
"v_accvgpr_read_b32 v196, a116\n"
"v_accvgpr_read_b32 v197, a117\n"
"v_accvgpr_read_b32 v198, a118\n"
"v_accvgpr_read_b32 v199, a119\n"
"v_mul_f32_dpp v192, v90, v192 row_newbcast:0 row_mask:0xf bank_mask:0xf\n"
"v_mul_f32_dpp v193, v90, v193 row_newbcast:4 row_mask:0xf bank_mask:0xf\n"
"v_mul_f32_dpp v194, v90, v194 row_newbcast:8 row_mask:0xf bank_mask:0xf\n"
"v_mul_f32_dpp v195, v90, v195 row_newbcast:12 row_mask:0xf bank_mask:0xf\n"
"v_mul_f32_dpp v196, v90, v196 row_newbcast:0 row_mask:0xf bank_mask:0xf\n"
"v_mul_f32_dpp v197, v90, v197 row_newbcast:4 row_mask:0xf bank_mask:0xf\n"
"v_mul_f32_dpp v198, v90, v198 row_newbcast:8 row_mask:0xf bank_mask:0xf\n"
"v_mul_f32_dpp v199, v90, v199 row_newbcast:12 row_mask:0xf bank_mask:0xf\n"
"v_accvgpr_write_b32 a112, v192\n"
"v_accvgpr_write_b32 a113, v193\n"
"v_accvgpr_write_b32 a114, v194\n"
"v_accvgpr_write_b32 a115, v195\n"
"v_accvgpr_write_b32 a116, v196\n"
"v_accvgpr_write_b32 a117, v197\n"
"v_accvgpr_write_b32 a118, v198\n"
"v_accvgpr_write_b32 a119, v199\n"
"v_accvgpr_read_b32 v192, a120\n"
"v_accvgpr_read_b32 v193, a121\n"
"v_accvgpr_read_b32 v194, a122\n"
"v_accvgpr_read_b32 v195, a123\n"
"v_accvgpr_read_b32 v196, a124\n"
"v_accvgpr_read_b32 v197, a125\n"
"v_accvgpr_read_b32 v198, a126\n"
"v_accvgpr_read_b32 v199, a127\n"
"v_mul_f32_dpp v192, v90, v192 row_newbcast:0 row_mask:0xf bank_mask:0xf\n"
"v_mul_f32_dpp v193, v90, v193 row_newbcast:4 row_mask:0xf bank_mask:0xf\n"
"v_mul_f32_dpp v194, v90, v194 row_newbcast:8 row_mask:0xf bank_mask:0xf\n"
"v_mul_f32_dpp v195, v90, v195 row_newbcast:12 row_mask:0xf bank_mask:0xf\n"
"v_mul_f32_dpp v196, v90, v196 row_newbcast:0 row_mask:0xf bank_mask:0xf\n"
"v_mul_f32_dpp v197, v90, v197 row_newbcast:4 row_mask:0xf bank_mask:0xf\n"
"v_mul_f32_dpp v198, v90, v198 row_newbcast:8 row_mask:0xf bank_mask:0xf\n"
"v_mul_f32_dpp v199, v90, v199 row_newbcast:12 row_mask:0xf bank_mask:0xf\n"
"v_accvgpr_write_b32 a120, v192\n"
"v_accvgpr_write_b32 a121, v193\n"
"v_accvgpr_write_b32 a122, v194\n"
"v_accvgpr_write_b32 a123, v195\n"
"v_accvgpr_write_b32 a124, v196\n"
"v_accvgpr_write_b32 a125, v197\n"
"v_accvgpr_write_b32 a126, v198\n"
"v_accvgpr_write_b32 a127, v199\n"
"v_accvgpr_read_b32 v192, a128\n"
"v_accvgpr_read_b32 v193, a129\n"
"v_accvgpr_read_b32 v194, a130\n"
"v_accvgpr_read_b32 v195, a131\n"
"v_accvgpr_read_b32 v196, a132\n"
"v_accvgpr_read_b32 v197, a133\n"
"v_accvgpr_read_b32 v198, a134\n"
"v_accvgpr_read_b32 v199, a135\n"
"v_mul_f32_dpp v192, v90, v192 row_newbcast:0 row_mask:0xf bank_mask:0xf\n"
"v_mul_f32_dpp v193, v90, v193 row_newbcast:4 row_mask:0xf bank_mask:0xf\n"
"v_mul_f32_dpp v194, v90, v194 row_newbcast:8 row_mask:0xf bank_mask:0xf\n"
"v_mul_f32_dpp v195, v90, v195 row_newbcast:12 row_mask:0xf bank_mask:0xf\n"
"v_mul_f32_dpp v196, v90, v196 row_newbcast:0 row_mask:0xf bank_mask:0xf\n"
"v_mul_f32_dpp v197, v90, v197 row_newbcast:4 row_mask:0xf bank_mask:0xf\n"
"v_mul_f32_dpp v198, v90, v198 row_newbcast:8 row_mask:0xf bank_mask:0xf\n"
"v_mul_f32_dpp v199, v90, v199 row_newbcast:12 row_mask:0xf bank_mask:0xf\n"
"v_accvgpr_write_b32 a128, v192\n"
"v_accvgpr_write_b32 a129, v193\n"
"v_accvgpr_write_b32 a130, v194\n"
"v_accvgpr_write_b32 a131, v195\n"
"v_accvgpr_write_b32 a132, v196\n"
"v_accvgpr_write_b32 a133, v197\n"
"v_accvgpr_write_b32 a134, v198\n"
"v_accvgpr_write_b32 a135, v199\n"
"v_accvgpr_read_b32 v192, a136\n"
"v_accvgpr_read_b32 v193, a137\n"
"v_accvgpr_read_b32 v194, a138\n"
"v_accvgpr_read_b32 v195, a139\n"
"v_accvgpr_read_b32 v196, a140\n"
"v_accvgpr_read_b32 v197, a141\n"
"v_accvgpr_read_b32 v198, a142\n"
"v_accvgpr_read_b32 v199, a143\n"
"v_mul_f32_dpp v192, v90, v192 row_newbcast:0 row_mask:0xf bank_mask:0xf\n"
"v_mul_f32_dpp v193, v90, v193 row_newbcast:4 row_mask:0xf bank_mask:0xf\n"
"v_mul_f32_dpp v194, v90, v194 row_newbcast:8 row_mask:0xf bank_mask:0xf\n"
"v_mul_f32_dpp v195, v90, v195 row_newbcast:12 row_mask:0xf bank_mask:0xf\n"
"v_mul_f32_dpp v196, v90, v196 row_newbcast:0 row_mask:0xf bank_mask:0xf\n"
"v_mul_f32_dpp v197, v90, v197 row_newbcast:4 row_mask:0xf bank_mask:0xf\n"
"v_mul_f32_dpp v198, v90, v198 row_newbcast:8 row_mask:0xf bank_mask:0xf\n"
"v_mul_f32_dpp v199, v90, v199 row_newbcast:12 row_mask:0xf bank_mask:0xf\n"
"v_accvgpr_write_b32 a136, v192\n"
"v_accvgpr_write_b32 a137, v193\n"
"v_accvgpr_write_b32 a138, v194\n"
"v_accvgpr_write_b32 a139, v195\n"
"v_accvgpr_write_b32 a140, v196\n"
"v_accvgpr_write_b32 a141, v197\n"
"v_accvgpr_write_b32 a142, v198\n"
"v_accvgpr_write_b32 a143, v199\n"
"v_accvgpr_read_b32 v192, a144\n"
"v_accvgpr_read_b32 v193, a145\n"
"v_accvgpr_read_b32 v194, a146\n"
"v_accvgpr_read_b32 v195, a147\n"
"v_accvgpr_read_b32 v196, a148\n"
"v_accvgpr_read_b32 v197, a149\n"
"v_accvgpr_read_b32 v198, a150\n"
"v_accvgpr_read_b32 v199, a151\n"
"s_waitcnt lgkmcnt(0)\n"
"v_mul_f32_dpp v192, v91, v192 row_newbcast:0 row_mask:0xf bank_mask:0xf\n"
"v_mul_f32_dpp v193, v91, v193 row_newbcast:4 row_mask:0xf bank_mask:0xf\n"
"v_mul_f32_dpp v194, v91, v194 row_newbcast:8 row_mask:0xf bank_mask:0xf\n"
"v_mul_f32_dpp v195, v91, v195 row_newbcast:12 row_mask:0xf bank_mask:0xf\n"
"v_mul_f32_dpp v196, v91, v196 row_newbcast:0 row_mask:0xf bank_mask:0xf\n"
"v_mul_f32_dpp v197, v91, v197 row_newbcast:4 row_mask:0xf bank_mask:0xf\n"
"v_mul_f32_dpp v198, v91, v198 row_newbcast:8 row_mask:0xf bank_mask:0xf\n"
"v_mul_f32_dpp v199, v91, v199 row_newbcast:12 row_mask:0xf bank_mask:0xf\n"
"v_accvgpr_write_b32 a144, v192\n"
"v_accvgpr_write_b32 a145, v193\n"
"v_accvgpr_write_b32 a146, v194\n"
"v_accvgpr_write_b32 a147, v195\n"
"v_accvgpr_write_b32 a148, v196\n"
"v_accvgpr_write_b32 a149, v197\n"
"v_accvgpr_write_b32 a150, v198\n"
"v_accvgpr_write_b32 a151, v199\n"
"v_accvgpr_read_b32 v192, a152\n"
"v_accvgpr_read_b32 v193, a153\n"
"v_accvgpr_read_b32 v194, a154\n"
"v_accvgpr_read_b32 v195, a155\n"
"v_accvgpr_read_b32 v196, a156\n"
"v_accvgpr_read_b32 v197, a157\n"
"v_accvgpr_read_b32 v198, a158\n"
"v_accvgpr_read_b32 v199, a159\n"
"v_mul_f32_dpp v192, v91, v192 row_newbcast:0 row_mask:0xf bank_mask:0xf\n"
"v_mul_f32_dpp v193, v91, v193 row_newbcast:4 row_mask:0xf bank_mask:0xf\n"
"v_mul_f32_dpp v194, v91, v194 row_newbcast:8 row_mask:0xf bank_mask:0xf\n"
"v_mul_f32_dpp v195, v91, v195 row_newbcast:12 row_mask:0xf bank_mask:0xf\n"
"v_mul_f32_dpp v196, v91, v196 row_newbcast:0 row_mask:0xf bank_mask:0xf\n"
"v_mul_f32_dpp v197, v91, v197 row_newbcast:4 row_mask:0xf bank_mask:0xf\n"
"v_mul_f32_dpp v198, v91, v198 row_newbcast:8 row_mask:0xf bank_mask:0xf\n"
"v_mul_f32_dpp v199, v91, v199 row_newbcast:12 row_mask:0xf bank_mask:0xf\n"
"v_accvgpr_write_b32 a152, v192\n"
"v_accvgpr_write_b32 a153, v193\n"
"v_accvgpr_write_b32 a154, v194\n"
"v_accvgpr_write_b32 a155, v195\n"
"v_accvgpr_write_b32 a156, v196\n"
"v_accvgpr_write_b32 a157, v197\n"
"v_accvgpr_write_b32 a158, v198\n"
"v_accvgpr_write_b32 a159, v199\n"
"v_accvgpr_read_b32 v192, a160\n"
"v_accvgpr_read_b32 v193, a161\n"
"v_accvgpr_read_b32 v194, a162\n"
"v_accvgpr_read_b32 v195, a163\n"
"v_accvgpr_read_b32 v196, a164\n"
"v_accvgpr_read_b32 v197, a165\n"
"v_accvgpr_read_b32 v198, a166\n"
"v_accvgpr_read_b32 v199, a167\n"
"v_mul_f32_dpp v192, v91, v192 row_newbcast:0 row_mask:0xf bank_mask:0xf\n"
"v_mul_f32_dpp v193, v91, v193 row_newbcast:4 row_mask:0xf bank_mask:0xf\n"
"v_mul_f32_dpp v194, v91, v194 row_newbcast:8 row_mask:0xf bank_mask:0xf\n"
"v_mul_f32_dpp v195, v91, v195 row_newbcast:12 row_mask:0xf bank_mask:0xf\n"
"v_mul_f32_dpp v196, v91, v196 row_newbcast:0 row_mask:0xf bank_mask:0xf\n"
"v_mul_f32_dpp v197, v91, v197 row_newbcast:4 row_mask:0xf bank_mask:0xf\n"
"v_mul_f32_dpp v198, v91, v198 row_newbcast:8 row_mask:0xf bank_mask:0xf\n"
"v_mul_f32_dpp v199, v91, v199 row_newbcast:12 row_mask:0xf bank_mask:0xf\n"
"v_accvgpr_write_b32 a160, v192\n"
"v_accvgpr_write_b32 a161, v193\n"
"v_accvgpr_write_b32 a162, v194\n"
"v_accvgpr_write_b32 a163, v195\n"
"v_accvgpr_write_b32 a164, v196\n"
"v_accvgpr_write_b32 a165, v197\n"
"v_accvgpr_write_b32 a166, v198\n"
"v_accvgpr_write_b32 a167, v199\n"
"v_accvgpr_read_b32 v192, a168\n"
"v_accvgpr_read_b32 v193, a169\n"
"v_accvgpr_read_b32 v194, a170\n"
"v_accvgpr_read_b32 v195, a171\n"
"v_accvgpr_read_b32 v196, a172\n"
"v_accvgpr_read_b32 v197, a173\n"
"v_accvgpr_read_b32 v198, a174\n"
"v_accvgpr_read_b32 v199, a175\n"
"v_mul_f32_dpp v192, v91, v192 row_newbcast:0 row_mask:0xf bank_mask:0xf\n"
"v_mul_f32_dpp v193, v91, v193 row_newbcast:4 row_mask:0xf bank_mask:0xf\n"
"v_mul_f32_dpp v194, v91, v194 row_newbcast:8 row_mask:0xf bank_mask:0xf\n"
"v_mul_f32_dpp v195, v91, v195 row_newbcast:12 row_mask:0xf bank_mask:0xf\n"
"v_mul_f32_dpp v196, v91, v196 row_newbcast:0 row_mask:0xf bank_mask:0xf\n"
"v_mul_f32_dpp v197, v91, v197 row_newbcast:4 row_mask:0xf bank_mask:0xf\n"
"v_mul_f32_dpp v198, v91, v198 row_newbcast:8 row_mask:0xf bank_mask:0xf\n"
"v_mul_f32_dpp v199, v91, v199 row_newbcast:12 row_mask:0xf bank_mask:0xf\n"
"v_accvgpr_write_b32 a168, v192\n"
"v_accvgpr_write_b32 a169, v193\n"
"v_accvgpr_write_b32 a170, v194\n"
"v_accvgpr_write_b32 a171, v195\n"
"v_accvgpr_write_b32 a172, v196\n"
"v_accvgpr_write_b32 a173, v197\n"
"v_accvgpr_write_b32 a174, v198\n"
"v_accvgpr_write_b32 a175, v199\n"
"v_accvgpr_read_b32 v192, a176\n"
"v_accvgpr_read_b32 v193, a177\n"
"v_accvgpr_read_b32 v194, a178\n"
"v_accvgpr_read_b32 v195, a179\n"
"v_accvgpr_read_b32 v196, a180\n"
"v_accvgpr_read_b32 v197, a181\n"
"v_accvgpr_read_b32 v198, a182\n"
"v_accvgpr_read_b32 v199, a183\n"
"v_mul_f32_dpp v192, v91, v192 row_newbcast:0 row_mask:0xf bank_mask:0xf\n"
"v_mul_f32_dpp v193, v91, v193 row_newbcast:4 row_mask:0xf bank_mask:0xf\n"
"v_mul_f32_dpp v194, v91, v194 row_newbcast:8 row_mask:0xf bank_mask:0xf\n"
"v_mul_f32_dpp v195, v91, v195 row_newbcast:12 row_mask:0xf bank_mask:0xf\n"
"v_mul_f32_dpp v196, v91, v196 row_newbcast:0 row_mask:0xf bank_mask:0xf\n"
"v_mul_f32_dpp v197, v91, v197 row_newbcast:4 row_mask:0xf bank_mask:0xf\n"
"v_mul_f32_dpp v198, v91, v198 row_newbcast:8 row_mask:0xf bank_mask:0xf\n"
"v_mul_f32_dpp v199, v91, v199 row_newbcast:12 row_mask:0xf bank_mask:0xf\n"
"v_accvgpr_write_b32 a176, v192\n"
"v_accvgpr_write_b32 a177, v193\n"
"v_accvgpr_write_b32 a178, v194\n"
"v_accvgpr_write_b32 a179, v195\n"
"v_accvgpr_write_b32 a180, v196\n"
"v_accvgpr_write_b32 a181, v197\n"
"v_accvgpr_write_b32 a182, v198\n"
"v_accvgpr_write_b32 a183, v199\n"
"v_accvgpr_read_b32 v192, a184\n"
"v_accvgpr_read_b32 v193, a185\n"
"v_accvgpr_read_b32 v194, a186\n"
"v_accvgpr_read_b32 v195, a187\n"
"v_accvgpr_read_b32 v196, a188\n"
"v_accvgpr_read_b32 v197, a189\n"
"v_accvgpr_read_b32 v198, a190\n"
"v_accvgpr_read_b32 v199, a191\n"
"v_mul_f32_dpp v192, v91, v192 row_newbcast:0 row_mask:0xf bank_mask:0xf\n"
"v_mul_f32_dpp v193, v91, v193 row_newbcast:4 row_mask:0xf bank_mask:0xf\n"
"v_mul_f32_dpp v194, v91, v194 row_newbcast:8 row_mask:0xf bank_mask:0xf\n"
"v_mul_f32_dpp v195, v91, v195 row_newbcast:12 row_mask:0xf bank_mask:0xf\n"
"v_mul_f32_dpp v196, v91, v196 row_newbcast:0 row_mask:0xf bank_mask:0xf\n"
"v_mul_f32_dpp v197, v91, v197 row_newbcast:4 row_mask:0xf bank_mask:0xf\n"
"v_mul_f32_dpp v198, v91, v198 row_newbcast:8 row_mask:0xf bank_mask:0xf\n"
"v_mul_f32_dpp v199, v91, v199 row_newbcast:12 row_mask:0xf bank_mask:0xf\n"
"v_accvgpr_write_b32 a184, v192\n"
"v_accvgpr_write_b32 a185, v193\n"
"v_accvgpr_write_b32 a186, v194\n"
"v_accvgpr_write_b32 a187, v195\n"
"v_accvgpr_write_b32 a188, v196\n"
"v_accvgpr_write_b32 a189, v197\n"
"v_accvgpr_write_b32 a190, v198\n"
"v_accvgpr_write_b32 a191, v199\n"
"v_sub_f32 v0, v0, v80\n"
"v_sub_f32 v1, v1, v80\n"
"v_sub_f32 v2, v2, v80\n"
"v_sub_f32 v3, v3, v80\n"
"v_sub_f32 v4, v4, v80\n"
"v_sub_f32 v5, v5, v80\n"
"v_sub_f32 v6, v6, v80\n"
"v_sub_f32 v7, v7, v80\n"
"v_sub_f32 v96, v96, v80\n"
"v_sub_f32 v97, v97, v80\n"
"v_sub_f32 v98, v98, v80\n"
"v_sub_f32 v99, v99, v80\n"
"v_sub_f32 v36, v36, v80\n"
"v_sub_f32 v37, v37, v80\n"
"v_sub_f32 v38, v38, v80\n"
"v_sub_f32 v39, v39, v80\n"
"v_sub_f32 v64, v64, v80\n"
"v_sub_f32 v65, v65, v80\n"
"v_sub_f32 v66, v66, v80\n"
"v_sub_f32 v67, v67, v80\n"
"v_sub_f32 v8, v8, v81\n"
"v_sub_f32 v9, v9, v81\n"
"v_sub_f32 v10, v10, v81\n"
"v_sub_f32 v11, v11, v81\n"
"v_sub_f32 v12, v12, v81\n"
"v_sub_f32 v13, v13, v81\n"
"v_sub_f32 v14, v14, v81\n"
"v_sub_f32 v15, v15, v81\n"
"v_sub_f32 v100, v100, v81\n"
"v_sub_f32 v101, v101, v81\n"
"v_sub_f32 v102, v102, v81\n"
"v_sub_f32 v103, v103, v81\n"
"v_sub_f32 v44, v44, v81\n"
"v_sub_f32 v45, v45, v81\n"
"v_sub_f32 v46, v46, v81\n"
"v_sub_f32 v47, v47, v81\n"
"v_sub_f32 v68, v68, v81\n"
"v_sub_f32 v69, v69, v81\n"
"v_sub_f32 v70, v70, v81\n"
"v_sub_f32 v71, v71, v81\n"
"v_sub_f32 v16, v16, v82\n"
"v_sub_f32 v17, v17, v82\n"
"v_sub_f32 v18, v18, v82\n"
"v_sub_f32 v19, v19, v82\n"
"v_sub_f32 v20, v20, v82\n"
"v_sub_f32 v21, v21, v82\n"
"v_sub_f32 v22, v22, v82\n"
"v_sub_f32 v23, v23, v82\n"
"v_sub_f32 v104, v104, v82\n"
"v_sub_f32 v105, v105, v82\n"
"v_sub_f32 v106, v106, v82\n"
"v_sub_f32 v107, v107, v82\n"
"v_sub_f32 v52, v52, v82\n"
"v_sub_f32 v53, v53, v82\n"
"v_sub_f32 v54, v54, v82\n"
"v_sub_f32 v55, v55, v82\n"
"v_sub_f32 v72, v72, v82\n"
"v_sub_f32 v73, v73, v82\n"
"v_sub_f32 v74, v74, v82\n"
"v_sub_f32 v75, v75, v82\n"
"v_sub_f32 v24, v24, v83\n"
"v_sub_f32 v25, v25, v83\n"
"v_sub_f32 v26, v26, v83\n"
"v_sub_f32 v27, v27, v83\n"
"v_sub_f32 v28, v28, v83\n"
"v_sub_f32 v29, v29, v83\n"
"v_sub_f32 v30, v30, v83\n"
"v_sub_f32 v31, v31, v83\n"
"v_sub_f32 v108, v108, v83\n"
"v_sub_f32 v109, v109, v83\n"
"v_sub_f32 v110, v110, v83\n"
"v_sub_f32 v111, v111, v83\n"
"v_sub_f32 v60, v60, v83\n"
"v_sub_f32 v61, v61, v83\n"
"v_sub_f32 v62, v62, v83\n"
"v_sub_f32 v63, v63, v83\n"
"v_sub_f32 v76, v76, v83\n"
"v_sub_f32 v77, v77, v83\n"
"v_sub_f32 v78, v78, v83\n"
"v_sub_f32 v79, v79, v83\n"
"v_exp_f32 v192, v0\n"
"s_nop 0\n"
"v_add_f32 v200, v204, v192\n"
"v_exp_f32 v193, v1\n"
"s_nop 0\n"
"v_add_f32 v200, v200, v193\n"
"v_exp_f32 v194, v2\n"
"v_cvt_pk_bf16_f32 v32, v192, v193\n"
"v_exp_f32 v195, v3\n"
"v_add_f32 v200, v200, v194\n"
"v_exp_f32 v196, v4\n"
"v_add_f32 v200, v200, v195\n"
"v_exp_f32 v197, v5\n"
"v_cvt_pk_bf16_f32 v33, v194, v195\n"
"v_exp_f32 v198, v6\n"
"v_add_f32 v200, v200, v196\n"
"v_add_f32 v200, v200, v197\n"
"v_cvt_pk_bf16_f32 v34, v196, v197\n"
"v_add_f32 v200, v200, v198\n"
"v_exp_f32 v199, v7\n"
"s_nop 0\n"
"v_add_f32 v200, v200, v199\n"
"v_exp_f32 v192, v8\n"
"v_cvt_pk_bf16_f32 v35, v198, v199\n"
"v_exp_f32 v193, v9\n"
"v_add_f32 v201, v205, v192\n"
"v_add_f32 v201, v201, v193\n"
"v_exp_f32 v194, v10\n"
"v_exp_f32 v195, v11\n"
"v_cvt_pk_bf16_f32 v40, v192, v193\n"
"v_exp_f32 v196, v12\n"
"v_add_f32 v201, v201, v194\n"
"v_add_f32 v201, v201, v195\n"
"v_exp_f32 v197, v13\n"
"v_cvt_pk_bf16_f32 v41, v194, v195\n"
"v_exp_f32 v198, v14\n"
"v_exp_f32 v199, v15\n"
"v_add_f32 v201, v201, v196\n"
"v_add_f32 v201, v201, v197\n"
"v_cvt_pk_bf16_f32 v42, v196, v197\n"
"v_exp_f32 v192, v16\n"
"v_add_f32 v201, v201, v198\n"
"v_exp_f32 v193, v17\n"
"v_exp_f32 v194, v18\n"
"v_add_f32 v201, v201, v199\n"
"v_cvt_pk_bf16_f32 v43, v198, v199\n"
"v_add_f32 v202, v206, v192\n"
"v_exp_f32 v195, v19\n"
"v_exp_f32 v196, v20\n"
"v_add_f32 v202, v202, v193\n"
"v_exp_f32 v197, v21\n"
"v_cvt_pk_bf16_f32 v48, v192, v193\n"
"v_add_f32 v202, v202, v194\n"
"v_add_f32 v202, v202, v195\n"
"v_exp_f32 v198, v22\n"
"v_exp_f32 v199, v23\n"
"v_cvt_pk_bf16_f32 v49, v194, v195\n"
"v_exp_f32 v192, v24\n"
"v_add_f32 v202, v202, v196\n"
"v_add_f32 v202, v202, v197\n"
"v_exp_f32 v193, v25\n"
"v_cvt_pk_bf16_f32 v50, v196, v197\n"
"v_exp_f32 v194, v26\n"
"v_exp_f32 v195, v27\n"
"v_add_f32 v202, v202, v198\n"
"v_add_f32 v202, v202, v199\n"
"v_cvt_pk_bf16_f32 v51, v198, v199\n"
"v_exp_f32 v196, v28\n"
"v_add_f32 v203, v207, v192\n"
"v_exp_f32 v197, v29\n"
"v_exp_f32 v198, v30\n"
"v_add_f32 v203, v203, v193\n"
"v_cvt_pk_bf16_f32 v56, v192, v193\n"
"v_exp_f32 v199, v31\n"
"v_add_f32 v203, v203, v194\n"
"v_add_f32 v203, v203, v195\n"
"v_cvt_pk_bf16_f32 v57, v194, v195\n"
"v_add_f32 v203, v203, v196\n"
"v_add_f32 v203, v203, v197\n"
"v_cvt_pk_bf16_f32 v58, v196, v197\n"
"v_add_f32 v203, v203, v198\n"
"v_add_f32 v203, v203, v199\n"
"v_cvt_pk_bf16_f32 v59, v198, v199\n"
"s_nop 2\n"
"s_setpc_b64 s[86:87]\n"
".Lat_rare3:\n"
"s_nop 15\n"
"v_log_f32 v84, v200\n"
"v_log_f32 v85, v201\n"
"v_log_f32 v86, v202\n"
"v_log_f32 v87, v203\n"
"v_max3_f32 v80, v96, v97, v98\n"
"v_max3_f32 v80, v80, v99, v36\n"
"v_max3_f32 v80, v80, v37, v38\n"
"v_max3_f32 v80, v80, v39, v84\n"
"v_max3_f32 v81, v100, v101, v102\n"
"v_max3_f32 v81, v81, v103, v44\n"
"v_max3_f32 v81, v81, v45, v46\n"
"v_max3_f32 v81, v81, v47, v85\n"
"v_max3_f32 v82, v104, v105, v106\n"
"v_max3_f32 v82, v82, v107, v52\n"
"v_max3_f32 v82, v82, v53, v54\n"
"v_max3_f32 v82, v82, v55, v86\n"
"v_max3_f32 v83, v108, v109, v110\n"
"v_max3_f32 v83, v83, v111, v60\n"
"v_max3_f32 v83, v83, v61, v62\n"
"v_max3_f32 v83, v83, v63, v87\n"
"v_mov_b32 v84, v80\n"
"v_mov_b32 v85, v81\n"
"v_mov_b32 v86, v82\n"
"v_mov_b32 v87, v83\n"
"s_nop 1\n"
"v_permlane16_swap_b32 v80, v84\n"
"v_permlane16_swap_b32 v81, v85\n"
"v_permlane16_swap_b32 v82, v86\n"
"v_permlane16_swap_b32 v83, v87\n"
"s_nop 0\n"
"v_max_f32 v80, v80, v84\n"
"v_max_f32 v81, v81, v85\n"
"v_max_f32 v82, v82, v86\n"
"v_max_f32 v83, v83, v87\n"
"v_mov_b32 v84, v80\n"
"v_mov_b32 v85, v81\n"
"v_mov_b32 v86, v82\n"
"v_mov_b32 v87, v83\n"
"s_nop 1\n"
"v_permlane32_swap_b32 v80, v84\n"
"v_permlane32_swap_b32 v81, v85\n"
"v_permlane32_swap_b32 v82, v86\n"
"v_permlane32_swap_b32 v83, v87\n"
"s_nop 0\n"
"v_max_f32 v80, v80, v84\n"
"v_max_f32 v81, v81, v85\n"
"v_max_f32 v82, v82, v86\n"
"v_max_f32 v83, v83, v87\n"
"v_max_f32 v80, 0, v80\n"
"v_max_f32 v81, 0, v81\n"
"v_max_f32 v82, 0, v82\n"
"v_max_f32 v83, 0, v83\n"
"v_sub_f32 v84, 0, v80\n"
"v_sub_f32 v85, 0, v81\n"
"v_sub_f32 v86, 0, v82\n"
"v_sub_f32 v87, 0, v83\n"
"v_exp_f32 v88, v84\n"
"v_exp_f32 v89, v85\n"
"v_exp_f32 v90, v86\n"
"v_exp_f32 v91, v87\n"
"s_nop 0\n"
"v_mul_f32 v200, v200, v88\n"
"v_lshlrev_b32 v192, 16, v32\n"
"v_and_b32 v193, 0xffff0000, v32\n"
"v_mul_f32 v192, v192, v88\n"
"v_mul_f32 v193, v193, v88\n"
"v_cvt_pk_bf16_f32 v32, v192, v193\n"
"v_lshlrev_b32 v192, 16, v33\n"
"v_and_b32 v193, 0xffff0000, v33\n"
"v_mul_f32 v192, v192, v88\n"
"v_mul_f32 v193, v193, v88\n"
"v_cvt_pk_bf16_f32 v33, v192, v193\n"
"v_lshlrev_b32 v192, 16, v34\n"
"v_and_b32 v193, 0xffff0000, v34\n"
"v_mul_f32 v192, v192, v88\n"
"v_mul_f32 v193, v193, v88\n"
"v_cvt_pk_bf16_f32 v34, v192, v193\n"
"v_lshlrev_b32 v192, 16, v35\n"
"v_and_b32 v193, 0xffff0000, v35\n"
"v_mul_f32 v192, v192, v88\n"
"v_mul_f32 v193, v193, v88\n"
"v_cvt_pk_bf16_f32 v35, v192, v193\n"
"v_lshlrev_b32 v192, 16, v112\n"
"v_and_b32 v193, 0xffff0000, v112\n"
"v_mul_f32 v192, v192, v88\n"
"v_mul_f32 v193, v193, v88\n"
"v_cvt_pk_bf16_f32 v112, v192, v193\n"
"v_lshlrev_b32 v192, 16, v113\n"
"v_and_b32 v193, 0xffff0000, v113\n"
"v_mul_f32 v192, v192, v88\n"
"v_mul_f32 v193, v193, v88\n"
"v_cvt_pk_bf16_f32 v113, v192, v193\n"
"v_lshlrev_b32 v192, 16, v114\n"
"v_and_b32 v193, 0xffff0000, v114\n"
"v_mul_f32 v192, v192, v88\n"
"v_mul_f32 v193, v193, v88\n"
"v_cvt_pk_bf16_f32 v114, v192, v193\n"
"v_lshlrev_b32 v192, 16, v115\n"
"v_and_b32 v193, 0xffff0000, v115\n"
"v_mul_f32 v192, v192, v88\n"
"v_mul_f32 v193, v193, v88\n"
"v_cvt_pk_bf16_f32 v115, v192, v193\n"
"v_mul_f32 v201, v201, v89\n"
"v_lshlrev_b32 v192, 16, v40\n"
"v_and_b32 v193, 0xffff0000, v40\n"
"v_mul_f32 v192, v192, v89\n"
"v_mul_f32 v193, v193, v89\n"
"v_cvt_pk_bf16_f32 v40, v192, v193\n"
"v_lshlrev_b32 v192, 16, v41\n"
"v_and_b32 v193, 0xffff0000, v41\n"
"v_mul_f32 v192, v192, v89\n"
"v_mul_f32 v193, v193, v89\n"
"v_cvt_pk_bf16_f32 v41, v192, v193\n"
"v_lshlrev_b32 v192, 16, v42\n"
"v_and_b32 v193, 0xffff0000, v42\n"
"v_mul_f32 v192, v192, v89\n"
"v_mul_f32 v193, v193, v89\n"
"v_cvt_pk_bf16_f32 v42, v192, v193\n"
"v_lshlrev_b32 v192, 16, v43\n"
"v_and_b32 v193, 0xffff0000, v43\n"
"v_mul_f32 v192, v192, v89\n"
"v_mul_f32 v193, v193, v89\n"
"v_cvt_pk_bf16_f32 v43, v192, v193\n"
"v_lshlrev_b32 v192, 16, v116\n"
"v_and_b32 v193, 0xffff0000, v116\n"
"v_mul_f32 v192, v192, v89\n"
"v_mul_f32 v193, v193, v89\n"
"v_cvt_pk_bf16_f32 v116, v192, v193\n"
"v_lshlrev_b32 v192, 16, v117\n"
"v_and_b32 v193, 0xffff0000, v117\n"
"v_mul_f32 v192, v192, v89\n"
"v_mul_f32 v193, v193, v89\n"
"v_cvt_pk_bf16_f32 v117, v192, v193\n"
"v_lshlrev_b32 v192, 16, v118\n"
"v_and_b32 v193, 0xffff0000, v118\n"
"v_mul_f32 v192, v192, v89\n"
"v_mul_f32 v193, v193, v89\n"
"v_cvt_pk_bf16_f32 v118, v192, v193\n"
"v_lshlrev_b32 v192, 16, v119\n"
"v_and_b32 v193, 0xffff0000, v119\n"
"v_mul_f32 v192, v192, v89\n"
"v_mul_f32 v193, v193, v89\n"
"v_cvt_pk_bf16_f32 v119, v192, v193\n"
"v_mul_f32 v202, v202, v90\n"
"v_lshlrev_b32 v192, 16, v48\n"
"v_and_b32 v193, 0xffff0000, v48\n"
"v_mul_f32 v192, v192, v90\n"
"v_mul_f32 v193, v193, v90\n"
"v_cvt_pk_bf16_f32 v48, v192, v193\n"
"v_lshlrev_b32 v192, 16, v49\n"
"v_and_b32 v193, 0xffff0000, v49\n"
"v_mul_f32 v192, v192, v90\n"
"v_mul_f32 v193, v193, v90\n"
"v_cvt_pk_bf16_f32 v49, v192, v193\n"
"v_lshlrev_b32 v192, 16, v50\n"
"v_and_b32 v193, 0xffff0000, v50\n"
"v_mul_f32 v192, v192, v90\n"
"v_mul_f32 v193, v193, v90\n"
"v_cvt_pk_bf16_f32 v50, v192, v193\n"
"v_lshlrev_b32 v192, 16, v51\n"
"v_and_b32 v193, 0xffff0000, v51\n"
"v_mul_f32 v192, v192, v90\n"
"v_mul_f32 v193, v193, v90\n"
"v_cvt_pk_bf16_f32 v51, v192, v193\n"
"v_lshlrev_b32 v192, 16, v120\n"
"v_and_b32 v193, 0xffff0000, v120\n"
"v_mul_f32 v192, v192, v90\n"
"v_mul_f32 v193, v193, v90\n"
"v_cvt_pk_bf16_f32 v120, v192, v193\n"
"v_lshlrev_b32 v192, 16, v121\n"
"v_and_b32 v193, 0xffff0000, v121\n"
"v_mul_f32 v192, v192, v90\n"
"v_mul_f32 v193, v193, v90\n"
"v_cvt_pk_bf16_f32 v121, v192, v193\n"
"v_lshlrev_b32 v192, 16, v122\n"
"v_and_b32 v193, 0xffff0000, v122\n"
"v_mul_f32 v192, v192, v90\n"
"v_mul_f32 v193, v193, v90\n"
"v_cvt_pk_bf16_f32 v122, v192, v193\n"
"v_lshlrev_b32 v192, 16, v123\n"
"v_and_b32 v193, 0xffff0000, v123\n"
"v_mul_f32 v192, v192, v90\n"
"v_mul_f32 v193, v193, v90\n"
"v_cvt_pk_bf16_f32 v123, v192, v193\n"
"v_mul_f32 v203, v203, v91\n"
"v_lshlrev_b32 v192, 16, v56\n"
"v_and_b32 v193, 0xffff0000, v56\n"
"v_mul_f32 v192, v192, v91\n"
"v_mul_f32 v193, v193, v91\n"
"v_cvt_pk_bf16_f32 v56, v192, v193\n"
"v_lshlrev_b32 v192, 16, v57\n"
"v_and_b32 v193, 0xffff0000, v57\n"
"v_mul_f32 v192, v192, v91\n"
"v_mul_f32 v193, v193, v91\n"
"v_cvt_pk_bf16_f32 v57, v192, v193\n"
"v_lshlrev_b32 v192, 16, v58\n"
"v_and_b32 v193, 0xffff0000, v58\n"
"v_mul_f32 v192, v192, v91\n"
"v_mul_f32 v193, v193, v91\n"
"v_cvt_pk_bf16_f32 v58, v192, v193\n"
"v_lshlrev_b32 v192, 16, v59\n"
"v_and_b32 v193, 0xffff0000, v59\n"
"v_mul_f32 v192, v192, v91\n"
"v_mul_f32 v193, v193, v91\n"
"v_cvt_pk_bf16_f32 v59, v192, v193\n"
"v_lshlrev_b32 v192, 16, v124\n"
"v_and_b32 v193, 0xffff0000, v124\n"
"v_mul_f32 v192, v192, v91\n"
"v_mul_f32 v193, v193, v91\n"
"v_cvt_pk_bf16_f32 v124, v192, v193\n"
"v_lshlrev_b32 v192, 16, v125\n"
"v_and_b32 v193, 0xffff0000, v125\n"
"v_mul_f32 v192, v192, v91\n"
"v_mul_f32 v193, v193, v91\n"
"v_cvt_pk_bf16_f32 v125, v192, v193\n"
"v_lshlrev_b32 v192, 16, v126\n"
"v_and_b32 v193, 0xffff0000, v126\n"
"v_mul_f32 v192, v192, v91\n"
"v_mul_f32 v193, v193, v91\n"
"v_cvt_pk_bf16_f32 v126, v192, v193\n"
"v_lshlrev_b32 v192, 16, v127\n"
"v_and_b32 v193, 0xffff0000, v127\n"
"v_mul_f32 v192, v192, v91\n"
"v_mul_f32 v193, v193, v91\n"
"v_cvt_pk_bf16_f32 v127, v192, v193\n"
"v_lshrrev_b32 v84, 4, v251\n"
"ds_bpermute_b32 v88, v84, v88 offset:0\n"
"ds_bpermute_b32 v89, v84, v89 offset:0\n"
"ds_bpermute_b32 v90, v84, v90 offset:0\n"
"ds_bpermute_b32 v91, v84, v91 offset:0\n"
"v_accvgpr_read_b32 v192, a0\n"
"v_accvgpr_read_b32 v193, a1\n"
"v_accvgpr_read_b32 v194, a2\n"
"v_accvgpr_read_b32 v195, a3\n"
"v_accvgpr_read_b32 v196, a4\n"
"v_accvgpr_read_b32 v197, a5\n"
"v_accvgpr_read_b32 v198, a6\n"
"v_accvgpr_read_b32 v199, a7\n"
"s_waitcnt lgkmcnt(3)\n"
"v_mul_f32_dpp v192, v88, v192 row_newbcast:0 row_mask:0xf bank_mask:0xf\n"
"v_mul_f32_dpp v193, v88, v193 row_newbcast:4 row_mask:0xf bank_mask:0xf\n"
"v_mul_f32_dpp v194, v88, v194 row_newbcast:8 row_mask:0xf bank_mask:0xf\n"
"v_mul_f32_dpp v195, v88, v195 row_newbcast:12 row_mask:0xf bank_mask:0xf\n"
"v_mul_f32_dpp v196, v88, v196 row_newbcast:0 row_mask:0xf bank_mask:0xf\n"
"v_mul_f32_dpp v197, v88, v197 row_newbcast:4 row_mask:0xf bank_mask:0xf\n"
"v_mul_f32_dpp v198, v88, v198 row_newbcast:8 row_mask:0xf bank_mask:0xf\n"
"v_mul_f32_dpp v199, v88, v199 row_newbcast:12 row_mask:0xf bank_mask:0xf\n"
"v_accvgpr_write_b32 a0, v192\n"
"v_accvgpr_write_b32 a1, v193\n"
"v_accvgpr_write_b32 a2, v194\n"
"v_accvgpr_write_b32 a3, v195\n"
"v_accvgpr_write_b32 a4, v196\n"
"v_accvgpr_write_b32 a5, v197\n"
"v_accvgpr_write_b32 a6, v198\n"
"v_accvgpr_write_b32 a7, v199\n"
"v_accvgpr_read_b32 v192, a8\n"
"v_accvgpr_read_b32 v193, a9\n"
"v_accvgpr_read_b32 v194, a10\n"
"v_accvgpr_read_b32 v195, a11\n"
"v_accvgpr_read_b32 v196, a12\n"
"v_accvgpr_read_b32 v197, a13\n"
"v_accvgpr_read_b32 v198, a14\n"
"v_accvgpr_read_b32 v199, a15\n"
"v_mul_f32_dpp v192, v88, v192 row_newbcast:0 row_mask:0xf bank_mask:0xf\n"
"v_mul_f32_dpp v193, v88, v193 row_newbcast:4 row_mask:0xf bank_mask:0xf\n"
"v_mul_f32_dpp v194, v88, v194 row_newbcast:8 row_mask:0xf bank_mask:0xf\n"
"v_mul_f32_dpp v195, v88, v195 row_newbcast:12 row_mask:0xf bank_mask:0xf\n"
"v_mul_f32_dpp v196, v88, v196 row_newbcast:0 row_mask:0xf bank_mask:0xf\n"
"v_mul_f32_dpp v197, v88, v197 row_newbcast:4 row_mask:0xf bank_mask:0xf\n"
"v_mul_f32_dpp v198, v88, v198 row_newbcast:8 row_mask:0xf bank_mask:0xf\n"
"v_mul_f32_dpp v199, v88, v199 row_newbcast:12 row_mask:0xf bank_mask:0xf\n"
"v_accvgpr_write_b32 a8, v192\n"
"v_accvgpr_write_b32 a9, v193\n"
"v_accvgpr_write_b32 a10, v194\n"
"v_accvgpr_write_b32 a11, v195\n"
"v_accvgpr_write_b32 a12, v196\n"
"v_accvgpr_write_b32 a13, v197\n"
"v_accvgpr_write_b32 a14, v198\n"
"v_accvgpr_write_b32 a15, v199\n"
"v_accvgpr_read_b32 v192, a16\n"
"v_accvgpr_read_b32 v193, a17\n"
"v_accvgpr_read_b32 v194, a18\n"
"v_accvgpr_read_b32 v195, a19\n"
"v_accvgpr_read_b32 v196, a20\n"
"v_accvgpr_read_b32 v197, a21\n"
"v_accvgpr_read_b32 v198, a22\n"
"v_accvgpr_read_b32 v199, a23\n"
"v_mul_f32_dpp v192, v88, v192 row_newbcast:0 row_mask:0xf bank_mask:0xf\n"
"v_mul_f32_dpp v193, v88, v193 row_newbcast:4 row_mask:0xf bank_mask:0xf\n"
"v_mul_f32_dpp v194, v88, v194 row_newbcast:8 row_mask:0xf bank_mask:0xf\n"
"v_mul_f32_dpp v195, v88, v195 row_newbcast:12 row_mask:0xf bank_mask:0xf\n"
"v_mul_f32_dpp v196, v88, v196 row_newbcast:0 row_mask:0xf bank_mask:0xf\n"
"v_mul_f32_dpp v197, v88, v197 row_newbcast:4 row_mask:0xf bank_mask:0xf\n"
"v_mul_f32_dpp v198, v88, v198 row_newbcast:8 row_mask:0xf bank_mask:0xf\n"
"v_mul_f32_dpp v199, v88, v199 row_newbcast:12 row_mask:0xf bank_mask:0xf\n"
"v_accvgpr_write_b32 a16, v192\n"
"v_accvgpr_write_b32 a17, v193\n"
"v_accvgpr_write_b32 a18, v194\n"
"v_accvgpr_write_b32 a19, v195\n"
"v_accvgpr_write_b32 a20, v196\n"
"v_accvgpr_write_b32 a21, v197\n"
"v_accvgpr_write_b32 a22, v198\n"
"v_accvgpr_write_b32 a23, v199\n"
"v_accvgpr_read_b32 v192, a24\n"
"v_accvgpr_read_b32 v193, a25\n"
"v_accvgpr_read_b32 v194, a26\n"
"v_accvgpr_read_b32 v195, a27\n"
"v_accvgpr_read_b32 v196, a28\n"
"v_accvgpr_read_b32 v197, a29\n"
"v_accvgpr_read_b32 v198, a30\n"
"v_accvgpr_read_b32 v199, a31\n"
"v_mul_f32_dpp v192, v88, v192 row_newbcast:0 row_mask:0xf bank_mask:0xf\n"
"v_mul_f32_dpp v193, v88, v193 row_newbcast:4 row_mask:0xf bank_mask:0xf\n"
"v_mul_f32_dpp v194, v88, v194 row_newbcast:8 row_mask:0xf bank_mask:0xf\n"
"v_mul_f32_dpp v195, v88, v195 row_newbcast:12 row_mask:0xf bank_mask:0xf\n"
"v_mul_f32_dpp v196, v88, v196 row_newbcast:0 row_mask:0xf bank_mask:0xf\n"
"v_mul_f32_dpp v197, v88, v197 row_newbcast:4 row_mask:0xf bank_mask:0xf\n"
"v_mul_f32_dpp v198, v88, v198 row_newbcast:8 row_mask:0xf bank_mask:0xf\n"
"v_mul_f32_dpp v199, v88, v199 row_newbcast:12 row_mask:0xf bank_mask:0xf\n"
"v_accvgpr_write_b32 a24, v192\n"
"v_accvgpr_write_b32 a25, v193\n"
"v_accvgpr_write_b32 a26, v194\n"
"v_accvgpr_write_b32 a27, v195\n"
"v_accvgpr_write_b32 a28, v196\n"
"v_accvgpr_write_b32 a29, v197\n"
"v_accvgpr_write_b32 a30, v198\n"
"v_accvgpr_write_b32 a31, v199\n"
"v_accvgpr_read_b32 v192, a32\n"
"v_accvgpr_read_b32 v193, a33\n"
"v_accvgpr_read_b32 v194, a34\n"
"v_accvgpr_read_b32 v195, a35\n"
"v_accvgpr_read_b32 v196, a36\n"
"v_accvgpr_read_b32 v197, a37\n"
"v_accvgpr_read_b32 v198, a38\n"
"v_accvgpr_read_b32 v199, a39\n"
"v_mul_f32_dpp v192, v88, v192 row_newbcast:0 row_mask:0xf bank_mask:0xf\n"
"v_mul_f32_dpp v193, v88, v193 row_newbcast:4 row_mask:0xf bank_mask:0xf\n"
"v_mul_f32_dpp v194, v88, v194 row_newbcast:8 row_mask:0xf bank_mask:0xf\n"
"v_mul_f32_dpp v195, v88, v195 row_newbcast:12 row_mask:0xf bank_mask:0xf\n"
"v_mul_f32_dpp v196, v88, v196 row_newbcast:0 row_mask:0xf bank_mask:0xf\n"
"v_mul_f32_dpp v197, v88, v197 row_newbcast:4 row_mask:0xf bank_mask:0xf\n"
"v_mul_f32_dpp v198, v88, v198 row_newbcast:8 row_mask:0xf bank_mask:0xf\n"
"v_mul_f32_dpp v199, v88, v199 row_newbcast:12 row_mask:0xf bank_mask:0xf\n"
"v_accvgpr_write_b32 a32, v192\n"
"v_accvgpr_write_b32 a33, v193\n"
"v_accvgpr_write_b32 a34, v194\n"
"v_accvgpr_write_b32 a35, v195\n"
"v_accvgpr_write_b32 a36, v196\n"
"v_accvgpr_write_b32 a37, v197\n"
"v_accvgpr_write_b32 a38, v198\n"
"v_accvgpr_write_b32 a39, v199\n"
"v_accvgpr_read_b32 v192, a40\n"
"v_accvgpr_read_b32 v193, a41\n"
"v_accvgpr_read_b32 v194, a42\n"
"v_accvgpr_read_b32 v195, a43\n"
"v_accvgpr_read_b32 v196, a44\n"
"v_accvgpr_read_b32 v197, a45\n"
"v_accvgpr_read_b32 v198, a46\n"
"v_accvgpr_read_b32 v199, a47\n"
"v_mul_f32_dpp v192, v88, v192 row_newbcast:0 row_mask:0xf bank_mask:0xf\n"
"v_mul_f32_dpp v193, v88, v193 row_newbcast:4 row_mask:0xf bank_mask:0xf\n"
"v_mul_f32_dpp v194, v88, v194 row_newbcast:8 row_mask:0xf bank_mask:0xf\n"
"v_mul_f32_dpp v195, v88, v195 row_newbcast:12 row_mask:0xf bank_mask:0xf\n"
"v_mul_f32_dpp v196, v88, v196 row_newbcast:0 row_mask:0xf bank_mask:0xf\n"
"v_mul_f32_dpp v197, v88, v197 row_newbcast:4 row_mask:0xf bank_mask:0xf\n"
"v_mul_f32_dpp v198, v88, v198 row_newbcast:8 row_mask:0xf bank_mask:0xf\n"
"v_mul_f32_dpp v199, v88, v199 row_newbcast:12 row_mask:0xf bank_mask:0xf\n"
"v_accvgpr_write_b32 a40, v192\n"
"v_accvgpr_write_b32 a41, v193\n"
"v_accvgpr_write_b32 a42, v194\n"
"v_accvgpr_write_b32 a43, v195\n"
"v_accvgpr_write_b32 a44, v196\n"
"v_accvgpr_write_b32 a45, v197\n"
"v_accvgpr_write_b32 a46, v198\n"
"v_accvgpr_write_b32 a47, v199\n"
"v_accvgpr_read_b32 v192, a48\n"
"v_accvgpr_read_b32 v193, a49\n"
"v_accvgpr_read_b32 v194, a50\n"
"v_accvgpr_read_b32 v195, a51\n"
"v_accvgpr_read_b32 v196, a52\n"
"v_accvgpr_read_b32 v197, a53\n"
"v_accvgpr_read_b32 v198, a54\n"
"v_accvgpr_read_b32 v199, a55\n"
"s_waitcnt lgkmcnt(2)\n"
"v_mul_f32_dpp v192, v89, v192 row_newbcast:0 row_mask:0xf bank_mask:0xf\n"
"v_mul_f32_dpp v193, v89, v193 row_newbcast:4 row_mask:0xf bank_mask:0xf\n"
"v_mul_f32_dpp v194, v89, v194 row_newbcast:8 row_mask:0xf bank_mask:0xf\n"
"v_mul_f32_dpp v195, v89, v195 row_newbcast:12 row_mask:0xf bank_mask:0xf\n"
"v_mul_f32_dpp v196, v89, v196 row_newbcast:0 row_mask:0xf bank_mask:0xf\n"
"v_mul_f32_dpp v197, v89, v197 row_newbcast:4 row_mask:0xf bank_mask:0xf\n"
"v_mul_f32_dpp v198, v89, v198 row_newbcast:8 row_mask:0xf bank_mask:0xf\n"
"v_mul_f32_dpp v199, v89, v199 row_newbcast:12 row_mask:0xf bank_mask:0xf\n"
"v_accvgpr_write_b32 a48, v192\n"
"v_accvgpr_write_b32 a49, v193\n"
"v_accvgpr_write_b32 a50, v194\n"
"v_accvgpr_write_b32 a51, v195\n"
"v_accvgpr_write_b32 a52, v196\n"
"v_accvgpr_write_b32 a53, v197\n"
"v_accvgpr_write_b32 a54, v198\n"
"v_accvgpr_write_b32 a55, v199\n"
"v_accvgpr_read_b32 v192, a56\n"
"v_accvgpr_read_b32 v193, a57\n"
"v_accvgpr_read_b32 v194, a58\n"
"v_accvgpr_read_b32 v195, a59\n"
"v_accvgpr_read_b32 v196, a60\n"
"v_accvgpr_read_b32 v197, a61\n"
"v_accvgpr_read_b32 v198, a62\n"
"v_accvgpr_read_b32 v199, a63\n"
"v_mul_f32_dpp v192, v89, v192 row_newbcast:0 row_mask:0xf bank_mask:0xf\n"
"v_mul_f32_dpp v193, v89, v193 row_newbcast:4 row_mask:0xf bank_mask:0xf\n"
"v_mul_f32_dpp v194, v89, v194 row_newbcast:8 row_mask:0xf bank_mask:0xf\n"
"v_mul_f32_dpp v195, v89, v195 row_newbcast:12 row_mask:0xf bank_mask:0xf\n"
"v_mul_f32_dpp v196, v89, v196 row_newbcast:0 row_mask:0xf bank_mask:0xf\n"
"v_mul_f32_dpp v197, v89, v197 row_newbcast:4 row_mask:0xf bank_mask:0xf\n"
"v_mul_f32_dpp v198, v89, v198 row_newbcast:8 row_mask:0xf bank_mask:0xf\n"
"v_mul_f32_dpp v199, v89, v199 row_newbcast:12 row_mask:0xf bank_mask:0xf\n"
"v_accvgpr_write_b32 a56, v192\n"
"v_accvgpr_write_b32 a57, v193\n"
"v_accvgpr_write_b32 a58, v194\n"
"v_accvgpr_write_b32 a59, v195\n"
"v_accvgpr_write_b32 a60, v196\n"
"v_accvgpr_write_b32 a61, v197\n"
"v_accvgpr_write_b32 a62, v198\n"
"v_accvgpr_write_b32 a63, v199\n"
"v_accvgpr_read_b32 v192, a64\n"
"v_accvgpr_read_b32 v193, a65\n"
"v_accvgpr_read_b32 v194, a66\n"
"v_accvgpr_read_b32 v195, a67\n"
"v_accvgpr_read_b32 v196, a68\n"
"v_accvgpr_read_b32 v197, a69\n"
"v_accvgpr_read_b32 v198, a70\n"
"v_accvgpr_read_b32 v199, a71\n"
"v_mul_f32_dpp v192, v89, v192 row_newbcast:0 row_mask:0xf bank_mask:0xf\n"
"v_mul_f32_dpp v193, v89, v193 row_newbcast:4 row_mask:0xf bank_mask:0xf\n"
"v_mul_f32_dpp v194, v89, v194 row_newbcast:8 row_mask:0xf bank_mask:0xf\n"
"v_mul_f32_dpp v195, v89, v195 row_newbcast:12 row_mask:0xf bank_mask:0xf\n"
"v_mul_f32_dpp v196, v89, v196 row_newbcast:0 row_mask:0xf bank_mask:0xf\n"
"v_mul_f32_dpp v197, v89, v197 row_newbcast:4 row_mask:0xf bank_mask:0xf\n"
"v_mul_f32_dpp v198, v89, v198 row_newbcast:8 row_mask:0xf bank_mask:0xf\n"
"v_mul_f32_dpp v199, v89, v199 row_newbcast:12 row_mask:0xf bank_mask:0xf\n"
"v_accvgpr_write_b32 a64, v192\n"
"v_accvgpr_write_b32 a65, v193\n"
"v_accvgpr_write_b32 a66, v194\n"
"v_accvgpr_write_b32 a67, v195\n"
"v_accvgpr_write_b32 a68, v196\n"
"v_accvgpr_write_b32 a69, v197\n"
"v_accvgpr_write_b32 a70, v198\n"
"v_accvgpr_write_b32 a71, v199\n"
"v_accvgpr_read_b32 v192, a72\n"
"v_accvgpr_read_b32 v193, a73\n"
"v_accvgpr_read_b32 v194, a74\n"
"v_accvgpr_read_b32 v195, a75\n"
"v_accvgpr_read_b32 v196, a76\n"
"v_accvgpr_read_b32 v197, a77\n"
"v_accvgpr_read_b32 v198, a78\n"
"v_accvgpr_read_b32 v199, a79\n"
"v_mul_f32_dpp v192, v89, v192 row_newbcast:0 row_mask:0xf bank_mask:0xf\n"
"v_mul_f32_dpp v193, v89, v193 row_newbcast:4 row_mask:0xf bank_mask:0xf\n"
"v_mul_f32_dpp v194, v89, v194 row_newbcast:8 row_mask:0xf bank_mask:0xf\n"
"v_mul_f32_dpp v195, v89, v195 row_newbcast:12 row_mask:0xf bank_mask:0xf\n"
"v_mul_f32_dpp v196, v89, v196 row_newbcast:0 row_mask:0xf bank_mask:0xf\n"
"v_mul_f32_dpp v197, v89, v197 row_newbcast:4 row_mask:0xf bank_mask:0xf\n"
"v_mul_f32_dpp v198, v89, v198 row_newbcast:8 row_mask:0xf bank_mask:0xf\n"
"v_mul_f32_dpp v199, v89, v199 row_newbcast:12 row_mask:0xf bank_mask:0xf\n"
"v_accvgpr_write_b32 a72, v192\n"
"v_accvgpr_write_b32 a73, v193\n"
"v_accvgpr_write_b32 a74, v194\n"
"v_accvgpr_write_b32 a75, v195\n"
"v_accvgpr_write_b32 a76, v196\n"
"v_accvgpr_write_b32 a77, v197\n"
"v_accvgpr_write_b32 a78, v198\n"
"v_accvgpr_write_b32 a79, v199\n"
"v_accvgpr_read_b32 v192, a80\n"
"v_accvgpr_read_b32 v193, a81\n"
"v_accvgpr_read_b32 v194, a82\n"
"v_accvgpr_read_b32 v195, a83\n"
"v_accvgpr_read_b32 v196, a84\n"
"v_accvgpr_read_b32 v197, a85\n"
"v_accvgpr_read_b32 v198, a86\n"
"v_accvgpr_read_b32 v199, a87\n"
"v_mul_f32_dpp v192, v89, v192 row_newbcast:0 row_mask:0xf bank_mask:0xf\n"
"v_mul_f32_dpp v193, v89, v193 row_newbcast:4 row_mask:0xf bank_mask:0xf\n"
"v_mul_f32_dpp v194, v89, v194 row_newbcast:8 row_mask:0xf bank_mask:0xf\n"
"v_mul_f32_dpp v195, v89, v195 row_newbcast:12 row_mask:0xf bank_mask:0xf\n"
"v_mul_f32_dpp v196, v89, v196 row_newbcast:0 row_mask:0xf bank_mask:0xf\n"
"v_mul_f32_dpp v197, v89, v197 row_newbcast:4 row_mask:0xf bank_mask:0xf\n"
"v_mul_f32_dpp v198, v89, v198 row_newbcast:8 row_mask:0xf bank_mask:0xf\n"
"v_mul_f32_dpp v199, v89, v199 row_newbcast:12 row_mask:0xf bank_mask:0xf\n"
"v_accvgpr_write_b32 a80, v192\n"
"v_accvgpr_write_b32 a81, v193\n"
"v_accvgpr_write_b32 a82, v194\n"
"v_accvgpr_write_b32 a83, v195\n"
"v_accvgpr_write_b32 a84, v196\n"
"v_accvgpr_write_b32 a85, v197\n"
"v_accvgpr_write_b32 a86, v198\n"
"v_accvgpr_write_b32 a87, v199\n"
"v_accvgpr_read_b32 v192, a88\n"
"v_accvgpr_read_b32 v193, a89\n"
"v_accvgpr_read_b32 v194, a90\n"
"v_accvgpr_read_b32 v195, a91\n"
"v_accvgpr_read_b32 v196, a92\n"
"v_accvgpr_read_b32 v197, a93\n"
"v_accvgpr_read_b32 v198, a94\n"
"v_accvgpr_read_b32 v199, a95\n"
"v_mul_f32_dpp v192, v89, v192 row_newbcast:0 row_mask:0xf bank_mask:0xf\n"
"v_mul_f32_dpp v193, v89, v193 row_newbcast:4 row_mask:0xf bank_mask:0xf\n"
"v_mul_f32_dpp v194, v89, v194 row_newbcast:8 row_mask:0xf bank_mask:0xf\n"
"v_mul_f32_dpp v195, v89, v195 row_newbcast:12 row_mask:0xf bank_mask:0xf\n"
"v_mul_f32_dpp v196, v89, v196 row_newbcast:0 row_mask:0xf bank_mask:0xf\n"
"v_mul_f32_dpp v197, v89, v197 row_newbcast:4 row_mask:0xf bank_mask:0xf\n"
"v_mul_f32_dpp v198, v89, v198 row_newbcast:8 row_mask:0xf bank_mask:0xf\n"
"v_mul_f32_dpp v199, v89, v199 row_newbcast:12 row_mask:0xf bank_mask:0xf\n"
"v_accvgpr_write_b32 a88, v192\n"
"v_accvgpr_write_b32 a89, v193\n"
"v_accvgpr_write_b32 a90, v194\n"
"v_accvgpr_write_b32 a91, v195\n"
"v_accvgpr_write_b32 a92, v196\n"
"v_accvgpr_write_b32 a93, v197\n"
"v_accvgpr_write_b32 a94, v198\n"
"v_accvgpr_write_b32 a95, v199\n"
"v_accvgpr_read_b32 v192, a96\n"
"v_accvgpr_read_b32 v193, a97\n"
"v_accvgpr_read_b32 v194, a98\n"
"v_accvgpr_read_b32 v195, a99\n"
"v_accvgpr_read_b32 v196, a100\n"
"v_accvgpr_read_b32 v197, a101\n"
"v_accvgpr_read_b32 v198, a102\n"
"v_accvgpr_read_b32 v199, a103\n"
"s_waitcnt lgkmcnt(1)\n"
"v_mul_f32_dpp v192, v90, v192 row_newbcast:0 row_mask:0xf bank_mask:0xf\n"
"v_mul_f32_dpp v193, v90, v193 row_newbcast:4 row_mask:0xf bank_mask:0xf\n"
"v_mul_f32_dpp v194, v90, v194 row_newbcast:8 row_mask:0xf bank_mask:0xf\n"
"v_mul_f32_dpp v195, v90, v195 row_newbcast:12 row_mask:0xf bank_mask:0xf\n"
"v_mul_f32_dpp v196, v90, v196 row_newbcast:0 row_mask:0xf bank_mask:0xf\n"
"v_mul_f32_dpp v197, v90, v197 row_newbcast:4 row_mask:0xf bank_mask:0xf\n"
"v_mul_f32_dpp v198, v90, v198 row_newbcast:8 row_mask:0xf bank_mask:0xf\n"
"v_mul_f32_dpp v199, v90, v199 row_newbcast:12 row_mask:0xf bank_mask:0xf\n"
"v_accvgpr_write_b32 a96, v192\n"
"v_accvgpr_write_b32 a97, v193\n"
"v_accvgpr_write_b32 a98, v194\n"
"v_accvgpr_write_b32 a99, v195\n"
"v_accvgpr_write_b32 a100, v196\n"
"v_accvgpr_write_b32 a101, v197\n"
"v_accvgpr_write_b32 a102, v198\n"
"v_accvgpr_write_b32 a103, v199\n"
"v_accvgpr_read_b32 v192, a104\n"
"v_accvgpr_read_b32 v193, a105\n"
"v_accvgpr_read_b32 v194, a106\n"
"v_accvgpr_read_b32 v195, a107\n"
"v_accvgpr_read_b32 v196, a108\n"
"v_accvgpr_read_b32 v197, a109\n"
"v_accvgpr_read_b32 v198, a110\n"
"v_accvgpr_read_b32 v199, a111\n"
"v_mul_f32_dpp v192, v90, v192 row_newbcast:0 row_mask:0xf bank_mask:0xf\n"
"v_mul_f32_dpp v193, v90, v193 row_newbcast:4 row_mask:0xf bank_mask:0xf\n"
"v_mul_f32_dpp v194, v90, v194 row_newbcast:8 row_mask:0xf bank_mask:0xf\n"
"v_mul_f32_dpp v195, v90, v195 row_newbcast:12 row_mask:0xf bank_mask:0xf\n"
"v_mul_f32_dpp v196, v90, v196 row_newbcast:0 row_mask:0xf bank_mask:0xf\n"
"v_mul_f32_dpp v197, v90, v197 row_newbcast:4 row_mask:0xf bank_mask:0xf\n"
"v_mul_f32_dpp v198, v90, v198 row_newbcast:8 row_mask:0xf bank_mask:0xf\n"
"v_mul_f32_dpp v199, v90, v199 row_newbcast:12 row_mask:0xf bank_mask:0xf\n"
"v_accvgpr_write_b32 a104, v192\n"
"v_accvgpr_write_b32 a105, v193\n"
"v_accvgpr_write_b32 a106, v194\n"
"v_accvgpr_write_b32 a107, v195\n"
"v_accvgpr_write_b32 a108, v196\n"
"v_accvgpr_write_b32 a109, v197\n"
"v_accvgpr_write_b32 a110, v198\n"
"v_accvgpr_write_b32 a111, v199\n"
"v_accvgpr_read_b32 v192, a112\n"
"v_accvgpr_read_b32 v193, a113\n"
"v_accvgpr_read_b32 v194, a114\n"
"v_accvgpr_read_b32 v195, a115\n"
"v_accvgpr_read_b32 v196, a116\n"
"v_accvgpr_read_b32 v197, a117\n"
"v_accvgpr_read_b32 v198, a118\n"
"v_accvgpr_read_b32 v199, a119\n"
"v_mul_f32_dpp v192, v90, v192 row_newbcast:0 row_mask:0xf bank_mask:0xf\n"
"v_mul_f32_dpp v193, v90, v193 row_newbcast:4 row_mask:0xf bank_mask:0xf\n"
"v_mul_f32_dpp v194, v90, v194 row_newbcast:8 row_mask:0xf bank_mask:0xf\n"
"v_mul_f32_dpp v195, v90, v195 row_newbcast:12 row_mask:0xf bank_mask:0xf\n"
"v_mul_f32_dpp v196, v90, v196 row_newbcast:0 row_mask:0xf bank_mask:0xf\n"
"v_mul_f32_dpp v197, v90, v197 row_newbcast:4 row_mask:0xf bank_mask:0xf\n"
"v_mul_f32_dpp v198, v90, v198 row_newbcast:8 row_mask:0xf bank_mask:0xf\n"
"v_mul_f32_dpp v199, v90, v199 row_newbcast:12 row_mask:0xf bank_mask:0xf\n"
"v_accvgpr_write_b32 a112, v192\n"
"v_accvgpr_write_b32 a113, v193\n"
"v_accvgpr_write_b32 a114, v194\n"
"v_accvgpr_write_b32 a115, v195\n"
"v_accvgpr_write_b32 a116, v196\n"
"v_accvgpr_write_b32 a117, v197\n"
"v_accvgpr_write_b32 a118, v198\n"
"v_accvgpr_write_b32 a119, v199\n"
"v_accvgpr_read_b32 v192, a120\n"
"v_accvgpr_read_b32 v193, a121\n"
"v_accvgpr_read_b32 v194, a122\n"
"v_accvgpr_read_b32 v195, a123\n"
"v_accvgpr_read_b32 v196, a124\n"
"v_accvgpr_read_b32 v197, a125\n"
"v_accvgpr_read_b32 v198, a126\n"
"v_accvgpr_read_b32 v199, a127\n"
"v_mul_f32_dpp v192, v90, v192 row_newbcast:0 row_mask:0xf bank_mask:0xf\n"
"v_mul_f32_dpp v193, v90, v193 row_newbcast:4 row_mask:0xf bank_mask:0xf\n"
"v_mul_f32_dpp v194, v90, v194 row_newbcast:8 row_mask:0xf bank_mask:0xf\n"
"v_mul_f32_dpp v195, v90, v195 row_newbcast:12 row_mask:0xf bank_mask:0xf\n"
"v_mul_f32_dpp v196, v90, v196 row_newbcast:0 row_mask:0xf bank_mask:0xf\n"
"v_mul_f32_dpp v197, v90, v197 row_newbcast:4 row_mask:0xf bank_mask:0xf\n"
"v_mul_f32_dpp v198, v90, v198 row_newbcast:8 row_mask:0xf bank_mask:0xf\n"
"v_mul_f32_dpp v199, v90, v199 row_newbcast:12 row_mask:0xf bank_mask:0xf\n"
"v_accvgpr_write_b32 a120, v192\n"
"v_accvgpr_write_b32 a121, v193\n"
"v_accvgpr_write_b32 a122, v194\n"
"v_accvgpr_write_b32 a123, v195\n"
"v_accvgpr_write_b32 a124, v196\n"
"v_accvgpr_write_b32 a125, v197\n"
"v_accvgpr_write_b32 a126, v198\n"
"v_accvgpr_write_b32 a127, v199\n"
"v_accvgpr_read_b32 v192, a128\n"
"v_accvgpr_read_b32 v193, a129\n"
"v_accvgpr_read_b32 v194, a130\n"
"v_accvgpr_read_b32 v195, a131\n"
"v_accvgpr_read_b32 v196, a132\n"
"v_accvgpr_read_b32 v197, a133\n"
"v_accvgpr_read_b32 v198, a134\n"
"v_accvgpr_read_b32 v199, a135\n"
"v_mul_f32_dpp v192, v90, v192 row_newbcast:0 row_mask:0xf bank_mask:0xf\n"
"v_mul_f32_dpp v193, v90, v193 row_newbcast:4 row_mask:0xf bank_mask:0xf\n"
"v_mul_f32_dpp v194, v90, v194 row_newbcast:8 row_mask:0xf bank_mask:0xf\n"
"v_mul_f32_dpp v195, v90, v195 row_newbcast:12 row_mask:0xf bank_mask:0xf\n"
"v_mul_f32_dpp v196, v90, v196 row_newbcast:0 row_mask:0xf bank_mask:0xf\n"
"v_mul_f32_dpp v197, v90, v197 row_newbcast:4 row_mask:0xf bank_mask:0xf\n"
"v_mul_f32_dpp v198, v90, v198 row_newbcast:8 row_mask:0xf bank_mask:0xf\n"
"v_mul_f32_dpp v199, v90, v199 row_newbcast:12 row_mask:0xf bank_mask:0xf\n"
"v_accvgpr_write_b32 a128, v192\n"
"v_accvgpr_write_b32 a129, v193\n"
"v_accvgpr_write_b32 a130, v194\n"
"v_accvgpr_write_b32 a131, v195\n"
"v_accvgpr_write_b32 a132, v196\n"
"v_accvgpr_write_b32 a133, v197\n"
"v_accvgpr_write_b32 a134, v198\n"
"v_accvgpr_write_b32 a135, v199\n"
"v_accvgpr_read_b32 v192, a136\n"
"v_accvgpr_read_b32 v193, a137\n"
"v_accvgpr_read_b32 v194, a138\n"
"v_accvgpr_read_b32 v195, a139\n"
"v_accvgpr_read_b32 v196, a140\n"
"v_accvgpr_read_b32 v197, a141\n"
"v_accvgpr_read_b32 v198, a142\n"
"v_accvgpr_read_b32 v199, a143\n"
"v_mul_f32_dpp v192, v90, v192 row_newbcast:0 row_mask:0xf bank_mask:0xf\n"
"v_mul_f32_dpp v193, v90, v193 row_newbcast:4 row_mask:0xf bank_mask:0xf\n"
"v_mul_f32_dpp v194, v90, v194 row_newbcast:8 row_mask:0xf bank_mask:0xf\n"
"v_mul_f32_dpp v195, v90, v195 row_newbcast:12 row_mask:0xf bank_mask:0xf\n"
"v_mul_f32_dpp v196, v90, v196 row_newbcast:0 row_mask:0xf bank_mask:0xf\n"
"v_mul_f32_dpp v197, v90, v197 row_newbcast:4 row_mask:0xf bank_mask:0xf\n"
"v_mul_f32_dpp v198, v90, v198 row_newbcast:8 row_mask:0xf bank_mask:0xf\n"
"v_mul_f32_dpp v199, v90, v199 row_newbcast:12 row_mask:0xf bank_mask:0xf\n"
"v_accvgpr_write_b32 a136, v192\n"
"v_accvgpr_write_b32 a137, v193\n"
"v_accvgpr_write_b32 a138, v194\n"
"v_accvgpr_write_b32 a139, v195\n"
"v_accvgpr_write_b32 a140, v196\n"
"v_accvgpr_write_b32 a141, v197\n"
"v_accvgpr_write_b32 a142, v198\n"
"v_accvgpr_write_b32 a143, v199\n"
"v_accvgpr_read_b32 v192, a144\n"
"v_accvgpr_read_b32 v193, a145\n"
"v_accvgpr_read_b32 v194, a146\n"
"v_accvgpr_read_b32 v195, a147\n"
"v_accvgpr_read_b32 v196, a148\n"
"v_accvgpr_read_b32 v197, a149\n"
"v_accvgpr_read_b32 v198, a150\n"
"v_accvgpr_read_b32 v199, a151\n"
"s_waitcnt lgkmcnt(0)\n"
"v_mul_f32_dpp v192, v91, v192 row_newbcast:0 row_mask:0xf bank_mask:0xf\n"
"v_mul_f32_dpp v193, v91, v193 row_newbcast:4 row_mask:0xf bank_mask:0xf\n"
"v_mul_f32_dpp v194, v91, v194 row_newbcast:8 row_mask:0xf bank_mask:0xf\n"
"v_mul_f32_dpp v195, v91, v195 row_newbcast:12 row_mask:0xf bank_mask:0xf\n"
"v_mul_f32_dpp v196, v91, v196 row_newbcast:0 row_mask:0xf bank_mask:0xf\n"
"v_mul_f32_dpp v197, v91, v197 row_newbcast:4 row_mask:0xf bank_mask:0xf\n"
"v_mul_f32_dpp v198, v91, v198 row_newbcast:8 row_mask:0xf bank_mask:0xf\n"
"v_mul_f32_dpp v199, v91, v199 row_newbcast:12 row_mask:0xf bank_mask:0xf\n"
"v_accvgpr_write_b32 a144, v192\n"
"v_accvgpr_write_b32 a145, v193\n"
"v_accvgpr_write_b32 a146, v194\n"
"v_accvgpr_write_b32 a147, v195\n"
"v_accvgpr_write_b32 a148, v196\n"
"v_accvgpr_write_b32 a149, v197\n"
"v_accvgpr_write_b32 a150, v198\n"
"v_accvgpr_write_b32 a151, v199\n"
"v_accvgpr_read_b32 v192, a152\n"
"v_accvgpr_read_b32 v193, a153\n"
"v_accvgpr_read_b32 v194, a154\n"
"v_accvgpr_read_b32 v195, a155\n"
"v_accvgpr_read_b32 v196, a156\n"
"v_accvgpr_read_b32 v197, a157\n"
"v_accvgpr_read_b32 v198, a158\n"
"v_accvgpr_read_b32 v199, a159\n"
"v_mul_f32_dpp v192, v91, v192 row_newbcast:0 row_mask:0xf bank_mask:0xf\n"
"v_mul_f32_dpp v193, v91, v193 row_newbcast:4 row_mask:0xf bank_mask:0xf\n"
"v_mul_f32_dpp v194, v91, v194 row_newbcast:8 row_mask:0xf bank_mask:0xf\n"
"v_mul_f32_dpp v195, v91, v195 row_newbcast:12 row_mask:0xf bank_mask:0xf\n"
"v_mul_f32_dpp v196, v91, v196 row_newbcast:0 row_mask:0xf bank_mask:0xf\n"
"v_mul_f32_dpp v197, v91, v197 row_newbcast:4 row_mask:0xf bank_mask:0xf\n"
"v_mul_f32_dpp v198, v91, v198 row_newbcast:8 row_mask:0xf bank_mask:0xf\n"
"v_mul_f32_dpp v199, v91, v199 row_newbcast:12 row_mask:0xf bank_mask:0xf\n"
"v_accvgpr_write_b32 a152, v192\n"
"v_accvgpr_write_b32 a153, v193\n"
"v_accvgpr_write_b32 a154, v194\n"
"v_accvgpr_write_b32 a155, v195\n"
"v_accvgpr_write_b32 a156, v196\n"
"v_accvgpr_write_b32 a157, v197\n"
"v_accvgpr_write_b32 a158, v198\n"
"v_accvgpr_write_b32 a159, v199\n"
"v_accvgpr_read_b32 v192, a160\n"
"v_accvgpr_read_b32 v193, a161\n"
"v_accvgpr_read_b32 v194, a162\n"
"v_accvgpr_read_b32 v195, a163\n"
"v_accvgpr_read_b32 v196, a164\n"
"v_accvgpr_read_b32 v197, a165\n"
"v_accvgpr_read_b32 v198, a166\n"
"v_accvgpr_read_b32 v199, a167\n"
"v_mul_f32_dpp v192, v91, v192 row_newbcast:0 row_mask:0xf bank_mask:0xf\n"
"v_mul_f32_dpp v193, v91, v193 row_newbcast:4 row_mask:0xf bank_mask:0xf\n"
"v_mul_f32_dpp v194, v91, v194 row_newbcast:8 row_mask:0xf bank_mask:0xf\n"
"v_mul_f32_dpp v195, v91, v195 row_newbcast:12 row_mask:0xf bank_mask:0xf\n"
"v_mul_f32_dpp v196, v91, v196 row_newbcast:0 row_mask:0xf bank_mask:0xf\n"
"v_mul_f32_dpp v197, v91, v197 row_newbcast:4 row_mask:0xf bank_mask:0xf\n"
"v_mul_f32_dpp v198, v91, v198 row_newbcast:8 row_mask:0xf bank_mask:0xf\n"
"v_mul_f32_dpp v199, v91, v199 row_newbcast:12 row_mask:0xf bank_mask:0xf\n"
"v_accvgpr_write_b32 a160, v192\n"
"v_accvgpr_write_b32 a161, v193\n"
"v_accvgpr_write_b32 a162, v194\n"
"v_accvgpr_write_b32 a163, v195\n"
"v_accvgpr_write_b32 a164, v196\n"
"v_accvgpr_write_b32 a165, v197\n"
"v_accvgpr_write_b32 a166, v198\n"
"v_accvgpr_write_b32 a167, v199\n"
"v_accvgpr_read_b32 v192, a168\n"
"v_accvgpr_read_b32 v193, a169\n"
"v_accvgpr_read_b32 v194, a170\n"
"v_accvgpr_read_b32 v195, a171\n"
"v_accvgpr_read_b32 v196, a172\n"
"v_accvgpr_read_b32 v197, a173\n"
"v_accvgpr_read_b32 v198, a174\n"
"v_accvgpr_read_b32 v199, a175\n"
"v_mul_f32_dpp v192, v91, v192 row_newbcast:0 row_mask:0xf bank_mask:0xf\n"
"v_mul_f32_dpp v193, v91, v193 row_newbcast:4 row_mask:0xf bank_mask:0xf\n"
"v_mul_f32_dpp v194, v91, v194 row_newbcast:8 row_mask:0xf bank_mask:0xf\n"
"v_mul_f32_dpp v195, v91, v195 row_newbcast:12 row_mask:0xf bank_mask:0xf\n"
"v_mul_f32_dpp v196, v91, v196 row_newbcast:0 row_mask:0xf bank_mask:0xf\n"
"v_mul_f32_dpp v197, v91, v197 row_newbcast:4 row_mask:0xf bank_mask:0xf\n"
"v_mul_f32_dpp v198, v91, v198 row_newbcast:8 row_mask:0xf bank_mask:0xf\n"
"v_mul_f32_dpp v199, v91, v199 row_newbcast:12 row_mask:0xf bank_mask:0xf\n"
"v_accvgpr_write_b32 a168, v192\n"
"v_accvgpr_write_b32 a169, v193\n"
"v_accvgpr_write_b32 a170, v194\n"
"v_accvgpr_write_b32 a171, v195\n"
"v_accvgpr_write_b32 a172, v196\n"
"v_accvgpr_write_b32 a173, v197\n"
"v_accvgpr_write_b32 a174, v198\n"
"v_accvgpr_write_b32 a175, v199\n"
"v_accvgpr_read_b32 v192, a176\n"
"v_accvgpr_read_b32 v193, a177\n"
"v_accvgpr_read_b32 v194, a178\n"
"v_accvgpr_read_b32 v195, a179\n"
"v_accvgpr_read_b32 v196, a180\n"
"v_accvgpr_read_b32 v197, a181\n"
"v_accvgpr_read_b32 v198, a182\n"
"v_accvgpr_read_b32 v199, a183\n"
"v_mul_f32_dpp v192, v91, v192 row_newbcast:0 row_mask:0xf bank_mask:0xf\n"
"v_mul_f32_dpp v193, v91, v193 row_newbcast:4 row_mask:0xf bank_mask:0xf\n"
"v_mul_f32_dpp v194, v91, v194 row_newbcast:8 row_mask:0xf bank_mask:0xf\n"
"v_mul_f32_dpp v195, v91, v195 row_newbcast:12 row_mask:0xf bank_mask:0xf\n"
"v_mul_f32_dpp v196, v91, v196 row_newbcast:0 row_mask:0xf bank_mask:0xf\n"
"v_mul_f32_dpp v197, v91, v197 row_newbcast:4 row_mask:0xf bank_mask:0xf\n"
"v_mul_f32_dpp v198, v91, v198 row_newbcast:8 row_mask:0xf bank_mask:0xf\n"
"v_mul_f32_dpp v199, v91, v199 row_newbcast:12 row_mask:0xf bank_mask:0xf\n"
"v_accvgpr_write_b32 a176, v192\n"
"v_accvgpr_write_b32 a177, v193\n"
"v_accvgpr_write_b32 a178, v194\n"
"v_accvgpr_write_b32 a179, v195\n"
"v_accvgpr_write_b32 a180, v196\n"
"v_accvgpr_write_b32 a181, v197\n"
"v_accvgpr_write_b32 a182, v198\n"
"v_accvgpr_write_b32 a183, v199\n"
"v_accvgpr_read_b32 v192, a184\n"
"v_accvgpr_read_b32 v193, a185\n"
"v_accvgpr_read_b32 v194, a186\n"
"v_accvgpr_read_b32 v195, a187\n"
"v_accvgpr_read_b32 v196, a188\n"
"v_accvgpr_read_b32 v197, a189\n"
"v_accvgpr_read_b32 v198, a190\n"
"v_accvgpr_read_b32 v199, a191\n"
"v_mul_f32_dpp v192, v91, v192 row_newbcast:0 row_mask:0xf bank_mask:0xf\n"
"v_mul_f32_dpp v193, v91, v193 row_newbcast:4 row_mask:0xf bank_mask:0xf\n"
"v_mul_f32_dpp v194, v91, v194 row_newbcast:8 row_mask:0xf bank_mask:0xf\n"
"v_mul_f32_dpp v195, v91, v195 row_newbcast:12 row_mask:0xf bank_mask:0xf\n"
"v_mul_f32_dpp v196, v91, v196 row_newbcast:0 row_mask:0xf bank_mask:0xf\n"
"v_mul_f32_dpp v197, v91, v197 row_newbcast:4 row_mask:0xf bank_mask:0xf\n"
"v_mul_f32_dpp v198, v91, v198 row_newbcast:8 row_mask:0xf bank_mask:0xf\n"
"v_mul_f32_dpp v199, v91, v199 row_newbcast:12 row_mask:0xf bank_mask:0xf\n"
"v_accvgpr_write_b32 a184, v192\n"
"v_accvgpr_write_b32 a185, v193\n"
"v_accvgpr_write_b32 a186, v194\n"
"v_accvgpr_write_b32 a187, v195\n"
"v_accvgpr_write_b32 a188, v196\n"
"v_accvgpr_write_b32 a189, v197\n"
"v_accvgpr_write_b32 a190, v198\n"
"v_accvgpr_write_b32 a191, v199\n"
"v_sub_f32 v96, v96, v80\n"
"v_sub_f32 v97, v97, v80\n"
"v_sub_f32 v98, v98, v80\n"
"v_sub_f32 v99, v99, v80\n"
"v_sub_f32 v36, v36, v80\n"
"v_sub_f32 v37, v37, v80\n"
"v_sub_f32 v38, v38, v80\n"
"v_sub_f32 v39, v39, v80\n"
"v_sub_f32 v0, v0, v80\n"
"v_sub_f32 v1, v1, v80\n"
"v_sub_f32 v2, v2, v80\n"
"v_sub_f32 v3, v3, v80\n"
"v_sub_f32 v4, v4, v80\n"
"v_sub_f32 v5, v5, v80\n"
"v_sub_f32 v6, v6, v80\n"
"v_sub_f32 v7, v7, v80\n"
"v_sub_f32 v64, v64, v80\n"
"v_sub_f32 v65, v65, v80\n"
"v_sub_f32 v66, v66, v80\n"
"v_sub_f32 v67, v67, v80\n"
"v_sub_f32 v100, v100, v81\n"
"v_sub_f32 v101, v101, v81\n"
"v_sub_f32 v102, v102, v81\n"
"v_sub_f32 v103, v103, v81\n"
"v_sub_f32 v44, v44, v81\n"
"v_sub_f32 v45, v45, v81\n"
"v_sub_f32 v46, v46, v81\n"
"v_sub_f32 v47, v47, v81\n"
"v_sub_f32 v8, v8, v81\n"
"v_sub_f32 v9, v9, v81\n"
"v_sub_f32 v10, v10, v81\n"
"v_sub_f32 v11, v11, v81\n"
"v_sub_f32 v12, v12, v81\n"
"v_sub_f32 v13, v13, v81\n"
"v_sub_f32 v14, v14, v81\n"
"v_sub_f32 v15, v15, v81\n"
"v_sub_f32 v68, v68, v81\n"
"v_sub_f32 v69, v69, v81\n"
"v_sub_f32 v70, v70, v81\n"
"v_sub_f32 v71, v71, v81\n"
"v_sub_f32 v104, v104, v82\n"
"v_sub_f32 v105, v105, v82\n"
"v_sub_f32 v106, v106, v82\n"
"v_sub_f32 v107, v107, v82\n"
"v_sub_f32 v52, v52, v82\n"
"v_sub_f32 v53, v53, v82\n"
"v_sub_f32 v54, v54, v82\n"
"v_sub_f32 v55, v55, v82\n"
"v_sub_f32 v16, v16, v82\n"
"v_sub_f32 v17, v17, v82\n"
"v_sub_f32 v18, v18, v82\n"
"v_sub_f32 v19, v19, v82\n"
"v_sub_f32 v20, v20, v82\n"
"v_sub_f32 v21, v21, v82\n"
"v_sub_f32 v22, v22, v82\n"
"v_sub_f32 v23, v23, v82\n"
"v_sub_f32 v72, v72, v82\n"
"v_sub_f32 v73, v73, v82\n"
"v_sub_f32 v74, v74, v82\n"
"v_sub_f32 v75, v75, v82\n"
"v_sub_f32 v108, v108, v83\n"
"v_sub_f32 v109, v109, v83\n"
"v_sub_f32 v110, v110, v83\n"
"v_sub_f32 v111, v111, v83\n"
"v_sub_f32 v60, v60, v83\n"
"v_sub_f32 v61, v61, v83\n"
"v_sub_f32 v62, v62, v83\n"
"v_sub_f32 v63, v63, v83\n"
"v_sub_f32 v24, v24, v83\n"
"v_sub_f32 v25, v25, v83\n"
"v_sub_f32 v26, v26, v83\n"
"v_sub_f32 v27, v27, v83\n"
"v_sub_f32 v28, v28, v83\n"
"v_sub_f32 v29, v29, v83\n"
"v_sub_f32 v30, v30, v83\n"
"v_sub_f32 v31, v31, v83\n"
"v_sub_f32 v76, v76, v83\n"
"v_sub_f32 v77, v77, v83\n"
"v_sub_f32 v78, v78, v83\n"
"v_sub_f32 v79, v79, v83\n"
"v_exp_f32 v192, v96\n"
"s_nop 0\n"
"v_add_f32 v204, v200, v192\n"
"v_exp_f32 v193, v97\n"
"s_nop 0\n"
"v_add_f32 v204, v204, v193\n"
"v_exp_f32 v194, v98\n"
"v_cvt_pk_bf16_f32 v80, v192, v193\n"
"v_exp_f32 v195, v99\n"
"v_add_f32 v204, v204, v194\n"
"v_exp_f32 v196, v36\n"
"v_add_f32 v204, v204, v195\n"
"v_exp_f32 v197, v37\n"
"v_cvt_pk_bf16_f32 v81, v194, v195\n"
"v_exp_f32 v198, v38\n"
"v_add_f32 v204, v204, v196\n"
"v_add_f32 v204, v204, v197\n"
"v_cvt_pk_bf16_f32 v82, v196, v197\n"
"v_add_f32 v204, v204, v198\n"
"v_exp_f32 v199, v39\n"
"s_nop 0\n"
"v_add_f32 v204, v204, v199\n"
"v_exp_f32 v192, v100\n"
"v_cvt_pk_bf16_f32 v83, v198, v199\n"
"v_exp_f32 v193, v101\n"
"v_add_f32 v205, v201, v192\n"
"v_add_f32 v205, v205, v193\n"
"v_exp_f32 v194, v102\n"
"v_exp_f32 v195, v103\n"
"v_cvt_pk_bf16_f32 v84, v192, v193\n"
"v_exp_f32 v196, v44\n"
"v_add_f32 v205, v205, v194\n"
"v_add_f32 v205, v205, v195\n"
"v_exp_f32 v197, v45\n"
"v_cvt_pk_bf16_f32 v85, v194, v195\n"
"v_exp_f32 v198, v46\n"
"v_exp_f32 v199, v47\n"
"v_add_f32 v205, v205, v196\n"
"v_add_f32 v205, v205, v197\n"
"v_cvt_pk_bf16_f32 v86, v196, v197\n"
"v_exp_f32 v192, v104\n"
"v_add_f32 v205, v205, v198\n"
"v_exp_f32 v193, v105\n"
"v_exp_f32 v194, v106\n"
"v_add_f32 v205, v205, v199\n"
"v_cvt_pk_bf16_f32 v87, v198, v199\n"
"v_add_f32 v206, v202, v192\n"
"v_exp_f32 v195, v107\n"
"v_exp_f32 v196, v52\n"
"v_add_f32 v206, v206, v193\n"
"v_exp_f32 v197, v53\n"
"v_cvt_pk_bf16_f32 v88, v192, v193\n"
"v_add_f32 v206, v206, v194\n"
"v_add_f32 v206, v206, v195\n"
"v_exp_f32 v198, v54\n"
"v_exp_f32 v199, v55\n"
"v_cvt_pk_bf16_f32 v89, v194, v195\n"
"v_exp_f32 v192, v108\n"
"v_add_f32 v206, v206, v196\n"
"v_add_f32 v206, v206, v197\n"
"v_exp_f32 v193, v109\n"
"v_cvt_pk_bf16_f32 v90, v196, v197\n"
"v_exp_f32 v194, v110\n"
"v_exp_f32 v195, v111\n"
"v_add_f32 v206, v206, v198\n"
"v_add_f32 v206, v206, v199\n"
"v_cvt_pk_bf16_f32 v91, v198, v199\n"
"v_exp_f32 v196, v60\n"
"v_add_f32 v207, v203, v192\n"
"v_exp_f32 v197, v61\n"
"v_exp_f32 v198, v62\n"
"v_add_f32 v207, v207, v193\n"
"v_cvt_pk_bf16_f32 v92, v192, v193\n"
"v_exp_f32 v199, v63\n"
"v_add_f32 v207, v207, v194\n"
"v_add_f32 v207, v207, v195\n"
"v_cvt_pk_bf16_f32 v93, v194, v195\n"
"v_add_f32 v207, v207, v196\n"
"v_add_f32 v207, v207, v197\n"
"v_cvt_pk_bf16_f32 v94, v196, v197\n"
"v_add_f32 v207, v207, v198\n"
"v_add_f32 v207, v207, v199\n"
"v_cvt_pk_bf16_f32 v95, v198, v199\n"
"s_nop 2\n"
"s_setpc_b64 s[86:87]\n"
".Lat_rare4:\n"
"s_nop 15\n"
"v_log_f32 v116, v204\n"
"v_log_f32 v117, v205\n"
"v_log_f32 v118, v206\n"
"v_log_f32 v119, v207\n"
"v_max3_f32 v112, v0, v1, v2\n"
"v_max3_f32 v112, v112, v3, v4\n"
"v_max3_f32 v112, v112, v5, v6\n"
"v_max3_f32 v112, v112, v7, v116\n"
"v_max3_f32 v113, v8, v9, v10\n"
"v_max3_f32 v113, v113, v11, v12\n"
"v_max3_f32 v113, v113, v13, v14\n"
"v_max3_f32 v113, v113, v15, v117\n"
"v_max3_f32 v114, v16, v17, v18\n"
"v_max3_f32 v114, v114, v19, v20\n"
"v_max3_f32 v114, v114, v21, v22\n"
"v_max3_f32 v114, v114, v23, v118\n"
"v_max3_f32 v115, v24, v25, v26\n"
"v_max3_f32 v115, v115, v27, v28\n"
"v_max3_f32 v115, v115, v29, v30\n"
"v_max3_f32 v115, v115, v31, v119\n"
"v_mov_b32 v116, v112\n"
"v_mov_b32 v117, v113\n"
"v_mov_b32 v118, v114\n"
"v_mov_b32 v119, v115\n"
"s_nop 1\n"
"v_permlane16_swap_b32 v112, v116\n"
"v_permlane16_swap_b32 v113, v117\n"
"v_permlane16_swap_b32 v114, v118\n"
"v_permlane16_swap_b32 v115, v119\n"
"s_nop 0\n"
"v_max_f32 v112, v112, v116\n"
"v_max_f32 v113, v113, v117\n"
"v_max_f32 v114, v114, v118\n"
"v_max_f32 v115, v115, v119\n"
"v_mov_b32 v116, v112\n"
"v_mov_b32 v117, v113\n"
"v_mov_b32 v118, v114\n"
"v_mov_b32 v119, v115\n"
"s_nop 1\n"
"v_permlane32_swap_b32 v112, v116\n"
"v_permlane32_swap_b32 v113, v117\n"
"v_permlane32_swap_b32 v114, v118\n"
"v_permlane32_swap_b32 v115, v119\n"
"s_nop 0\n"
"v_max_f32 v112, v112, v116\n"
"v_max_f32 v113, v113, v117\n"
"v_max_f32 v114, v114, v118\n"
"v_max_f32 v115, v115, v119\n"
"v_sub_f32 v0, v0, v112\n"
"v_sub_f32 v1, v1, v112\n"
"v_sub_f32 v2, v2, v112\n"
"v_sub_f32 v3, v3, v112\n"
"v_sub_f32 v4, v4, v112\n"
"v_sub_f32 v5, v5, v112\n"
"v_sub_f32 v6, v6, v112\n"
"v_sub_f32 v7, v7, v112\n"
"v_sub_f32 v32, v32, v112\n"
"v_sub_f32 v33, v33, v112\n"
"v_sub_f32 v34, v34, v112\n"
"v_sub_f32 v35, v35, v112\n"
"v_sub_f32 v36, v36, v112\n"
"v_sub_f32 v37, v37, v112\n"
"v_sub_f32 v38, v38, v112\n"
"v_sub_f32 v39, v39, v112\n"
"v_sub_f32 v64, v64, v112\n"
"v_sub_f32 v65, v65, v112\n"
"v_sub_f32 v66, v66, v112\n"
"v_sub_f32 v67, v67, v112\n"
"v_sub_f32 v8, v8, v113\n"
"v_sub_f32 v9, v9, v113\n"
"v_sub_f32 v10, v10, v113\n"
"v_sub_f32 v11, v11, v113\n"
"v_sub_f32 v12, v12, v113\n"
"v_sub_f32 v13, v13, v113\n"
"v_sub_f32 v14, v14, v113\n"
"v_sub_f32 v15, v15, v113\n"
"v_sub_f32 v40, v40, v113\n"
"v_sub_f32 v41, v41, v113\n"
"v_sub_f32 v42, v42, v113\n"
"v_sub_f32 v43, v43, v113\n"
"v_sub_f32 v44, v44, v113\n"
"v_sub_f32 v45, v45, v113\n"
"v_sub_f32 v46, v46, v113\n"
"v_sub_f32 v47, v47, v113\n"
"v_sub_f32 v68, v68, v113\n"
"v_sub_f32 v69, v69, v113\n"
"v_sub_f32 v70, v70, v113\n"
"v_sub_f32 v71, v71, v113\n"
"v_sub_f32 v16, v16, v114\n"
"v_sub_f32 v17, v17, v114\n"
"v_sub_f32 v18, v18, v114\n"
"v_sub_f32 v19, v19, v114\n"
"v_sub_f32 v20, v20, v114\n"
"v_sub_f32 v21, v21, v114\n"
"v_sub_f32 v22, v22, v114\n"
"v_sub_f32 v23, v23, v114\n"
"v_sub_f32 v48, v48, v114\n"
"v_sub_f32 v49, v49, v114\n"
"v_sub_f32 v50, v50, v114\n"
"v_sub_f32 v51, v51, v114\n"
"v_sub_f32 v52, v52, v114\n"
"v_sub_f32 v53, v53, v114\n"
"v_sub_f32 v54, v54, v114\n"
"v_sub_f32 v55, v55, v114\n"
"v_sub_f32 v72, v72, v114\n"
"v_sub_f32 v73, v73, v114\n"
"v_sub_f32 v74, v74, v114\n"
"v_sub_f32 v75, v75, v114\n"
"v_sub_f32 v24, v24, v115\n"
"v_sub_f32 v25, v25, v115\n"
"v_sub_f32 v26, v26, v115\n"
"v_sub_f32 v27, v27, v115\n"
"v_sub_f32 v28, v28, v115\n"
"v_sub_f32 v29, v29, v115\n"
"v_sub_f32 v30, v30, v115\n"
"v_sub_f32 v31, v31, v115\n"
"v_sub_f32 v56, v56, v115\n"
"v_sub_f32 v57, v57, v115\n"
"v_sub_f32 v58, v58, v115\n"
"v_sub_f32 v59, v59, v115\n"
"v_sub_f32 v60, v60, v115\n"
"v_sub_f32 v61, v61, v115\n"
"v_sub_f32 v62, v62, v115\n"
"v_sub_f32 v63, v63, v115\n"
"v_sub_f32 v76, v76, v115\n"
"v_sub_f32 v77, v77, v115\n"
"v_sub_f32 v78, v78, v115\n"
"v_sub_f32 v79, v79, v115\n"
"v_exp_f32 v192, v0\n"
"s_nop 0\n"
"v_add_f32 v200, v204, v192\n"
"v_exp_f32 v193, v1\n"
"s_nop 0\n"
"v_add_f32 v200, v200, v193\n"
"v_exp_f32 v194, v2\n"
"v_cvt_pk_bf16_f32 v96, v192, v193\n"
"v_exp_f32 v195, v3\n"
"v_add_f32 v200, v200, v194\n"
"v_exp_f32 v196, v4\n"
"v_add_f32 v200, v200, v195\n"
"v_exp_f32 v197, v5\n"
"v_cvt_pk_bf16_f32 v97, v194, v195\n"
"v_exp_f32 v198, v6\n"
"v_add_f32 v200, v200, v196\n"
"v_add_f32 v200, v200, v197\n"
"v_cvt_pk_bf16_f32 v98, v196, v197\n"
"v_add_f32 v200, v200, v198\n"
"v_exp_f32 v199, v7\n"
"s_nop 0\n"
"v_add_f32 v200, v200, v199\n"
"v_exp_f32 v192, v8\n"
"v_cvt_pk_bf16_f32 v99, v198, v199\n"
"v_exp_f32 v193, v9\n"
"v_add_f32 v201, v205, v192\n"
"v_add_f32 v201, v201, v193\n"
"v_exp_f32 v194, v10\n"
"v_exp_f32 v195, v11\n"
"v_cvt_pk_bf16_f32 v100, v192, v193\n"
"v_exp_f32 v196, v12\n"
"v_add_f32 v201, v201, v194\n"
"v_add_f32 v201, v201, v195\n"
"v_exp_f32 v197, v13\n"
"v_cvt_pk_bf16_f32 v101, v194, v195\n"
"v_exp_f32 v198, v14\n"
"v_exp_f32 v199, v15\n"
"v_add_f32 v201, v201, v196\n"
"v_add_f32 v201, v201, v197\n"
"v_cvt_pk_bf16_f32 v102, v196, v197\n"
"v_exp_f32 v192, v16\n"
"v_add_f32 v201, v201, v198\n"
"v_exp_f32 v193, v17\n"
"v_exp_f32 v194, v18\n"
"v_add_f32 v201, v201, v199\n"
"v_cvt_pk_bf16_f32 v103, v198, v199\n"
"v_add_f32 v202, v206, v192\n"
"v_exp_f32 v195, v19\n"
"v_exp_f32 v196, v20\n"
"v_add_f32 v202, v202, v193\n"
"v_exp_f32 v197, v21\n"
"v_cvt_pk_bf16_f32 v104, v192, v193\n"
"v_add_f32 v202, v202, v194\n"
"v_add_f32 v202, v202, v195\n"
"v_exp_f32 v198, v22\n"
"v_exp_f32 v199, v23\n"
"v_cvt_pk_bf16_f32 v105, v194, v195\n"
"v_exp_f32 v192, v24\n"
"v_add_f32 v202, v202, v196\n"
"v_add_f32 v202, v202, v197\n"
"v_exp_f32 v193, v25\n"
"v_cvt_pk_bf16_f32 v106, v196, v197\n"
"v_exp_f32 v194, v26\n"
"v_exp_f32 v195, v27\n"
"v_add_f32 v202, v202, v198\n"
"v_add_f32 v202, v202, v199\n"
"v_cvt_pk_bf16_f32 v107, v198, v199\n"
"v_exp_f32 v196, v28\n"
"v_add_f32 v203, v207, v192\n"
"v_exp_f32 v197, v29\n"
"v_exp_f32 v198, v30\n"
"v_add_f32 v203, v203, v193\n"
"v_cvt_pk_bf16_f32 v108, v192, v193\n"
"v_exp_f32 v199, v31\n"
"v_add_f32 v203, v203, v194\n"
"v_add_f32 v203, v203, v195\n"
"v_cvt_pk_bf16_f32 v109, v194, v195\n"
"v_add_f32 v203, v203, v196\n"
"v_add_f32 v203, v203, v197\n"
"v_cvt_pk_bf16_f32 v110, v196, v197\n"
"v_add_f32 v203, v203, v198\n"
"v_add_f32 v203, v203, v199\n"
"v_cvt_pk_bf16_f32 v111, v198, v199\n"
"s_nop 2\n"
"s_setpc_b64 s[86:87]\n"
".Lat_end:\n"

      : "+{v0}"(tid), "+{s[0:1]}"(args), "+{s2}"(bid), "+{s63}"(lds0)
      :
      : "memory", "vcc", "scc",
      "v1", "v2", "v3", "v4", "v5", "v6", "v7", "v8", "v9", "v10", "v11", "v12", "v13", "v14", "v15", "v16", "v17", "v18", "v19", "v20", "v21", "v22", "v23", "v24", "v25", "v26", "v27", "v28", "v29", "v30", "v31", "v32", "v33", "v34", "v35", "v36", "v37", "v38", "v39", "v40", "v41", "v42", "v43", "v44", "v45", "v46", "v47", "v48", "v49", "v50", "v51", "v52", "v53", "v54", "v55", "v56", "v57", "v58", "v59", "v60", "v61", "v62", "v63", "v64", "v65", "v66", "v67", "v68", "v69", "v70", "v71", "v72", "v73", "v74", "v75", "v76", "v77", "v78", "v79", "v80", "v81", "v82", "v83", "v84", "v85", "v86", "v87", "v88", "v89", "v90", "v91", "v92", "v93", "v94", "v95", "v96", "v97", "v98", "v99", "v100", "v101", "v102", "v103", "v104", "v105", "v106", "v107", "v108", "v109", "v110", "v111", "v112", "v113", "v114", "v115", "v116", "v117", "v118", "v119", "v120", "v121", "v122", "v123", "v124", "v125", "v126", "v127", "v128", "v129", "v130", "v131", "v132", "v133", "v134", "v135", "v136", "v137", "v138", "v139", "v140", "v141", "v142", "v143", "v144", "v145", "v146", "v147", "v148", "v149", "v150", "v151", "v152", "v153", "v154", "v155", "v156", "v157", "v158", "v159", "v160", "v161", "v162", "v163", "v164", "v165", "v166", "v167", "v168", "v169", "v170", "v171", "v172", "v173", "v174", "v175", "v176", "v177", "v178", "v179", "v180", "v181", "v182", "v183", "v184", "v185", "v186", "v187", "v188", "v189", "v190", "v191", "v192", "v193", "v194", "v195", "v196", "v197", "v198", "v199", "v200", "v201", "v202", "v203", "v204", "v205", "v206", "v207", "v208", "v209", "v210", "v211", "v212", "v213", "v214", "v215", "v216", "v217", "v218", "v219", "v220", "v221", "v222", "v223", "v224", "v225", "v226", "v227", "v228", "v229", "v230", "v231", "v232", "v233", "v234", "v235", "v236", "v237", "v238", "v239", "v240", "v241", "v242", "v243", "v244", "v245", "v246", "v247", "v248", "v249", "v250", "v251", "v252", "v253", "v254", "v255",
      "a0", "a1", "a2", "a3", "a4", "a5", "a6", "a7", "a8", "a9", "a10", "a11", "a12", "a13", "a14", "a15", "a16", "a17", "a18", "a19", "a20", "a21", "a22", "a23", "a24", "a25", "a26", "a27", "a28", "a29", "a30", "a31", "a32", "a33", "a34", "a35", "a36", "a37", "a38", "a39", "a40", "a41", "a42", "a43", "a44", "a45", "a46", "a47", "a48", "a49", "a50", "a51", "a52", "a53", "a54", "a55", "a56", "a57", "a58", "a59", "a60", "a61", "a62", "a63", "a64", "a65", "a66", "a67", "a68", "a69", "a70", "a71", "a72", "a73", "a74", "a75", "a76", "a77", "a78", "a79", "a80", "a81", "a82", "a83", "a84", "a85", "a86", "a87", "a88", "a89", "a90", "a91", "a92", "a93", "a94", "a95", "a96", "a97", "a98", "a99", "a100", "a101", "a102", "a103", "a104", "a105", "a106", "a107", "a108", "a109", "a110", "a111", "a112", "a113", "a114", "a115", "a116", "a117", "a118", "a119", "a120", "a121", "a122", "a123", "a124", "a125", "a126", "a127", "a128", "a129", "a130", "a131", "a132", "a133", "a134", "a135", "a136", "a137", "a138", "a139", "a140", "a141", "a142", "a143", "a144", "a145", "a146", "a147", "a148", "a149", "a150", "a151", "a152", "a153", "a154", "a155", "a156", "a157", "a158", "a159", "a160", "a161", "a162", "a163", "a164", "a165", "a166", "a167", "a168", "a169", "a170", "a171", "a172", "a173", "a174", "a175", "a176", "a177", "a178", "a179", "a180", "a181", "a182", "a183", "a184", "a185", "a186", "a187", "a188", "a189", "a190", "a191", "a192", "a193", "a194", "a195", "a196", "a197", "a198", "a199", "a200", "a201", "a202", "a203", "a204", "a205", "a206", "a207", "a208", "a209", "a210", "a211", "a212", "a213", "a214", "a215", "a216", "a217", "a218", "a219", "a220", "a221", "a222", "a223", "a224", "a225", "a226", "a227", "a228", "a229", "a230", "a231", "a232", "a233", "a234", "a235", "a236", "a237", "a238", "a239", "a240", "a241", "a242", "a243", "a244", "a245", "a246", "a247", "a248", "a249", "a250", "a251", "a252", "a253", "a254", "a255",
      "s3", "s4", "s5", "s6", "s7", "s8", "s9", "s10", "s11", "s12", "s13", "s14", "s15", "s16", "s17", "s18", "s19", "s20", "s21", "s22", "s23", "s24", "s25", "s26", "s27", "s28", "s29", "s30", "s31", "s33", "s34", "s35", "s36", "s37", "s40", "s41", "s42", "s43", "s44", "s45", "s46", "s47", "s48", "s49", "s50", "s51", "s52", "s53", "s54", "s55", "s56", "s57", "s58", "s59", "s60", "s61", "s62", "s64", "s65", "s66", "s67", "s68", "s69", "s70", "s71", "s72", "s73", "s74", "s75", "s76", "s77", "s78", "s79", "s80", "s81", "s82", "s83", "s84", "s85", "s86", "s87", "s88", "s89", "s90", "s91", "s92", "s93", "s94", "s95", "s96", "s97", "s98", "s99");
}

extern "C" void kernel_launch(void* const* d_in, const int* in_sizes, int n_in, void* d_out, int out_size, void* d_ws,
                              size_t ws_size, hipStream_t stream) {
  const bf16* const* in = (const bf16* const*)d_in;
  attn_kernel<<<dim3(256), dim3(256), 0, stream>>>(in[0], in[3], in[1], in[2], in[4], in[5], in[8], in[6], in[7], in[9], d_ws, (bf16*)d_out);
}
```
